```python
import torch

def _softmax(x, axis=-1):
    return torch.softmax(x, dim=axis)
def _gelu(x):
    return torch.nn.functional.gelu(x, approximate="tanh")
def _silu(x):
    return torch.nn.functional.silu(x)
def _sigmoid(x):
    return torch.sigmoid(x)
def _layernorm(x, axis=-1, eps=1e-5):
    m = x.mean(dim=axis, keepdim=True)
    v = x.var(dim=axis, keepdim=True, unbiased=False)
    return (x - m) / torch.sqrt(v + eps)
def _scatter_add(tgt, idx, src, axis=0):
    return tgt.clone().index_add_(axis, idx, src)
def _segment_sum(src, seg, n):
    out = torch.zeros((n,) + src.shape[1:], dtype=src.dtype)
    return out.index_add_(0, seg, src)
def _pad(x, pw):
    flat = tuple(v for pair in reversed(pw) for v in pair)
    return torch.nn.functional.pad(x, flat)

kernel_name = 'concat_heads__mha_block__sliding_local_dilated__prompt'

def setup_inputs():

    _w = (lambda a: (lambda t: t.to(torch.bfloat16) if t.is_floating_point() else t)(torch.as_tensor(a)))
    def _A_setup():
        B, S, D, H = 1, 2048, 1024, 32
        return {
            "x": _w((lambda *s: torch.randn(*s, dtype=torch.bfloat16) * 0.1)(B, S, D)),
            "wqkv": _w((lambda *s: torch.randn(*s, dtype=torch.bfloat16) * 0.4)(D, 3 * D)),
            "wo": _w((lambda *s: torch.randn(*s, dtype=torch.bfloat16) * 0.8)(D, D)),
        }
    _A_in = _A_setup()

    def _B_setup():
        B, S, D = 1, 2048, 1024
        return {
            "x": _w((lambda *s: torch.randn(*s, dtype=torch.bfloat16) * 0.1)(B, S, D)),
            "wqkv": _w((lambda *s: torch.randn(*s, dtype=torch.bfloat16) * 0.4)(D, 3 * D)),
            "wo": _w((lambda *s: torch.randn(*s, dtype=torch.bfloat16) * 0.8)(D, D)),
        }
    _B_in = _B_setup()

    return {
        "A_x": _A_in["x"],
        "A_wqkv": _A_in["wqkv"],
        "A_wo": _A_in["wo"],
        "B_x": _B_in["x"],
        "B_wqkv": _B_in["wqkv"],
        "B_wo": _B_in["wo"],
    }

def reference(A_x, A_wqkv, A_wo, B_x, B_wqkv, B_wo):
    def _A_ref(x, wqkv, wo):
        B, S, D = x.shape
        H = 32
        Dh = D // H
        qkv = (lambda x, *d: x.permute(*d))((x @ wqkv).reshape(B, S, 3, H, Dh), 2, 0, 3, 1, 4)
        q, k, v = qkv[0], qkv[1], qkv[2]
        scores = torch.einsum("bhqd,bhkd->bhqk", q, k) * (Dh ** -0.5)
        p = _softmax(scores, axis=-1)
        o = torch.einsum("bhqk,bhkd->bhqd", p, v)
        return (lambda x, *d: x.permute(*d))(o, 0, 2, 1, 3).reshape(B, S, D) @ wo

    def _B_ref(x, wqkv, wo):
        B, S, D = x.shape
        H = 32; Dh = D // H
        W, dil = 8, 2
        qkv = (lambda x, *d: x.permute(*d))((x @ wqkv).reshape(B, S, 3, H, Dh), 2, 0, 3, 1, 4)
        q, k, v = qkv[0], qkv[1], qkv[2]
        scale = Dh ** -0.5
        pos = torch.arange(S)
        off = torch.arange(W) * dil
        idx = pos[:, None] - off[None, :]
        mask = idx >= 0
        idx_c = torch.where(mask, idx, 0).reshape(-1)
        kg = k[:, :, idx_c, :].reshape(B, H, S, W, Dh)
        vg = v[:, :, idx_c, :].reshape(B, H, S, W, Dh)
        logits = torch.einsum("bhsd,bhswd->bhsw", q, kg) * scale
        logits = torch.where(mask[None, None, :, :], logits, -1e9)
        attn = _softmax(logits, axis=-1)
        o = torch.einsum("bhsw,bhswd->bhsd", attn, vg)
        return (lambda x, *d: x.permute(*d))(o, 0, 2, 1, 3).reshape(B, S, D) @ wo

    a = _A_ref(A_x, A_wqkv, A_wo)
    b = _B_ref(B_x, B_wqkv, B_wo)
    return torch.concatenate([a, b], axis=-1)
```

```cpp
#include <hip/hip_runtime.h>
#include <hip/hip_bf16.h>
typedef unsigned short u16;
__device__ __forceinline__ float bf2f(u16 b){ return __uint_as_float(((unsigned)b)<<16); }
__device__ __forceinline__ u16 f2bf(float f){ __hip_bfloat16 h = __float2bfloat16(f); return *(u16*)&h; }

__global__ void gemm_naive(const u16* A, const u16* B, u16* C, int M, int N, int K, int ldc){
  __shared__ float As[16][65], Bs[16][65];
  int tx = threadIdx.x & 15, ty = threadIdx.x >> 4;
  int m0 = blockIdx.y*64, n0 = blockIdx.x*64;
  float acc[4][4] = {};
  for (int k0=0;k0<K;k0+=16){
    for (int i=threadIdx.x;i<16*64;i+=256){ int r=i/16,c=i%16; As[c][r]=bf2f(A[(size_t)(m0+r)*K+k0+c]); }
    for (int i=threadIdx.x;i<16*64;i+=256){ int r=i/64,c=i%64; Bs[r][c]=bf2f(B[(size_t)(k0+r)*N+n0+c]); }
    __syncthreads();
    for (int k=0;k<16;k++){
      float a[4],b[4];
      for(int i=0;i<4;i++){a[i]=As[k][ty*4+i]; b[i]=Bs[k][tx*4+i];}
      for(int i=0;i<4;i++)for(int j=0;j<4;j++)acc[i][j]+=a[i]*b[j];
    }
    __syncthreads();
  }
  for(int i=0;i<4;i++)for(int j=0;j<4;j++) C[(size_t)(m0+ty*4+i)*ldc+n0+tx*4+j]=f2bf(acc[i][j]);
}
__global__ void attnA_naive(const u16* qkv, u16* o){
  int q = blockIdx.x*64 + threadIdx.x; int h = blockIdx.y;
  float qr[32]; for(int d=0;d<32;d++) qr[d]=bf2f(qkv[(size_t)q*3072+h*32+d])*0.17677669529663687f;
  float m=-1e30f,l=0.f,acc[32]; for(int d=0;d<32;d++)acc[d]=0.f;
  for(int k=0;k<2048;k++){
    float s=0; for(int d=0;d<32;d++) s+=qr[d]*bf2f(qkv[(size_t)k*3072+1024+h*32+d]);
    float mn=fmaxf(m,s); float a=__expf(m-mn), p=__expf(s-mn); l=l*a+p;
    for(int d=0;d<32;d++) acc[d]=acc[d]*a+p*bf2f(qkv[(size_t)k*3072+2048+h*32+d]);
    m=mn;
  }
  for(int d=0;d<32;d++) o[(size_t)q*1024+h*32+d]=f2bf(acc[d]/l);
}
__global__ void attnB_naive(const u16* qkv, u16* o){
  int q = blockIdx.x*64 + threadIdx.x; int h = blockIdx.y;
  float qr[32]; for(int d=0;d<32;d++) qr[d]=bf2f(qkv[(size_t)q*3072+h*32+d])*0.17677669529663687f;
  float s[8]; float m=-1e30f;
  for(int w=0;w<8;w++){ int k=q-2*w; if(k>=0){ float t=0; for(int d=0;d<32;d++) t+=qr[d]*bf2f(qkv[(size_t)k*3072+1024+h*32+d]); s[w]=t;} else s[w]=-1e9f; m=fmaxf(m,s[w]); }
  float l=0; for(int w=0;w<8;w++){ s[w]=__expf(s[w]-m); l+=s[w]; }
  for(int d=0;d<32;d++){ float a=0; for(int w=0;w<8;w++){ int k=q-2*w; if(k<0)k=0; a+=s[w]*bf2f(qkv[(size_t)k*3072+2048+h*32+d]); } o[(size_t)q*1024+h*32+d]=f2bf(a/l); }
}
extern "C" void kernel_launch(void* const* d_in, const int* in_sizes, int n_in, void* d_out, int out_size, void* d_ws, size_t ws_size, hipStream_t stream){
  const u16* Ax=(const u16*)d_in[0]; const u16* Aw=(const u16*)d_in[1]; const u16* Ao=(const u16*)d_in[2];
  const u16* Bx=(const u16*)d_in[3]; const u16* Bw=(const u16*)d_in[4]; const u16* Bo=(const u16*)d_in[5];
  u16* out=(u16*)d_out; u16* ws=(u16*)d_ws;
  u16* qkvA=ws; u16* qkvB=ws+2048*3072; u16* oA=qkvB+2048*3072; u16* oB=oA+2048*1024;
  gemm_naive<<<dim3(48,32),256,0,stream>>>(Ax,Aw,qkvA,2048,3072,1024,3072);
  gemm_naive<<<dim3(48,32),256,0,stream>>>(Bx,Bw,qkvB,2048,3072,1024,3072);
  attnA_naive<<<dim3(32,32),64,0,stream>>>(qkvA,oA);
  attnB_naive<<<dim3(32,32),64,0,stream>>>(qkvB,oB);
  gemm_naive<<<dim3(16,32),256,0,stream>>>(oA,Ao,out,2048,1024,1024,2048);
  gemm_naive<<<dim3(16,32),256,0,stream>>>(oB,Bo,out+1024,2048,1024,1024,2048);
}
```

```cpp
#include <hip/hip_runtime.h>
#include <stdint.h>

typedef unsigned short u16;
typedef __bf16 bf16x8 __attribute__((ext_vector_type(8)));
typedef __bf16 bf16x2 __attribute__((ext_vector_type(2)));
typedef short s16x4 __attribute__((ext_vector_type(4)));
typedef float f32x16 __attribute__((ext_vector_type(16)));
typedef float f32x4 __attribute__((ext_vector_type(4)));
typedef float f32x2 __attribute__((ext_vector_type(2)));
typedef unsigned u32x4 __attribute__((ext_vector_type(4)));
typedef unsigned u32x2 __attribute__((ext_vector_type(2)));
#define LAS __attribute__((address_space(3)))
#define DI __device__ __forceinline__
#define SBAR() __builtin_amdgcn_sched_barrier(0)
#define PIN(x) asm volatile("" : "+v"(x))
#define MFMA32(a, b, c) __builtin_amdgcn_mfma_f32_32x32x16_bf16((a), (b), (c), 0, 0, 0)
#define MFMA16(a, b, c) __builtin_amdgcn_mfma_f32_16x16x32_bf16((a), (b), (c), 0, 0, 0)

constexpr int S_LEN = 2048, DM = 1024, NH = 32;
constexpr float QSCALE = 0.17677669529663687f * 1.4426950408889634f;

constexpr size_t WS_QF = 0;
constexpr size_t WS_KV = WS_QF + (4u << 20);
constexpr size_t WS_OF = WS_KV + (8u << 20);
constexpr size_t WS_HALO = WS_OF + (8u << 20);

DI unsigned cvtpk(float lo, float hi) { f32x2 v = {lo, hi}; bf16x2 b = __builtin_convertvector(v, bf16x2); return __builtin_bit_cast(unsigned, b); }
DI u32x4 pack8(const f32x16& a, int s) { u32x4 r; r[0] = cvtpk(a[8 * s], a[8 * s + 1]); r[1] = cvtpk(a[8 * s + 2], a[8 * s + 3]); r[2] = cvtpk(a[8 * s + 4], a[8 * s + 5]); r[3] = cvtpk(a[8 * s + 6], a[8 * s + 7]); return r; }
DI bf16x8 asbf(u32x4 v) { return __builtin_bit_cast(bf16x8, v); }
DI void glds16(const void* sbase, unsigned voff, unsigned lds) {
    asm volatile("s_mov_b32 m0, %2\n\ts_nop 0\n\tglobal_load_lds_dwordx4 %0, %1" :: "v"(voff), "s"(sbase), "s"(lds) : "memory");
}
DI bf16x8 ldsrd128(unsigned addr) { return *(const LAS bf16x8*)(uintptr_t)addr; }
DI s16x4 ldstr(unsigned addr) { return __builtin_amdgcn_ds_read_tr16_b64_v4i16((LAS s16x4*)(uintptr_t)addr); }
DI bf16x8 cat8(s16x4 lo, s16x4 hi) { typedef short s16x8 __attribute__((ext_vector_type(8))); s16x8 v = {lo[0], lo[1], lo[2], lo[3], hi[0], hi[1], hi[2], hi[3]}; return __builtin_bit_cast(bf16x8, v); }
DI float swap_max(float m) { auto r = __builtin_amdgcn_permlane32_swap(__float_as_uint(m), __float_as_uint(m), false, false); return __builtin_fmaxf(__uint_as_float(r[0]), __uint_as_float(r[1])); }
DI float swap_add(float m) { auto r = __builtin_amdgcn_permlane32_swap(__float_as_uint(m), __float_as_uint(m), false, false); return __uint_as_float(r[0]) + __uint_as_float(r[1]); }

DI void local_attn(bf16x8 q0, bf16x8 q1, bf16x8 k0, bf16x8 k1, bf16x8 v0, bf16x8 v1, bf16x8 kp0, bf16x8 kp1, bf16x8 vp1, bool has_prev, int lane, u32x4& o0, u32x4& o1) {
    const int i = lane & 31, h = lane >> 5;
    f32x16 z = {};
    f32x16 so = MFMA32(k0, q0, z); so = MFMA32(k1, q1, so);
    f32x16 sp = MFMA32(kp0, q0, z); sp = MFMA32(kp1, q1, sp);
    const float NEG = -__builtin_inff();
    float m = NEG;
#pragma unroll
    for (int r = 0; r < 16; ++r) { const int j = (r & 3) + 8 * (r >> 2) + 4 * h; const bool ok = (unsigned)(i - j) <= 7u; so[r] = ok ? so[r] : NEG; m = __builtin_fmaxf(m, so[r]); }
#pragma unroll
    for (int r = 12; r < 16; ++r) { const int j = (r & 3) + 24 + 4 * h; const bool ok = has_prev && (j >= i + 25); sp[r] = ok ? sp[r] : NEG; m = __builtin_fmaxf(m, sp[r]); }
    m = swap_max(m);
    float l = 0.f;
#pragma unroll
    for (int r = 0; r < 16; ++r) { so[r] = __builtin_amdgcn_exp2f(so[r] - m); l += so[r]; }
#pragma unroll
    for (int r = 12; r < 16; ++r) { sp[r] = __builtin_amdgcn_exp2f(sp[r] - m); l += sp[r]; }
    l = swap_add(l);
    u32x4 pp = {0u, 0u, cvtpk(sp[12], sp[13]), cvtpk(sp[14], sp[15])};
    f32x16 o = MFMA32(v0, asbf(pack8(so, 0)), z); o = MFMA32(v1, asbf(pack8(so, 1)), o); o = MFMA32(vp1, asbf(pp), o);
    const float inv = 1.0f / l;
#pragma unroll
    for (int r = 0; r < 16; ++r) o[r] *= inv;
    o0 = pack8(o, 0); o1 = pack8(o, 1);
}

constexpr int K1_X = 0, K1_W = 65536, K1_WB = 24576, K1_LDS = K1_W + 3 * K1_WB;
__global__ __launch_bounds__(512, 2) void k1_qkv(const u16* __restrict__ xA, const u16* __restrict__ wA, const u16* __restrict__ xB, const u16* __restrict__ wB, char* __restrict__ ws) {
    __shared__ __attribute__((aligned(1024))) char smem[K1_LDS];
    const unsigned lds0 = (unsigned)(uintptr_t)(LAS char*)smem;
    const int tid = threadIdx.x, lane = tid & 63, wid = __builtin_amdgcn_readfirstlane(tid >> 6);
    const int g = wid >> 2, wi = wid & 3, mw = wi >> 1, hd = wi & 1, h = lane >> 5;
    const int b = blockIdx.x, xcd = b & 7, j = b >> 3, br = xcd >> 2, qd = xcd & 3;
    const int mt = (qd & 1) * 4 + (j & 3), hp = (qd >> 1) * 8 + (j >> 2);
    const char* x = (const char*)(br ? xB : xA) + (size_t)mt * 256 * 2048;
    const char* w = (const char*)(br ? wB : wA) + hp * 128;
    unsigned vx[4], vw[3];
#pragma unroll
    for (int ii = 0; ii < 4; ++ii) {
        const int row = br ? ((wi >> 1) * 64 + (wi & 1) + ii * 16 + 2 * (lane >> 3)) : (wi * 32 + ii * 8 + (lane >> 3));
        vx[ii] = row * 2048 + (((lane & 7) ^ (((ii & 1) * 4 + (lane >> 4)) & 7)) << 4);
    }
#pragma unroll
    for (int jj = 0; jj < 3; ++jj) {
        const int jw = 12 * g + 3 * wi + jj, n = jw >> 3, k0 = (jw & 7) * 8 + (lane >> 3);
        vw[jj] = k0 * 6144 + n * 2048 + (((lane & 7) ^ (((lane >> 4) & 1) << 2)) << 4);
    }
    unsigned ax[4];
    { const int row = mw * 64 + (lane & 31); const unsigned a0 = lds0 + K1_X + g * 32768 + row * 128 + ((h ^ ((row >> 1) & 7)) << 4);
#pragma unroll
      for (int s = 0; s < 4; ++s) ax[s] = a0 ^ (s << 5); }
    unsigned aw;
    { const int q4 = (lane & 15) >> 2, p = lane & 3, cb = (lane >> 4) & 1; const int c = hd * 4 + cb * 2 + (p >> 1);
      aw = lds0 + K1_W + (8 * h + q4) * 128 + ((c ^ (((q4 >> 1) & 1) << 2)) << 4) + (p & 1) * 8; }
    const unsigned xdst_o = lds0 + K1_X + (1 - g) * 32768 + wi * 4096;
    const unsigned wdst = lds0 + K1_W + (12 * g + 3 * wi) * 1024;
    const char* xo = x + (size_t)(1 - g) * 128 * 2048;
#define STAGE_X(src, kt, dst) do { _Pragma("unroll") for (int ii = 0; ii < 4; ++ii) glds16((src) + (kt) * 128, vx[ii], (dst) + ii * 1024); } while (0)
#define STAGE_W(kt, buf) do { _Pragma("unroll") for (int jj = 0; jj < 3; ++jj) glds16(w + (size_t)(kt) * 64 * 6144, vw[jj], wdst + (buf) * K1_WB + jj * 1024); } while (0)
    STAGE_X(xo, 0, xdst_o); STAGE_W(0, 0);
    STAGE_W(1, 1);
    if (g == 1) { STAGE_X(xo, 1, xdst_o + 16384); asm volatile("s_waitcnt vmcnt(7)\n\ts_barrier" ::: "memory"); }
    else { asm volatile("s_waitcnt vmcnt(3)\n\ts_barrier" ::: "memory"); }
    if (g == 1) asm volatile("s_barrier" ::: "memory");
    f32x16 acc[2][3];
#pragma unroll
    for (int mb = 0; mb < 2; ++mb)
#pragma unroll
        for (int n = 0; n < 3; ++n) acc[mb][n] = f32x16{};
    SBAR();
#pragma unroll
    for (int t = 0; t < 16; ++t) {
        bf16x8 xf[2][4], wf[3][4];
        const int tx = t + 1;
        if (g == 0) { if (t + 1 < 16) STAGE_X(xo, t + 1, xdst_o + ((t + 1) & 1) * 16384); }
        else        { if (t + 2 < 16) STAGE_X(xo, t + 2, xdst_o + ((t + 2) & 1) * 16384); }
        if (t + 2 < 16) STAGE_W(t + 2, (t + 2) % 3);
        (void)tx;
#pragma unroll
        for (int s = 0; s < 4; ++s) {
#pragma unroll
            for (int mb = 0; mb < 2; ++mb) xf[mb][s] = ldsrd128(ax[s] + (t & 1) * 16384 + mb * 4096);
#pragma unroll
            for (int n = 0; n < 3; ++n) wf[n][s] = cat8(ldstr(aw + (t % 3) * K1_WB + n * 8192 + s * 2048), ldstr(aw + (t % 3) * K1_WB + n * 8192 + s * 2048 + 512));
        }
        if (t + 2 < 16) asm volatile("s_waitcnt vmcnt(7) lgkmcnt(0)\n\ts_barrier" ::: "memory");
        else if (t == 14 && g == 0) asm volatile("s_waitcnt vmcnt(4) lgkmcnt(0)\n\ts_barrier" ::: "memory");
        else asm volatile("s_waitcnt vmcnt(0) lgkmcnt(0)\n\ts_barrier" ::: "memory");
        SBAR();
        __builtin_amdgcn_s_setprio(1);
#pragma unroll
        for (int s = 0; s < 4; ++s)
#pragma unroll
            for (int mb = 0; mb < 2; ++mb) {
                acc[mb][0] = MFMA32(wf[0][s], xf[mb][s], acc[mb][0]);
                acc[mb][1] = MFMA32(wf[1][s], xf[mb][s], acc[mb][1]);
                acc[mb][2] = MFMA32(xf[mb][s], wf[2][s], acc[mb][2]);
            }
        __builtin_amdgcn_s_setprio(0);
        SBAR();
        asm volatile("s_barrier" ::: "memory");
        SBAR();
    }
    if (g == 0) asm volatile("s_barrier" ::: "memory");
    const int head = 2 * hp + hd, wr = 2 * g + mw, blk0 = mt * 8 + wr * 2;
    u32x4 fq[2][2], fk[2][2], fv[2][2];
#pragma unroll
    for (int mb = 0; mb < 2; ++mb) {
#pragma unroll
        for (int r = 0; r < 16; ++r) acc[mb][0][r] *= QSCALE;
#pragma unroll
        for (int s = 0; s < 2; ++s) { fq[mb][s] = pack8(acc[mb][0], s); fk[mb][s] = pack8(acc[mb][1], s); fv[mb][s] = pack8(acc[mb][2], s); }
    }
    if (br == 0) {
#pragma unroll
        for (int mb = 0; mb < 2; ++mb) {
            char* qp = ws + WS_QF + (size_t)((head * 64 + blk0 + mb) * 2) * 1024 + lane * 16;
            char* kp = ws + WS_KV + (size_t)((head * 64 + blk0 + mb) * 4) * 1024 + lane * 16;
            *(u32x4*)(qp) = fq[mb][0]; *(u32x4*)(qp + 1024) = fq[mb][1];
            *(u32x4*)(kp) = fk[mb][0]; *(u32x4*)(kp + 1024) = fk[mb][1]; *(u32x4*)(kp + 2048) = fv[mb][0]; *(u32x4*)(kp + 3072) = fv[mb][1];
        }
    } else {
        LAS char* ex = (LAS char*)smem + (wr * 2 + hd) * 6144 + lane * 16;
#pragma unroll
        for (int mb = 0; mb < 2; ++mb) { *(LAS u32x4*)(ex + mb * 3072) = fk[mb][0]; *(LAS u32x4*)(ex + mb * 3072 + 1024) = fk[mb][1]; *(LAS u32x4*)(ex + mb * 3072 + 2048) = fv[mb][1]; }
        char* hl = ws + WS_HALO + lane * 16;
        if (wr == 0 && mt > 0) {
#pragma unroll
            for (int mb = 0; mb < 2; ++mb) { char* p = hl + (size_t)((mt * 32 + head) * 2 + mb) * 9216;
                *(u32x4*)(p) = fq[mb][0]; *(u32x4*)(p + 1024) = fq[mb][1]; *(u32x4*)(p + 2048) = fk[mb][0]; *(u32x4*)(p + 3072) = fk[mb][1]; *(u32x4*)(p + 4096) = fv[mb][0]; *(u32x4*)(p + 5120) = fv[mb][1]; }
        }
        if (wr == 3 && mt < 7) {
#pragma unroll
            for (int mb = 0; mb < 2; ++mb) { char* p = hl + (size_t)(((mt + 1) * 32 + head) * 2 + mb) * 9216;
                *(u32x4*)(p + 6144) = fk[mb][0]; *(u32x4*)(p + 7168) = fk[mb][1]; *(u32x4*)(p + 8192) = fv[mb][1]; }
        }
        __syncthreads();
        if (wr > 0 || mt == 0) {
            const LAS char* pr = (const LAS char*)smem + ((wr > 0 ? wr - 1 : 0) * 2 + hd) * 6144 + lane * 16;
#pragma unroll
            for (int mb = 0; mb < 2; ++mb) {
                const bf16x8 kp0 = *(const LAS bf16x8*)(pr + mb * 3072), kp1 = *(const LAS bf16x8*)(pr + mb * 3072 + 1024), vp1 = *(const LAS bf16x8*)(pr + mb * 3072 + 2048);
                u32x4 o0, o1;
                local_attn(asbf(fq[mb][0]), asbf(fq[mb][1]), asbf(fk[mb][0]), asbf(fk[mb][1]), asbf(fv[mb][0]), asbf(fv[mb][1]), kp0, kp1, vp1, wr > 0, lane, o0, o1);
                char* op = ws + WS_OF + (size_t)(((64 + blk0 + mb) * 32 + head) * 2) * 1024 + lane * 16;
                *(u32x4*)(op) = o0; *(u32x4*)(op + 1024) = o1;
            }
        }
    }
#undef STAGE_X
#undef STAGE_W
}

DI void halo_fix(char* ws, int task, int lane) {
    const int mb = task & 1, head = (task >> 1) & 31, mt = (task >> 6) + 1;
    const char* p = ws + WS_HALO + (size_t)((mt * 32 + head) * 2 + mb) * 9216 + lane * 16;
    bf16x8 f[9];
#pragma unroll
    for (int i = 0; i < 9; ++i) f[i] = *(const bf16x8*)(p + i * 1024);
    u32x4 o0, o1;
    local_attn(f[0], f[1], f[2], f[3], f[4], f[5], f[6], f[7], f[8], true, lane, o0, o1);
    char* op = ws + WS_OF + (size_t)(((64 + mt * 8 + mb) * 32 + head) * 2) * 1024 + lane * 16;
    *(u32x4*)(op) = o0; *(u32x4*)(op + 1024) = o1;
}
__global__ __launch_bounds__(64) void k_halo(char* __restrict__ ws) { halo_fix(ws, blockIdx.x, threadIdx.x); }

constexpr int K2_SLOT = 16384, K2_LDS = 4 * K2_SLOT;
#define EXP2(x) __builtin_amdgcn_exp2f(x)
__global__ __launch_bounds__(512, 2) void k2_attn(char* __restrict__ ws) {
    __shared__ __attribute__((aligned(1024))) char smem[K2_LDS];
    const unsigned lds0 = (unsigned)(uintptr_t)(LAS char*)smem;
    const int tid = threadIdx.x, lane = tid & 63, wid = __builtin_amdgcn_readfirstlane(tid >> 6);
    const int b = blockIdx.x, xcd = b & 7, j = b >> 3, head = xcd * 4 + (j >> 3), qt = j & 7, blk = qt * 8 + wid;
    const char* kv = ws + WS_KV + (size_t)head * 262144;
    const char* qp = ws + WS_QF + (size_t)((head * 64 + blk) * 2) * 1024 + lane * 16;
    const bf16x8 q0 = *(const bf16x8*)qp, q1 = *(const bf16x8*)(qp + 1024);
    const unsigned voff = wid * 2048 + lane * 16, dst = lds0 + wid * 2048;
#define STAGE(c) do { glds16(kv + (size_t)(c) * K2_SLOT, voff, dst + ((c) & 3) * K2_SLOT); glds16(kv + (size_t)(c) * K2_SLOT + 1024, voff, dst + ((c) & 3) * K2_SLOT + 1024); } while (0)
    STAGE(0); STAGE(1); STAGE(2);
    const unsigned one2 = (((lane >> 4) & 1) == ((lane >> 2) & 1)) ? 0x3F803F80u : 0u;
    const bf16x8 ones = asbf(u32x4{one2, one2, one2, one2});
    asm volatile("s_waitcnt vmcnt(4)\n\ts_barrier" ::: "memory");
    unsigned cur = lds0 + lane * 16, nxt = cur + K2_SLOT;
    bf16x8 k0 = ldsrd128(cur), k1 = ldsrd128(cur + 1024), v0 = ldsrd128(cur + 2048), v1;
    f32x16 z = {}, sa, sb, negm, o = {};
    f32x4 l = {};
    sa = MFMA32(k0, q0, z); sa = MFMA32(k1, q1, sa);
    { float m = sa[0];
#pragma unroll
      for (int r = 1; r < 16; ++r) m = __builtin_fmaxf(m, sa[r]);
      m = -swap_max(m);
#pragma unroll
      for (int r = 0; r < 16; ++r) { negm[r] = m; sa[r] += m; } }
    PIN(negm);
    k0 = ldsrd128(cur + 4096); k1 = ldsrd128(cur + 4096 + 1024);
    u32x4 pl, ph = {0u, 0u, 0u, 0u};
    v1 = v0;
#define E(SC, r) SC[r] = EXP2(SC[r])
#define STEP(SC, SN, KA, V1A, V0A, NEXT, LOADK, LOADV0) do { \
        SBAR(); if (NEXT) SN = MFMA32(k0, q0, negm); E(SC, 0); E(SC, 1); E(SC, 2); PIN(SC); SBAR(); \
        if (NEXT) SN = MFMA32(k1, q1, SN); if (LOADK) { k0 = ldsrd128(KA); k1 = ldsrd128((KA) + 1024); } \
        E(SC, 3); pl[0] = cvtpk(SC[0], SC[1]); pl[1] = cvtpk(SC[2], SC[3]); E(SC, 4); PIN(SC); PIN(pl); SBAR(); \
        o = MFMA32(v1, asbf(ph), o); v1 = ldsrd128(V1A); E(SC, 5); E(SC, 6); E(SC, 7); PIN(SC); SBAR(); \
        l = MFMA16(ones, asbf(ph), l); pl[2] = cvtpk(SC[4], SC[5]); pl[3] = cvtpk(SC[6], SC[7]); E(SC, 8); E(SC, 9); PIN(SC); PIN(pl); SBAR(); \
        o = MFMA32(v0, asbf(pl), o); if (LOADV0) v0 = ldsrd128(V0A); E(SC, 10); E(SC, 11); E(SC, 12); PIN(SC); SBAR(); \
        l = MFMA16(ones, asbf(pl), l); E(SC, 13); E(SC, 14); E(SC, 15); ph[0] = cvtpk(SC[8], SC[9]); ph[1] = cvtpk(SC[10], SC[11]); ph[2] = cvtpk(SC[12], SC[13]); ph[3] = cvtpk(SC[14], SC[15]); PIN(ph); SBAR(); \
    } while (0)
    STEP(sa, sb, cur + 2 * 4096, cur + 3072, cur + 4096 + 2048, true, true, true);
    STEP(sb, sa, cur + 3 * 4096, cur + 4096 + 3072, cur + 2 * 4096 + 2048, true, true, true);
    for (int c = 0; c < 15; ++c) {
        if (c < 14) asm volatile("s_waitcnt vmcnt(2)\n\ts_barrier" ::: "memory"); else asm volatile("s_waitcnt vmcnt(0)\n\ts_barrier" ::: "memory");
        if (c < 13) STAGE(c + 3);
        STEP(sa, sb, nxt, cur + 2 * 4096 + 3072, cur + 3 * 4096 + 2048, true, true, true);
        STEP(sb, sa, nxt + 4096, cur + 3 * 4096 + 3072, nxt + 2048, true, true, true);
        STEP(sa, sb, nxt + 2 * 4096, nxt + 3072, nxt + 4096 + 2048, true, true, true);
        STEP(sb, sa, nxt + 3 * 4096, nxt + 4096 + 3072, nxt + 2 * 4096 + 2048, true, true, true);
        cur = nxt; nxt = lds0 + lane * 16 + ((c + 2) & 3) * K2_SLOT;
    }
    STEP(sa, sb, cur, cur + 2 * 4096 + 3072, cur + 3 * 4096 + 2048, true, false, true);
    STEP(sb, sa, cur, cur + 3 * 4096 + 3072, cur, false, false, false);
    o = MFMA32(v1, asbf(ph), o); l = MFMA16(ones, asbf(ph), l);
    float lsum = l[0];
    bool ok = lsum < 1.0e18f;
#pragma unroll
    for (int r = 0; r < 16; ++r) ok = ok && (__builtin_fabsf(o[r]) < 3.0e38f);
    if (__builtin_expect(__any(!ok), 0)) {
        const char* kg = kv + lane * 16;
        float m = -__builtin_inff();
        for (int t = 0; t < 64; ++t) {
            f32x16 s = MFMA32(*(const bf16x8*)(kg + t * 4096), q0, z); s = MFMA32(*(const bf16x8*)(kg + t * 4096 + 1024), q1, s);
#pragma unroll
            for (int r = 0; r < 16; ++r) m = __builtin_fmaxf(m, s[r]);
        }
        m = swap_max(m);
        o = z; l = f32x4{};
        for (int t = 0; t < 64; ++t) {
            f32x16 s = MFMA32(*(const bf16x8*)(kg + t * 4096), q0, z); s = MFMA32(*(const bf16x8*)(kg + t * 4096 + 1024), q1, s);
#pragma unroll
            for (int r = 0; r < 16; ++r) s[r] = EXP2(s[r] - m);
            const bf16x8 p0 = asbf(pack8(s, 0)), p1 = asbf(pack8(s, 1));
            o = MFMA32(*(const bf16x8*)(kg + t * 4096 + 2048), p0, o); o = MFMA32(*(const bf16x8*)(kg + t * 4096 + 3072), p1, o);
            l = MFMA16(ones, p0, l); l = MFMA16(ones, p1, l);
        }
        lsum = l[0];
    }
    const float inv = 1.0f / lsum;
#pragma unroll
    for (int r = 0; r < 16; ++r) o[r] *= inv;
    char* op = ws + WS_OF + (size_t)((blk * 32 + head) * 2) * 1024 + lane * 16;
    *(u32x4*)(op) = pack8(o, 0); *(u32x4*)(op + 1024) = pack8(o, 1);
#undef STAGE
#undef STEP
#undef E
}

constexpr int K3_ST = 32768, K3_LDS = 4 * K3_ST;
__global__ __launch_bounds__(512, 2) void k3_out(const u16* __restrict__ woA, const u16* __restrict__ woB, const char* __restrict__ ws, u16* __restrict__ out) {
    __shared__ __attribute__((aligned(1024))) char smem[K3_LDS];
    const unsigned lds0 = (unsigned)(uintptr_t)(LAS char*)smem;
    const int tid = threadIdx.x, lane = tid & 63, wid = __builtin_amdgcn_readfirstlane(tid >> 6), h = lane >> 5;
    const int wm = wid >> 1, wn = wid & 1;
    const int b = blockIdx.x, xcd = b & 7, j = b >> 3, br = xcd >> 2, qd = xcd & 3;
    const int mt = qd * 4 + (j & 3), nt = j >> 2;
    const char* of = ws + WS_OF + (size_t)(br * 64 + mt * 4) * 65536;
    const char* wo = (const char*)(br ? woB : woA) + nt * 256;
    unsigned vo[2], vw[2];
#pragma unroll
    for (int ii = 0; ii < 2; ++ii) {
        const int i = 2 * wid + ii;
        vo[ii] = (i >> 2) * 65536 + (i & 3) * 1024 + lane * 16;
        const int k = 4 * i + (lane >> 4), c = lane & 15;
        vw[ii] = k * 2048 + ((c ^ (((k & 1) << 1) | ((k & 2) << 2))) << 4);
    }
    const unsigned dst = lds0 + wid * 2048;
#define STAGE(t) do { _Pragma("unroll") for (int ii = 0; ii < 2; ++ii) { glds16(of + (t) * 4096, vo[ii], dst + ((t) & 3) * K3_ST + ii * 1024); \
        glds16(wo + (size_t)(t) * 64 * 2048, vw[ii], dst + ((t) & 3) * K3_ST + 16384 + ii * 1024); } } while (0)
    const unsigned ao = lds0 + wm * 4096 + lane * 16;
    unsigned aw[2];
    { const int q4 = (lane & 15) >> 2, p = lane & 3, cb = (lane >> 4) & 1;
#pragma unroll
      for (int nb = 0; nb < 2; ++nb) { const int col = wn * 64 + 32 * (p & 1) + 16 * nb + 4 * (2 * cb + (p >> 1)); const int c = col >> 3;
          aw[nb] = lds0 + 16384 + (4 * h + q4) * 256 + ((c ^ (((q4 & 1) << 1) | ((q4 & 2) << 2))) << 4) + ((col >> 2) & 1) * 8; } }
    STAGE(0); STAGE(1); STAGE(2);
    f32x16 acc[2] = {f32x16{}, f32x16{}};
#pragma unroll
    for (int t = 0; t < 16; ++t) {
        if (t < 14) asm volatile("s_waitcnt vmcnt(8)\n\ts_barrier" ::: "memory");
        else if (t == 14) asm volatile("s_waitcnt vmcnt(4)\n\ts_barrier" ::: "memory");
        else asm volatile("s_waitcnt vmcnt(0)\n\ts_barrier" ::: "memory");
        if (t + 3 < 16) STAGE(t + 3);
        const unsigned so = (t & 3) * K3_ST;
#pragma unroll
        for (int s = 0; s < 4; ++s) {
            const bf16x8 of_ = ldsrd128(ao + so + s * 1024);
#pragma unroll
            for (int nb = 0; nb < 2; ++nb) {
                const bf16x8 wf = cat8(ldstr(aw[nb] + so + s * 4096), ldstr(aw[nb] + so + s * 4096 + 2048));
                acc[nb] = MFMA32(wf, of_, acc[nb]);
            }
        }
        asm volatile("s_waitcnt lgkmcnt(0)" ::: "memory");
    }
    const int blk = mt * 4 + wm, i = lane & 31;
    const int row = br ? ((blk >> 1) * 64 + 2 * i + (blk & 1)) : (blk * 32 + i);
    char* op = (char*)out + (size_t)row * 4096 + br * 2048 + nt * 256 + wn * 128 + h * 64;
    *(u32x4*)(op) = pack8(acc[0], 0); *(u32x4*)(op + 16) = pack8(acc[0], 1); *(u32x4*)(op + 32) = pack8(acc[1], 0); *(u32x4*)(op + 48) = pack8(acc[1], 1);
#undef STAGE
}

extern "C" void kernel_launch(void* const* d_in, const int* in_sizes, int n_in, void* d_out, int out_size, void* d_ws, size_t ws_size, hipStream_t stream) {
    const u16* xA = (const u16*)d_in[0]; const u16* wA = (const u16*)d_in[1]; const u16* woA = (const u16*)d_in[2];
    const u16* xB = (const u16*)d_in[3]; const u16* wB = (const u16*)d_in[4]; const u16* woB = (const u16*)d_in[5];
    char* ws = (char*)d_ws;
    k1_qkv<<<256, 512, 0, stream>>>(xA, wA, xB, wB, ws);
    k_halo<<<448, 64, 0, stream>>>(ws);
    k2_attn<<<256, 512, 0, stream>>>(ws);
    k3_out<<<256, 512, 0, stream>>>(woA, woB, ws, (u16*)d_out);
}
```

```cpp
#define NWB 3
#include <hip/hip_runtime.h>
#include <stdint.h>
#define K2V 0

typedef unsigned short u16;
typedef __bf16 bf16x8 __attribute__((ext_vector_type(8)));
typedef __bf16 bf16x2 __attribute__((ext_vector_type(2)));
typedef short s16x4 __attribute__((ext_vector_type(4)));
typedef float f32x16 __attribute__((ext_vector_type(16)));
typedef float f32x4 __attribute__((ext_vector_type(4)));
typedef float f32x2 __attribute__((ext_vector_type(2)));
typedef unsigned u32x4 __attribute__((ext_vector_type(4)));
typedef unsigned u32x2 __attribute__((ext_vector_type(2)));
#define LAS __attribute__((address_space(3)))
#define DI __device__ __forceinline__
#define SBAR() __builtin_amdgcn_sched_barrier(0)
#define PIN(x) asm volatile("" : "+v"(x))
#define MFMA32(a, b, c) __builtin_amdgcn_mfma_f32_32x32x16_bf16((a), (b), (c), 0, 0, 0)
#define MFMA16(a, b, c) __builtin_amdgcn_mfma_f32_16x16x32_bf16((a), (b), (c), 0, 0, 0)

constexpr int S_LEN = 2048, DM = 1024, NH = 32;
constexpr float QSCALE = 0.17677669529663687f * 1.4426950408889634f;

constexpr size_t WS_QF = 0;
constexpr size_t WS_KV = WS_QF + (4u << 20);
constexpr size_t WS_OF = WS_KV + (8u << 20);
constexpr size_t WS_HALO = WS_OF + (8u << 20);

#define STAMP_AT(K, I)
DI unsigned cvtpk(float lo, float hi) { f32x2 v = {lo, hi}; bf16x2 b = __builtin_convertvector(v, bf16x2); return __builtin_bit_cast(unsigned, b); }
DI u32x4 pack8(const f32x16& a, int s) { u32x4 r; r[0] = cvtpk(a[8 * s], a[8 * s + 1]); r[1] = cvtpk(a[8 * s + 2], a[8 * s + 3]); r[2] = cvtpk(a[8 * s + 4], a[8 * s + 5]); r[3] = cvtpk(a[8 * s + 6], a[8 * s + 7]); return r; }
DI bf16x8 asbf(u32x4 v) { return __builtin_bit_cast(bf16x8, v); }
DI void glds16(const void* sbase, unsigned voff, unsigned lds) {
    asm volatile("s_mov_b32 m0, %2\n\ts_nop 0\n\tglobal_load_lds_dwordx4 %0, %1" :: "v"(voff), "s"(sbase), "s"(lds) : "memory");
}
DI void touch4(const void* sbase, unsigned voff, unsigned lds) {
    asm volatile("s_mov_b32 m0, %2\n\ts_nop 0\n\tglobal_load_lds_dword %0, %1" :: "v"(voff), "s"(sbase), "s"(lds) : "memory");
}
DI bf16x8 ldsrd128(unsigned addr) { return *(const LAS bf16x8*)(uintptr_t)addr; }
DI s16x4 ldstr(unsigned addr) { return __builtin_amdgcn_ds_read_tr16_b64_v4i16((LAS s16x4*)(uintptr_t)addr); }
DI bf16x8 cat8(s16x4 lo, s16x4 hi) { typedef short s16x8 __attribute__((ext_vector_type(8))); s16x8 v = {lo[0], lo[1], lo[2], lo[3], hi[0], hi[1], hi[2], hi[3]}; return __builtin_bit_cast(bf16x8, v); }
DI float swap_max(float m) { auto r = __builtin_amdgcn_permlane32_swap(__float_as_uint(m), __float_as_uint(m), false, false); return __builtin_fmaxf(__uint_as_float(r[0]), __uint_as_float(r[1])); }
DI float swap_add(float m) { auto r = __builtin_amdgcn_permlane32_swap(__float_as_uint(m), __float_as_uint(m), false, false); return __uint_as_float(r[0]) + __uint_as_float(r[1]); }

DI void local_attn(bf16x8 q0, bf16x8 q1, bf16x8 k0, bf16x8 k1, bf16x8 v0, bf16x8 v1, bf16x8 kp0, bf16x8 kp1, bf16x8 vp1, bool has_prev, int lane, u32x4& o0, u32x4& o1) {
    const int i = lane & 31, h = lane >> 5;
    f32x16 z = {};
    f32x16 so = MFMA32(k0, q0, z); so = MFMA32(k1, q1, so);
    f32x16 sp = MFMA32(kp0, q0, z); sp = MFMA32(kp1, q1, sp);
    const float NEG = -__builtin_inff();
    float m = NEG;
#pragma unroll
    for (int r = 0; r < 16; ++r) { const int j = (r & 3) + 8 * (r >> 2) + 4 * h; const bool ok = (unsigned)(i - j) <= 7u; so[r] = ok ? so[r] : NEG; m = __builtin_fmaxf(m, so[r]); }
#pragma unroll
    for (int r = 12; r < 16; ++r) { const int j = (r & 3) + 24 + 4 * h; const bool ok = has_prev && (j >= i + 25); sp[r] = ok ? sp[r] : NEG; m = __builtin_fmaxf(m, sp[r]); }
    m = swap_max(m);
    float l = 0.f;
#pragma unroll
    for (int r = 0; r < 16; ++r) { so[r] = __builtin_amdgcn_exp2f(so[r] - m); l += so[r]; }
#pragma unroll
    for (int r = 12; r < 16; ++r) { sp[r] = __builtin_amdgcn_exp2f(sp[r] - m); l += sp[r]; }
    l = swap_add(l);
    u32x4 pp = {0u, 0u, cvtpk(sp[12], sp[13]), cvtpk(sp[14], sp[15])};
    f32x16 o = MFMA32(v0, asbf(pack8(so, 0)), z); o = MFMA32(v1, asbf(pack8(so, 1)), o); o = MFMA32(vp1, asbf(pp), o);
    const float inv = 1.0f / l;
#pragma unroll
    for (int r = 0; r < 16; ++r) o[r] *= inv;
    o0 = pack8(o, 0); o1 = pack8(o, 1);
}

#define WAITBAR_(n) asm volatile("s_waitcnt vmcnt(%0) lgkmcnt(0)\n\ts_barrier" :: "i"(n) : "memory")
#define WAITBAR(n) do { switch (n) { case 0: WAITBAR_(0); break; case 3: WAITBAR_(3); break; case 4: WAITBAR_(4); break; case 7: WAITBAR_(7); break; case 10: WAITBAR_(10); break; default: WAITBAR_(0); } } while (0)
constexpr int K1_X = 0, K1_W = 65536, K1_WB = 24576, K1_LDS = K1_W + NWB * K1_WB;
__global__ __launch_bounds__(512, 2) void k1_qkv(const u16* __restrict__ xA, const u16* __restrict__ wA, const u16* __restrict__ xB, const u16* __restrict__ wB, const u16* __restrict__ woA, const u16* __restrict__ woB, char* __restrict__ ws) {
    __shared__ __attribute__((aligned(1024))) char smem[K1_LDS];
    const unsigned lds0 = (unsigned)(uintptr_t)(LAS char*)smem;
    const int tid = threadIdx.x, lane = tid & 63, wid = __builtin_amdgcn_readfirstlane(tid >> 6);
    const int g = wid >> 2, wi = wid & 3, mw = wi >> 1, hd = wi & 1, h = lane >> 5;
    const int b = blockIdx.x, xcd = b & 7, j = b >> 3, br = xcd >> 2, qd = xcd & 3;
    const int mt = (qd & 1) * 4 + (j & 3), hp = (qd >> 1) * 8 + (j >> 2);
    const char* x = (const char*)(br ? xB : xA) + (size_t)mt * 256 * 2048;
    const char* w = (const char*)(br ? wB : wA) + hp * 128;
    STAMP_AT(0, 0);
    unsigned vx[4], vw[3];
#pragma unroll
    for (int ii = 0; ii < 4; ++ii) {
        const int row = br ? ((wi >> 1) * 64 + (wi & 1) + ii * 16 + 2 * (lane >> 3)) : (wi * 32 + ii * 8 + (lane >> 3));
        vx[ii] = row * 2048 + (((lane & 7) ^ (((ii & 1) * 4 + (lane >> 4)) & 7)) << 4);
    }
#pragma unroll
    for (int jj = 0; jj < 3; ++jj) {
        const int jw = 12 * g + 3 * wi + jj, n = jw >> 3, k0 = (jw & 7) * 8 + (lane >> 3);
        vw[jj] = k0 * 6144 + n * 2048 + (((lane & 7) ^ (((lane >> 4) & 1) << 2)) << 4);
    }
    unsigned ax[4];
    { const int row = mw * 64 + (lane & 31); const unsigned a0 = lds0 + K1_X + g * 32768 + row * 128 + ((h ^ ((row >> 1) & 7)) << 4);
#pragma unroll
      for (int s = 0; s < 4; ++s) ax[s] = a0 ^ (s << 5); }
    unsigned aw;
    { const int q4 = (lane & 15) >> 2, p = lane & 3, cb = (lane >> 4) & 1; const int c = hd * 4 + cb * 2 + (p >> 1);
      aw = lds0 + K1_W + (8 * h + q4) * 128 + ((c ^ (((q4 >> 1) & 1) << 2)) << 4) + (p & 1) * 8; }
    const unsigned xdst_o = lds0 + K1_X + (1 - g) * 32768 + wi * 4096;
    const unsigned wdst = lds0 + K1_W + (12 * g + 3 * wi) * 1024;
    const char* xo = x + (size_t)(1 - g) * 128 * 2048;
#define STAGE_X(src, kt, dst) do { _Pragma("unroll") for (int ii = 0; ii < 4; ++ii) glds16((src) + (kt) * 128, vx[ii], (dst) + ii * 1024); } while (0)
#define STAGE_W(kt, buf) do { _Pragma("unroll") for (int jj = 0; jj < 3; ++jj) glds16(w + (size_t)(kt) * 64 * 6144, vw[jj], wdst + (buf) * K1_WB + jj * 1024); } while (0)
    STAGE_X(xo, 0, xdst_o); STAGE_W(0, 0);
    STAGE_W(1, 1);
    if (g == 1) STAGE_X(xo, 1, xdst_o + 16384);
    if (NWB == 4) STAGE_W(2, 2);
    if (g == 1) { asm volatile("s_waitcnt vmcnt(%0)\n\ts_barrier" :: "i"(NWB == 4 ? 10 : 7) : "memory"); }
    else { asm volatile("s_waitcnt vmcnt(%0)\n\ts_barrier" :: "i"(NWB == 4 ? 6 : 3) : "memory"); }
    STAMP_AT(0, 1);
    if (g == 1) asm volatile("s_barrier" ::: "memory");
    f32x16 acc[2][3];
#pragma unroll
    for (int mb = 0; mb < 2; ++mb)
#pragma unroll
        for (int n = 0; n < 3; ++n) acc[mb][n] = f32x16{};
    SBAR();
#pragma unroll
    for (int t = 0; t < 16; ++t) {
        bf16x8 xf[2][4], wf[3][4];
        const int tx = t + 1;
        if (g == 0) { if (t + 1 < 16) STAGE_X(xo, t + 1, xdst_o + ((t + 1) & 1) * 16384); }
        else        { if (t + 2 < 16) STAGE_X(xo, t + 2, xdst_o + ((t + 2) & 1) * 16384); }
        if (t + NWB - 1 < 16) STAGE_W(t + NWB - 1, (t + NWB - 1) % NWB);
        (void)tx;
#pragma unroll
        for (int s = 0; s < 4; ++s) {
#pragma unroll
            for (int mb = 0; mb < 2; ++mb) xf[mb][s] = ldsrd128(ax[s] + (t & 1) * 16384 + mb * 4096);
#pragma unroll
            for (int n = 0; n < 3; ++n) wf[n][s] = cat8(ldstr(aw + (t % NWB) * K1_WB + n * 8192 + s * 2048), ldstr(aw + (t % NWB) * K1_WB + n * 8192 + s * 2048 + 512));
        }
        { constexpr int WN = 3;
          const int w_now = (t + NWB - 1 < 16) ? WN : 0, w_prev = (NWB == 4 && t + 2 < 16) ? WN : 0;
          const int n0 = ((t + 1 < 16) ? 4 : 0) + w_now + w_prev, n1 = ((t + 2 < 16) ? 4 : 0) + w_now + w_prev;
          if (n0 == n1) WAITBAR(n0); else if (g == 0) WAITBAR(n0); else WAITBAR(n1); }
        SBAR();
        __builtin_amdgcn_s_setprio(1);
#pragma unroll
        for (int s = 0; s < 4; ++s)
#pragma unroll
            for (int mb = 0; mb < 2; ++mb) {
                acc[mb][0] = MFMA32(wf[0][s], xf[mb][s], acc[mb][0]);
                acc[mb][1] = MFMA32(wf[1][s], xf[mb][s], acc[mb][1]);
                acc[mb][2] = MFMA32(xf[mb][s], wf[2][s], acc[mb][2]);
            }
        __builtin_amdgcn_s_setprio(0);
        SBAR();
        asm volatile("s_barrier" ::: "memory");
        SBAR();
    }
    if (g == 0) asm volatile("s_barrier" ::: "memory");
    STAMP_AT(0, 2);
    const int head = 2 * hp + hd, wr = 2 * g + mw, blk0 = mt * 8 + wr * 2;
    u32x4 fq[2][2], fk[2][2], fv[2][2];
#pragma unroll
    for (int mb = 0; mb < 2; ++mb) {
#pragma unroll
        for (int r = 0; r < 16; ++r) acc[mb][0][r] *= QSCALE;
#pragma unroll
        for (int s = 0; s < 2; ++s) { fq[mb][s] = pack8(acc[mb][0], s); fk[mb][s] = pack8(acc[mb][1], s); fv[mb][s] = pack8(acc[mb][2], s); }
    }
    if (br == 0) {
        if (wid < 4) touch4((wid & 2) ? woB : woA, (unsigned)(((xcd * 32 + j) * 2 + (wid & 1)) * 64 + lane) * 128u, lds0);
#pragma unroll
        for (int mb = 0; mb < 2; ++mb) {
            char* qp = ws + WS_QF + (size_t)((head * 64 + blk0 + mb) * 2) * 1024 + lane * 16;
            char* kp = ws + WS_KV + (size_t)((head * 64 + blk0 + mb) * 4) * 1024 + lane * 16;
            *(u32x4*)(qp) = fq[mb][0]; *(u32x4*)(qp + 1024) = fq[mb][1];
            *(u32x4*)(kp) = fk[mb][0]; *(u32x4*)(kp + 1024) = fk[mb][1]; *(u32x4*)(kp + 2048) = fv[mb][0]; *(u32x4*)(kp + 3072) = fv[mb][1];
        }
    } else {
        LAS char* ex = (LAS char*)smem + (wr * 2 + hd) * 6144 + lane * 16;
#pragma unroll
        for (int mb = 0; mb < 2; ++mb) { *(LAS u32x4*)(ex + mb * 3072) = fk[mb][0]; *(LAS u32x4*)(ex + mb * 3072 + 1024) = fk[mb][1]; *(LAS u32x4*)(ex + mb * 3072 + 2048) = fv[mb][1]; }
        char* hl = ws + WS_HALO + lane * 16;
        if (wr == 0 && mt > 0) {
#pragma unroll
            for (int mb = 0; mb < 2; ++mb) { char* p = hl + (size_t)((mt * 32 + head) * 2 + mb) * 9216;
                *(u32x4*)(p) = fq[mb][0]; *(u32x4*)(p + 1024) = fq[mb][1]; *(u32x4*)(p + 2048) = fk[mb][0]; *(u32x4*)(p + 3072) = fk[mb][1]; *(u32x4*)(p + 4096) = fv[mb][0]; *(u32x4*)(p + 5120) = fv[mb][1]; }
        }
        if (wr == 3 && mt < 7) {
#pragma unroll
            for (int mb = 0; mb < 2; ++mb) { char* p = hl + (size_t)(((mt + 1) * 32 + head) * 2 + mb) * 9216;
                *(u32x4*)(p + 6144) = fk[mb][0]; *(u32x4*)(p + 7168) = fk[mb][1]; *(u32x4*)(p + 8192) = fv[mb][1]; }
        }
        __syncthreads();
        if (wr > 0 || mt == 0) {
            const LAS char* pr = (const LAS char*)smem + ((wr > 0 ? wr - 1 : 0) * 2 + hd) * 6144 + lane * 16;
#pragma unroll
            for (int mb = 0; mb < 2; ++mb) {
                const bf16x8 kp0 = *(const LAS bf16x8*)(pr + mb * 3072), kp1 = *(const LAS bf16x8*)(pr + mb * 3072 + 1024), vp1 = *(const LAS bf16x8*)(pr + mb * 3072 + 2048);
                u32x4 o0, o1;
                local_attn(asbf(fq[mb][0]), asbf(fq[mb][1]), asbf(fk[mb][0]), asbf(fk[mb][1]), asbf(fv[mb][0]), asbf(fv[mb][1]), kp0, kp1, vp1, wr > 0, lane, o0, o1);
                char* op = ws + WS_OF + (size_t)(((64 + blk0 + mb) * 32 + head) * 2) * 1024 + lane * 16;
                *(u32x4*)(op) = o0; *(u32x4*)(op + 1024) = o1;
            }
        }
    }
    STAMP_AT(0, 3);
#undef STAGE_X
#undef STAGE_W
}

DI void halo_fix(char* ws, int task, int lane) {
    const int mb = task & 1, head = (task >> 1) & 31, mt = (task >> 6) + 1;
    const char* p = ws + WS_HALO + (size_t)((mt * 32 + head) * 2 + mb) * 9216 + lane * 16;
    bf16x8 f[9];
#pragma unroll
    for (int i = 0; i < 9; ++i) f[i] = *(const bf16x8*)(p + i * 1024);
    u32x4 o0, o1;
    local_attn(f[0], f[1], f[2], f[3], f[4], f[5], f[6], f[7], f[8], true, lane, o0, o1);
    char* op = ws + WS_OF + (size_t)(((64 + mt * 8 + mb) * 32 + head) * 2) * 1024 + lane * 16;
    *(u32x4*)(op) = o0; *(u32x4*)(op + 1024) = o1;
}

#define K2_LOOP_ASM \
    "s_waitcnt lgkmcnt(0)\n\t" \
    "v_mfma_f32_32x32x16_bf16 v[48:63], v[80:83], v[64:67], v[16:31]\n\t" \
    "v_exp_f32_e32 v32, v32\n\t" \
    "v_exp_f32_e32 v33, v33\n\t" \
    "v_exp_f32_e32 v34, v34\n\t" \
    "v_mfma_f32_32x32x16_bf16 v[48:63], v[84:87], v[68:71], v[48:63]\n\t" \
    "ds_read_b128 v[80:83], v112 offset:8192\n\t" \
    "ds_read_b128 v[84:87], v112 offset:9216\n\t" \
    "v_exp_f32_e32 v35, v35\n\t" \
    "v_exp_f32_e32 v36, v36\n\t" \
    "v_cvt_pk_bf16_f32 v104, v32, v33\n\t" \
    "v_cvt_pk_bf16_f32 v105, v34, v35\n\t" \
    "ds_read_b128 v[100:103], v112 offset:3072\n\t" \
    "ds_read_b128 v[96:99], v112 offset:6144\n\t" \
    "v_exp_f32_e32 v37, v37\n\t" \
    "v_exp_f32_e32 v38, v38\n\t" \
    "v_exp_f32_e32 v39, v39\n\t" \
    "v_exp_f32_e32 v40, v40\n\t" \
    "v_cvt_pk_bf16_f32 v106, v36, v37\n\t" \
    "v_cvt_pk_bf16_f32 v107, v38, v39\n\t" \
    "v_exp_f32_e32 v41, v41\n\t" \
    "v_exp_f32_e32 v42, v42\n\t" \
    "v_mfma_f32_32x32x16_bf16 v[0:15], v[88:91], v[104:107], 0\n\t" \
    "v_exp_f32_e32 v43, v43\n\t" \
    "v_exp_f32_e32 v44, v44\n\t" \
    "v_exp_f32_e32 v45, v45\n\t" \
    "v_mfma_f32_16x16x32_bf16 v[76:79], v[72:75], v[104:107], 0\n\t" \
    "v_exp_f32_e32 v46, v46\n\t" \
    "v_exp_f32_e32 v47, v47\n\t" \
    "v_cvt_pk_bf16_f32 v108, v40, v41\n\t" \
    "v_cvt_pk_bf16_f32 v109, v42, v43\n\t" \
    "v_cvt_pk_bf16_f32 v110, v44, v45\n\t" \
    "v_cvt_pk_bf16_f32 v111, v46, v47\n\t" \
    "s_waitcnt lgkmcnt(0)\n\t" \
    "v_mfma_f32_32x32x16_bf16 v[32:47], v[80:83], v[64:67], v[16:31]\n\t" \
    "v_exp_f32_e32 v48, v48\n\t" \
    "v_exp_f32_e32 v49, v49\n\t" \
    "v_exp_f32_e32 v50, v50\n\t" \
    "v_mfma_f32_32x32x16_bf16 v[32:47], v[84:87], v[68:71], v[32:47]\n\t" \
    "ds_read_b128 v[80:83], v112 offset:12288\n\t" \
    "ds_read_b128 v[84:87], v112 offset:13312\n\t" \
    "v_exp_f32_e32 v51, v51\n\t" \
    "v_exp_f32_e32 v52, v52\n\t" \
    "v_cvt_pk_bf16_f32 v104, v48, v49\n\t" \
    "v_cvt_pk_bf16_f32 v105, v50, v51\n\t" \
    "v_mfma_f32_32x32x16_bf16 v[0:15], v[100:103], v[108:111], v[0:15]\n\t" \
    "ds_read_b128 v[92:95], v112 offset:7168\n\t" \
    "ds_read_b128 v[88:91], v112 offset:10240\n\t" \
    "v_exp_f32_e32 v53, v53\n\t" \
    "v_exp_f32_e32 v54, v54\n\t" \
    "v_exp_f32_e32 v55, v55\n\t" \
    "v_mfma_f32_16x16x32_bf16 v[76:79], v[72:75], v[108:111], v[76:79]\n\t" \
    "v_exp_f32_e32 v56, v56\n\t" \
    "v_cvt_pk_bf16_f32 v106, v52, v53\n\t" \
    "v_cvt_pk_bf16_f32 v107, v54, v55\n\t" \
    "v_exp_f32_e32 v57, v57\n\t" \
    "v_exp_f32_e32 v58, v58\n\t" \
    "v_mfma_f32_32x32x16_bf16 v[0:15], v[96:99], v[104:107], v[0:15]\n\t" \
    "v_exp_f32_e32 v59, v59\n\t" \
    "v_exp_f32_e32 v60, v60\n\t" \
    "v_exp_f32_e32 v61, v61\n\t" \
    "v_mfma_f32_16x16x32_bf16 v[76:79], v[72:75], v[104:107], v[76:79]\n\t" \
    "v_exp_f32_e32 v62, v62\n\t" \
    "v_exp_f32_e32 v63, v63\n\t" \
    "v_cvt_pk_bf16_f32 v108, v56, v57\n\t" \
    "v_cvt_pk_bf16_f32 v109, v58, v59\n\t" \
    "v_cvt_pk_bf16_f32 v110, v60, v61\n\t" \
    "v_cvt_pk_bf16_f32 v111, v62, v63\n\t" \
    "s_waitcnt lgkmcnt(0)\n\t" \
    "v_mfma_f32_32x32x16_bf16 v[48:63], v[80:83], v[64:67], v[16:31]\n\t" \
    "v_exp_f32_e32 v32, v32\n\t" \
    "v_exp_f32_e32 v33, v33\n\t" \
    "v_exp_f32_e32 v34, v34\n\t" \
    "v_mfma_f32_32x32x16_bf16 v[48:63], v[84:87], v[68:71], v[48:63]\n\t" \
    "ds_read_b128 v[80:83], v112 offset:16384\n\t" \
    "ds_read_b128 v[84:87], v112 offset:17408\n\t" \
    "v_exp_f32_e32 v35, v35\n\t" \
    "v_exp_f32_e32 v36, v36\n\t" \
    "v_cvt_pk_bf16_f32 v104, v32, v33\n\t" \
    "v_cvt_pk_bf16_f32 v105, v34, v35\n\t" \
    "v_mfma_f32_32x32x16_bf16 v[0:15], v[92:95], v[108:111], v[0:15]\n\t" \
    "ds_read_b128 v[100:103], v112 offset:11264\n\t" \
    "ds_read_b128 v[96:99], v112 offset:14336\n\t" \
    "v_exp_f32_e32 v37, v37\n\t" \
    "v_exp_f32_e32 v38, v38\n\t" \
    "v_exp_f32_e32 v39, v39\n\t" \
    "v_mfma_f32_16x16x32_bf16 v[76:79], v[72:75], v[108:111], v[76:79]\n\t" \
    "v_exp_f32_e32 v40, v40\n\t" \
    "v_cvt_pk_bf16_f32 v106, v36, v37\n\t" \
    "v_cvt_pk_bf16_f32 v107, v38, v39\n\t" \
    "v_exp_f32_e32 v41, v41\n\t" \
    "v_exp_f32_e32 v42, v42\n\t" \
    "v_mfma_f32_32x32x16_bf16 v[0:15], v[88:91], v[104:107], v[0:15]\n\t" \
    "v_exp_f32_e32 v43, v43\n\t" \
    "v_exp_f32_e32 v44, v44\n\t" \
    "v_exp_f32_e32 v45, v45\n\t" \
    "v_mfma_f32_16x16x32_bf16 v[76:79], v[72:75], v[104:107], v[76:79]\n\t" \
    "v_exp_f32_e32 v46, v46\n\t" \
    "v_exp_f32_e32 v47, v47\n\t" \
    "v_cvt_pk_bf16_f32 v108, v40, v41\n\t" \
    "v_cvt_pk_bf16_f32 v109, v42, v43\n\t" \
    "v_cvt_pk_bf16_f32 v110, v44, v45\n\t" \
    "v_cvt_pk_bf16_f32 v111, v46, v47\n\t" \
    "s_waitcnt lgkmcnt(0)\n\t" \
    "v_mfma_f32_32x32x16_bf16 v[32:47], v[80:83], v[64:67], v[16:31]\n\t" \
    "v_exp_f32_e32 v48, v48\n\t" \
    "v_exp_f32_e32 v49, v49\n\t" \
    "v_exp_f32_e32 v50, v50\n\t" \
    "v_mfma_f32_32x32x16_bf16 v[32:47], v[84:87], v[68:71], v[32:47]\n\t" \
    "ds_read_b128 v[80:83], v112 offset:20480\n\t" \
    "ds_read_b128 v[84:87], v112 offset:21504\n\t" \
    "v_exp_f32_e32 v51, v51\n\t" \
    "v_exp_f32_e32 v52, v52\n\t" \
    "v_cvt_pk_bf16_f32 v104, v48, v49\n\t" \
    "v_cvt_pk_bf16_f32 v105, v50, v51\n\t" \
    "v_mfma_f32_32x32x16_bf16 v[0:15], v[100:103], v[108:111], v[0:15]\n\t" \
    "ds_read_b128 v[92:95], v112 offset:15360\n\t" \
    "ds_read_b128 v[88:91], v112 offset:18432\n\t" \
    "v_exp_f32_e32 v53, v53\n\t" \
    "v_exp_f32_e32 v54, v54\n\t" \
    "v_exp_f32_e32 v55, v55\n\t" \
    "v_mfma_f32_16x16x32_bf16 v[76:79], v[72:75], v[108:111], v[76:79]\n\t" \
    "v_exp_f32_e32 v56, v56\n\t" \
    "v_cvt_pk_bf16_f32 v106, v52, v53\n\t" \
    "v_cvt_pk_bf16_f32 v107, v54, v55\n\t" \
    "v_exp_f32_e32 v57, v57\n\t" \
    "v_exp_f32_e32 v58, v58\n\t" \
    "v_mfma_f32_32x32x16_bf16 v[0:15], v[96:99], v[104:107], v[0:15]\n\t" \
    "v_exp_f32_e32 v59, v59\n\t" \
    "v_exp_f32_e32 v60, v60\n\t" \
    "v_exp_f32_e32 v61, v61\n\t" \
    "v_mfma_f32_16x16x32_bf16 v[76:79], v[72:75], v[104:107], v[76:79]\n\t" \
    "v_exp_f32_e32 v62, v62\n\t" \
    "v_exp_f32_e32 v63, v63\n\t" \
    "v_cvt_pk_bf16_f32 v108, v56, v57\n\t" \
    "v_cvt_pk_bf16_f32 v109, v58, v59\n\t" \
    "v_cvt_pk_bf16_f32 v110, v60, v61\n\t" \
    "v_cvt_pk_bf16_f32 v111, v62, v63\n\t" \
    "s_waitcnt lgkmcnt(0)\n\t" \
    "v_mfma_f32_32x32x16_bf16 v[48:63], v[80:83], v[64:67], v[16:31]\n\t" \
    "v_exp_f32_e32 v32, v32\n\t" \
    "v_exp_f32_e32 v33, v33\n\t" \
    "v_exp_f32_e32 v34, v34\n\t" \
    "v_mfma_f32_32x32x16_bf16 v[48:63], v[84:87], v[68:71], v[48:63]\n\t" \
    "ds_read_b128 v[80:83], v112 offset:24576\n\t" \
    "ds_read_b128 v[84:87], v112 offset:25600\n\t" \
    "v_exp_f32_e32 v35, v35\n\t" \
    "v_exp_f32_e32 v36, v36\n\t" \
    "v_cvt_pk_bf16_f32 v104, v32, v33\n\t" \
    "v_cvt_pk_bf16_f32 v105, v34, v35\n\t" \
    "v_mfma_f32_32x32x16_bf16 v[0:15], v[92:95], v[108:111], v[0:15]\n\t" \
    "ds_read_b128 v[100:103], v112 offset:19456\n\t" \
    "ds_read_b128 v[96:99], v112 offset:22528\n\t" \
    "v_exp_f32_e32 v37, v37\n\t" \
    "v_exp_f32_e32 v38, v38\n\t" \
    "v_exp_f32_e32 v39, v39\n\t" \
    "v_mfma_f32_16x16x32_bf16 v[76:79], v[72:75], v[108:111], v[76:79]\n\t" \
    "v_exp_f32_e32 v40, v40\n\t" \
    "v_cvt_pk_bf16_f32 v106, v36, v37\n\t" \
    "v_cvt_pk_bf16_f32 v107, v38, v39\n\t" \
    "v_exp_f32_e32 v41, v41\n\t" \
    "v_exp_f32_e32 v42, v42\n\t" \
    "v_mfma_f32_32x32x16_bf16 v[0:15], v[88:91], v[104:107], v[0:15]\n\t" \
    "v_exp_f32_e32 v43, v43\n\t" \
    "v_exp_f32_e32 v44, v44\n\t" \
    "v_exp_f32_e32 v45, v45\n\t" \
    "v_mfma_f32_16x16x32_bf16 v[76:79], v[72:75], v[104:107], v[76:79]\n\t" \
    "v_exp_f32_e32 v46, v46\n\t" \
    "v_exp_f32_e32 v47, v47\n\t" \
    "v_cvt_pk_bf16_f32 v108, v40, v41\n\t" \
    "v_cvt_pk_bf16_f32 v109, v42, v43\n\t" \
    "v_cvt_pk_bf16_f32 v110, v44, v45\n\t" \
    "v_cvt_pk_bf16_f32 v111, v46, v47\n\t" \
    "s_waitcnt lgkmcnt(0)\n\t" \
    "v_mfma_f32_32x32x16_bf16 v[32:47], v[80:83], v[64:67], v[16:31]\n\t" \
    "v_exp_f32_e32 v48, v48\n\t" \
    "v_exp_f32_e32 v49, v49\n\t" \
    "v_exp_f32_e32 v50, v50\n\t" \
    "v_mfma_f32_32x32x16_bf16 v[32:47], v[84:87], v[68:71], v[32:47]\n\t" \
    "ds_read_b128 v[80:83], v112 offset:28672\n\t" \
    "ds_read_b128 v[84:87], v112 offset:29696\n\t" \
    "v_exp_f32_e32 v51, v51\n\t" \
    "v_exp_f32_e32 v52, v52\n\t" \
    "v_cvt_pk_bf16_f32 v104, v48, v49\n\t" \
    "v_cvt_pk_bf16_f32 v105, v50, v51\n\t" \
    "v_mfma_f32_32x32x16_bf16 v[0:15], v[100:103], v[108:111], v[0:15]\n\t" \
    "ds_read_b128 v[92:95], v112 offset:23552\n\t" \
    "ds_read_b128 v[88:91], v112 offset:26624\n\t" \
    "v_exp_f32_e32 v53, v53\n\t" \
    "v_exp_f32_e32 v54, v54\n\t" \
    "v_exp_f32_e32 v55, v55\n\t" \
    "v_mfma_f32_16x16x32_bf16 v[76:79], v[72:75], v[108:111], v[76:79]\n\t" \
    "v_exp_f32_e32 v56, v56\n\t" \
    "v_cvt_pk_bf16_f32 v106, v52, v53\n\t" \
    "v_cvt_pk_bf16_f32 v107, v54, v55\n\t" \
    "v_exp_f32_e32 v57, v57\n\t" \
    "v_exp_f32_e32 v58, v58\n\t" \
    "v_mfma_f32_32x32x16_bf16 v[0:15], v[96:99], v[104:107], v[0:15]\n\t" \
    "v_exp_f32_e32 v59, v59\n\t" \
    "v_exp_f32_e32 v60, v60\n\t" \
    "v_exp_f32_e32 v61, v61\n\t" \
    "v_mfma_f32_16x16x32_bf16 v[76:79], v[72:75], v[104:107], v[76:79]\n\t" \
    "v_exp_f32_e32 v62, v62\n\t" \
    "v_exp_f32_e32 v63, v63\n\t" \
    "v_cvt_pk_bf16_f32 v108, v56, v57\n\t" \
    "v_cvt_pk_bf16_f32 v109, v58, v59\n\t" \
    "v_cvt_pk_bf16_f32 v110, v60, v61\n\t" \
    "v_cvt_pk_bf16_f32 v111, v62, v63\n\t" \
    "1:\n" \
    "s_waitcnt vmcnt(0)\n\t" \
    "s_barrier\n\t" \
    "s_cmp_eq_u32 %[cnt], 1\n\t" \
    "s_cbranch_scc1 2f\n\t" \
    "s_mov_b32 m0, %[dst]\n\t" \
    "s_add_u32 %[dst], %[dst], 0x8000\n\t" \
    "global_load_lds_dwordx4 v114, %[src] offset:0\n\t" \
    "global_load_lds_dwordx4 v114, %[src] offset:1024\n\t" \
    "global_load_lds_dwordx4 v114, %[src] offset:2048\n\t" \
    "global_load_lds_dwordx4 v114, %[src] offset:3072\n\t" \
    "s_cmp_eq_u32 %[dst], %[dend]\n\t" \
    "s_cselect_b32 %[dst], %[dbeg], %[dst]\n\t" \
    "v_add_u32_e32 v114, 0x8000, v114\n\t" \
    "2:\n" \
    "s_setprio 3\n\t" \
    "s_waitcnt lgkmcnt(0)\n\t" \
    "v_mfma_f32_32x32x16_bf16 v[48:63], v[80:83], v[64:67], v[16:31]\n\t" \
    "v_exp_f32_e32 v32, v32\n\t" \
    "v_exp_f32_e32 v33, v33\n\t" \
    "v_exp_f32_e32 v34, v34\n\t" \
    "v_mfma_f32_32x32x16_bf16 v[48:63], v[84:87], v[68:71], v[48:63]\n\t" \
    "ds_read_b128 v[80:83], v113 offset:0\n\t" \
    "ds_read_b128 v[84:87], v113 offset:1024\n\t" \
    "v_exp_f32_e32 v35, v35\n\t" \
    "v_exp_f32_e32 v36, v36\n\t" \
    "v_cvt_pk_bf16_f32 v104, v32, v33\n\t" \
    "v_cvt_pk_bf16_f32 v105, v34, v35\n\t" \
    "v_mfma_f32_32x32x16_bf16 v[0:15], v[92:95], v[108:111], v[0:15]\n\t" \
    "ds_read_b128 v[100:103], v112 offset:27648\n\t" \
    "ds_read_b128 v[96:99], v112 offset:30720\n\t" \
    "v_exp_f32_e32 v37, v37\n\t" \
    "v_exp_f32_e32 v38, v38\n\t" \
    "v_exp_f32_e32 v39, v39\n\t" \
    "v_mfma_f32_16x16x32_bf16 v[76:79], v[72:75], v[108:111], v[76:79]\n\t" \
    "v_exp_f32_e32 v40, v40\n\t" \
    "v_cvt_pk_bf16_f32 v106, v36, v37\n\t" \
    "v_cvt_pk_bf16_f32 v107, v38, v39\n\t" \
    "v_exp_f32_e32 v41, v41\n\t" \
    "v_exp_f32_e32 v42, v42\n\t" \
    "v_mfma_f32_32x32x16_bf16 v[0:15], v[88:91], v[104:107], v[0:15]\n\t" \
    "v_exp_f32_e32 v43, v43\n\t" \
    "v_exp_f32_e32 v44, v44\n\t" \
    "v_exp_f32_e32 v45, v45\n\t" \
    "v_mfma_f32_16x16x32_bf16 v[76:79], v[72:75], v[104:107], v[76:79]\n\t" \
    "v_exp_f32_e32 v46, v46\n\t" \
    "v_exp_f32_e32 v47, v47\n\t" \
    "v_cvt_pk_bf16_f32 v108, v40, v41\n\t" \
    "v_cvt_pk_bf16_f32 v109, v42, v43\n\t" \
    "v_cvt_pk_bf16_f32 v110, v44, v45\n\t" \
    "v_cvt_pk_bf16_f32 v111, v46, v47\n\t" \
    "s_waitcnt lgkmcnt(0)\n\t" \
    "v_mfma_f32_32x32x16_bf16 v[32:47], v[80:83], v[64:67], v[16:31]\n\t" \
    "v_exp_f32_e32 v48, v48\n\t" \
    "v_exp_f32_e32 v49, v49\n\t" \
    "v_exp_f32_e32 v50, v50\n\t" \
    "v_mfma_f32_32x32x16_bf16 v[32:47], v[84:87], v[68:71], v[32:47]\n\t" \
    "ds_read_b128 v[80:83], v113 offset:4096\n\t" \
    "ds_read_b128 v[84:87], v113 offset:5120\n\t" \
    "v_exp_f32_e32 v51, v51\n\t" \
    "v_exp_f32_e32 v52, v52\n\t" \
    "v_cvt_pk_bf16_f32 v104, v48, v49\n\t" \
    "v_cvt_pk_bf16_f32 v105, v50, v51\n\t" \
    "v_mfma_f32_32x32x16_bf16 v[0:15], v[100:103], v[108:111], v[0:15]\n\t" \
    "ds_read_b128 v[92:95], v112 offset:31744\n\t" \
    "ds_read_b128 v[88:91], v113 offset:2048\n\t" \
    "v_exp_f32_e32 v53, v53\n\t" \
    "v_exp_f32_e32 v54, v54\n\t" \
    "v_exp_f32_e32 v55, v55\n\t" \
    "v_mfma_f32_16x16x32_bf16 v[76:79], v[72:75], v[108:111], v[76:79]\n\t" \
    "v_exp_f32_e32 v56, v56\n\t" \
    "v_cvt_pk_bf16_f32 v106, v52, v53\n\t" \
    "v_cvt_pk_bf16_f32 v107, v54, v55\n\t" \
    "v_exp_f32_e32 v57, v57\n\t" \
    "v_exp_f32_e32 v58, v58\n\t" \
    "v_mfma_f32_32x32x16_bf16 v[0:15], v[96:99], v[104:107], v[0:15]\n\t" \
    "v_exp_f32_e32 v59, v59\n\t" \
    "v_exp_f32_e32 v60, v60\n\t" \
    "v_exp_f32_e32 v61, v61\n\t" \
    "v_mfma_f32_16x16x32_bf16 v[76:79], v[72:75], v[104:107], v[76:79]\n\t" \
    "v_exp_f32_e32 v62, v62\n\t" \
    "v_exp_f32_e32 v63, v63\n\t" \
    "v_cvt_pk_bf16_f32 v108, v56, v57\n\t" \
    "v_cvt_pk_bf16_f32 v109, v58, v59\n\t" \
    "v_cvt_pk_bf16_f32 v110, v60, v61\n\t" \
    "v_cvt_pk_bf16_f32 v111, v62, v63\n\t" \
    "s_setprio 2\n\t" \
    "s_waitcnt lgkmcnt(0)\n\t" \
    "v_mfma_f32_32x32x16_bf16 v[48:63], v[80:83], v[64:67], v[16:31]\n\t" \
    "v_exp_f32_e32 v32, v32\n\t" \
    "v_exp_f32_e32 v33, v33\n\t" \
    "v_exp_f32_e32 v34, v34\n\t" \
    "v_mfma_f32_32x32x16_bf16 v[48:63], v[84:87], v[68:71], v[48:63]\n\t" \
    "ds_read_b128 v[80:83], v113 offset:8192\n\t" \
    "ds_read_b128 v[84:87], v113 offset:9216\n\t" \
    "v_exp_f32_e32 v35, v35\n\t" \
    "v_exp_f32_e32 v36, v36\n\t" \
    "v_cvt_pk_bf16_f32 v104, v32, v33\n\t" \
    "v_cvt_pk_bf16_f32 v105, v34, v35\n\t" \
    "v_mfma_f32_32x32x16_bf16 v[0:15], v[92:95], v[108:111], v[0:15]\n\t" \
    "ds_read_b128 v[100:103], v113 offset:3072\n\t" \
    "ds_read_b128 v[96:99], v113 offset:6144\n\t" \
    "v_exp_f32_e32 v37, v37\n\t" \
    "v_exp_f32_e32 v38, v38\n\t" \
    "v_exp_f32_e32 v39, v39\n\t" \
    "v_mfma_f32_16x16x32_bf16 v[76:79], v[72:75], v[108:111], v[76:79]\n\t" \
    "v_exp_f32_e32 v40, v40\n\t" \
    "v_cvt_pk_bf16_f32 v106, v36, v37\n\t" \
    "v_cvt_pk_bf16_f32 v107, v38, v39\n\t" \
    "v_exp_f32_e32 v41, v41\n\t" \
    "v_exp_f32_e32 v42, v42\n\t" \
    "v_mfma_f32_32x32x16_bf16 v[0:15], v[88:91], v[104:107], v[0:15]\n\t" \
    "v_exp_f32_e32 v43, v43\n\t" \
    "v_exp_f32_e32 v44, v44\n\t" \
    "v_exp_f32_e32 v45, v45\n\t" \
    "v_mfma_f32_16x16x32_bf16 v[76:79], v[72:75], v[104:107], v[76:79]\n\t" \
    "v_exp_f32_e32 v46, v46\n\t" \
    "v_exp_f32_e32 v47, v47\n\t" \
    "v_cvt_pk_bf16_f32 v108, v40, v41\n\t" \
    "v_cvt_pk_bf16_f32 v109, v42, v43\n\t" \
    "v_cvt_pk_bf16_f32 v110, v44, v45\n\t" \
    "v_cvt_pk_bf16_f32 v111, v46, v47\n\t" \
    "s_waitcnt lgkmcnt(0)\n\t" \
    "v_mfma_f32_32x32x16_bf16 v[32:47], v[80:83], v[64:67], v[16:31]\n\t" \
    "v_exp_f32_e32 v48, v48\n\t" \
    "v_exp_f32_e32 v49, v49\n\t" \
    "v_exp_f32_e32 v50, v50\n\t" \
    "v_mfma_f32_32x32x16_bf16 v[32:47], v[84:87], v[68:71], v[32:47]\n\t" \
    "ds_read_b128 v[80:83], v113 offset:12288\n\t" \
    "ds_read_b128 v[84:87], v113 offset:13312\n\t" \
    "v_exp_f32_e32 v51, v51\n\t" \
    "v_exp_f32_e32 v52, v52\n\t" \
    "v_cvt_pk_bf16_f32 v104, v48, v49\n\t" \
    "v_cvt_pk_bf16_f32 v105, v50, v51\n\t" \
    "v_mfma_f32_32x32x16_bf16 v[0:15], v[100:103], v[108:111], v[0:15]\n\t" \
    "ds_read_b128 v[92:95], v113 offset:7168\n\t" \
    "ds_read_b128 v[88:91], v113 offset:10240\n\t" \
    "v_exp_f32_e32 v53, v53\n\t" \
    "v_exp_f32_e32 v54, v54\n\t" \
    "v_exp_f32_e32 v55, v55\n\t" \
    "v_mfma_f32_16x16x32_bf16 v[76:79], v[72:75], v[108:111], v[76:79]\n\t" \
    "v_exp_f32_e32 v56, v56\n\t" \
    "v_cvt_pk_bf16_f32 v106, v52, v53\n\t" \
    "v_cvt_pk_bf16_f32 v107, v54, v55\n\t" \
    "v_exp_f32_e32 v57, v57\n\t" \
    "v_exp_f32_e32 v58, v58\n\t" \
    "v_mfma_f32_32x32x16_bf16 v[0:15], v[96:99], v[104:107], v[0:15]\n\t" \
    "v_exp_f32_e32 v59, v59\n\t" \
    "v_exp_f32_e32 v60, v60\n\t" \
    "v_exp_f32_e32 v61, v61\n\t" \
    "v_mfma_f32_16x16x32_bf16 v[76:79], v[72:75], v[104:107], v[76:79]\n\t" \
    "v_exp_f32_e32 v62, v62\n\t" \
    "v_exp_f32_e32 v63, v63\n\t" \
    "v_cvt_pk_bf16_f32 v108, v56, v57\n\t" \
    "v_cvt_pk_bf16_f32 v109, v58, v59\n\t" \
    "v_cvt_pk_bf16_f32 v110, v60, v61\n\t" \
    "v_cvt_pk_bf16_f32 v111, v62, v63\n\t" \
    "s_setprio 1\n\t" \
    "s_waitcnt lgkmcnt(0)\n\t" \
    "v_mfma_f32_32x32x16_bf16 v[48:63], v[80:83], v[64:67], v[16:31]\n\t" \
    "v_exp_f32_e32 v32, v32\n\t" \
    "v_exp_f32_e32 v33, v33\n\t" \
    "v_exp_f32_e32 v34, v34\n\t" \
    "v_mfma_f32_32x32x16_bf16 v[48:63], v[84:87], v[68:71], v[48:63]\n\t" \
    "ds_read_b128 v[80:83], v113 offset:16384\n\t" \
    "ds_read_b128 v[84:87], v113 offset:17408\n\t" \
    "v_exp_f32_e32 v35, v35\n\t" \
    "v_exp_f32_e32 v36, v36\n\t" \
    "v_cvt_pk_bf16_f32 v104, v32, v33\n\t" \
    "v_cvt_pk_bf16_f32 v105, v34, v35\n\t" \
    "v_mfma_f32_32x32x16_bf16 v[0:15], v[92:95], v[108:111], v[0:15]\n\t" \
    "ds_read_b128 v[100:103], v113 offset:11264\n\t" \
    "ds_read_b128 v[96:99], v113 offset:14336\n\t" \
    "v_exp_f32_e32 v37, v37\n\t" \
    "v_exp_f32_e32 v38, v38\n\t" \
    "v_exp_f32_e32 v39, v39\n\t" \
    "v_mfma_f32_16x16x32_bf16 v[76:79], v[72:75], v[108:111], v[76:79]\n\t" \
    "v_exp_f32_e32 v40, v40\n\t" \
    "v_cvt_pk_bf16_f32 v106, v36, v37\n\t" \
    "v_cvt_pk_bf16_f32 v107, v38, v39\n\t" \
    "v_exp_f32_e32 v41, v41\n\t" \
    "v_exp_f32_e32 v42, v42\n\t" \
    "v_mfma_f32_32x32x16_bf16 v[0:15], v[88:91], v[104:107], v[0:15]\n\t" \
    "v_exp_f32_e32 v43, v43\n\t" \
    "v_exp_f32_e32 v44, v44\n\t" \
    "v_exp_f32_e32 v45, v45\n\t" \
    "v_mfma_f32_16x16x32_bf16 v[76:79], v[72:75], v[104:107], v[76:79]\n\t" \
    "v_exp_f32_e32 v46, v46\n\t" \
    "v_exp_f32_e32 v47, v47\n\t" \
    "v_cvt_pk_bf16_f32 v108, v40, v41\n\t" \
    "v_cvt_pk_bf16_f32 v109, v42, v43\n\t" \
    "v_cvt_pk_bf16_f32 v110, v44, v45\n\t" \
    "v_cvt_pk_bf16_f32 v111, v46, v47\n\t" \
    "s_waitcnt lgkmcnt(0)\n\t" \
    "v_mfma_f32_32x32x16_bf16 v[32:47], v[80:83], v[64:67], v[16:31]\n\t" \
    "v_exp_f32_e32 v48, v48\n\t" \
    "v_exp_f32_e32 v49, v49\n\t" \
    "v_exp_f32_e32 v50, v50\n\t" \
    "v_mfma_f32_32x32x16_bf16 v[32:47], v[84:87], v[68:71], v[32:47]\n\t" \
    "ds_read_b128 v[80:83], v113 offset:20480\n\t" \
    "ds_read_b128 v[84:87], v113 offset:21504\n\t" \
    "v_exp_f32_e32 v51, v51\n\t" \
    "v_exp_f32_e32 v52, v52\n\t" \
    "v_cvt_pk_bf16_f32 v104, v48, v49\n\t" \
    "v_cvt_pk_bf16_f32 v105, v50, v51\n\t" \
    "v_mfma_f32_32x32x16_bf16 v[0:15], v[100:103], v[108:111], v[0:15]\n\t" \
    "ds_read_b128 v[92:95], v113 offset:15360\n\t" \
    "ds_read_b128 v[88:91], v113 offset:18432\n\t" \
    "v_exp_f32_e32 v53, v53\n\t" \
    "v_exp_f32_e32 v54, v54\n\t" \
    "v_exp_f32_e32 v55, v55\n\t" \
    "v_mfma_f32_16x16x32_bf16 v[76:79], v[72:75], v[108:111], v[76:79]\n\t" \
    "v_exp_f32_e32 v56, v56\n\t" \
    "v_cvt_pk_bf16_f32 v106, v52, v53\n\t" \
    "v_cvt_pk_bf16_f32 v107, v54, v55\n\t" \
    "v_exp_f32_e32 v57, v57\n\t" \
    "v_exp_f32_e32 v58, v58\n\t" \
    "v_mfma_f32_32x32x16_bf16 v[0:15], v[96:99], v[104:107], v[0:15]\n\t" \
    "v_exp_f32_e32 v59, v59\n\t" \
    "v_exp_f32_e32 v60, v60\n\t" \
    "v_exp_f32_e32 v61, v61\n\t" \
    "v_mfma_f32_16x16x32_bf16 v[76:79], v[72:75], v[104:107], v[76:79]\n\t" \
    "v_exp_f32_e32 v62, v62\n\t" \
    "v_exp_f32_e32 v63, v63\n\t" \
    "v_cvt_pk_bf16_f32 v108, v56, v57\n\t" \
    "v_cvt_pk_bf16_f32 v109, v58, v59\n\t" \
    "v_cvt_pk_bf16_f32 v110, v60, v61\n\t" \
    "v_cvt_pk_bf16_f32 v111, v62, v63\n\t" \
    "s_setprio 0\n\t" \
    "s_waitcnt lgkmcnt(0)\n\t" \
    "v_mfma_f32_32x32x16_bf16 v[48:63], v[80:83], v[64:67], v[16:31]\n\t" \
    "v_exp_f32_e32 v32, v32\n\t" \
    "v_exp_f32_e32 v33, v33\n\t" \
    "v_exp_f32_e32 v34, v34\n\t" \
    "v_mfma_f32_32x32x16_bf16 v[48:63], v[84:87], v[68:71], v[48:63]\n\t" \
    "ds_read_b128 v[80:83], v113 offset:24576\n\t" \
    "ds_read_b128 v[84:87], v113 offset:25600\n\t" \
    "v_exp_f32_e32 v35, v35\n\t" \
    "v_exp_f32_e32 v36, v36\n\t" \
    "v_cvt_pk_bf16_f32 v104, v32, v33\n\t" \
    "v_cvt_pk_bf16_f32 v105, v34, v35\n\t" \
    "v_mfma_f32_32x32x16_bf16 v[0:15], v[92:95], v[108:111], v[0:15]\n\t" \
    "ds_read_b128 v[100:103], v113 offset:19456\n\t" \
    "ds_read_b128 v[96:99], v113 offset:22528\n\t" \
    "v_exp_f32_e32 v37, v37\n\t" \
    "v_exp_f32_e32 v38, v38\n\t" \
    "v_exp_f32_e32 v39, v39\n\t" \
    "v_mfma_f32_16x16x32_bf16 v[76:79], v[72:75], v[108:111], v[76:79]\n\t" \
    "v_exp_f32_e32 v40, v40\n\t" \
    "v_cvt_pk_bf16_f32 v106, v36, v37\n\t" \
    "v_cvt_pk_bf16_f32 v107, v38, v39\n\t" \
    "v_exp_f32_e32 v41, v41\n\t" \
    "v_exp_f32_e32 v42, v42\n\t" \
    "v_mfma_f32_32x32x16_bf16 v[0:15], v[88:91], v[104:107], v[0:15]\n\t" \
    "v_exp_f32_e32 v43, v43\n\t" \
    "v_exp_f32_e32 v44, v44\n\t" \
    "v_exp_f32_e32 v45, v45\n\t" \
    "v_mfma_f32_16x16x32_bf16 v[76:79], v[72:75], v[104:107], v[76:79]\n\t" \
    "v_exp_f32_e32 v46, v46\n\t" \
    "v_exp_f32_e32 v47, v47\n\t" \
    "v_cvt_pk_bf16_f32 v108, v40, v41\n\t" \
    "v_cvt_pk_bf16_f32 v109, v42, v43\n\t" \
    "v_cvt_pk_bf16_f32 v110, v44, v45\n\t" \
    "v_cvt_pk_bf16_f32 v111, v46, v47\n\t" \
    "s_waitcnt lgkmcnt(0)\n\t" \
    "v_mfma_f32_32x32x16_bf16 v[32:47], v[80:83], v[64:67], v[16:31]\n\t" \
    "v_exp_f32_e32 v48, v48\n\t" \
    "v_exp_f32_e32 v49, v49\n\t" \
    "v_exp_f32_e32 v50, v50\n\t" \
    "v_mfma_f32_32x32x16_bf16 v[32:47], v[84:87], v[68:71], v[32:47]\n\t" \
    "ds_read_b128 v[80:83], v113 offset:28672\n\t" \
    "ds_read_b128 v[84:87], v113 offset:29696\n\t" \
    "v_exp_f32_e32 v51, v51\n\t" \
    "v_exp_f32_e32 v52, v52\n\t" \
    "v_cvt_pk_bf16_f32 v104, v48, v49\n\t" \
    "v_cvt_pk_bf16_f32 v105, v50, v51\n\t" \
    "v_mfma_f32_32x32x16_bf16 v[0:15], v[100:103], v[108:111], v[0:15]\n\t" \
    "ds_read_b128 v[92:95], v113 offset:23552\n\t" \
    "ds_read_b128 v[88:91], v113 offset:26624\n\t" \
    "v_exp_f32_e32 v53, v53\n\t" \
    "v_exp_f32_e32 v54, v54\n\t" \
    "v_exp_f32_e32 v55, v55\n\t" \
    "v_mfma_f32_16x16x32_bf16 v[76:79], v[72:75], v[108:111], v[76:79]\n\t" \
    "v_exp_f32_e32 v56, v56\n\t" \
    "v_cvt_pk_bf16_f32 v106, v52, v53\n\t" \
    "v_cvt_pk_bf16_f32 v107, v54, v55\n\t" \
    "v_exp_f32_e32 v57, v57\n\t" \
    "v_exp_f32_e32 v58, v58\n\t" \
    "v_mfma_f32_32x32x16_bf16 v[0:15], v[96:99], v[104:107], v[0:15]\n\t" \
    "v_exp_f32_e32 v59, v59\n\t" \
    "v_exp_f32_e32 v60, v60\n\t" \
    "v_exp_f32_e32 v61, v61\n\t" \
    "v_mfma_f32_16x16x32_bf16 v[76:79], v[72:75], v[104:107], v[76:79]\n\t" \
    "v_exp_f32_e32 v62, v62\n\t" \
    "v_exp_f32_e32 v63, v63\n\t" \
    "v_cvt_pk_bf16_f32 v108, v56, v57\n\t" \
    "v_cvt_pk_bf16_f32 v109, v58, v59\n\t" \
    "v_cvt_pk_bf16_f32 v110, v60, v61\n\t" \
    "v_cvt_pk_bf16_f32 v111, v62, v63\n\t" \
    "v_mov_b32_e32 v112, v113\n\t" \
    "s_add_u32 %[nx], %[nx], 0x8000\n\t" \
    "s_cmp_eq_u32 %[nx], 0x18000\n\t" \
    "s_cselect_b32 %[nx], 0, %[nx]\n\t" \
    "v_add_u32_e32 v113, %[nx], v115\n\t" \
    "s_sub_u32 %[cnt], %[cnt], 1\n\t" \
    "s_cmp_lg_u32 %[cnt], 0\n\t" \
    "s_cbranch_scc1 1b\n\t" \
    "s_setprio 0\n\t" \
    "s_waitcnt lgkmcnt(0)\n\t" \
    "v_mfma_f32_32x32x16_bf16 v[48:63], v[80:83], v[64:67], v[16:31]\n\t" \
    "v_exp_f32_e32 v32, v32\n\t" \
    "v_exp_f32_e32 v33, v33\n\t" \
    "v_exp_f32_e32 v34, v34\n\t" \
    "v_mfma_f32_32x32x16_bf16 v[48:63], v[84:87], v[68:71], v[48:63]\n\t" \
    "v_exp_f32_e32 v35, v35\n\t" \
    "v_exp_f32_e32 v36, v36\n\t" \
    "v_cvt_pk_bf16_f32 v104, v32, v33\n\t" \
    "v_cvt_pk_bf16_f32 v105, v34, v35\n\t" \
    "v_mfma_f32_32x32x16_bf16 v[0:15], v[92:95], v[108:111], v[0:15]\n\t" \
    "ds_read_b128 v[100:103], v112 offset:27648\n\t" \
    "ds_read_b128 v[96:99], v112 offset:30720\n\t" \
    "v_exp_f32_e32 v37, v37\n\t" \
    "v_exp_f32_e32 v38, v38\n\t" \
    "v_exp_f32_e32 v39, v39\n\t" \
    "v_mfma_f32_16x16x32_bf16 v[76:79], v[72:75], v[108:111], v[76:79]\n\t" \
    "v_exp_f32_e32 v40, v40\n\t" \
    "v_cvt_pk_bf16_f32 v106, v36, v37\n\t" \
    "v_cvt_pk_bf16_f32 v107, v38, v39\n\t" \
    "v_exp_f32_e32 v41, v41\n\t" \
    "v_exp_f32_e32 v42, v42\n\t" \
    "v_mfma_f32_32x32x16_bf16 v[0:15], v[88:91], v[104:107], v[0:15]\n\t" \
    "v_exp_f32_e32 v43, v43\n\t" \
    "v_exp_f32_e32 v44, v44\n\t" \
    "v_exp_f32_e32 v45, v45\n\t" \
    "v_mfma_f32_16x16x32_bf16 v[76:79], v[72:75], v[104:107], v[76:79]\n\t" \
    "v_exp_f32_e32 v46, v46\n\t" \
    "v_exp_f32_e32 v47, v47\n\t" \
    "v_cvt_pk_bf16_f32 v108, v40, v41\n\t" \
    "v_cvt_pk_bf16_f32 v109, v42, v43\n\t" \
    "v_cvt_pk_bf16_f32 v110, v44, v45\n\t" \
    "v_cvt_pk_bf16_f32 v111, v46, v47\n\t" \
    "s_waitcnt lgkmcnt(0)\n\t" \
    "v_exp_f32_e32 v48, v48\n\t" \
    "v_exp_f32_e32 v49, v49\n\t" \
    "v_exp_f32_e32 v50, v50\n\t" \
    "v_exp_f32_e32 v51, v51\n\t" \
    "v_exp_f32_e32 v52, v52\n\t" \
    "v_cvt_pk_bf16_f32 v104, v48, v49\n\t" \
    "v_cvt_pk_bf16_f32 v105, v50, v51\n\t" \
    "v_mfma_f32_32x32x16_bf16 v[0:15], v[100:103], v[108:111], v[0:15]\n\t" \
    "ds_read_b128 v[92:95], v112 offset:31744\n\t" \
    "v_exp_f32_e32 v53, v53\n\t" \
    "v_exp_f32_e32 v54, v54\n\t" \
    "v_exp_f32_e32 v55, v55\n\t" \
    "v_mfma_f32_16x16x32_bf16 v[76:79], v[72:75], v[108:111], v[76:79]\n\t" \
    "v_exp_f32_e32 v56, v56\n\t" \
    "v_cvt_pk_bf16_f32 v106, v52, v53\n\t" \
    "v_cvt_pk_bf16_f32 v107, v54, v55\n\t" \
    "v_exp_f32_e32 v57, v57\n\t" \
    "v_exp_f32_e32 v58, v58\n\t" \
    "v_mfma_f32_32x32x16_bf16 v[0:15], v[96:99], v[104:107], v[0:15]\n\t" \
    "v_exp_f32_e32 v59, v59\n\t" \
    "v_exp_f32_e32 v60, v60\n\t" \
    "v_exp_f32_e32 v61, v61\n\t" \
    "v_mfma_f32_16x16x32_bf16 v[76:79], v[72:75], v[104:107], v[76:79]\n\t" \
    "v_exp_f32_e32 v62, v62\n\t" \
    "v_exp_f32_e32 v63, v63\n\t" \
    "v_cvt_pk_bf16_f32 v108, v56, v57\n\t" \
    "v_cvt_pk_bf16_f32 v109, v58, v59\n\t" \
    "v_cvt_pk_bf16_f32 v110, v60, v61\n\t" \
    "v_cvt_pk_bf16_f32 v111, v62, v63\n\t" \
    "s_waitcnt lgkmcnt(0)\n\t" \
    "v_mfma_f32_32x32x16_bf16 v[0:15], v[92:95], v[108:111], v[0:15]\n\t" \
    "v_mfma_f32_16x16x32_bf16 v[76:79], v[72:75], v[108:111], v[76:79]\n\t" \
    "s_nop 15\n\t"
#define K2_LOOP_CLOB "v48", "v49", "v50", "v51", "v52", "v53", "v54", "v55", "v56", "v57", "v58", "v59", "v60", "v61", "v62", "v63", "v92", "v93", "v94", "v95", "v96", "v97", "v98", "v99", "v100", "v101", "v102", "v103", "v104", "v105", "v106", "v107", "v108", "v109", "v110", "v111"

constexpr int K2_SLOT = 32768, K2_LDS = 3 * K2_SLOT;
#define EXP2(x) __builtin_amdgcn_exp2f(x)
__global__ __launch_bounds__(512, 2) void k2_attn(char* __restrict__ ws) {
    __shared__ __attribute__((aligned(1024))) char smem[K2_LDS];
    const unsigned lds0 = (unsigned)(uintptr_t)(LAS char*)smem;
    const int tid = threadIdx.x, lane = tid & 63, wid = __builtin_amdgcn_readfirstlane(tid >> 6);
    const int b = blockIdx.x, xcd = b & 7, j = b >> 3, head = xcd * 4 + (j >> 3), qt = j & 7, blk = qt * 8 + wid;
    STAMP_AT(1, 0);
    const char* kv = ws + WS_KV + (size_t)head * 262144;
    const char* qp = ws + WS_QF + (size_t)((head * 64 + blk) * 2) * 1024 + lane * 16;
    const bf16x8 q0 = *(const bf16x8*)qp, q1 = *(const bf16x8*)(qp + 1024);
    unsigned voff = wid * 4096 + lane * 16;
    const unsigned dbeg = lds0 + wid * 4096;
    asm volatile("s_mov_b32 m0, %2\n\ts_nop 0\n\tglobal_load_lds_dwordx4 %0, %1\n\tglobal_load_lds_dwordx4 %0, %1 offset:1024\n\tglobal_load_lds_dwordx4 %0, %1 offset:2048\n\tglobal_load_lds_dwordx4 %0, %1 offset:3072"
                 :: "v"(voff), "s"(kv), "s"(dbeg) : "memory");
    asm volatile("s_mov_b32 m0, %2\n\ts_nop 0\n\tglobal_load_lds_dwordx4 %0, %1\n\tglobal_load_lds_dwordx4 %0, %1 offset:1024\n\tglobal_load_lds_dwordx4 %0, %1 offset:2048\n\tglobal_load_lds_dwordx4 %0, %1 offset:3072"
                 :: "v"(voff), "s"(kv + K2_SLOT), "s"(dbeg + K2_SLOT) : "memory");
    asm volatile("s_waitcnt vmcnt(4)\n\ts_barrier" ::: "memory");
    if (wid < 2 && wid * 256 + b < 448) halo_fix(ws, wid * 256 + b, lane);
    STAMP_AT(1, 1);
    const unsigned one2 = (((lane >> 4) & 1) == ((lane >> 2) & 1)) ? 0x3F803F80u : 0u;
    const bf16x8 ones = asbf(u32x4{one2, one2, one2, one2});
    unsigned lbase = lds0 + lane * 16, cur = lbase, nxt = lbase + K2_SLOT;
    bf16x8 k0 = ldsrd128(cur), k1 = ldsrd128(cur + 1024), v0 = ldsrd128(cur + 2048);
    f32x16 z = {}, sa, negm, o;
    f32x4 l;
    sa = MFMA32(k0, q0, z); sa = MFMA32(k1, q1, sa);
    k0 = ldsrd128(cur + 4096); k1 = ldsrd128(cur + 4096 + 1024);
    { float m = sa[0];
#pragma unroll
      for (int r = 1; r < 16; ++r) m = __builtin_fmaxf(m, sa[r]);
      m = -swap_max(m);
#pragma unroll
      for (int r = 0; r < 16; ++r) { negm[r] = m; sa[r] += m; } }
    voff += 2 * K2_SLOT;
    unsigned cnt = 7, dst = dbeg + 2 * K2_SLOT, nx = K2_SLOT;
    asm volatile("s_nop 1\n\t" K2_LOOP_ASM
                 : "={v[0:15]}"(o), "={v[76:79]}"(l), "+{v[32:47]}"(sa), "+{v[80:83]}"(k0), "+{v[84:87]}"(k1), "+{v[88:91]}"(v0), "+{v112}"(cur), "+{v113}"(nxt), "+{v114}"(voff),
                   [cnt] "+s"(cnt), [dst] "+s"(dst), [nx] "+s"(nx)
                 : "{v[16:31]}"(negm), "{v[64:67]}"(q0), "{v[68:71]}"(q1), "{v[72:75]}"(ones), "{v115}"(lbase), [src] "s"(kv), [dbeg] "s"(dbeg), [dend] "s"(dbeg + 3 * K2_SLOT)
                 : K2_LOOP_CLOB, "scc", "memory");
    STAMP_AT(1, 2);
    float lsum = l[0];
    bool ok = lsum < 1.0e18f;
#pragma unroll
    for (int r = 0; r < 16; ++r) ok = ok && (__builtin_fabsf(o[r]) < 3.0e38f);
    if (__builtin_expect(__any(!ok), 0)) {
        const char* kg = kv + lane * 16;
        float m = -__builtin_inff();
        for (int t = 0; t < 64; ++t) {
            f32x16 s = MFMA32(*(const bf16x8*)(kg + t * 4096), q0, z); s = MFMA32(*(const bf16x8*)(kg + t * 4096 + 1024), q1, s);
#pragma unroll
            for (int r = 0; r < 16; ++r) m = __builtin_fmaxf(m, s[r]);
        }
        m = swap_max(m);
        o = z; l = f32x4{};
        for (int t = 0; t < 64; ++t) {
            f32x16 s = MFMA32(*(const bf16x8*)(kg + t * 4096), q0, z); s = MFMA32(*(const bf16x8*)(kg + t * 4096 + 1024), q1, s);
#pragma unroll
            for (int r = 0; r < 16; ++r) s[r] = EXP2(s[r] - m);
            const bf16x8 p0 = asbf(pack8(s, 0)), p1 = asbf(pack8(s, 1));
            o = MFMA32(*(const bf16x8*)(kg + t * 4096 + 2048), p0, o); o = MFMA32(*(const bf16x8*)(kg + t * 4096 + 3072), p1, o);
            l = MFMA16(ones, p0, l); l = MFMA16(ones, p1, l);
        }
        lsum = l[0];
    }
    const float inv = 1.0f / lsum;
#pragma unroll
    for (int r = 0; r < 16; ++r) o[r] *= inv;
    char* op = ws + WS_OF + (size_t)((blk * 32 + head) * 2) * 1024 + lane * 16;
    *(u32x4*)(op) = pack8(o, 0); *(u32x4*)(op + 1024) = pack8(o, 1);
    STAMP_AT(1, 3);
}

constexpr int K3_ST = 32768, K3_LDS = 4 * K3_ST;
__global__ __launch_bounds__(512, 2) void k3_out(const u16* __restrict__ woA, const u16* __restrict__ woB, const char* __restrict__ ws, u16* __restrict__ out) {
    __shared__ __attribute__((aligned(1024))) char smem[K3_LDS];
    const unsigned lds0 = (unsigned)(uintptr_t)(LAS char*)smem;
    const int tid = threadIdx.x, lane = tid & 63, wid = __builtin_amdgcn_readfirstlane(tid >> 6), h = lane >> 5;
    const int wm = wid >> 1, wn = wid & 1;
    const int b = blockIdx.x, xcd = b & 7, j = b >> 3, br = xcd >> 2, qd = xcd & 3;
    const int mt = qd * 4 + (j & 3), nt = j >> 2;
    STAMP_AT(2, 0);
    const char* of = ws + WS_OF + (size_t)(br * 64 + mt * 4) * 65536;
    const char* wo = (const char*)(br ? woB : woA) + nt * 256;
    unsigned vo[2], vw[2];
#pragma unroll
    for (int ii = 0; ii < 2; ++ii) {
        const int i = 2 * wid + ii;
        vo[ii] = (i >> 2) * 65536 + (i & 3) * 1024 + lane * 16;
        const int k = 4 * i + (lane >> 4), c = lane & 15;
        vw[ii] = k * 2048 + ((c ^ (((k & 1) << 1) | ((k & 2) << 2))) << 4);
    }
    const unsigned dst = lds0 + wid * 2048;
#define STAGE(t) do { _Pragma("unroll") for (int ii = 0; ii < 2; ++ii) { glds16(of + (t) * 4096, vo[ii], dst + ((t) & 3) * K3_ST + ii * 1024); \
        glds16(wo + (size_t)(t) * 64 * 2048, vw[ii], dst + ((t) & 3) * K3_ST + 16384 + ii * 1024); } } while (0)
    const unsigned ao = lds0 + wm * 4096 + lane * 16;
    unsigned aw[2];
    { const int q4 = (lane & 15) >> 2, p = lane & 3, cb = (lane >> 4) & 1;
#pragma unroll
      for (int nb = 0; nb < 2; ++nb) { const int col = wn * 64 + 32 * (p & 1) + 16 * nb + 4 * (2 * cb + (p >> 1)); const int c = col >> 3;
          aw[nb] = lds0 + 16384 + (4 * h + q4) * 256 + ((c ^ (((q4 & 1) << 1) | ((q4 & 2) << 2))) << 4) + ((col >> 2) & 1) * 8; } }
    STAGE(0); STAGE(1); STAGE(2);
    f32x16 acc[2] = {f32x16{}, f32x16{}};
#pragma unroll
    for (int t = 0; t < 16; ++t) {
        if (t < 14) asm volatile("s_waitcnt vmcnt(8)\n\ts_barrier" ::: "memory");
        else if (t == 14) asm volatile("s_waitcnt vmcnt(4)\n\ts_barrier" ::: "memory");
        else asm volatile("s_waitcnt vmcnt(0)\n\ts_barrier" ::: "memory");
        if (t == 0) STAMP_AT(2, 1);
        if (t + 3 < 16) STAGE(t + 3);
        const unsigned so = (t & 3) * K3_ST;
#pragma unroll
        for (int s = 0; s < 4; ++s) {
            const bf16x8 of_ = ldsrd128(ao + so + s * 1024);
#pragma unroll
            for (int nb = 0; nb < 2; ++nb) {
                const bf16x8 wf = cat8(ldstr(aw[nb] + so + s * 4096), ldstr(aw[nb] + so + s * 4096 + 2048));
                acc[nb] = MFMA32(wf, of_, acc[nb]);
            }
        }
        asm volatile("s_waitcnt lgkmcnt(0)" ::: "memory");
    }
    STAMP_AT(2, 2);
    const int blk = mt * 4 + wm, i = lane & 31;
    const int row = br ? ((blk >> 1) * 64 + 2 * i + (blk & 1)) : (blk * 32 + i);
    char* op = (char*)out + (size_t)row * 4096 + br * 2048 + nt * 256 + wn * 128 + h * 64;
    *(u32x4*)(op) = pack8(acc[0], 0); *(u32x4*)(op + 16) = pack8(acc[0], 1); *(u32x4*)(op + 32) = pack8(acc[1], 0); *(u32x4*)(op + 48) = pack8(acc[1], 1);
    STAMP_AT(2, 3);
#undef STAGE
}

extern "C" void kernel_launch(void* const* d_in, const int* in_sizes, int n_in, void* d_out, int out_size, void* d_ws, size_t ws_size, hipStream_t stream) {
    const u16* xA = (const u16*)d_in[0]; const u16* wA = (const u16*)d_in[1]; const u16* woA = (const u16*)d_in[2];
    const u16* xB = (const u16*)d_in[3]; const u16* wB = (const u16*)d_in[4]; const u16* woB = (const u16*)d_in[5];
    char* ws = (char*)d_ws;
    k1_qkv<<<256, 512, 0, stream>>>(xA, wA, xB, wB, woA, woB, ws);
    k2_attn<<<256, 512, 0, stream>>>(ws);
    k3_out<<<256, 512, 0, stream>>>(woA, woB, ws, (u16*)d_out);
}
```

```cpp
#define SPREAD
#include <hip/hip_runtime.h>
#include <stdint.h>

typedef unsigned short u16;
typedef unsigned long long u64;
typedef __bf16 bf16x8 __attribute__((ext_vector_type(8)));
typedef __bf16 bf16x2 __attribute__((ext_vector_type(2)));
typedef short s16x4 __attribute__((ext_vector_type(4)));
typedef float f32x16 __attribute__((ext_vector_type(16)));
typedef float f32x4 __attribute__((ext_vector_type(4)));
typedef float f32x2 __attribute__((ext_vector_type(2)));
typedef unsigned u32x4 __attribute__((ext_vector_type(4)));
typedef unsigned u32x2 __attribute__((ext_vector_type(2)));
typedef unsigned u32x8 __attribute__((ext_vector_type(8)));
typedef unsigned u32x16 __attribute__((ext_vector_type(16)));
#define LAS __attribute__((address_space(3)))
#define DI __device__ __forceinline__
#define SBAR() __builtin_amdgcn_sched_barrier(0)
#define MFMA32(a, b, c) __builtin_amdgcn_mfma_f32_32x32x16_bf16((a), (b), (c), 0, 0, 0)
#define MFMA16(a, b, c) __builtin_amdgcn_mfma_f32_16x16x32_bf16((a), (b), (c), 0, 0, 0)
#define EXP2(x) __builtin_amdgcn_exp2f(x)

constexpr float QSCALE = 0.17677669529663687f * 1.4426950408889634f;

constexpr size_t WS_QF = 0;
constexpr size_t WS_KV = WS_QF + (4u << 20);
constexpr size_t WS_OF = WS_KV + (8u << 20);
constexpr size_t WS_HALO = WS_OF + (8u << 20);
constexpr size_t WS_XP = WS_HALO + (2u << 20);
constexpr size_t WS_FX = WS_XP + (5u << 20);
#define FS 128
constexpr size_t WS_FQ = WS_FX + 1024 * FS;
constexpr size_t WS_FK = WS_FQ + 1024 * FS;
constexpr size_t WS_FO = WS_FK + 1024 * FS;
constexpr size_t WS_FH = WS_FO + 4096 * FS;
constexpr size_t WS_EP = WS_FH + 256 * FS;

#define SLP 8
#define STAMP_AT(K, I)
#define SA_(I)
#define SB_(I)

#define SC_(I)
DI unsigned cvtpk(float lo, float hi) { f32x2 v = {lo, hi}; bf16x2 b = __builtin_convertvector(v, bf16x2); return __builtin_bit_cast(unsigned, b); }
DI u32x4 pack8(const f32x16& a, int s) { u32x4 r; r[0] = cvtpk(a[8 * s], a[8 * s + 1]); r[1] = cvtpk(a[8 * s + 2], a[8 * s + 3]); r[2] = cvtpk(a[8 * s + 4], a[8 * s + 5]); r[3] = cvtpk(a[8 * s + 6], a[8 * s + 7]); return r; }
DI u32x4 asu(bf16x8 v) { return __builtin_bit_cast(u32x4, v); }
DI u32x16 cat16(u32x4 a, u32x4 b, u32x4 c, u32x4 d) { return u32x16{a[0], a[1], a[2], a[3], b[0], b[1], b[2], b[3], c[0], c[1], c[2], c[3], d[0], d[1], d[2], d[3]}; }
DI bf16x8 asbf(u32x4 v) { return __builtin_bit_cast(bf16x8, v); }
DI void glds16(const void* sbase, unsigned voff, unsigned lds) {
    asm volatile("s_mov_b32 m0, %2\n\ts_nop 0\n\tglobal_load_lds_dwordx4 %0, %1" :: "v"(voff), "s"(sbase), "s"(lds) : "memory");
}
DI void glds16_dev(const void* sbase, unsigned voff, unsigned lds) {
    asm volatile("s_mov_b32 m0, %2\n\ts_nop 0\n\tglobal_load_lds_dwordx4 %0, %1 sc1" :: "v"(voff), "s"(sbase), "s"(lds) : "memory");
}
DI void glds4k_dev(const void* sbase, unsigned voff, unsigned lds) {
    asm volatile("s_mov_b32 m0, %2\n\ts_nop 0\n\tglobal_load_lds_dwordx4 %0, %1 sc1\n\tglobal_load_lds_dwordx4 %0, %1 offset:1024 sc1\n\t"
                 "global_load_lds_dwordx4 %0, %1 offset:2048 sc1\n\tglobal_load_lds_dwordx4 %0, %1 offset:3072 sc1" :: "v"(voff), "s"(sbase), "s"(lds) : "memory");
}
template <int OFF> DI void st16_dev(const void* sbase, unsigned voff, u32x4 v) {
    asm volatile("global_store_dwordx4 %0, %1, %2 offset:%3 sc1\n\ts_nop 1" :: "v"(voff), "v"(v), "s"(sbase), "i"(OFF) : "memory");
}
DI void drain() { asm volatile("s_waitcnt vmcnt(0)" ::: "memory"); }
DI void flag_set(const void* sbase, unsigned voff, u64 tag) {
    asm volatile("global_store_dwordx2 %0, %1, %2 sc1" :: "v"(voff), "v"(tag), "s"(sbase) : "memory");
}
DI void flag_wait(const void* sbase, unsigned voff, u64 tag) {
    const u64 t0 = __builtin_amdgcn_s_memrealtime();
    for (;;) {
        u64 v;
        asm volatile("global_load_dwordx2 %0, %1, %2 sc1\n\ts_waitcnt vmcnt(0)" : "=v"(v) : "v"(voff), "s"(sbase) : "memory");
        if (__all(v == tag)) break;
        if (__builtin_amdgcn_s_memrealtime() - t0 > 2000000ull) break;
        __builtin_amdgcn_s_sleep(SLP);
    }
}
DI u64 flag_wait_some(const void* sbase, unsigned voff, u64 tag, u64 need) {
    const u64 t0 = __builtin_amdgcn_s_memrealtime();
    for (;;) {
        u64 v;
        asm volatile("global_load_dwordx2 %0, %1, %2 sc1\n\ts_waitcnt vmcnt(0)" : "=v"(v) : "v"(voff), "s"(sbase) : "memory");
        const u64 m = __ballot(v == tag);
        if ((m & need) == need || __builtin_amdgcn_s_memrealtime() - t0 > 2000000ull) return m;
        __builtin_amdgcn_s_sleep(SLP);
    }
}
DI bf16x8 ldsrd128(unsigned addr) { return *(const LAS bf16x8*)(uintptr_t)addr; }
DI void ldswr128(unsigned addr, u32x4 v) { *(LAS u32x4*)(uintptr_t)addr = v; }
DI s16x4 ldstr(unsigned addr) { return __builtin_amdgcn_ds_read_tr16_b64_v4i16((LAS s16x4*)(uintptr_t)addr); }
DI bf16x8 cat8(s16x4 lo, s16x4 hi) { typedef short s16x8 __attribute__((ext_vector_type(8))); s16x8 v = {lo[0], lo[1], lo[2], lo[3], hi[0], hi[1], hi[2], hi[3]}; return __builtin_bit_cast(bf16x8, v); }
DI float swap_max(float m) { auto r = __builtin_amdgcn_permlane32_swap(__float_as_uint(m), __float_as_uint(m), false, false); return __builtin_fmaxf(__uint_as_float(r[0]), __uint_as_float(r[1])); }
DI float swap_add(float m) { auto r = __builtin_amdgcn_permlane32_swap(__float_as_uint(m), __float_as_uint(m), false, false); return __uint_as_float(r[0]) + __uint_as_float(r[1]); }
#define FRESH() asm volatile("" : "+v"(lane), "+s"(ws))
#define BARRIER_L() asm volatile("s_waitcnt lgkmcnt(0)\n\ts_barrier" ::: "memory")
#define WAITBAR_(n) asm volatile("s_waitcnt vmcnt(%0) lgkmcnt(0)\n\ts_barrier" :: "i"(n) : "memory")
#define WAITBAR(n) do { switch (n) { case 0: WAITBAR_(0); break; case 2: WAITBAR_(2); break; case 3: WAITBAR_(3); break; case 4: WAITBAR_(4); break; case 6: WAITBAR_(6); break; \
    case 7: WAITBAR_(7); break; case 8: WAITBAR_(8); break; case 10: WAITBAR_(10); break; case 12: WAITBAR_(12); break; default: WAITBAR_(0); } } while (0)

struct LocalPart { f32x16 o; float m, l; };
DI f32x16 band_mask(int lane) {
    const int i = lane & 31, h = lane >> 5;
    f32x16 c;
#pragma unroll
    for (int r = 0; r < 16; ++r) { const int j = (r & 3) + 8 * (r >> 2) + 4 * h; c[r] = ((unsigned)(i - j) <= 7u) ? 0.f : -__builtin_inff(); }
    return c;
}
DI float max3(float a, float b, float c) { float r; asm("v_max3_f32 %0, %1, %2, %3" : "=v"(r) : "v"(a), "v"(b), "v"(c)); return r; }
DI void local_own(bf16x8 q0, bf16x8 q1, bf16x8 k0, bf16x8 k1, bf16x8 v0, bf16x8 v1, const f32x16& band, LocalPart& P) {
    const f32x16 z = {};
    f32x16 so = MFMA32(k0, q0, band); so = MFMA32(k1, q1, so);
    float m = __builtin_fmaxf(so[0], so[1]);
#pragma unroll
    for (int r = 2; r < 16; r += 2) m = __builtin_fmaxf(__builtin_fmaxf(m, so[r]), so[r + 1]);
    m = swap_max(m);
    float l = 0.f;
#pragma unroll
    for (int r = 0; r < 16; ++r) { so[r] = EXP2(so[r] - m); l += so[r]; }
    P.o = MFMA32(v0, asbf(pack8(so, 0)), z); P.o = MFMA32(v1, asbf(pack8(so, 1)), P.o);
    P.m = m; P.l = l;
}
DI void local_prev(bf16x8 q0, bf16x8 q1, bf16x8 kp0, bf16x8 kp1, bf16x8 vp1, int lane, LocalPart& P) {
    const int i = lane & 31, h = lane >> 5;
    const f32x16 z = {};
    f32x16 sp = MFMA32(kp0, q0, z); sp = MFMA32(kp1, q1, sp);
    const float NEG = -__builtin_inff();
    float m = P.m;
#pragma unroll
    for (int r = 12; r < 16; ++r) { const int j = (r & 3) + 24 + 4 * h; sp[r] = (j >= i + 25) ? sp[r] : NEG; m = __builtin_fmaxf(m, sp[r]); }
    m = swap_max(m);
    const float f = EXP2(P.m - m);
    float l = P.l * f;
#pragma unroll
    for (int r = 12; r < 16; ++r) { sp[r] = EXP2(sp[r] - m); l += sp[r]; }
#pragma unroll
    for (int r = 0; r < 16; ++r) P.o[r] *= f;
    P.o = MFMA32(vp1, asbf(u32x4{0u, 0u, cvtpk(sp[12], sp[13]), cvtpk(sp[14], sp[15])}), P.o);
    P.m = m; P.l = l;
}
DI void local_fin(LocalPart& P) {
    const float inv = 1.0f / swap_add(P.l);
#pragma unroll
    for (int r = 0; r < 16; ++r) P.o[r] *= inv;
    const u32x4 o0 = pack8(P.o, 0), o1 = pack8(P.o, 1);
#pragma unroll
    for (int r = 0; r < 4; ++r) { P.o[r] = __uint_as_float(o0[r]); P.o[4 + r] = __uint_as_float(o1[r]); }
}
DI u32x4 quad(const f32x16& a, int i) { return __builtin_bit_cast(u32x4, f32x4{a[4 * i], a[4 * i + 1], a[4 * i + 2], a[4 * i + 3]}); }

#define ATT_L6C \
    "s_nop 1\n\t" \
    "s_cmp_eq_u32 %[hl], 0\n\t" \
    "s_cbranch_scc1 10f\n\t" \
    "global_load_dwordx4 v[144:147], v143, %[ws] offset:-3072 sc1\n\t" \
    "global_load_dwordx4 v[148:151], v143, %[ws] offset:-2048 sc1\n\t" \
    "global_load_dwordx4 v[152:155], v143, %[ws] offset:-1024 sc1\n\t" \
    "global_load_dwordx4 v[156:159], v143, %[ws] offset:0 sc1\n\t" \
    "global_load_dwordx4 v[160:163], v143, %[ws] offset:1024 sc1\n\t" \
    "global_load_dwordx4 v[164:167], v143, %[ws] offset:2048 sc1\n\t" \
    "10:\n" \
    "s_setprio 3\n\t" \
    "s_waitcnt lgkmcnt(0)\n\t" \
    "v_mfma_f32_32x32x16_bf16 v[48:63], v[80:83], v[64:67], v[16:31]\n\t" \
    "v_exp_f32_e32 v32, v32\n\t" \
    "v_exp_f32_e32 v33, v33\n\t" \
    "v_exp_f32_e32 v34, v34\n\t" \
    "v_mfma_f32_32x32x16_bf16 v[48:63], v[84:87], v[68:71], v[48:63]\n\t" \
    "ds_read_b128 v[80:83], v112 offset:8192\n\t" \
    "ds_read_b128 v[84:87], v112 offset:9216\n\t" \
    "v_exp_f32_e32 v35, v35\n\t" \
    "v_exp_f32_e32 v36, v36\n\t" \
    "v_cvt_pk_bf16_f32 v104, v32, v33\n\t" \
    "v_cvt_pk_bf16_f32 v105, v34, v35\n\t" \
    "ds_read_b128 v[100:103], v112 offset:3072\n\t" \
    "ds_read_b128 v[96:99], v112 offset:6144\n\t" \
    "v_exp_f32_e32 v37, v37\n\t" \
    "v_exp_f32_e32 v38, v38\n\t" \
    "v_exp_f32_e32 v39, v39\n\t" \
    "v_exp_f32_e32 v40, v40\n\t" \
    "v_cvt_pk_bf16_f32 v106, v36, v37\n\t" \
    "v_cvt_pk_bf16_f32 v107, v38, v39\n\t" \
    "v_exp_f32_e32 v41, v41\n\t" \
    "v_exp_f32_e32 v42, v42\n\t" \
    "v_mfma_f32_32x32x16_bf16 v[0:15], v[88:91], v[104:107], 0\n\t" \
    "v_exp_f32_e32 v43, v43\n\t" \
    "v_exp_f32_e32 v44, v44\n\t" \
    "v_exp_f32_e32 v45, v45\n\t" \
    "v_mfma_f32_16x16x32_bf16 v[76:79], v[72:75], v[104:107], 0\n\t" \
    "v_exp_f32_e32 v46, v46\n\t" \
    "v_exp_f32_e32 v47, v47\n\t" \
    "v_cvt_pk_bf16_f32 v108, v40, v41\n\t" \
    "v_cvt_pk_bf16_f32 v109, v42, v43\n\t" \
    "v_cvt_pk_bf16_f32 v110, v44, v45\n\t" \
    "v_cvt_pk_bf16_f32 v111, v46, v47\n\t" \
    "s_waitcnt lgkmcnt(0)\n\t" \
    "v_mfma_f32_32x32x16_bf16 v[32:47], v[80:83], v[64:67], v[16:31]\n\t" \
    "v_exp_f32_e32 v48, v48\n\t" \
    "v_exp_f32_e32 v49, v49\n\t" \
    "v_exp_f32_e32 v50, v50\n\t" \
    "v_mfma_f32_32x32x16_bf16 v[32:47], v[84:87], v[68:71], v[32:47]\n\t" \
    "ds_read_b128 v[80:83], v112 offset:12288\n\t" \
    "ds_read_b128 v[84:87], v112 offset:13312\n\t" \
    "v_exp_f32_e32 v51, v51\n\t" \
    "v_exp_f32_e32 v52, v52\n\t" \
    "v_cvt_pk_bf16_f32 v104, v48, v49\n\t" \
    "v_cvt_pk_bf16_f32 v105, v50, v51\n\t" \
    "v_mfma_f32_32x32x16_bf16 v[0:15], v[100:103], v[108:111], v[0:15]\n\t" \
    "ds_read_b128 v[92:95], v112 offset:7168\n\t" \
    "ds_read_b128 v[88:91], v112 offset:10240\n\t" \
    "v_exp_f32_e32 v53, v53\n\t" \
    "v_exp_f32_e32 v54, v54\n\t" \
    "v_exp_f32_e32 v55, v55\n\t" \
    "v_mfma_f32_16x16x32_bf16 v[76:79], v[72:75], v[108:111], v[76:79]\n\t" \
    "v_exp_f32_e32 v56, v56\n\t" \
    "v_cvt_pk_bf16_f32 v106, v52, v53\n\t" \
    "v_cvt_pk_bf16_f32 v107, v54, v55\n\t" \
    "v_exp_f32_e32 v57, v57\n\t" \
    "v_exp_f32_e32 v58, v58\n\t" \
    "v_mfma_f32_32x32x16_bf16 v[0:15], v[96:99], v[104:107], v[0:15]\n\t" \
    "v_exp_f32_e32 v59, v59\n\t" \
    "v_exp_f32_e32 v60, v60\n\t" \
    "v_exp_f32_e32 v61, v61\n\t" \
    "v_mfma_f32_16x16x32_bf16 v[76:79], v[72:75], v[104:107], v[76:79]\n\t" \
    "v_exp_f32_e32 v62, v62\n\t" \
    "v_exp_f32_e32 v63, v63\n\t" \
    "v_cvt_pk_bf16_f32 v108, v56, v57\n\t" \
    "v_cvt_pk_bf16_f32 v109, v58, v59\n\t" \
    "v_cvt_pk_bf16_f32 v110, v60, v61\n\t" \
    "v_cvt_pk_bf16_f32 v111, v62, v63\n\t" \
    "s_setprio 2\n\t" \
    "s_waitcnt lgkmcnt(0)\n\t" \
    "v_mfma_f32_32x32x16_bf16 v[48:63], v[80:83], v[64:67], v[16:31]\n\t" \
    "v_exp_f32_e32 v32, v32\n\t" \
    "v_exp_f32_e32 v33, v33\n\t" \
    "v_exp_f32_e32 v34, v34\n\t" \
    "v_mfma_f32_32x32x16_bf16 v[48:63], v[84:87], v[68:71], v[48:63]\n\t" \
    "ds_read_b128 v[80:83], v112 offset:16384\n\t" \
    "ds_read_b128 v[84:87], v112 offset:17408\n\t" \
    "v_exp_f32_e32 v35, v35\n\t" \
    "v_exp_f32_e32 v36, v36\n\t" \
    "v_cvt_pk_bf16_f32 v104, v32, v33\n\t" \
    "v_cvt_pk_bf16_f32 v105, v34, v35\n\t" \
    "v_mfma_f32_32x32x16_bf16 v[0:15], v[92:95], v[108:111], v[0:15]\n\t" \
    "ds_read_b128 v[100:103], v112 offset:11264\n\t" \
    "ds_read_b128 v[96:99], v112 offset:14336\n\t" \
    "v_exp_f32_e32 v37, v37\n\t" \
    "v_exp_f32_e32 v38, v38\n\t" \
    "v_exp_f32_e32 v39, v39\n\t" \
    "v_mfma_f32_16x16x32_bf16 v[76:79], v[72:75], v[108:111], v[76:79]\n\t" \
    "v_exp_f32_e32 v40, v40\n\t" \
    "v_cvt_pk_bf16_f32 v106, v36, v37\n\t" \
    "v_cvt_pk_bf16_f32 v107, v38, v39\n\t" \
    "v_exp_f32_e32 v41, v41\n\t" \
    "v_exp_f32_e32 v42, v42\n\t" \
    "v_mfma_f32_32x32x16_bf16 v[0:15], v[88:91], v[104:107], v[0:15]\n\t" \
    "v_exp_f32_e32 v43, v43\n\t" \
    "v_exp_f32_e32 v44, v44\n\t" \
    "v_exp_f32_e32 v45, v45\n\t" \
    "v_mfma_f32_16x16x32_bf16 v[76:79], v[72:75], v[104:107], v[76:79]\n\t" \
    "v_exp_f32_e32 v46, v46\n\t" \
    "v_exp_f32_e32 v47, v47\n\t" \
    "v_cvt_pk_bf16_f32 v108, v40, v41\n\t" \
    "v_cvt_pk_bf16_f32 v109, v42, v43\n\t" \
    "v_cvt_pk_bf16_f32 v110, v44, v45\n\t" \
    "v_cvt_pk_bf16_f32 v111, v46, v47\n\t" \
    "s_waitcnt lgkmcnt(0)\n\t" \
    "v_mfma_f32_32x32x16_bf16 v[32:47], v[80:83], v[64:67], v[16:31]\n\t" \
    "v_exp_f32_e32 v48, v48\n\t" \
    "v_exp_f32_e32 v49, v49\n\t" \
    "v_exp_f32_e32 v50, v50\n\t" \
    "v_mfma_f32_32x32x16_bf16 v[32:47], v[84:87], v[68:71], v[32:47]\n\t" \
    "ds_read_b128 v[80:83], v112 offset:20480\n\t" \
    "ds_read_b128 v[84:87], v112 offset:21504\n\t" \
    "v_exp_f32_e32 v51, v51\n\t" \
    "v_exp_f32_e32 v52, v52\n\t" \
    "v_cvt_pk_bf16_f32 v104, v48, v49\n\t" \
    "v_cvt_pk_bf16_f32 v105, v50, v51\n\t" \
    "v_mfma_f32_32x32x16_bf16 v[0:15], v[100:103], v[108:111], v[0:15]\n\t" \
    "ds_read_b128 v[92:95], v112 offset:15360\n\t" \
    "ds_read_b128 v[88:91], v112 offset:18432\n\t" \
    "v_exp_f32_e32 v53, v53\n\t" \
    "v_exp_f32_e32 v54, v54\n\t" \
    "v_exp_f32_e32 v55, v55\n\t" \
    "v_mfma_f32_16x16x32_bf16 v[76:79], v[72:75], v[108:111], v[76:79]\n\t" \
    "v_exp_f32_e32 v56, v56\n\t" \
    "v_cvt_pk_bf16_f32 v106, v52, v53\n\t" \
    "v_cvt_pk_bf16_f32 v107, v54, v55\n\t" \
    "v_exp_f32_e32 v57, v57\n\t" \
    "v_exp_f32_e32 v58, v58\n\t" \
    "v_mfma_f32_32x32x16_bf16 v[0:15], v[96:99], v[104:107], v[0:15]\n\t" \
    "v_exp_f32_e32 v59, v59\n\t" \
    "v_exp_f32_e32 v60, v60\n\t" \
    "v_exp_f32_e32 v61, v61\n\t" \
    "v_mfma_f32_16x16x32_bf16 v[76:79], v[72:75], v[104:107], v[76:79]\n\t" \
    "v_exp_f32_e32 v62, v62\n\t" \
    "v_exp_f32_e32 v63, v63\n\t" \
    "v_cvt_pk_bf16_f32 v108, v56, v57\n\t" \
    "v_cvt_pk_bf16_f32 v109, v58, v59\n\t" \
    "v_cvt_pk_bf16_f32 v110, v60, v61\n\t" \
    "v_cvt_pk_bf16_f32 v111, v62, v63\n\t" \
    "s_mov_b32 m0, %[fld]\n\t" \
    "s_nop 0\n\t" \
    "global_load_lds_dwordx4 v142, %[ws] sc1\n\t" \
    "s_setprio 1\n\t" \
    "s_waitcnt lgkmcnt(0)\n\t" \
    "v_mfma_f32_32x32x16_bf16 v[48:63], v[80:83], v[64:67], v[16:31]\n\t" \
    "v_exp_f32_e32 v32, v32\n\t" \
    "v_exp_f32_e32 v33, v33\n\t" \
    "v_exp_f32_e32 v34, v34\n\t" \
    "v_mfma_f32_32x32x16_bf16 v[48:63], v[84:87], v[68:71], v[48:63]\n\t" \
    "ds_read_b128 v[80:83], v112 offset:24576\n\t" \
    "ds_read_b128 v[84:87], v112 offset:25600\n\t" \
    "v_exp_f32_e32 v35, v35\n\t" \
    "v_exp_f32_e32 v36, v36\n\t" \
    "v_cvt_pk_bf16_f32 v104, v32, v33\n\t" \
    "v_cvt_pk_bf16_f32 v105, v34, v35\n\t" \
    "v_mfma_f32_32x32x16_bf16 v[0:15], v[92:95], v[108:111], v[0:15]\n\t" \
    "ds_read_b128 v[100:103], v112 offset:19456\n\t" \
    "ds_read_b128 v[96:99], v112 offset:22528\n\t" \
    "v_exp_f32_e32 v37, v37\n\t" \
    "v_exp_f32_e32 v38, v38\n\t" \
    "v_exp_f32_e32 v39, v39\n\t" \
    "v_mfma_f32_16x16x32_bf16 v[76:79], v[72:75], v[108:111], v[76:79]\n\t" \
    "v_exp_f32_e32 v40, v40\n\t" \
    "v_cvt_pk_bf16_f32 v106, v36, v37\n\t" \
    "v_cvt_pk_bf16_f32 v107, v38, v39\n\t" \
    "v_exp_f32_e32 v41, v41\n\t" \
    "v_exp_f32_e32 v42, v42\n\t" \
    "v_mfma_f32_32x32x16_bf16 v[0:15], v[88:91], v[104:107], v[0:15]\n\t" \
    "v_exp_f32_e32 v43, v43\n\t" \
    "v_exp_f32_e32 v44, v44\n\t" \
    "v_exp_f32_e32 v45, v45\n\t" \
    "v_mfma_f32_16x16x32_bf16 v[76:79], v[72:75], v[104:107], v[76:79]\n\t" \
    "v_exp_f32_e32 v46, v46\n\t" \
    "v_exp_f32_e32 v47, v47\n\t" \
    "v_cvt_pk_bf16_f32 v108, v40, v41\n\t" \
    "v_cvt_pk_bf16_f32 v109, v42, v43\n\t" \
    "v_cvt_pk_bf16_f32 v110, v44, v45\n\t" \
    "v_cvt_pk_bf16_f32 v111, v46, v47\n\t" \
    "s_waitcnt lgkmcnt(0)\n\t" \
    "v_mfma_f32_32x32x16_bf16 v[32:47], v[80:83], v[64:67], v[16:31]\n\t" \
    "v_exp_f32_e32 v48, v48\n\t" \
    "v_exp_f32_e32 v49, v49\n\t" \
    "v_exp_f32_e32 v50, v50\n\t" \
    "v_mfma_f32_32x32x16_bf16 v[32:47], v[84:87], v[68:71], v[32:47]\n\t" \
    "ds_read_b128 v[80:83], v112 offset:28672\n\t" \
    "ds_read_b128 v[84:87], v112 offset:29696\n\t" \
    "v_exp_f32_e32 v51, v51\n\t" \
    "v_exp_f32_e32 v52, v52\n\t" \
    "v_cvt_pk_bf16_f32 v104, v48, v49\n\t" \
    "v_cvt_pk_bf16_f32 v105, v50, v51\n\t" \
    "v_mfma_f32_32x32x16_bf16 v[0:15], v[100:103], v[108:111], v[0:15]\n\t" \
    "ds_read_b128 v[92:95], v112 offset:23552\n\t" \
    "ds_read_b128 v[88:91], v112 offset:26624\n\t" \
    "v_exp_f32_e32 v53, v53\n\t" \
    "v_exp_f32_e32 v54, v54\n\t" \
    "v_exp_f32_e32 v55, v55\n\t" \
    "v_mfma_f32_16x16x32_bf16 v[76:79], v[72:75], v[108:111], v[76:79]\n\t" \
    "v_exp_f32_e32 v56, v56\n\t" \
    "v_cvt_pk_bf16_f32 v106, v52, v53\n\t" \
    "v_cvt_pk_bf16_f32 v107, v54, v55\n\t" \
    "v_exp_f32_e32 v57, v57\n\t" \
    "v_exp_f32_e32 v58, v58\n\t" \
    "v_mfma_f32_32x32x16_bf16 v[0:15], v[96:99], v[104:107], v[0:15]\n\t" \
    "v_exp_f32_e32 v59, v59\n\t" \
    "v_exp_f32_e32 v60, v60\n\t" \
    "v_exp_f32_e32 v61, v61\n\t" \
    "v_mfma_f32_16x16x32_bf16 v[76:79], v[72:75], v[104:107], v[76:79]\n\t" \
    "v_exp_f32_e32 v62, v62\n\t" \
    "v_exp_f32_e32 v63, v63\n\t" \
    "v_cvt_pk_bf16_f32 v108, v56, v57\n\t" \
    "v_cvt_pk_bf16_f32 v109, v58, v59\n\t" \
    "v_cvt_pk_bf16_f32 v110, v60, v61\n\t" \
    "v_cvt_pk_bf16_f32 v111, v62, v63\n\t" \
    "s_setprio 0\n\t" \
    "s_cmp_eq_u32 %[hl], 0\n\t" \
    "s_cbranch_scc1 11f\n\t" \
    "s_waitcnt vmcnt(1)\n\t" \
    "11:\n" \
    "s_waitcnt lgkmcnt(0)\n\t" \
    "s_nop 15\n\t"
#define ATT_XT \
    "s_nop 1\n\t" \
    "s_waitcnt lgkmcnt(0)\n\t" \
    "v_mfma_f32_32x32x16_bf16 v[48:63], v[80:83], v[64:67], v[16:31]\n\t" \
    "v_exp_f32_e32 v32, v32\n\t" \
    "v_exp_f32_e32 v33, v33\n\t" \
    "v_exp_f32_e32 v34, v34\n\t" \
    "v_mfma_f32_32x32x16_bf16 v[48:63], v[84:87], v[68:71], v[48:63]\n\t" \
    "v_exp_f32_e32 v35, v35\n\t" \
    "v_exp_f32_e32 v36, v36\n\t" \
    "v_cvt_pk_bf16_f32 v104, v32, v33\n\t" \
    "v_cvt_pk_bf16_f32 v105, v34, v35\n\t" \
    "v_mfma_f32_32x32x16_bf16 v[0:15], v[92:95], v[108:111], v[0:15]\n\t" \
    "ds_read_b128 v[100:103], v112 offset:27648\n\t" \
    "ds_read_b128 v[96:99], v112 offset:30720\n\t" \
    "v_exp_f32_e32 v37, v37\n\t" \
    "v_exp_f32_e32 v38, v38\n\t" \
    "v_exp_f32_e32 v39, v39\n\t" \
    "v_mfma_f32_16x16x32_bf16 v[76:79], v[72:75], v[108:111], v[76:79]\n\t" \
    "v_exp_f32_e32 v40, v40\n\t" \
    "v_cvt_pk_bf16_f32 v106, v36, v37\n\t" \
    "v_cvt_pk_bf16_f32 v107, v38, v39\n\t" \
    "v_exp_f32_e32 v41, v41\n\t" \
    "v_exp_f32_e32 v42, v42\n\t" \
    "v_mfma_f32_32x32x16_bf16 v[0:15], v[88:91], v[104:107], v[0:15]\n\t" \
    "v_exp_f32_e32 v43, v43\n\t" \
    "v_exp_f32_e32 v44, v44\n\t" \
    "v_exp_f32_e32 v45, v45\n\t" \
    "v_mfma_f32_16x16x32_bf16 v[76:79], v[72:75], v[104:107], v[76:79]\n\t" \
    "v_exp_f32_e32 v46, v46\n\t" \
    "v_exp_f32_e32 v47, v47\n\t" \
    "v_cvt_pk_bf16_f32 v108, v40, v41\n\t" \
    "v_cvt_pk_bf16_f32 v109, v42, v43\n\t" \
    "v_cvt_pk_bf16_f32 v110, v44, v45\n\t" \
    "v_cvt_pk_bf16_f32 v111, v46, v47\n\t" \
    "s_waitcnt lgkmcnt(0)\n\t" \
    "v_exp_f32_e32 v48, v48\n\t" \
    "v_exp_f32_e32 v49, v49\n\t" \
    "v_exp_f32_e32 v50, v50\n\t" \
    "v_exp_f32_e32 v51, v51\n\t" \
    "v_exp_f32_e32 v52, v52\n\t" \
    "v_cvt_pk_bf16_f32 v104, v48, v49\n\t" \
    "v_cvt_pk_bf16_f32 v105, v50, v51\n\t" \
    "v_mfma_f32_32x32x16_bf16 v[0:15], v[100:103], v[108:111], v[0:15]\n\t" \
    "ds_read_b128 v[92:95], v112 offset:31744\n\t" \
    "v_exp_f32_e32 v53, v53\n\t" \
    "v_exp_f32_e32 v54, v54\n\t" \
    "v_exp_f32_e32 v55, v55\n\t" \
    "v_mfma_f32_16x16x32_bf16 v[76:79], v[72:75], v[108:111], v[76:79]\n\t" \
    "v_exp_f32_e32 v56, v56\n\t" \
    "v_cvt_pk_bf16_f32 v106, v52, v53\n\t" \
    "v_cvt_pk_bf16_f32 v107, v54, v55\n\t" \
    "v_exp_f32_e32 v57, v57\n\t" \
    "v_exp_f32_e32 v58, v58\n\t" \
    "v_mfma_f32_32x32x16_bf16 v[0:15], v[96:99], v[104:107], v[0:15]\n\t" \
    "v_exp_f32_e32 v59, v59\n\t" \
    "v_exp_f32_e32 v60, v60\n\t" \
    "v_exp_f32_e32 v61, v61\n\t" \
    "v_mfma_f32_16x16x32_bf16 v[76:79], v[72:75], v[104:107], v[76:79]\n\t" \
    "v_exp_f32_e32 v62, v62\n\t" \
    "v_exp_f32_e32 v63, v63\n\t" \
    "v_cvt_pk_bf16_f32 v108, v56, v57\n\t" \
    "v_cvt_pk_bf16_f32 v109, v58, v59\n\t" \
    "v_cvt_pk_bf16_f32 v110, v60, v61\n\t" \
    "v_cvt_pk_bf16_f32 v111, v62, v63\n\t" \
    "s_waitcnt lgkmcnt(0)\n\t" \
    "v_mfma_f32_32x32x16_bf16 v[0:15], v[92:95], v[108:111], v[0:15]\n\t" \
    "v_mfma_f32_16x16x32_bf16 v[76:79], v[72:75], v[108:111], v[76:79]\n\t" \
    "s_nop 15\n\t"
#define ATT_MAIN \
    "s_mov_b64 s[48:49], %[tab]\n\t" \
    "s_mov_b32 s46, %[k0]\n\t" \
    "s_and_b32 s40, s48, 63\n\t" \
    "s_lshl_b32 s40, s40, 12\n\t" \
    "s_add_u32 s44, %[srclo], s40\n\t" \
    "s_addc_u32 s45, %[srchi], 0\n\t" \
    "s_lshr_b64 s[48:49], s[48:49], 6\n\t" \
    "s_add_u32 s46, s46, 1\n\t" \
    "s_mov_b64 vcc, -1\n\t" \
    "s_waitcnt vmcnt(0)\n\t" \
    "ds_read_b64 v[120:121], v173\n\t" \
    "s_waitcnt lgkmcnt(0)\n\t" \
    "1:\n" \
    "s_cmp_eq_u32 %[cnt], 4\n\t" \
    "s_cbranch_scc1 5f\n\t" \
    "s_waitcnt vmcnt(0)\n\t" \
    "5:\n" \
    "s_waitcnt vmcnt(9)\n\t" \
    "s_barrier\n\t" \
    "s_bitcmp1_b32 %[act], %[cnt]\n\t" \
    "s_cbranch_scc1 30f\n\t" \
    "2:\n" \
    "s_setprio 3\n\t" \
    "s_waitcnt lgkmcnt(0)\n\t" \
    "v_mfma_f32_32x32x16_bf16 v[48:63], v[80:83], v[64:67], v[16:31]\n\t" \
    "v_exp_f32_e32 v32, v32\n\t" \
    "v_exp_f32_e32 v33, v33\n\t" \
    "v_exp_f32_e32 v34, v34\n\t" \
    "v_mfma_f32_32x32x16_bf16 v[48:63], v[84:87], v[68:71], v[48:63]\n\t" \
    "ds_read_b128 v[80:83], v113 offset:0\n\t" \
    "ds_read_b128 v[84:87], v113 offset:1024\n\t" \
    "v_exp_f32_e32 v35, v35\n\t" \
    "v_exp_f32_e32 v36, v36\n\t" \
    "v_cvt_pk_bf16_f32 v104, v32, v33\n\t" \
    "v_cvt_pk_bf16_f32 v105, v34, v35\n\t" \
    "v_mfma_f32_32x32x16_bf16 v[0:15], v[92:95], v[108:111], v[0:15]\n\t" \
    "ds_read_b128 v[100:103], v112 offset:27648\n\t" \
    "ds_read_b128 v[96:99], v112 offset:30720\n\t" \
    "v_exp_f32_e32 v37, v37\n\t" \
    "v_exp_f32_e32 v38, v38\n\t" \
    "v_exp_f32_e32 v39, v39\n\t" \
    "v_mfma_f32_16x16x32_bf16 v[76:79], v[72:75], v[108:111], v[76:79]\n\t" \
    "v_exp_f32_e32 v40, v40\n\t" \
    "v_cvt_pk_bf16_f32 v106, v36, v37\n\t" \
    "v_cvt_pk_bf16_f32 v107, v38, v39\n\t" \
    "v_exp_f32_e32 v41, v41\n\t" \
    "v_exp_f32_e32 v42, v42\n\t" \
    "v_mfma_f32_32x32x16_bf16 v[0:15], v[88:91], v[104:107], v[0:15]\n\t" \
    "s_cbranch_vccz 20f\n\t" \
    "v_cmp_eq_u64_e64 s[42:43], v[120:121], v[140:141]\n\t" \
    "s_mov_b32 m0, %[dst]\n\t" \
    "s_nop 3\n\t" \
    "s_bitcmp1_b64 s[42:43], s46\n\t" \
    "s_cbranch_scc0 9f\n\t" \
    "12:\n" \
    "global_load_lds_dwordx4 v114, s[44:45] offset:0 sc1\n\t" \
    "20:\n" \
    "v_exp_f32_e32 v43, v43\n\t" \
    "v_exp_f32_e32 v44, v44\n\t" \
    "v_exp_f32_e32 v45, v45\n\t" \
    "v_mfma_f32_16x16x32_bf16 v[76:79], v[72:75], v[104:107], v[76:79]\n\t" \
    "v_exp_f32_e32 v46, v46\n\t" \
    "v_exp_f32_e32 v47, v47\n\t" \
    "v_cvt_pk_bf16_f32 v108, v40, v41\n\t" \
    "v_cvt_pk_bf16_f32 v109, v42, v43\n\t" \
    "v_cvt_pk_bf16_f32 v110, v44, v45\n\t" \
    "v_cvt_pk_bf16_f32 v111, v46, v47\n\t" \
    "s_waitcnt lgkmcnt(0)\n\t" \
    "v_mfma_f32_32x32x16_bf16 v[32:47], v[80:83], v[64:67], v[16:31]\n\t" \
    "v_exp_f32_e32 v48, v48\n\t" \
    "v_exp_f32_e32 v49, v49\n\t" \
    "v_exp_f32_e32 v50, v50\n\t" \
    "v_mfma_f32_32x32x16_bf16 v[32:47], v[84:87], v[68:71], v[32:47]\n\t" \
    "ds_read_b128 v[80:83], v113 offset:4096\n\t" \
    "ds_read_b128 v[84:87], v113 offset:5120\n\t" \
    "v_exp_f32_e32 v51, v51\n\t" \
    "v_exp_f32_e32 v52, v52\n\t" \
    "v_cvt_pk_bf16_f32 v104, v48, v49\n\t" \
    "v_cvt_pk_bf16_f32 v105, v50, v51\n\t" \
    "v_mfma_f32_32x32x16_bf16 v[0:15], v[100:103], v[108:111], v[0:15]\n\t" \
    "ds_read_b128 v[92:95], v112 offset:31744\n\t" \
    "ds_read_b128 v[88:91], v113 offset:2048\n\t" \
    "v_exp_f32_e32 v53, v53\n\t" \
    "v_exp_f32_e32 v54, v54\n\t" \
    "v_exp_f32_e32 v55, v55\n\t" \
    "v_mfma_f32_16x16x32_bf16 v[76:79], v[72:75], v[108:111], v[76:79]\n\t" \
    "v_exp_f32_e32 v56, v56\n\t" \
    "v_cvt_pk_bf16_f32 v106, v52, v53\n\t" \
    "v_cvt_pk_bf16_f32 v107, v54, v55\n\t" \
    "v_exp_f32_e32 v57, v57\n\t" \
    "v_exp_f32_e32 v58, v58\n\t" \
    "v_mfma_f32_32x32x16_bf16 v[0:15], v[96:99], v[104:107], v[0:15]\n\t" \
    "s_cbranch_vccz 21f\n\t" \
    "global_load_lds_dwordx4 v114, s[44:45] offset:1024 sc1\n\t" \
    "21:\n" \
    "v_exp_f32_e32 v59, v59\n\t" \
    "v_exp_f32_e32 v60, v60\n\t" \
    "v_exp_f32_e32 v61, v61\n\t" \
    "v_mfma_f32_16x16x32_bf16 v[76:79], v[72:75], v[104:107], v[76:79]\n\t" \
    "v_exp_f32_e32 v62, v62\n\t" \
    "v_exp_f32_e32 v63, v63\n\t" \
    "v_cvt_pk_bf16_f32 v108, v56, v57\n\t" \
    "v_cvt_pk_bf16_f32 v109, v58, v59\n\t" \
    "v_cvt_pk_bf16_f32 v110, v60, v61\n\t" \
    "v_cvt_pk_bf16_f32 v111, v62, v63\n\t" \
    "s_setprio 2\n\t" \
    "s_waitcnt lgkmcnt(0)\n\t" \
    "v_mfma_f32_32x32x16_bf16 v[48:63], v[80:83], v[64:67], v[16:31]\n\t" \
    "v_exp_f32_e32 v32, v32\n\t" \
    "v_exp_f32_e32 v33, v33\n\t" \
    "v_exp_f32_e32 v34, v34\n\t" \
    "v_mfma_f32_32x32x16_bf16 v[48:63], v[84:87], v[68:71], v[48:63]\n\t" \
    "ds_read_b128 v[80:83], v113 offset:8192\n\t" \
    "ds_read_b128 v[84:87], v113 offset:9216\n\t" \
    "v_exp_f32_e32 v35, v35\n\t" \
    "v_exp_f32_e32 v36, v36\n\t" \
    "v_cvt_pk_bf16_f32 v104, v32, v33\n\t" \
    "v_cvt_pk_bf16_f32 v105, v34, v35\n\t" \
    "v_mfma_f32_32x32x16_bf16 v[0:15], v[92:95], v[108:111], v[0:15]\n\t" \
    "ds_read_b128 v[100:103], v113 offset:3072\n\t" \
    "ds_read_b128 v[96:99], v113 offset:6144\n\t" \
    "v_exp_f32_e32 v37, v37\n\t" \
    "v_exp_f32_e32 v38, v38\n\t" \
    "v_exp_f32_e32 v39, v39\n\t" \
    "v_mfma_f32_16x16x32_bf16 v[76:79], v[72:75], v[108:111], v[76:79]\n\t" \
    "v_exp_f32_e32 v40, v40\n\t" \
    "v_cvt_pk_bf16_f32 v106, v36, v37\n\t" \
    "v_cvt_pk_bf16_f32 v107, v38, v39\n\t" \
    "v_exp_f32_e32 v41, v41\n\t" \
    "v_exp_f32_e32 v42, v42\n\t" \
    "v_mfma_f32_32x32x16_bf16 v[0:15], v[88:91], v[104:107], v[0:15]\n\t" \
    "s_cbranch_vccz 22f\n\t" \
    "global_load_lds_dwordx4 v114, s[44:45] offset:2048 sc1\n\t" \
    "22:\n" \
    "v_exp_f32_e32 v43, v43\n\t" \
    "v_exp_f32_e32 v44, v44\n\t" \
    "v_exp_f32_e32 v45, v45\n\t" \
    "v_mfma_f32_16x16x32_bf16 v[76:79], v[72:75], v[104:107], v[76:79]\n\t" \
    "v_exp_f32_e32 v46, v46\n\t" \
    "v_exp_f32_e32 v47, v47\n\t" \
    "v_cvt_pk_bf16_f32 v108, v40, v41\n\t" \
    "v_cvt_pk_bf16_f32 v109, v42, v43\n\t" \
    "v_cvt_pk_bf16_f32 v110, v44, v45\n\t" \
    "v_cvt_pk_bf16_f32 v111, v46, v47\n\t" \
    "s_waitcnt lgkmcnt(0)\n\t" \
    "v_mfma_f32_32x32x16_bf16 v[32:47], v[80:83], v[64:67], v[16:31]\n\t" \
    "v_exp_f32_e32 v48, v48\n\t" \
    "v_exp_f32_e32 v49, v49\n\t" \
    "v_exp_f32_e32 v50, v50\n\t" \
    "v_mfma_f32_32x32x16_bf16 v[32:47], v[84:87], v[68:71], v[32:47]\n\t" \
    "ds_read_b128 v[80:83], v113 offset:12288\n\t" \
    "ds_read_b128 v[84:87], v113 offset:13312\n\t" \
    "v_exp_f32_e32 v51, v51\n\t" \
    "v_exp_f32_e32 v52, v52\n\t" \
    "v_cvt_pk_bf16_f32 v104, v48, v49\n\t" \
    "v_cvt_pk_bf16_f32 v105, v50, v51\n\t" \
    "v_mfma_f32_32x32x16_bf16 v[0:15], v[100:103], v[108:111], v[0:15]\n\t" \
    "ds_read_b128 v[92:95], v113 offset:7168\n\t" \
    "ds_read_b128 v[88:91], v113 offset:10240\n\t" \
    "v_exp_f32_e32 v53, v53\n\t" \
    "v_exp_f32_e32 v54, v54\n\t" \
    "v_exp_f32_e32 v55, v55\n\t" \
    "v_mfma_f32_16x16x32_bf16 v[76:79], v[72:75], v[108:111], v[76:79]\n\t" \
    "v_exp_f32_e32 v56, v56\n\t" \
    "v_cvt_pk_bf16_f32 v106, v52, v53\n\t" \
    "v_cvt_pk_bf16_f32 v107, v54, v55\n\t" \
    "v_exp_f32_e32 v57, v57\n\t" \
    "v_exp_f32_e32 v58, v58\n\t" \
    "v_mfma_f32_32x32x16_bf16 v[0:15], v[96:99], v[104:107], v[0:15]\n\t" \
    "s_cbranch_vccz 23f\n\t" \
    "global_load_lds_dwordx4 v114, s[44:45] offset:3072 sc1\n\t" \
    "global_load_dwordx2 v[120:121], v170, %[ws] sc1\n\t" \
    "23:\n" \
    "v_exp_f32_e32 v59, v59\n\t" \
    "v_exp_f32_e32 v60, v60\n\t" \
    "v_exp_f32_e32 v61, v61\n\t" \
    "v_mfma_f32_16x16x32_bf16 v[76:79], v[72:75], v[104:107], v[76:79]\n\t" \
    "v_exp_f32_e32 v62, v62\n\t" \
    "v_exp_f32_e32 v63, v63\n\t" \
    "v_cvt_pk_bf16_f32 v108, v56, v57\n\t" \
    "v_cvt_pk_bf16_f32 v109, v58, v59\n\t" \
    "v_cvt_pk_bf16_f32 v110, v60, v61\n\t" \
    "v_cvt_pk_bf16_f32 v111, v62, v63\n\t" \
    "s_setprio 1\n\t" \
    "s_waitcnt lgkmcnt(0)\n\t" \
    "v_mfma_f32_32x32x16_bf16 v[48:63], v[80:83], v[64:67], v[16:31]\n\t" \
    "v_exp_f32_e32 v32, v32\n\t" \
    "v_exp_f32_e32 v33, v33\n\t" \
    "v_exp_f32_e32 v34, v34\n\t" \
    "v_mfma_f32_32x32x16_bf16 v[48:63], v[84:87], v[68:71], v[48:63]\n\t" \
    "ds_read_b128 v[80:83], v113 offset:16384\n\t" \
    "ds_read_b128 v[84:87], v113 offset:17408\n\t" \
    "v_exp_f32_e32 v35, v35\n\t" \
    "v_exp_f32_e32 v36, v36\n\t" \
    "v_cvt_pk_bf16_f32 v104, v32, v33\n\t" \
    "v_cvt_pk_bf16_f32 v105, v34, v35\n\t" \
    "v_mfma_f32_32x32x16_bf16 v[0:15], v[92:95], v[108:111], v[0:15]\n\t" \
    "ds_read_b128 v[100:103], v113 offset:11264\n\t" \
    "ds_read_b128 v[96:99], v113 offset:14336\n\t" \
    "v_exp_f32_e32 v37, v37\n\t" \
    "v_exp_f32_e32 v38, v38\n\t" \
    "v_exp_f32_e32 v39, v39\n\t" \
    "v_mfma_f32_16x16x32_bf16 v[76:79], v[72:75], v[108:111], v[76:79]\n\t" \
    "v_exp_f32_e32 v40, v40\n\t" \
    "v_cvt_pk_bf16_f32 v106, v36, v37\n\t" \
    "v_cvt_pk_bf16_f32 v107, v38, v39\n\t" \
    "v_exp_f32_e32 v41, v41\n\t" \
    "v_exp_f32_e32 v42, v42\n\t" \
    "v_mfma_f32_32x32x16_bf16 v[0:15], v[88:91], v[104:107], v[0:15]\n\t" \
    "s_add_u32 %[dst], %[dst], 0x8000\n\t" \
    "s_cmp_eq_u32 %[dst], %[dend]\n\t" \
    "s_cselect_b32 %[dst], %[dbeg], %[dst]\n\t" \
    "s_add_u32 %[nx], %[nx], 0x8000\n\t" \
    "s_cmp_eq_u32 %[nx], 0x18000\n\t" \
    "s_cselect_b32 %[nx], 0, %[nx]\n\t" \
    "s_cmp_eq_u32 %[cnt], 5\n\t" \
    "s_cbranch_scc1 40f\n\t" \
    "24:\n" \
    "v_exp_f32_e32 v43, v43\n\t" \
    "v_exp_f32_e32 v44, v44\n\t" \
    "v_exp_f32_e32 v45, v45\n\t" \
    "v_mfma_f32_16x16x32_bf16 v[76:79], v[72:75], v[104:107], v[76:79]\n\t" \
    "v_exp_f32_e32 v46, v46\n\t" \
    "v_exp_f32_e32 v47, v47\n\t" \
    "v_cvt_pk_bf16_f32 v108, v40, v41\n\t" \
    "v_cvt_pk_bf16_f32 v109, v42, v43\n\t" \
    "v_cvt_pk_bf16_f32 v110, v44, v45\n\t" \
    "v_cvt_pk_bf16_f32 v111, v46, v47\n\t" \
    "s_waitcnt lgkmcnt(0)\n\t" \
    "v_mfma_f32_32x32x16_bf16 v[32:47], v[80:83], v[64:67], v[16:31]\n\t" \
    "v_exp_f32_e32 v48, v48\n\t" \
    "v_exp_f32_e32 v49, v49\n\t" \
    "v_exp_f32_e32 v50, v50\n\t" \
    "v_mfma_f32_32x32x16_bf16 v[32:47], v[84:87], v[68:71], v[32:47]\n\t" \
    "ds_read_b128 v[80:83], v113 offset:20480\n\t" \
    "ds_read_b128 v[84:87], v113 offset:21504\n\t" \
    "v_exp_f32_e32 v51, v51\n\t" \
    "v_exp_f32_e32 v52, v52\n\t" \
    "v_cvt_pk_bf16_f32 v104, v48, v49\n\t" \
    "v_cvt_pk_bf16_f32 v105, v50, v51\n\t" \
    "v_mfma_f32_32x32x16_bf16 v[0:15], v[100:103], v[108:111], v[0:15]\n\t" \
    "ds_read_b128 v[92:95], v113 offset:15360\n\t" \
    "ds_read_b128 v[88:91], v113 offset:18432\n\t" \
    "v_exp_f32_e32 v53, v53\n\t" \
    "v_exp_f32_e32 v54, v54\n\t" \
    "v_exp_f32_e32 v55, v55\n\t" \
    "v_mfma_f32_16x16x32_bf16 v[76:79], v[72:75], v[108:111], v[76:79]\n\t" \
    "v_exp_f32_e32 v56, v56\n\t" \
    "v_cvt_pk_bf16_f32 v106, v52, v53\n\t" \
    "v_cvt_pk_bf16_f32 v107, v54, v55\n\t" \
    "v_exp_f32_e32 v57, v57\n\t" \
    "v_exp_f32_e32 v58, v58\n\t" \
    "v_mfma_f32_32x32x16_bf16 v[0:15], v[96:99], v[104:107], v[0:15]\n\t" \
    "s_cmp_eq_u32 %[cnt], 5\n\t" \
    "s_cbranch_scc1 41f\n\t" \
    "25:\n" \
    "v_exp_f32_e32 v59, v59\n\t" \
    "v_exp_f32_e32 v60, v60\n\t" \
    "v_exp_f32_e32 v61, v61\n\t" \
    "v_mfma_f32_16x16x32_bf16 v[76:79], v[72:75], v[104:107], v[76:79]\n\t" \
    "v_exp_f32_e32 v62, v62\n\t" \
    "v_exp_f32_e32 v63, v63\n\t" \
    "v_cvt_pk_bf16_f32 v108, v56, v57\n\t" \
    "v_cvt_pk_bf16_f32 v109, v58, v59\n\t" \
    "v_cvt_pk_bf16_f32 v110, v60, v61\n\t" \
    "v_cvt_pk_bf16_f32 v111, v62, v63\n\t" \
    "s_setprio 0\n\t" \
    "s_waitcnt lgkmcnt(0)\n\t" \
    "v_mfma_f32_32x32x16_bf16 v[48:63], v[80:83], v[64:67], v[16:31]\n\t" \
    "v_exp_f32_e32 v32, v32\n\t" \
    "v_exp_f32_e32 v33, v33\n\t" \
    "v_exp_f32_e32 v34, v34\n\t" \
    "v_mfma_f32_32x32x16_bf16 v[48:63], v[84:87], v[68:71], v[48:63]\n\t" \
    "ds_read_b128 v[80:83], v113 offset:24576\n\t" \
    "ds_read_b128 v[84:87], v113 offset:25600\n\t" \
    "v_exp_f32_e32 v35, v35\n\t" \
    "v_exp_f32_e32 v36, v36\n\t" \
    "v_cvt_pk_bf16_f32 v104, v32, v33\n\t" \
    "v_cvt_pk_bf16_f32 v105, v34, v35\n\t" \
    "v_mfma_f32_32x32x16_bf16 v[0:15], v[92:95], v[108:111], v[0:15]\n\t" \
    "ds_read_b128 v[100:103], v113 offset:19456\n\t" \
    "ds_read_b128 v[96:99], v113 offset:22528\n\t" \
    "v_exp_f32_e32 v37, v37\n\t" \
    "v_exp_f32_e32 v38, v38\n\t" \
    "v_exp_f32_e32 v39, v39\n\t" \
    "v_mfma_f32_16x16x32_bf16 v[76:79], v[72:75], v[108:111], v[76:79]\n\t" \
    "v_exp_f32_e32 v40, v40\n\t" \
    "v_cvt_pk_bf16_f32 v106, v36, v37\n\t" \
    "v_cvt_pk_bf16_f32 v107, v38, v39\n\t" \
    "v_exp_f32_e32 v41, v41\n\t" \
    "v_exp_f32_e32 v42, v42\n\t" \
    "v_mfma_f32_32x32x16_bf16 v[0:15], v[88:91], v[104:107], v[0:15]\n\t" \
    "s_and_b32 s40, s48, 63\n\t" \
    "s_lshl_b32 s40, s40, 12\n\t" \
    "s_add_u32 s44, %[srclo], s40\n\t" \
    "s_addc_u32 s45, %[srchi], 0\n\t" \
    "s_lshr_b64 s[48:49], s[48:49], 6\n\t" \
    "s_add_u32 s46, s46, 1\n\t" \
    "s_cmp_eq_u32 %[cnt], 5\n\t" \
    "s_cbranch_scc1 42f\n\t" \
    "26:\n" \
    "v_exp_f32_e32 v43, v43\n\t" \
    "v_exp_f32_e32 v44, v44\n\t" \
    "v_exp_f32_e32 v45, v45\n\t" \
    "v_mfma_f32_16x16x32_bf16 v[76:79], v[72:75], v[104:107], v[76:79]\n\t" \
    "v_exp_f32_e32 v46, v46\n\t" \
    "v_exp_f32_e32 v47, v47\n\t" \
    "v_cvt_pk_bf16_f32 v108, v40, v41\n\t" \
    "v_cvt_pk_bf16_f32 v109, v42, v43\n\t" \
    "v_cvt_pk_bf16_f32 v110, v44, v45\n\t" \
    "v_cvt_pk_bf16_f32 v111, v46, v47\n\t" \
    "s_waitcnt lgkmcnt(0)\n\t" \
    "v_mfma_f32_32x32x16_bf16 v[32:47], v[80:83], v[64:67], v[16:31]\n\t" \
    "v_exp_f32_e32 v48, v48\n\t" \
    "v_exp_f32_e32 v49, v49\n\t" \
    "v_exp_f32_e32 v50, v50\n\t" \
    "v_mfma_f32_32x32x16_bf16 v[32:47], v[84:87], v[68:71], v[32:47]\n\t" \
    "ds_read_b128 v[80:83], v113 offset:28672\n\t" \
    "ds_read_b128 v[84:87], v113 offset:29696\n\t" \
    "v_exp_f32_e32 v51, v51\n\t" \
    "v_exp_f32_e32 v52, v52\n\t" \
    "v_cvt_pk_bf16_f32 v104, v48, v49\n\t" \
    "v_cvt_pk_bf16_f32 v105, v50, v51\n\t" \
    "v_mfma_f32_32x32x16_bf16 v[0:15], v[100:103], v[108:111], v[0:15]\n\t" \
    "ds_read_b128 v[92:95], v113 offset:23552\n\t" \
    "ds_read_b128 v[88:91], v113 offset:26624\n\t" \
    "v_exp_f32_e32 v53, v53\n\t" \
    "v_exp_f32_e32 v54, v54\n\t" \
    "v_exp_f32_e32 v55, v55\n\t" \
    "v_mfma_f32_16x16x32_bf16 v[76:79], v[72:75], v[108:111], v[76:79]\n\t" \
    "v_exp_f32_e32 v56, v56\n\t" \
    "v_cvt_pk_bf16_f32 v106, v52, v53\n\t" \
    "v_cvt_pk_bf16_f32 v107, v54, v55\n\t" \
    "v_exp_f32_e32 v57, v57\n\t" \
    "v_exp_f32_e32 v58, v58\n\t" \
    "v_mfma_f32_32x32x16_bf16 v[0:15], v[96:99], v[104:107], v[0:15]\n\t" \
    "s_cmp_eq_u32 %[cnt], 5\n\t" \
    "s_cbranch_scc1 43f\n\t" \
    "27:\n" \
    "v_mov_b32_e32 v112, v113\n\t" \
    "v_add_u32_e32 v113, %[nx], v115\n\t" \
    "s_sub_u32 %[cnt], %[cnt], 1\n\t" \
    "s_cmp_lg_u32 %[cnt], 0\n\t" \
    "v_exp_f32_e32 v59, v59\n\t" \
    "v_exp_f32_e32 v60, v60\n\t" \
    "v_exp_f32_e32 v61, v61\n\t" \
    "v_mfma_f32_16x16x32_bf16 v[76:79], v[72:75], v[104:107], v[76:79]\n\t" \
    "v_exp_f32_e32 v62, v62\n\t" \
    "v_exp_f32_e32 v63, v63\n\t" \
    "v_cvt_pk_bf16_f32 v108, v56, v57\n\t" \
    "v_cvt_pk_bf16_f32 v109, v58, v59\n\t" \
    "v_cvt_pk_bf16_f32 v110, v60, v61\n\t" \
    "v_cvt_pk_bf16_f32 v111, v62, v63\n\t" \
    "s_cbranch_scc1 1b\n\t" \
    "7:\n" \
    "s_setprio 0\n\t" \
    "s_waitcnt lgkmcnt(0)\n\t" \
    "v_mfma_f32_32x32x16_bf16 v[48:63], v[80:83], v[64:67], v[16:31]\n\t" \
    "v_exp_f32_e32 v32, v32\n\t" \
    "v_exp_f32_e32 v33, v33\n\t" \
    "v_exp_f32_e32 v34, v34\n\t" \
    "v_mfma_f32_32x32x16_bf16 v[48:63], v[84:87], v[68:71], v[48:63]\n\t" \
    "v_exp_f32_e32 v35, v35\n\t" \
    "v_exp_f32_e32 v36, v36\n\t" \
    "v_cvt_pk_bf16_f32 v104, v32, v33\n\t" \
    "v_cvt_pk_bf16_f32 v105, v34, v35\n\t" \
    "v_mfma_f32_32x32x16_bf16 v[0:15], v[92:95], v[108:111], v[0:15]\n\t" \
    "ds_read_b128 v[100:103], v112 offset:27648\n\t" \
    "ds_read_b128 v[96:99], v112 offset:30720\n\t" \
    "v_exp_f32_e32 v37, v37\n\t" \
    "v_exp_f32_e32 v38, v38\n\t" \
    "v_exp_f32_e32 v39, v39\n\t" \
    "v_mfma_f32_16x16x32_bf16 v[76:79], v[72:75], v[108:111], v[76:79]\n\t" \
    "v_exp_f32_e32 v40, v40\n\t" \
    "v_cvt_pk_bf16_f32 v106, v36, v37\n\t" \
    "v_cvt_pk_bf16_f32 v107, v38, v39\n\t" \
    "v_exp_f32_e32 v41, v41\n\t" \
    "v_exp_f32_e32 v42, v42\n\t" \
    "v_mfma_f32_32x32x16_bf16 v[0:15], v[88:91], v[104:107], v[0:15]\n\t" \
    "v_exp_f32_e32 v43, v43\n\t" \
    "v_exp_f32_e32 v44, v44\n\t" \
    "v_exp_f32_e32 v45, v45\n\t" \
    "v_mfma_f32_16x16x32_bf16 v[76:79], v[72:75], v[104:107], v[76:79]\n\t" \
    "v_exp_f32_e32 v46, v46\n\t" \
    "v_exp_f32_e32 v47, v47\n\t" \
    "v_cvt_pk_bf16_f32 v108, v40, v41\n\t" \
    "v_cvt_pk_bf16_f32 v109, v42, v43\n\t" \
    "v_cvt_pk_bf16_f32 v110, v44, v45\n\t" \
    "v_cvt_pk_bf16_f32 v111, v46, v47\n\t" \
    "s_waitcnt lgkmcnt(0)\n\t" \
    "v_exp_f32_e32 v48, v48\n\t" \
    "v_exp_f32_e32 v49, v49\n\t" \
    "v_exp_f32_e32 v50, v50\n\t" \
    "v_exp_f32_e32 v51, v51\n\t" \
    "v_exp_f32_e32 v52, v52\n\t" \
    "v_cvt_pk_bf16_f32 v104, v48, v49\n\t" \
    "v_cvt_pk_bf16_f32 v105, v50, v51\n\t" \
    "v_mfma_f32_32x32x16_bf16 v[0:15], v[100:103], v[108:111], v[0:15]\n\t" \
    "ds_read_b128 v[92:95], v112 offset:31744\n\t" \
    "v_exp_f32_e32 v53, v53\n\t" \
    "v_exp_f32_e32 v54, v54\n\t" \
    "v_exp_f32_e32 v55, v55\n\t" \
    "v_mfma_f32_16x16x32_bf16 v[76:79], v[72:75], v[108:111], v[76:79]\n\t" \
    "v_exp_f32_e32 v56, v56\n\t" \
    "v_cvt_pk_bf16_f32 v106, v52, v53\n\t" \
    "v_cvt_pk_bf16_f32 v107, v54, v55\n\t" \
    "v_exp_f32_e32 v57, v57\n\t" \
    "v_exp_f32_e32 v58, v58\n\t" \
    "v_mfma_f32_32x32x16_bf16 v[0:15], v[96:99], v[104:107], v[0:15]\n\t" \
    "v_exp_f32_e32 v59, v59\n\t" \
    "v_exp_f32_e32 v60, v60\n\t" \
    "v_exp_f32_e32 v61, v61\n\t" \
    "v_mfma_f32_16x16x32_bf16 v[76:79], v[72:75], v[104:107], v[76:79]\n\t" \
    "v_exp_f32_e32 v62, v62\n\t" \
    "v_exp_f32_e32 v63, v63\n\t" \
    "v_cvt_pk_bf16_f32 v108, v56, v57\n\t" \
    "v_cvt_pk_bf16_f32 v109, v58, v59\n\t" \
    "v_cvt_pk_bf16_f32 v110, v60, v61\n\t" \
    "v_cvt_pk_bf16_f32 v111, v62, v63\n\t" \
    "s_waitcnt lgkmcnt(0)\n\t" \
    "v_mfma_f32_32x32x16_bf16 v[0:15], v[92:95], v[108:111], v[0:15]\n\t" \
    "v_mfma_f32_16x16x32_bf16 v[76:79], v[72:75], v[108:111], v[76:79]\n\t" \
    "s_nop 15\n\t" \
    "s_branch 99f\n\t" \
    "9:\n" \
    "s_mov_b32 s47, 0x40000\n\t" \
    "10:\n" \
    "s_sub_u32 s47, s47, 1\n\t" \
    "s_cbranch_scc1 12b\n\t" \
    "global_load_dwordx2 v[120:121], v170, %[ws] sc1\n\t" \
    "s_waitcnt vmcnt(0)\n\t" \
    "v_cmp_eq_u64_e64 s[42:43], v[120:121], v[140:141]\n\t" \
    "s_nop 4\n\t" \
    "s_bitcmp1_b64 s[42:43], s46\n\t" \
    "s_cbranch_scc1 12b\n\t" \
    "s_sleep 4\n\t" \
    "s_branch 10b\n\t" \
    "40:\n" \
    "s_add_u32 m0, %[wdst], 0\n\t" \
    "s_nop 0\n\t" \
    "global_load_lds_dwordx4 v116, %[wo01]\n\t" \
    "global_load_lds_dwordx4 v117, %[wo01] offset:1024\n\t" \
    "v_add_u32_e32 v116, 0x20000, v116\n\t" \
    "v_add_u32_e32 v117, 0x20000, v117\n\t" \
    "s_branch 24b\n\t" \
    "41:\n" \
    "s_add_u32 m0, %[wdst], 16384\n\t" \
    "s_nop 0\n\t" \
    "global_load_lds_dwordx4 v116, %[wo01]\n\t" \
    "global_load_lds_dwordx4 v117, %[wo01] offset:1024\n\t" \
    "v_subrev_u32_e32 v116, 0x20000, v116\n\t" \
    "v_subrev_u32_e32 v117, 0x20000, v117\n\t" \
    "s_branch 25b\n\t" \
    "42:\n" \
    "s_add_u32 m0, %[wdst], 32768\n\t" \
    "s_nop 0\n\t" \
    "global_load_lds_dwordx4 v116, %[wo23]\n\t" \
    "global_load_lds_dwordx4 v117, %[wo23] offset:1024\n\t" \
    "v_add_u32_e32 v116, 0x20000, v116\n\t" \
    "v_add_u32_e32 v117, 0x20000, v117\n\t" \
    "s_branch 26b\n\t" \
    "43:\n" \
    "s_add_u32 m0, %[wdst], 49152\n\t" \
    "s_nop 0\n\t" \
    "global_load_lds_dwordx4 v116, %[wo23]\n\t" \
    "global_load_lds_dwordx4 v117, %[wo23] offset:1024\n\t" \
    "global_load_dword v168, v119, %[wo]\n\t" \
    "s_branch 27b\n\t" \
    "30:\n" \
    "s_cmp_eq_u32 %[cnt], 1\n\t" \
    "s_cbranch_scc0 31f\n\t" \
    "s_mov_b64 vcc, 0\n\t" \
    "s_bitcmp1_b32 %[mode], 1\n\t" \
    "s_cbranch_scc1 7b\n\t" \
    "s_branch 2b\n\t" \
    "31:\n" \
    "33:\n" \
    "s_cmp_eq_u32 %[cnt], 5\n\t" \
    "s_cbranch_scc0 34f\n\t" \
    "global_store_dwordx2 v118, v[140:141], %[ws] sc1\n\t" \
    "s_branch 2b\n\t" \
    "34:\n" \
    "s_cmp_eq_u32 %[cnt], 3\n\t" \
    "s_cbranch_scc0 35f\n\t" \
    "global_load_dwordx2 v[168:169], v142, %[ws] sc1\n\t" \
    "s_branch 2b\n\t" \
    "35:\n" \
    "v_cmp_eq_u64_e64 s[42:43], v[168:169], v[140:141]\n\t" \
    "s_nop 4\n\t" \
    "s_cmp_eq_u64 s[42:43], -1\n\t" \
    "s_cbranch_scc0 2b\n\t" \
    "global_load_dwordx4 v[122:125], v143, %[ws] offset:-2048 sc1\n\t" \
    "global_load_dwordx4 v[126:129], v143, %[ws] offset:-1024 sc1\n\t" \
    "global_load_dwordx4 v[130:133], v143, %[ws] offset:0 sc1\n\t" \
    "global_load_dwordx4 v[134:137], v143, %[ws] offset:1024 sc1\n\t" \
    "global_load_dwordx2 v[138:139], v143, %[ws] offset:2048 sc1\n\t" \
    "s_bitset1_b32 %[mode], 2\n\t" \
    "s_branch 2b\n\t" \
    "99:\n"
#define ATT_TMP "v48", "v49", "v50", "v51", "v52", "v53", "v54", "v55", "v56", "v57", "v58", "v59", "v60", "v61", "v62", "v63", "v96", "v97", "v98", "v99", "v100", "v101", "v102", "v103", "v104", "v105", "v106", "v107"

constexpr int K1_X = 0, K1_W = 65536, K1_WB = 24576;
constexpr int K2_SLOT = 32768, K2_QX = 3 * K2_SLOT, K2_FL = 147456;
constexpr int LDS_BYTES = 163840;
constexpr int p3_wslot(int t) { return (t % 6) < 4 ? 98304 + (t % 6) * 16384 : 65536 + ((t % 6) - 4) * 16384; }
constexpr int p3_allow(int t) { return ((t - 2 >= 1 && t + 3 < 16) ? 4 : 0) + (t + 3 < 16 ? 4 : 0) + ((t >= 1 && t + 5 < 16) ? 4 : 0); }

__global__ __launch_bounds__(512, 2) void mk_fused(const u16* __restrict__ xA, const u16* __restrict__ wA, const u16* __restrict__ woA,
                                                   const u16* __restrict__ xB, const u16* __restrict__ wB, const u16* __restrict__ woB, char* ws0, u16* __restrict__ out) {
    char* ws = ws0;
    __shared__ __attribute__((aligned(1024))) char smem[LDS_BYTES];
    const unsigned lds0 = (unsigned)(uintptr_t)(LAS char*)smem;
    const int wid = __builtin_amdgcn_readfirstlane(threadIdx.x >> 6);
    int lane = threadIdx.x & 63;
    const int b = blockIdx.x, xcd = b & 7, j = b >> 3, br = j >> 4;
    const int mt = (xcd & 1) * 4 + (j & 3), hp = (xcd >> 1) * 4 + ((j >> 2) & 3);
    const int mt3 = (xcd >> 1) * 4 + (j & 3), nt3 = (xcd & 1) * 4 + ((j >> 2) & 3);
    u64 tag;
    u32x4 fq[2][2], fk[2][2], fv[2][2];
    STAMP_AT(0, 0);
    {
        const int g = wid >> 2, wi = wid & 3, mw = wi >> 1, hd = wi & 1, h = lane >> 5;
        const char* x = (const char*)(br ? xB : xA) + (size_t)mt * 256 * 2048;
        const char* w = (const char*)(br ? wB : wA) + hp * 128;
        unsigned vx[4], vw[3];
#pragma unroll
        for (int ii = 0; ii < 4; ++ii) {
            const int row = br ? ((wi >> 1) * 64 + (wi & 1) + ii * 16 + 2 * (lane >> 3)) : (wi * 32 + ii * 8 + (lane >> 3));
            vx[ii] = row * 2048 + (((lane & 7) ^ (((ii & 1) * 4 + (lane >> 4)) & 7)) << 4);
        }
#pragma unroll
        for (int jj = 0; jj < 3; ++jj) {
            const int jw = 12 * g + 3 * wi + jj, n = jw >> 3, k0 = (jw & 7) * 8 + (lane >> 3);
            vw[jj] = k0 * 6144 + n * 2048 + (((lane & 7) ^ (((lane >> 4) & 1) << 2)) << 4);
        }
        const unsigned xdst_o = lds0 + K1_X + (1 - g) * 32768 + wi * 4096;
        const unsigned wdst = lds0 + K1_W + (12 * g + 3 * wi) * 1024;
        const char* xo = x + (size_t)(1 - g) * 128 * 2048;
        const int kt0 = 4 * (j & 3);
#define STAGE_X(kt, dst) do { const char* s_ = xo + (((kt) + kt0) & 15) * 128; _Pragma("unroll") for (int ii = 0; ii < 4; ++ii) glds16(s_, vx[ii], (dst) + ii * 1024); } while (0)
#define STAGE_W(kt, buf) do { const char* s_ = w + (size_t)(((kt) + kt0) & 15) * 64 * 6144; _Pragma("unroll") for (int jj = 0; jj < 3; ++jj) glds16(s_, vw[jj], wdst + (buf) * K1_WB + jj * 1024); } while (0)
        STAGE_X(0, xdst_o); STAGE_W(0, 0); STAGE_W(1, 1);
        if (g == 1) STAGE_X(1, xdst_o + 16384);
        { u64 e; asm volatile("s_load_dwordx2 %0, %1, 0x0\n\ts_waitcnt lgkmcnt(0)" : "=s"(e) : "s"(ws + WS_EP) : "memory");
          e += 0x9E3779B97F4A7C15ull; e = (e ^ (e >> 30)) * 0xBF58476D1CE4E5B9ull; e = (e ^ (e >> 27)) * 0x94D049BB133111EBull; tag = e ^ (e >> 31); }
        unsigned ax[4];
        { const int row = mw * 64 + (lane & 31); const unsigned a0 = lds0 + K1_X + g * 32768 + row * 128 + ((h ^ ((row >> 1) & 7)) << 4);
#pragma unroll
          for (int s = 0; s < 4; ++s) ax[s] = a0 ^ (s << 5); }
        unsigned aw;
        { const int q4 = (lane & 15) >> 2, p = lane & 3, cb = (lane >> 4) & 1; const int c = hd * 4 + cb * 2 + (p >> 1);
          aw = lds0 + K1_W + (8 * h + q4) * 128 + ((c ^ (((q4 >> 1) & 1) << 2)) << 4) + (p & 1) * 8; }
        if (g == 1) WAITBAR_(7); else WAITBAR_(3);
        SA_(1);
        if (g == 1) asm volatile("s_barrier" ::: "memory");
        f32x16 acc[2][3];
#pragma unroll
        for (int mb = 0; mb < 2; ++mb)
#pragma unroll
            for (int n = 0; n < 3; ++n) acc[mb][n] = f32x16{};
        SBAR();
#pragma unroll
        for (int t = 0; t < 16; ++t) {
            bf16x8 xf[2][4], wf[3][4];
            const int tx = t + 1 + g;
            const char* sx = xo + ((tx + kt0) & 15) * 128; const char* sw = w + (size_t)((t + 2 + kt0) & 15) * 64 * 6144;
            const unsigned dx = xdst_o + (tx & 1) * 16384, dw = wdst + ((t + 2) % 3) * K1_WB;
            const bool dox = t < 14 || (t == 14 && g == 0);
#pragma unroll
            for (int s = 0; s < 4; ++s) {
#pragma unroll
                for (int mb = 0; mb < 2; ++mb) xf[mb][s] = ldsrd128(ax[s] + (t & 1) * 16384 + mb * 4096);
                wf[0][s] = cat8(ldstr(aw + (t % 3) * K1_WB + s * 2048), ldstr(aw + (t % 3) * K1_WB + s * 2048 + 512));
                if (t < 15 && dox) glds16(sx, vx[s], dx + s * 1024);
#pragma unroll
                for (int n = 1; n < 3; ++n) wf[n][s] = cat8(ldstr(aw + (t % 3) * K1_WB + n * 8192 + s * 2048), ldstr(aw + (t % 3) * K1_WB + n * 8192 + s * 2048 + 512));
                if (t + 2 < 16 && s < 3) glds16(sw, vw[s], dw + s * 1024);
            }
            if (t + 2 < 16) WAITBAR_(7); else if (t == 14 && g == 0) WAITBAR_(4); else WAITBAR_(0);
            SBAR();
            __builtin_amdgcn_s_setprio(1);
#pragma unroll
            for (int s = 0; s < 4; ++s)
#pragma unroll
                for (int mb = 0; mb < 2; ++mb) {
                    acc[mb][0] = MFMA32(wf[0][s], xf[mb][s], acc[mb][0]);
                    acc[mb][1] = MFMA32(wf[1][s], xf[mb][s], acc[mb][1]);
                    acc[mb][2] = MFMA32(xf[mb][s], wf[2][s], acc[mb][2]);
                }
            __builtin_amdgcn_s_setprio(0);
            SBAR();
            if (t < 15 || g == 0) asm volatile("s_barrier" ::: "memory");
            SBAR();
        }
#undef STAGE_X
#undef STAGE_W
        SA_(2); SC_(1);
        FRESH();
#pragma unroll
        for (int mb = 0; mb < 2; ++mb) {
#pragma unroll
            for (int r = 0; r < 16; ++r) acc[mb][0][r] *= QSCALE;
#pragma unroll
            for (int s = 0; s < 2; ++s) { fq[mb][s] = pack8(acc[mb][0], s); fk[mb][s] = pack8(acc[mb][1], s); fv[mb][s] = pack8(acc[mb][2], s); }
        }
    }
    const char* wo = (const char*)(br ? woB : woA) + nt3 * 256;
    const int k30 = 2 * nt3;
    {
        const int g = wid >> 2, wr = wid >> 1, hd = wid & 1;
        const int ph1 = 2 * hp + hd, pb1 = mt * 8 + wr * 2;
        const int head = 2 * hp + br, blk = mt * 8 + wid;
#define RBLK(e) ((((e) % 7) + (((e) % 7) >= mt)) * 8 + (e) / 7)
        const char* kv = ws + WS_KV + (size_t)head * 262144;
        unsigned l16 = lane * 16;
        const unsigned dbeg = lds0 + wid * 4096, lbase = lds0 + l16;
        const unsigned fk0 = (unsigned)WS_FK + head * 32 * FS;
        const unsigned fxo = (unsigned)WS_FX + ((hp * 8 + mt) * 8 + wid) * FS, xrec0 = (unsigned)WS_XP + ((hp * 8 + mt) * 8 + wid) * 5120 + 2048;
        const f32x16 z = {};
        f32x16 sa, negm, o;
        u32x16 qol, kvf; u32x4 ph;

#define FIRST_TILE(base, q0, q1) do { const unsigned one2 = (((lane >> 4) & 1) == ((lane >> 2) & 1)) ? 0x3F803F80u : 0u; const u32x4 ones = {one2, one2, one2, one2}; \
            const bf16x8 k0_ = ldsrd128(base), k1_ = ldsrd128((base) + 1024); kvf = cat16(asu(ldsrd128((base) + 4096)), asu(ldsrd128((base) + 5120)), asu(ldsrd128((base) + 2048)), ones); \
            sa = MFMA32(k0_, q0, z); sa = MFMA32(k1_, q1, sa); float m_ = sa[0]; \
            _Pragma("unroll") for (int r = 1; r < 16; ++r) m_ = __builtin_fmaxf(m_, sa[r]); \
            m_ = -swap_max(m_); _Pragma("unroll") for (int r = 0; r < 16; ++r) { negm[r] = m_; sa[r] += m_; } \
            qol = cat16(asu(q0), asu(q1), ones, ones); } while (0)
#define L6_OUT "={v[0:15]}"(o), "+{v[32:47]}"(sa), "+{v[64:79]}"(qol), "+{v[80:95]}"(kvf), "={v[108:111]}"(ph)
#define L6C(HL, BASE) asm volatile(ATT_L6C : L6_OUT, "={v[144:159]}"(h0), "={v[160:167]}"(h1) \
            : "{v[16:31]}"(negm), "{v112}"(BASE), "{v[140:143]}"(u32x4{0u, 0u, FLADDR, (unsigned)WS_HALO + (mt * 32 + ph1) * 6144 + 3072 + l16}), \
              [ws] "s"(ws), [hl] "s"(HL), [fld] "s"(lds0 + K2_FL + wid * 1024) : ATT_TMP, "scc", "memory")
#define FLADDR (fk0 + ((((lane & 7) == 7) ? blk : RBLK(8 * (lane & 7) + wid)) >> 1) * FS)
        const unsigned own = (ph1 * 32 + mt * 4 + wr) * FS;
        if (br == 0) {
            const char* kp = ws + WS_KV + (size_t)((ph1 * 64 + pb1) * 4) * 1024;
#define PUB_KV() do { st16_dev<0>(kp, l16, fk[0][0]); st16_dev<1024>(kp, l16, fk[0][1]); st16_dev<2048>(kp, l16, fv[0][0]); st16_dev<3072>(kp, l16, fv[0][1]); \
            st16_dev<0>(kp + 4096, l16, fk[1][0]); st16_dev<1024>(kp + 4096, l16, fk[1][1]); st16_dev<2048>(kp + 4096, l16, fv[1][0]); st16_dev<3072>(kp + 4096, l16, fv[1][1]); } while (0)
            if (hd == 1) {
                const char* qp = ws + WS_QF + (size_t)((ph1 * 64 + pb1) * 2) * 1024;
                st16_dev<0>(qp, l16, fq[0][0]); st16_dev<1024>(qp, l16, fq[0][1]); st16_dev<2048>(qp, l16, fq[1][0]); st16_dev<3072>(qp, l16, fq[1][1]);
            }
            if (g == 0) PUB_KV();
#pragma unroll
            for (int mb = 0; mb < 2; ++mb) {
                const unsigned a = lbase + hd * 2 * K2_SLOT + (wr * 2 + mb) * 4096, aq = lbase + K2_QX + hd * 16384 + (wr * 2 + mb) * 2048;
                ldswr128(a, fk[mb][0]); ldswr128(a + 1024, fk[mb][1]); ldswr128(a + 2048, fv[mb][0]); ldswr128(a + 3072, fv[mb][1]);
                if (hd == 0 || g == 0) { ldswr128(aq, fq[mb][0]); ldswr128(aq + 1024, fq[mb][1]); }
            }
            BARRIER_L();
            if (g == 0 || hd == 1) {
                drain();
                if (lane == 0) { if (g == 0) flag_set(ws + WS_FK, own, tag); if (hd == 1) flag_set(ws + WS_FQ, own, tag); }
            }
            const unsigned qa = lbase + K2_QX + (1 - g) * 16384 + wid * 2048, cx = lbase + (1 - g) * 2 * K2_SLOT;
            SA_(3); SC_(2);
            u32x16 h0; u32x8 h1;
            { const bf16x8 q0 = ldsrd128(qa), q1 = ldsrd128(qa + 1024); FIRST_TILE(cx, q0, q1); }
            L6C(0, cx);
            SC_(3);
            if (g == 0) {
                asm volatile(ATT_XT : "+{v[0:15]}"(o), "+{v[32:47]}"(sa), "+{v[64:79]}"(qol), "+{v[80:95]}"(kvf), "+{v[108:111]}"(ph) : "{v[16:31]}"(negm), "{v112}"(cx) : ATT_TMP, "memory");
                SC_(4);
                st16_dev<-2048>(ws, xrec0 + l16, quad(o, 0)); st16_dev<-1024>(ws, xrec0 + l16, quad(o, 1)); st16_dev<0>(ws, xrec0 + l16, quad(o, 2)); st16_dev<1024>(ws, xrec0 + l16, quad(o, 3));
                st16_dev<2048>(ws, xrec0 + l16, u32x4{qol[12], __float_as_uint(negm[0]), 0u, 0u});
                SC_(5);
                const bf16x8 q0 = ldsrd128(lbase + K2_QX + wid * 2048), q1 = ldsrd128(lbase + K2_QX + wid * 2048 + 1024);
                FIRST_TILE(lbase, q0, q1);
                L6C(0, lbase);
            } else {
                PUB_KV();
                SC_(4);
                const int b1 = RBLK(wid), b0 = RBLK(wid - 4);
                flag_wait(ws, fk0 + (((lane & 1) ? b1 : b0) >> 1) * FS, tag);
                if (lane == 0) flag_set(ws + WS_FK, own, tag);
                SC_(5);
                glds4k_dev(kv + b1 * 4096, l16, dbeg + K2_SLOT);
                glds4k_dev(kv + b0 * 4096, l16, dbeg + K2_SLOT - 16384);
                glds16_dev(ws, FLADDR, lds0 + K2_FL + wid * 1024);
            }
#undef PUB_KV
        } else {
            if (wr == 3 && mt < 7) {
                const char* p = ws + WS_HALO + (size_t)(((mt + 1) * 32 + ph1) * 6) * 1024;
                st16_dev<0>(p, l16, fk[0][0]); st16_dev<1024>(p, l16, fk[0][1]); st16_dev<2048>(p, l16, fv[0][1]);
                st16_dev<0>(p + 3072, l16, fk[1][0]); st16_dev<1024>(p + 3072, l16, fk[1][1]); st16_dev<2048>(p + 3072, l16, fv[1][1]);
            }
            const unsigned ex = lbase + K2_QX + wid * 6144;
#pragma unroll
            for (int mb = 0; mb < 2; ++mb) { ldswr128(ex + mb * 3072, fk[mb][0]); ldswr128(ex + mb * 3072 + 1024, fk[mb][1]); ldswr128(ex + mb * 3072 + 2048, fv[mb][1]); }
            BARRIER_L();
            if (wr == 3 && mt < 7) { drain(); if (lane == 0) flag_set(ws + WS_FH, (ph1 * 8 + mt + 1) * FS, tag); }
            SC_(2);
            LocalPart lp[2];
            const f32x16 band = band_mask(lane);
#define LOCAL_BLOCK(mb) do { local_own(asbf(fq[mb][0]), asbf(fq[mb][1]), asbf(fk[mb][0]), asbf(fk[mb][1]), asbf(fv[mb][0]), asbf(fv[mb][1]), band, lp[mb]); \
                if (wr > 0) local_prev(asbf(fq[mb][0]), asbf(fq[mb][1]), ldsrd128(ex - 12288 + mb * 3072), ldsrd128(ex - 12288 + mb * 3072 + 1024), ldsrd128(ex - 12288 + mb * 3072 + 2048), lane, lp[mb]); \
                if (wr > 0 || mt == 0) local_fin(lp[mb]); } while (0)
            LOCAL_BLOCK(0);
            SC_(3);
            const int hl = (wid < 2) ? mt : 0;
            const unsigned fh = (unsigned)WS_FH + (ph1 * 8 + mt) * FS;
            const int b0 = RBLK(wid), b1 = RBLK(8 + wid);
            const int q = lane & 3;
            const u64 fm = flag_wait_some(ws, q == 0 ? fk0 - 1024 * FS + (blk >> 1) * FS : q == 1 ? fk0 + (b0 >> 1) * FS : q == 2 ? fk0 + (b1 >> 1) * FS : fh, tag, 3);
            SC_(4);
            glds4k_dev(kv + b0 * 4096, l16, dbeg);
            const char* qp = ws + WS_QF + (size_t)((head * 64 + blk) * 2) * 1024;
            glds16_dev(qp, l16, dbeg + 2 * K2_SLOT); glds16_dev(qp + 1024, l16, dbeg + 2 * K2_SLOT + 1024);
            if (fm & 4) glds4k_dev(kv + b1 * 4096, l16, dbeg + K2_SLOT);
            LOCAL_BLOCK(1);
#undef LOCAL_BLOCK
            if (fm & 4) WAITBAR_(4);
            else {
                WAITBAR_(0);
                flag_wait(ws, fk0 + (b1 >> 1) * FS, tag);
                glds4k_dev(kv + b1 * 4096, l16, dbeg + K2_SLOT);
            }
            const bf16x8 q0 = ldsrd128(dbeg + 2 * K2_SLOT + l16), q1 = ldsrd128(dbeg + 2 * K2_SLOT + 1024 + l16);
            SA_(3); SC_(5);
            if (hl && !(fm & 8)) flag_wait(ws, fh, tag);
            FIRST_TILE(lbase, q0, q1);
            u32x16 h0; u32x8 h1;
            L6C(hl, lbase);
            if (hl) {
                local_prev(asbf(fq[0][0]), asbf(fq[0][1]), asbf(u32x4{h0[0], h0[1], h0[2], h0[3]}), asbf(u32x4{h0[4], h0[5], h0[6], h0[7]}), asbf(u32x4{h0[8], h0[9], h0[10], h0[11]}), lane, lp[0]);
                local_prev(asbf(fq[1][0]), asbf(fq[1][1]), asbf(u32x4{h0[12], h0[13], h0[14], h0[15]}), asbf(u32x4{h1[0], h1[1], h1[2], h1[3]}), asbf(u32x4{h1[4], h1[5], h1[6], h1[7]}), lane, lp[1]);
                local_fin(lp[0]); local_fin(lp[1]);
            }
            const char* op = ws + WS_OF + (size_t)(((64 + pb1) * 32 + ph1) * 2) * 1024;
            st16_dev<0>(op, l16, quad(lp[0].o, 0)); st16_dev<1024>(op, l16, quad(lp[0].o, 1)); st16_dev<0>(op + 65536, l16, quad(lp[1].o, 0)); st16_dev<1024>(op + 65536, l16, quad(lp[1].o, 1));
        }
#undef FIRST_TILE
#undef L6_OUT
#undef L6C
        FRESH();
        l16 = lane * 16;
        SA_(4); SC_(6);
        const int isx = br & (1 - g);
        unsigned cnt = 7, dst = dbeg + 2 * K2_SLOT, nx = K2_SLOT;
        unsigned mode = isx << 1;
        const unsigned act = 2u | (isx * 12u) | ((br | (1 - g)) << 5);
        u64 tab = (u64)blk << 42;
#pragma unroll
        for (int k = 1; k < 7; ++k) tab |= (u64)RBLK(8 * k + wid) << (6 * k);
        tab >>= 6 + 6 * br;
        u32x8 adr;
        { const int k = 8 * wid + (lane >> 4), c = lane & 15;
          adr = u32x8{lds0 + l16, lds0 + l16 + K2_SLOT, l16, lds0 + l16, (unsigned)(k * 2048 + ((c ^ ((k & 7) << 1)) << 4)), (unsigned)((k + 4) * 2048 + ((c ^ (((k + 4) & 7) << 1)) << 4) - 1024),
                      br ? (unsigned)WS_FO + ((64 + pb1 + (lane & 1)) * 32 + ph1) * FS : fxo, (unsigned)((((j & 3) * 8 + wid) * 32 + (lane >> 1)) * 2048 + (lane & 1) * 128)}; }
        f32x16 xo; f32x2 xl;
#define DBGOUT
        asm volatile(ATT_MAIN
                     : DBGOUT"+{v[0:15]}"(o), "+{v[32:47]}"(sa), "+{v[64:79]}"(qol), "+{v[80:95]}"(kvf), "+{v[108:111]}"(ph), "+{v[112:119]}"(adr), "={v[122:137]}"(xo), "={v[138:139]}"(xl),
                       [cnt] "+s"(cnt), [dst] "+s"(dst), [nx] "+s"(nx), [mode] "+s"(mode)
                     : "{v[16:31]}"(negm), "{v[140:143]}"(u32x4{(unsigned)tag, (unsigned)(tag >> 32), fxo, xrec0 + l16}),
                       "{v[170:173]}"(u32x4{FLADDR, 0u, 0u, lds0 + K2_FL + wid * 1024 + l16}),
                       [srclo] "s"((unsigned)(uintptr_t)kv), [srchi] "s"((unsigned)((uintptr_t)kv >> 32)), [dbeg] "s"(dbeg), [dend] "s"(dbeg + 3 * K2_SLOT),
                       [wo01] "s"(wo + (size_t)k30 * 131072), [wo23] "s"(wo + (size_t)((k30 + 2) & 15) * 131072), [wo] "s"(wo), [wdst] "s"(lds0 + 98304 + wid * 2048), [act] "s"(act), [tab] "s"(tab), [k0] "s"(br), [ws] "s"(ws)
                     : ATT_TMP, "v120", "v121", "v168", "v169", "s40", "s41", "s42", "s43", "s44", "s45", "s46", "s47", "s48", "s49", "s50", "s51", "vcc", "scc", "memory");
        FRESH();
        l16 = lane * 16;
        const unsigned xrec = xrec0 + l16;
        const bf16x8 q0 = asbf(u32x4{qol[0], qol[1], qol[2], qol[3]}), q1 = asbf(u32x4{qol[4], qol[5], qol[6], qol[7]});
        const u32x4 ones = {qol[8], qol[9], qol[10], qol[11]};
        SA_(5); SB_(1);
        float lsum = __uint_as_float(qol[12]);
        bool ok = lsum < 1.0e18f;
        if (isx) {
            if (__builtin_expect(!(mode & 4), 0)) {
                flag_wait(ws, fxo, tag);
                f32x4 a0, a1, a2, a3;
                asm volatile("global_load_dwordx4 %0, %5, %6 offset:-2048 sc1\n\tglobal_load_dwordx4 %1, %5, %6 offset:-1024 sc1\n\tglobal_load_dwordx4 %2, %5, %6 sc1\n\t"
                             "global_load_dwordx4 %3, %5, %6 offset:1024 sc1\n\tglobal_load_dwordx2 %4, %5, %6 offset:2048 sc1\n\ts_waitcnt vmcnt(0)"
                             : "=&v"(a0), "=&v"(a1), "=&v"(a2), "=&v"(a3), "=&v"(xl) : "v"(xrec), "s"(ws) : "memory");
#pragma unroll
                for (int r = 0; r < 4; ++r) { xo[r] = a0[r]; xo[4 + r] = a1[r]; xo[8 + r] = a2[r]; xo[12 + r] = a3[r]; }
            }
            const float nm = __builtin_fminf(negm[0], xl[1]), fa = EXP2(nm - negm[0]), fx = EXP2(nm - xl[1]);
            ok = ok && (xl[0] < 1.0e18f);
            lsum = lsum * fa + xl[0] * fx;
#pragma unroll
            for (int r = 0; r < 16; ++r) o[r] = o[r] * fa + xo[r] * fx;
        }
#pragma unroll
        for (int r = 0; r < 16; ++r) ok = ok && (__builtin_fabsf(o[r]) < 3.0e38f);
        if (__builtin_expect(__any(!ok), 0)) {
            float m = -__builtin_inff();
            f32x4 l;
            for (int pass = 0; pass < 2; ++pass) {
                o = z; l = f32x4{};
                for (int t = 0; t < 64; ++t) {
                    bf16x8 f0, f1, f2, f3;
                    asm volatile("global_load_dwordx4 %0, %4, %5 sc1\n\tglobal_load_dwordx4 %1, %4, %5 offset:1024 sc1\n\tglobal_load_dwordx4 %2, %4, %5 offset:2048 sc1\n\t"
                                 "global_load_dwordx4 %3, %4, %5 offset:3072 sc1\n\ts_waitcnt vmcnt(0)" : "=&v"(f0), "=&v"(f1), "=&v"(f2), "=&v"(f3) : "v"(l16 + t * 4096), "s"(kv) : "memory");
                    f32x16 s = MFMA32(f0, q0, z); s = MFMA32(f1, q1, s);
                    if (pass == 0) {
#pragma unroll
                        for (int r = 0; r < 16; ++r) m = __builtin_fmaxf(m, s[r]);
                    } else {
#pragma unroll
                        for (int r = 0; r < 16; ++r) s[r] = EXP2(s[r] - m);
                        const bf16x8 p0 = asbf(pack8(s, 0)), p1 = asbf(pack8(s, 1));
                        o = MFMA32(f2, p0, o); o = MFMA32(f3, p1, o);
                        l = MFMA16(asbf(ones), p0, l); l = MFMA16(asbf(ones), p1, l);
                    }
                }
                if (pass == 0) m = swap_max(m);
            }
            lsum = l[0];
        }
        const float inv = 1.0f / lsum;
#pragma unroll
        for (int r = 0; r < 16; ++r) o[r] *= inv;
        const char* op = ws + WS_OF + (size_t)((blk * 32 + head) * 2) * 1024;
        st16_dev<0>(op, l16, pack8(o, 0)); st16_dev<1024>(op, l16, pack8(o, 1));
        drain();
        SB_(2);
        if (lane == 0) flag_set(ws + WS_FO, (blk * 32 + head) * FS, tag);
    }
    {
        FRESH();
        const int g = wid >> 2, wi = wid & 3, wm = wi >> 1, wn = wi & 1, g4 = lane >> 4;
        const char* of = ws + WS_OF + (size_t)(br * 64 + mt3 * 4 + wi) * 65536;
        unsigned vw4[2];
#pragma unroll
        for (int ii = 0; ii < 2; ++ii) { const int k = 16 * wi + 4 * ii + g4; vw4[ii] = k * 2048 + (((lane & 15) ^ ((k & 7) << 1)) << 4); }
        const unsigned l16 = lane * 16, sdst = lds0 + wi * 4096;
#define STAGE_O(t) glds4k_dev(of + (((t) + k30) & 15) * 4096, l16, sdst + ((t) & 3) * 16384)
#define STAGE_WO(t, slot) do { const char* s_ = wo + (size_t)(((t) + k30) & 15) * 131072; glds16(s_, vw4[0], sdst + (slot)); glds16(s_, vw4[1], sdst + (slot) + 1024); \
            glds16(s_ + 16384, vw4[0], sdst + (slot) + 2048); glds16(s_ + 16384, vw4[1], sdst + (slot) + 3072); } while (0)
        asm volatile("s_barrier" ::: "memory");
        SB_(3);
        STAGE_WO(5 - g, 65536 + (1 - g) * 16384);
        flag_wait(ws + WS_FO, ((br * 64 + mt3 * 4 + wi) * 32 + 4 * ((lane & 15) >> 1) + 2 * (1 - g) + (lane & 1)) * FS, tag);
        STAGE_O(1 - g);
        if (g == 1) STAGE_O(2);
        SA_(6); SB_(4);
        const unsigned ao = lds0 + g * 16384 + wm * 8192 + (g4 >> 1) * 1024 + ((lane & 15) + 32 * (g4 & 1)) * 16;
        unsigned aw[4];
        { const int q4 = (lane & 15) >> 2, p = lane & 3, k = 16 * (g4 >> 1) + 4 * (g4 & 1) + q4;
          const unsigned a0 = lds0 + g * 16384 + k * 256 + (((8 * wn + (p >> 1)) ^ ((k & 7) << 1)) << 4) + (p & 1) * 8;
#pragma unroll
          for (int nb = 0; nb < 4; ++nb) aw[nb] = a0 ^ (nb << 5); }
        f32x4 acc[4][4];
#pragma unroll
        for (int mb = 0; mb < 4; ++mb)
#pragma unroll
            for (int nb = 0; nb < 4; ++nb) acc[mb][nb] = f32x4{};
        if (g == 1) { WAITBAR_(4); asm volatile("s_barrier" ::: "memory"); } else asm volatile("s_barrier" ::: "memory");
        SB_(5);
        SBAR();
#pragma unroll
        for (int u = 0; u < 8; ++u) {
            bf16x8 of_[4][2], wf[4][2];
            const int to = 2 * u + 3 + g, tw = to + 2;
            const char* so = of + ((to + k30) & 15) * 4096; const char* sw = wo + (size_t)((tw + k30) & 15) * 131072;
            const unsigned dob = sdst + (to & 3) * 16384, dwb = sdst + (g ? p3_wslot(2 * u + 6) : p3_wslot(2 * u + 5));
            const bool doo = u < 6 || (u == 6 && g == 0), dow = (u >= 1 || g == 1) && (u < 5 || (u == 5 && g == 0));
#pragma unroll
            for (int i = 0; i < 8; ++i) {
                const int hh = i >> 2, q = i & 3;
                of_[q][hh] = ldsrd128(ao + ((2 * u) & 3) * 16384 + (q >> 1) * 4096 + (q & 1) * 256 + hh * 2048);
                wf[q][hh] = cat8(ldstr(aw[q] + p3_wslot(2 * u) + hh * 8192), ldstr(aw[q] + p3_wslot(2 * u) + hh * 8192 + 2048));
                if (i < 4) { if (u < 7 && doo) glds16_dev(so + i * 1024, l16, dob + i * 1024); }
                else if (u < 6 && dow) glds16(sw + ((i & 2) ? 16384 : 0), vw4[i & 1], dwb + (i - 4) * 1024);
            }
            if (p3_allow(2 * u) == p3_allow(2 * u + 1)) WAITBAR(p3_allow(2 * u)); else if (g == 0) WAITBAR(p3_allow(2 * u)); else WAITBAR(p3_allow(2 * u + 1));
            SBAR();
            __builtin_amdgcn_s_setprio(1);
#pragma unroll
            for (int mb = 0; mb < 4; ++mb)
#pragma unroll
                for (int nb = 0; nb < 4; ++nb) { acc[mb][nb] = MFMA16(wf[nb][0], of_[mb][0], acc[mb][nb]); acc[mb][nb] = MFMA16(wf[nb][1], of_[mb][1], acc[mb][nb]); }
            __builtin_amdgcn_s_setprio(0);
            SBAR();
            if (u < 7 || g == 0) asm volatile("s_barrier" ::: "memory");
            SBAR();
        }
        SB_(6);
        FRESH();
        const unsigned xw = lds0 + wid * 8192 + l16, xr = lds0 + (wid ^ 4) * 8192 + l16;
#pragma unroll
        for (int i = 0; i < 8; ++i) { const f32x4 v = g ? acc[i >> 2][i & 3] : acc[2 + (i >> 2)][i & 3]; ldswr128(xw + i * 1024, __builtin_bit_cast(u32x4, v)); }
        BARRIER_L();
        const unsigned st = lds0 + 65536 + wid * 4096;
#pragma unroll
        for (int i = 0; i < 8; ++i) {
            const f32x4 v = (g ? acc[2 + (i >> 2)][i & 3] : acc[i >> 2][i & 3]) + __builtin_bit_cast(f32x4, ldsrd128(xr + i * 1024));
            const int row = 16 * (i >> 2) + (lane & 15), c = 2 * (i & 3) + (g4 >> 1);
            *(LAS u32x2*)(uintptr_t)(st + row * 128 + ((c ^ (row & 7)) << 4) + (g4 & 1) * 8) = u32x2{cvtpk(v[0], v[1]), cvtpk(v[2], v[3])};
        }
        const int blk = mt3 * 4 + 2 * wm + g;
        char* op = (char*)out + br * 2048 + nt3 * 256 + wn * 128 + (lane & 7) * 16;
#pragma unroll
        for (int i = 0; i < 4; ++i) {
            const int r = 8 * i + (lane >> 3), row = br ? ((blk >> 1) * 64 + 2 * r + (blk & 1)) : (blk * 32 + r);
            *(u32x4*)(op + (size_t)row * 4096) = __builtin_bit_cast(u32x4, ldsrd128(st + r * 128 + (((lane & 7) ^ (r & 7)) << 4)));
        }
#undef STAGE_O
#undef STAGE_WO
    }
    STAMP_AT(0, 7);
    if (b == 0 && wid == 0) {
#pragma unroll
        for (int i = 0; i < 2; ++i) { const int u = i * 64 + lane;
            flag_wait(ws0, (unsigned)WS_FK + ((2 * (u >> 3)) * 32 + (u & 7) * 4) * FS, tag);
            flag_wait(ws0, (unsigned)WS_FO + ((64 + (u & 7) * 8) * 32 + 2 * (u >> 3)) * FS, tag); }
        if (lane == 0) flag_set(ws0, (unsigned)WS_EP, tag);
    }
}

extern "C" void kernel_launch(void* const* d_in, const int* in_sizes, int n_in, void* d_out, int out_size, void* d_ws, size_t ws_size, hipStream_t stream) {
    mk_fused<<<256, 512, 0, stream>>>((const u16*)d_in[0], (const u16*)d_in[1], (const u16*)d_in[2], (const u16*)d_in[3], (const u16*)d_in[4], (const u16*)d_in[5], (char*)d_ws, (u16*)d_out);
}
```

```cpp
#include <hip/hip_runtime.h>
#include <stdint.h>

typedef unsigned short u16;
typedef unsigned long long u64;
typedef __bf16 bf16x8 __attribute__((ext_vector_type(8)));
typedef __bf16 bf16x2 __attribute__((ext_vector_type(2)));
typedef short s16x4 __attribute__((ext_vector_type(4)));
typedef float f32x16 __attribute__((ext_vector_type(16)));
typedef float f32x4 __attribute__((ext_vector_type(4)));
typedef float f32x2 __attribute__((ext_vector_type(2)));
typedef unsigned u32x4 __attribute__((ext_vector_type(4)));
typedef unsigned u32x2 __attribute__((ext_vector_type(2)));
typedef unsigned u32x8 __attribute__((ext_vector_type(8)));
typedef unsigned u32x16 __attribute__((ext_vector_type(16)));
#define LAS __attribute__((address_space(3)))
#define DI __device__ __forceinline__
#define SBAR() __builtin_amdgcn_sched_barrier(0)
#define MFMA32(a, b, c) __builtin_amdgcn_mfma_f32_32x32x16_bf16((a), (b), (c), 0, 0, 0)
#define MFMA16(a, b, c) __builtin_amdgcn_mfma_f32_16x16x32_bf16((a), (b), (c), 0, 0, 0)
#define EXP2(x) __builtin_amdgcn_exp2f(x)

constexpr float QSCALE = 0.17677669529663687f * 1.4426950408889634f;

constexpr size_t WS_QF = 0;
constexpr size_t WS_KV = WS_QF + (4u << 20);
constexpr size_t WS_OF = WS_KV + (8u << 20);
constexpr size_t WS_HALO = WS_OF + (8u << 20);
constexpr size_t WS_XP = WS_HALO + (2u << 20);
constexpr size_t WS_FX = WS_XP + (5u << 20);
#define FS 128
constexpr size_t WS_FQ = WS_FX + 1024 * FS;
constexpr size_t WS_FK = WS_FQ + 1024 * FS;
constexpr size_t WS_FO = WS_FK + 1024 * FS;
constexpr size_t WS_FH = WS_FO + 4096 * FS;
constexpr size_t WS_EP = WS_FH + 256 * FS;

#define SLP 8
#define STAMP_AT(K, I)
#define SA_(I)
#define SB_(I)

#define SC_(I)
DI unsigned cvtpk(float lo, float hi) { f32x2 v = {lo, hi}; bf16x2 b = __builtin_convertvector(v, bf16x2); return __builtin_bit_cast(unsigned, b); }
DI u32x4 pack8(const f32x16& a, int s) { u32x4 r; r[0] = cvtpk(a[8 * s], a[8 * s + 1]); r[1] = cvtpk(a[8 * s + 2], a[8 * s + 3]); r[2] = cvtpk(a[8 * s + 4], a[8 * s + 5]); r[3] = cvtpk(a[8 * s + 6], a[8 * s + 7]); return r; }
DI u32x4 pack44(f32x4 a, f32x4 c) { return u32x4{cvtpk(a[0], a[1]), cvtpk(a[2], a[3]), cvtpk(c[0], c[1]), cvtpk(c[2], c[3])}; }
DI u32x4 asu(bf16x8 v) { return __builtin_bit_cast(u32x4, v); }
DI u32x16 cat16(u32x4 a, u32x4 b, u32x4 c, u32x4 d) { return u32x16{a[0], a[1], a[2], a[3], b[0], b[1], b[2], b[3], c[0], c[1], c[2], c[3], d[0], d[1], d[2], d[3]}; }
DI bf16x8 asbf(u32x4 v) { return __builtin_bit_cast(bf16x8, v); }
DI void glds16(const void* sbase, unsigned voff, unsigned lds) {
    asm volatile("s_mov_b32 m0, %2\n\ts_nop 0\n\tglobal_load_lds_dwordx4 %0, %1" :: "v"(voff), "s"(sbase), "s"(lds) : "memory");
}
DI void glds16_dev(const void* sbase, unsigned voff, unsigned lds) {
    asm volatile("s_mov_b32 m0, %2\n\ts_nop 0\n\tglobal_load_lds_dwordx4 %0, %1 sc1" :: "v"(voff), "s"(sbase), "s"(lds) : "memory");
}
DI void glds4k_dev(const void* sbase, unsigned voff, unsigned lds) {
    asm volatile("s_mov_b32 m0, %2\n\ts_nop 0\n\tglobal_load_lds_dwordx4 %0, %1 sc1\n\tglobal_load_lds_dwordx4 %0, %1 offset:1024 sc1\n\t"
                 "global_load_lds_dwordx4 %0, %1 offset:2048 sc1\n\tglobal_load_lds_dwordx4 %0, %1 offset:3072 sc1" :: "v"(voff), "s"(sbase), "s"(lds) : "memory");
}
template <int OFF> DI void st16_dev(const void* sbase, unsigned voff, u32x4 v) {
    asm volatile("global_store_dwordx4 %0, %1, %2 offset:%3 sc1\n\ts_nop 1" :: "v"(voff), "v"(v), "s"(sbase), "i"(OFF) : "memory");
}
DI void drain() { asm volatile("s_waitcnt vmcnt(0)" ::: "memory"); }
DI void flag_set(const void* sbase, unsigned voff, u64 tag) {
    asm volatile("global_store_dwordx2 %0, %1, %2 sc1" :: "v"(voff), "v"(tag), "s"(sbase) : "memory");
}
DI void flag_wait(const void* sbase, unsigned voff, u64 tag) {
    const u64 t0 = __builtin_amdgcn_s_memrealtime();
    for (;;) {
        u64 v;
        asm volatile("global_load_dwordx2 %0, %1, %2 sc1\n\ts_waitcnt vmcnt(0)" : "=v"(v) : "v"(voff), "s"(sbase) : "memory");
        if (__all(v == tag)) break;
        if (__builtin_amdgcn_s_memrealtime() - t0 > 2000000ull) break;
        __builtin_amdgcn_s_sleep(SLP);
    }
}
DI u64 flag_wait_some(const void* sbase, unsigned voff, u64 tag, u64 need) {
    const u64 t0 = __builtin_amdgcn_s_memrealtime();
    for (;;) {
        u64 v;
        asm volatile("global_load_dwordx2 %0, %1, %2 sc1\n\ts_waitcnt vmcnt(0)" : "=v"(v) : "v"(voff), "s"(sbase) : "memory");
        const u64 m = __ballot(v == tag);
        if ((m & need) == need || __builtin_amdgcn_s_memrealtime() - t0 > 2000000ull) return m;
        __builtin_amdgcn_s_sleep(SLP);
    }
}
DI bf16x8 ldsrd128(unsigned addr) { return *(const LAS bf16x8*)(uintptr_t)addr; }
DI void ldswr128(unsigned addr, u32x4 v) { *(LAS u32x4*)(uintptr_t)addr = v; }
DI s16x4 ldstr(unsigned addr) { return __builtin_amdgcn_ds_read_tr16_b64_v4i16((LAS s16x4*)(uintptr_t)addr); }
DI bf16x8 cat8(s16x4 lo, s16x4 hi) { typedef short s16x8 __attribute__((ext_vector_type(8))); s16x8 v = {lo[0], lo[1], lo[2], lo[3], hi[0], hi[1], hi[2], hi[3]}; return __builtin_bit_cast(bf16x8, v); }
DI float swap_max(float m) { auto r = __builtin_amdgcn_permlane32_swap(__float_as_uint(m), __float_as_uint(m), false, false); return __builtin_fmaxf(__uint_as_float(r[0]), __uint_as_float(r[1])); }
DI float swap_add(float m) { auto r = __builtin_amdgcn_permlane32_swap(__float_as_uint(m), __float_as_uint(m), false, false); return __uint_as_float(r[0]) + __uint_as_float(r[1]); }
#define FRESH() asm volatile("" : "+v"(lane), "+s"(ws))
#define BARRIER_L() asm volatile("s_waitcnt lgkmcnt(0)\n\ts_barrier" ::: "memory")
#define WAITBAR_(n) asm volatile("s_waitcnt vmcnt(%0) lgkmcnt(0)\n\ts_barrier" :: "i"(n) : "memory")
#define WAITBAR(n) do { switch (n) { case 0: WAITBAR_(0); break; case 2: WAITBAR_(2); break; case 3: WAITBAR_(3); break; case 4: WAITBAR_(4); break; case 6: WAITBAR_(6); break; \
    case 7: WAITBAR_(7); break; case 8: WAITBAR_(8); break; case 10: WAITBAR_(10); break; case 12: WAITBAR_(12); break; default: WAITBAR_(0); } } while (0)

struct LocalPart { f32x16 o; float m, l; };
DI f32x16 band_mask(int lane) {
    const int i = lane & 31, h = lane >> 5;
    f32x16 c;
#pragma unroll
    for (int r = 0; r < 16; ++r) { const int j = (r & 3) + 8 * (r >> 2) + 4 * h; c[r] = ((unsigned)(i - j) <= 7u) ? 0.f : -__builtin_inff(); }
    return c;
}
DI float max3(float a, float b, float c) { float r; asm("v_max3_f32 %0, %1, %2, %3" : "=v"(r) : "v"(a), "v"(b), "v"(c)); return r; }
DI void local_own(bf16x8 q0, bf16x8 q1, bf16x8 k0, bf16x8 k1, bf16x8 v0, bf16x8 v1, const f32x16& band, LocalPart& P) {
    const f32x16 z = {};
    f32x16 so = MFMA32(k0, q0, band); so = MFMA32(k1, q1, so);
    float m = __builtin_fmaxf(so[0], so[1]);
#pragma unroll
    for (int r = 2; r < 16; r += 2) m = __builtin_fmaxf(__builtin_fmaxf(m, so[r]), so[r + 1]);
    m = swap_max(m);
    float l = 0.f;
#pragma unroll
    for (int r = 0; r < 16; ++r) { so[r] = EXP2(so[r] - m); l += so[r]; }
    P.o = MFMA32(v0, asbf(pack8(so, 0)), z); P.o = MFMA32(v1, asbf(pack8(so, 1)), P.o);
    P.m = m; P.l = l;
}
DI void local_prev(bf16x8 q0, bf16x8 q1, bf16x8 kp0, bf16x8 kp1, bf16x8 vp1, int lane, LocalPart& P) {
    const int i = lane & 31, h = lane >> 5;
    const f32x16 z = {};
    f32x16 sp = MFMA32(kp0, q0, z); sp = MFMA32(kp1, q1, sp);
    const float NEG = -__builtin_inff();
    float m = P.m;
#pragma unroll
    for (int r = 12; r < 16; ++r) { const int j = (r & 3) + 24 + 4 * h; sp[r] = (j >= i + 25) ? sp[r] : NEG; m = __builtin_fmaxf(m, sp[r]); }
    m = swap_max(m);
    const float f = EXP2(P.m - m);
    float l = P.l * f;
#pragma unroll
    for (int r = 12; r < 16; ++r) { sp[r] = EXP2(sp[r] - m); l += sp[r]; }
#pragma unroll
    for (int r = 0; r < 16; ++r) P.o[r] *= f;
    P.o = MFMA32(vp1, asbf(u32x4{0u, 0u, cvtpk(sp[12], sp[13]), cvtpk(sp[14], sp[15])}), P.o);
    P.m = m; P.l = l;
}
DI void local_fin(LocalPart& P) {
    const float inv = 1.0f / swap_add(P.l);
#pragma unroll
    for (int r = 0; r < 16; ++r) P.o[r] *= inv;
    const u32x4 o0 = pack8(P.o, 0), o1 = pack8(P.o, 1);
#pragma unroll
    for (int r = 0; r < 4; ++r) { P.o[r] = __uint_as_float(o0[r]); P.o[4 + r] = __uint_as_float(o1[r]); }
}
DI u32x4 quad(const f32x16& a, int i) { return __builtin_bit_cast(u32x4, f32x4{a[4 * i], a[4 * i + 1], a[4 * i + 2], a[4 * i + 3]}); }

#define ATT_L6C \
    "s_nop 1\n\t" \
    "s_cmp_eq_u32 %[hl], 0\n\t" \
    "s_cbranch_scc1 10f\n\t" \
    "global_load_dwordx4 v[144:147], v143, %[ws] offset:-3072 sc1\n\t" \
    "global_load_dwordx4 v[148:151], v143, %[ws] offset:-2048 sc1\n\t" \
    "global_load_dwordx4 v[152:155], v143, %[ws] offset:-1024 sc1\n\t" \
    "global_load_dwordx4 v[156:159], v143, %[ws] offset:0 sc1\n\t" \
    "global_load_dwordx4 v[160:163], v143, %[ws] offset:1024 sc1\n\t" \
    "global_load_dwordx4 v[164:167], v143, %[ws] offset:2048 sc1\n\t" \
    "10:\n" \
    "s_setprio 3\n\t" \
    "s_waitcnt lgkmcnt(0)\n\t" \
    "v_mfma_f32_32x32x16_bf16 v[48:63], v[80:83], v[64:67], v[16:31]\n\t" \
    "v_exp_f32_e32 v32, v32\n\t" \
    "v_exp_f32_e32 v33, v33\n\t" \
    "v_exp_f32_e32 v34, v34\n\t" \
    "v_mfma_f32_32x32x16_bf16 v[48:63], v[84:87], v[68:71], v[48:63]\n\t" \
    "ds_read_b128 v[80:83], v112 offset:8192\n\t" \
    "ds_read_b128 v[84:87], v112 offset:9216\n\t" \
    "v_exp_f32_e32 v35, v35\n\t" \
    "v_exp_f32_e32 v36, v36\n\t" \
    "v_cvt_pk_bf16_f32 v104, v32, v33\n\t" \
    "v_cvt_pk_bf16_f32 v105, v34, v35\n\t" \
    "ds_read_b128 v[100:103], v112 offset:3072\n\t" \
    "ds_read_b128 v[96:99], v112 offset:6144\n\t" \
    "v_exp_f32_e32 v37, v37\n\t" \
    "v_exp_f32_e32 v38, v38\n\t" \
    "v_exp_f32_e32 v39, v39\n\t" \
    "v_exp_f32_e32 v40, v40\n\t" \
    "v_cvt_pk_bf16_f32 v106, v36, v37\n\t" \
    "v_cvt_pk_bf16_f32 v107, v38, v39\n\t" \
    "v_exp_f32_e32 v41, v41\n\t" \
    "v_exp_f32_e32 v42, v42\n\t" \
    "v_mfma_f32_32x32x16_bf16 v[0:15], v[88:91], v[104:107], 0\n\t" \
    "v_exp_f32_e32 v43, v43\n\t" \
    "v_exp_f32_e32 v44, v44\n\t" \
    "v_exp_f32_e32 v45, v45\n\t" \
    "v_mfma_f32_16x16x32_bf16 v[76:79], v[72:75], v[104:107], 0\n\t" \
    "v_exp_f32_e32 v46, v46\n\t" \
    "v_exp_f32_e32 v47, v47\n\t" \
    "v_cvt_pk_bf16_f32 v108, v40, v41\n\t" \
    "v_cvt_pk_bf16_f32 v109, v42, v43\n\t" \
    "v_cvt_pk_bf16_f32 v110, v44, v45\n\t" \
    "v_cvt_pk_bf16_f32 v111, v46, v47\n\t" \
    "s_waitcnt lgkmcnt(0)\n\t" \
    "v_mfma_f32_32x32x16_bf16 v[32:47], v[80:83], v[64:67], v[16:31]\n\t" \
    "v_exp_f32_e32 v48, v48\n\t" \
    "v_exp_f32_e32 v49, v49\n\t" \
    "v_exp_f32_e32 v50, v50\n\t" \
    "v_mfma_f32_32x32x16_bf16 v[32:47], v[84:87], v[68:71], v[32:47]\n\t" \
    "ds_read_b128 v[80:83], v112 offset:12288\n\t" \
    "ds_read_b128 v[84:87], v112 offset:13312\n\t" \
    "v_exp_f32_e32 v51, v51\n\t" \
    "v_exp_f32_e32 v52, v52\n\t" \
    "v_cvt_pk_bf16_f32 v104, v48, v49\n\t" \
    "v_cvt_pk_bf16_f32 v105, v50, v51\n\t" \
    "v_mfma_f32_32x32x16_bf16 v[0:15], v[100:103], v[108:111], v[0:15]\n\t" \
    "ds_read_b128 v[92:95], v112 offset:7168\n\t" \
    "ds_read_b128 v[88:91], v112 offset:10240\n\t" \
    "v_exp_f32_e32 v53, v53\n\t" \
    "v_exp_f32_e32 v54, v54\n\t" \
    "v_exp_f32_e32 v55, v55\n\t" \
    "v_mfma_f32_16x16x32_bf16 v[76:79], v[72:75], v[108:111], v[76:79]\n\t" \
    "v_exp_f32_e32 v56, v56\n\t" \
    "v_cvt_pk_bf16_f32 v106, v52, v53\n\t" \
    "v_cvt_pk_bf16_f32 v107, v54, v55\n\t" \
    "v_exp_f32_e32 v57, v57\n\t" \
    "v_exp_f32_e32 v58, v58\n\t" \
    "v_mfma_f32_32x32x16_bf16 v[0:15], v[96:99], v[104:107], v[0:15]\n\t" \
    "v_exp_f32_e32 v59, v59\n\t" \
    "v_exp_f32_e32 v60, v60\n\t" \
    "v_exp_f32_e32 v61, v61\n\t" \
    "v_mfma_f32_16x16x32_bf16 v[76:79], v[72:75], v[104:107], v[76:79]\n\t" \
    "v_exp_f32_e32 v62, v62\n\t" \
    "v_exp_f32_e32 v63, v63\n\t" \
    "v_cvt_pk_bf16_f32 v108, v56, v57\n\t" \
    "v_cvt_pk_bf16_f32 v109, v58, v59\n\t" \
    "v_cvt_pk_bf16_f32 v110, v60, v61\n\t" \
    "v_cvt_pk_bf16_f32 v111, v62, v63\n\t" \
    "s_setprio 2\n\t" \
    "s_waitcnt lgkmcnt(0)\n\t" \
    "v_mfma_f32_32x32x16_bf16 v[48:63], v[80:83], v[64:67], v[16:31]\n\t" \
    "v_exp_f32_e32 v32, v32\n\t" \
    "v_exp_f32_e32 v33, v33\n\t" \
    "v_exp_f32_e32 v34, v34\n\t" \
    "v_mfma_f32_32x32x16_bf16 v[48:63], v[84:87], v[68:71], v[48:63]\n\t" \
    "ds_read_b128 v[80:83], v112 offset:16384\n\t" \
    "ds_read_b128 v[84:87], v112 offset:17408\n\t" \
    "v_exp_f32_e32 v35, v35\n\t" \
    "v_exp_f32_e32 v36, v36\n\t" \
    "v_cvt_pk_bf16_f32 v104, v32, v33\n\t" \
    "v_cvt_pk_bf16_f32 v105, v34, v35\n\t" \
    "v_mfma_f32_32x32x16_bf16 v[0:15], v[92:95], v[108:111], v[0:15]\n\t" \
    "ds_read_b128 v[100:103], v112 offset:11264\n\t" \
    "ds_read_b128 v[96:99], v112 offset:14336\n\t" \
    "v_exp_f32_e32 v37, v37\n\t" \
    "v_exp_f32_e32 v38, v38\n\t" \
    "v_exp_f32_e32 v39, v39\n\t" \
    "v_mfma_f32_16x16x32_bf16 v[76:79], v[72:75], v[108:111], v[76:79]\n\t" \
    "v_exp_f32_e32 v40, v40\n\t" \
    "v_cvt_pk_bf16_f32 v106, v36, v37\n\t" \
    "v_cvt_pk_bf16_f32 v107, v38, v39\n\t" \
    "v_exp_f32_e32 v41, v41\n\t" \
    "v_exp_f32_e32 v42, v42\n\t" \
    "v_mfma_f32_32x32x16_bf16 v[0:15], v[88:91], v[104:107], v[0:15]\n\t" \
    "v_exp_f32_e32 v43, v43\n\t" \
    "v_exp_f32_e32 v44, v44\n\t" \
    "v_exp_f32_e32 v45, v45\n\t" \
    "v_mfma_f32_16x16x32_bf16 v[76:79], v[72:75], v[104:107], v[76:79]\n\t" \
    "v_exp_f32_e32 v46, v46\n\t" \
    "v_exp_f32_e32 v47, v47\n\t" \
    "v_cvt_pk_bf16_f32 v108, v40, v41\n\t" \
    "v_cvt_pk_bf16_f32 v109, v42, v43\n\t" \
    "v_cvt_pk_bf16_f32 v110, v44, v45\n\t" \
    "v_cvt_pk_bf16_f32 v111, v46, v47\n\t" \
    "s_waitcnt lgkmcnt(0)\n\t" \
    "v_mfma_f32_32x32x16_bf16 v[32:47], v[80:83], v[64:67], v[16:31]\n\t" \
    "v_exp_f32_e32 v48, v48\n\t" \
    "v_exp_f32_e32 v49, v49\n\t" \
    "v_exp_f32_e32 v50, v50\n\t" \
    "v_mfma_f32_32x32x16_bf16 v[32:47], v[84:87], v[68:71], v[32:47]\n\t" \
    "ds_read_b128 v[80:83], v112 offset:20480\n\t" \
    "ds_read_b128 v[84:87], v112 offset:21504\n\t" \
    "v_exp_f32_e32 v51, v51\n\t" \
    "v_exp_f32_e32 v52, v52\n\t" \
    "v_cvt_pk_bf16_f32 v104, v48, v49\n\t" \
    "v_cvt_pk_bf16_f32 v105, v50, v51\n\t" \
    "v_mfma_f32_32x32x16_bf16 v[0:15], v[100:103], v[108:111], v[0:15]\n\t" \
    "ds_read_b128 v[92:95], v112 offset:15360\n\t" \
    "ds_read_b128 v[88:91], v112 offset:18432\n\t" \
    "v_exp_f32_e32 v53, v53\n\t" \
    "v_exp_f32_e32 v54, v54\n\t" \
    "v_exp_f32_e32 v55, v55\n\t" \
    "v_mfma_f32_16x16x32_bf16 v[76:79], v[72:75], v[108:111], v[76:79]\n\t" \
    "v_exp_f32_e32 v56, v56\n\t" \
    "v_cvt_pk_bf16_f32 v106, v52, v53\n\t" \
    "v_cvt_pk_bf16_f32 v107, v54, v55\n\t" \
    "v_exp_f32_e32 v57, v57\n\t" \
    "v_exp_f32_e32 v58, v58\n\t" \
    "v_mfma_f32_32x32x16_bf16 v[0:15], v[96:99], v[104:107], v[0:15]\n\t" \
    "v_exp_f32_e32 v59, v59\n\t" \
    "v_exp_f32_e32 v60, v60\n\t" \
    "v_exp_f32_e32 v61, v61\n\t" \
    "v_mfma_f32_16x16x32_bf16 v[76:79], v[72:75], v[104:107], v[76:79]\n\t" \
    "v_exp_f32_e32 v62, v62\n\t" \
    "v_exp_f32_e32 v63, v63\n\t" \
    "v_cvt_pk_bf16_f32 v108, v56, v57\n\t" \
    "v_cvt_pk_bf16_f32 v109, v58, v59\n\t" \
    "v_cvt_pk_bf16_f32 v110, v60, v61\n\t" \
    "v_cvt_pk_bf16_f32 v111, v62, v63\n\t" \
    "s_mov_b32 m0, %[fld]\n\t" \
    "s_nop 0\n\t" \
    "global_load_lds_dwordx4 v142, %[ws] sc1\n\t" \
    "s_setprio 1\n\t" \
    "s_waitcnt lgkmcnt(0)\n\t" \
    "v_mfma_f32_32x32x16_bf16 v[48:63], v[80:83], v[64:67], v[16:31]\n\t" \
    "v_exp_f32_e32 v32, v32\n\t" \
    "v_exp_f32_e32 v33, v33\n\t" \
    "v_exp_f32_e32 v34, v34\n\t" \
    "v_mfma_f32_32x32x16_bf16 v[48:63], v[84:87], v[68:71], v[48:63]\n\t" \
    "ds_read_b128 v[80:83], v112 offset:24576\n\t" \
    "ds_read_b128 v[84:87], v112 offset:25600\n\t" \
    "v_exp_f32_e32 v35, v35\n\t" \
    "v_exp_f32_e32 v36, v36\n\t" \
    "v_cvt_pk_bf16_f32 v104, v32, v33\n\t" \
    "v_cvt_pk_bf16_f32 v105, v34, v35\n\t" \
    "v_mfma_f32_32x32x16_bf16 v[0:15], v[92:95], v[108:111], v[0:15]\n\t" \
    "ds_read_b128 v[100:103], v112 offset:19456\n\t" \
    "ds_read_b128 v[96:99], v112 offset:22528\n\t" \
    "v_exp_f32_e32 v37, v37\n\t" \
    "v_exp_f32_e32 v38, v38\n\t" \
    "v_exp_f32_e32 v39, v39\n\t" \
    "v_mfma_f32_16x16x32_bf16 v[76:79], v[72:75], v[108:111], v[76:79]\n\t" \
    "v_exp_f32_e32 v40, v40\n\t" \
    "v_cvt_pk_bf16_f32 v106, v36, v37\n\t" \
    "v_cvt_pk_bf16_f32 v107, v38, v39\n\t" \
    "v_exp_f32_e32 v41, v41\n\t" \
    "v_exp_f32_e32 v42, v42\n\t" \
    "v_mfma_f32_32x32x16_bf16 v[0:15], v[88:91], v[104:107], v[0:15]\n\t" \
    "v_exp_f32_e32 v43, v43\n\t" \
    "v_exp_f32_e32 v44, v44\n\t" \
    "v_exp_f32_e32 v45, v45\n\t" \
    "v_mfma_f32_16x16x32_bf16 v[76:79], v[72:75], v[104:107], v[76:79]\n\t" \
    "v_exp_f32_e32 v46, v46\n\t" \
    "v_exp_f32_e32 v47, v47\n\t" \
    "v_cvt_pk_bf16_f32 v108, v40, v41\n\t" \
    "v_cvt_pk_bf16_f32 v109, v42, v43\n\t" \
    "v_cvt_pk_bf16_f32 v110, v44, v45\n\t" \
    "v_cvt_pk_bf16_f32 v111, v46, v47\n\t" \
    "s_waitcnt lgkmcnt(0)\n\t" \
    "v_mfma_f32_32x32x16_bf16 v[32:47], v[80:83], v[64:67], v[16:31]\n\t" \
    "v_exp_f32_e32 v48, v48\n\t" \
    "v_exp_f32_e32 v49, v49\n\t" \
    "v_exp_f32_e32 v50, v50\n\t" \
    "v_mfma_f32_32x32x16_bf16 v[32:47], v[84:87], v[68:71], v[32:47]\n\t" \
    "ds_read_b128 v[80:83], v112 offset:28672\n\t" \
    "ds_read_b128 v[84:87], v112 offset:29696\n\t" \
    "v_exp_f32_e32 v51, v51\n\t" \
    "v_exp_f32_e32 v52, v52\n\t" \
    "v_cvt_pk_bf16_f32 v104, v48, v49\n\t" \
    "v_cvt_pk_bf16_f32 v105, v50, v51\n\t" \
    "v_mfma_f32_32x32x16_bf16 v[0:15], v[100:103], v[108:111], v[0:15]\n\t" \
    "ds_read_b128 v[92:95], v112 offset:23552\n\t" \
    "ds_read_b128 v[88:91], v112 offset:26624\n\t" \
    "v_exp_f32_e32 v53, v53\n\t" \
    "v_exp_f32_e32 v54, v54\n\t" \
    "v_exp_f32_e32 v55, v55\n\t" \
    "v_mfma_f32_16x16x32_bf16 v[76:79], v[72:75], v[108:111], v[76:79]\n\t" \
    "v_exp_f32_e32 v56, v56\n\t" \
    "v_cvt_pk_bf16_f32 v106, v52, v53\n\t" \
    "v_cvt_pk_bf16_f32 v107, v54, v55\n\t" \
    "v_exp_f32_e32 v57, v57\n\t" \
    "v_exp_f32_e32 v58, v58\n\t" \
    "v_mfma_f32_32x32x16_bf16 v[0:15], v[96:99], v[104:107], v[0:15]\n\t" \
    "v_exp_f32_e32 v59, v59\n\t" \
    "v_exp_f32_e32 v60, v60\n\t" \
    "v_exp_f32_e32 v61, v61\n\t" \
    "v_mfma_f32_16x16x32_bf16 v[76:79], v[72:75], v[104:107], v[76:79]\n\t" \
    "v_exp_f32_e32 v62, v62\n\t" \
    "v_exp_f32_e32 v63, v63\n\t" \
    "v_cvt_pk_bf16_f32 v108, v56, v57\n\t" \
    "v_cvt_pk_bf16_f32 v109, v58, v59\n\t" \
    "v_cvt_pk_bf16_f32 v110, v60, v61\n\t" \
    "v_cvt_pk_bf16_f32 v111, v62, v63\n\t" \
    "s_setprio 0\n\t" \
    "s_cmp_eq_u32 %[hl], 0\n\t" \
    "s_cbranch_scc1 11f\n\t" \
    "s_waitcnt vmcnt(1)\n\t" \
    "11:\n" \
    "s_waitcnt lgkmcnt(0)\n\t" \
    "s_nop 15\n\t"
#define ATT_XT \
    "s_nop 1\n\t" \
    "s_waitcnt lgkmcnt(0)\n\t" \
    "v_mfma_f32_32x32x16_bf16 v[48:63], v[80:83], v[64:67], v[16:31]\n\t" \
    "v_exp_f32_e32 v32, v32\n\t" \
    "v_exp_f32_e32 v33, v33\n\t" \
    "v_exp_f32_e32 v34, v34\n\t" \
    "v_mfma_f32_32x32x16_bf16 v[48:63], v[84:87], v[68:71], v[48:63]\n\t" \
    "v_exp_f32_e32 v35, v35\n\t" \
    "v_exp_f32_e32 v36, v36\n\t" \
    "v_cvt_pk_bf16_f32 v104, v32, v33\n\t" \
    "v_cvt_pk_bf16_f32 v105, v34, v35\n\t" \
    "v_mfma_f32_32x32x16_bf16 v[0:15], v[92:95], v[108:111], v[0:15]\n\t" \
    "ds_read_b128 v[100:103], v112 offset:27648\n\t" \
    "ds_read_b128 v[96:99], v112 offset:30720\n\t" \
    "v_exp_f32_e32 v37, v37\n\t" \
    "v_exp_f32_e32 v38, v38\n\t" \
    "v_exp_f32_e32 v39, v39\n\t" \
    "v_mfma_f32_16x16x32_bf16 v[76:79], v[72:75], v[108:111], v[76:79]\n\t" \
    "v_exp_f32_e32 v40, v40\n\t" \
    "v_cvt_pk_bf16_f32 v106, v36, v37\n\t" \
    "v_cvt_pk_bf16_f32 v107, v38, v39\n\t" \
    "v_exp_f32_e32 v41, v41\n\t" \
    "v_exp_f32_e32 v42, v42\n\t" \
    "v_mfma_f32_32x32x16_bf16 v[0:15], v[88:91], v[104:107], v[0:15]\n\t" \
    "v_exp_f32_e32 v43, v43\n\t" \
    "v_exp_f32_e32 v44, v44\n\t" \
    "v_exp_f32_e32 v45, v45\n\t" \
    "v_mfma_f32_16x16x32_bf16 v[76:79], v[72:75], v[104:107], v[76:79]\n\t" \
    "v_exp_f32_e32 v46, v46\n\t" \
    "v_exp_f32_e32 v47, v47\n\t" \
    "v_cvt_pk_bf16_f32 v108, v40, v41\n\t" \
    "v_cvt_pk_bf16_f32 v109, v42, v43\n\t" \
    "v_cvt_pk_bf16_f32 v110, v44, v45\n\t" \
    "v_cvt_pk_bf16_f32 v111, v46, v47\n\t" \
    "s_waitcnt lgkmcnt(0)\n\t" \
    "v_exp_f32_e32 v48, v48\n\t" \
    "v_exp_f32_e32 v49, v49\n\t" \
    "v_exp_f32_e32 v50, v50\n\t" \
    "v_exp_f32_e32 v51, v51\n\t" \
    "v_exp_f32_e32 v52, v52\n\t" \
    "v_cvt_pk_bf16_f32 v104, v48, v49\n\t" \
    "v_cvt_pk_bf16_f32 v105, v50, v51\n\t" \
    "v_mfma_f32_32x32x16_bf16 v[0:15], v[100:103], v[108:111], v[0:15]\n\t" \
    "ds_read_b128 v[92:95], v112 offset:31744\n\t" \
    "v_exp_f32_e32 v53, v53\n\t" \
    "v_exp_f32_e32 v54, v54\n\t" \
    "v_exp_f32_e32 v55, v55\n\t" \
    "v_mfma_f32_16x16x32_bf16 v[76:79], v[72:75], v[108:111], v[76:79]\n\t" \
    "v_exp_f32_e32 v56, v56\n\t" \
    "v_cvt_pk_bf16_f32 v106, v52, v53\n\t" \
    "v_cvt_pk_bf16_f32 v107, v54, v55\n\t" \
    "v_exp_f32_e32 v57, v57\n\t" \
    "v_exp_f32_e32 v58, v58\n\t" \
    "v_mfma_f32_32x32x16_bf16 v[0:15], v[96:99], v[104:107], v[0:15]\n\t" \
    "v_exp_f32_e32 v59, v59\n\t" \
    "v_exp_f32_e32 v60, v60\n\t" \
    "v_exp_f32_e32 v61, v61\n\t" \
    "v_mfma_f32_16x16x32_bf16 v[76:79], v[72:75], v[104:107], v[76:79]\n\t" \
    "v_exp_f32_e32 v62, v62\n\t" \
    "v_exp_f32_e32 v63, v63\n\t" \
    "v_cvt_pk_bf16_f32 v108, v56, v57\n\t" \
    "v_cvt_pk_bf16_f32 v109, v58, v59\n\t" \
    "v_cvt_pk_bf16_f32 v110, v60, v61\n\t" \
    "v_cvt_pk_bf16_f32 v111, v62, v63\n\t" \
    "s_waitcnt lgkmcnt(0)\n\t" \
    "v_mfma_f32_32x32x16_bf16 v[0:15], v[92:95], v[108:111], v[0:15]\n\t" \
    "v_mfma_f32_16x16x32_bf16 v[76:79], v[72:75], v[108:111], v[76:79]\n\t" \
    "s_nop 15\n\t"
#define ATT_MAIN \
    "s_mov_b64 s[48:49], %[tab]\n\t" \
    "s_mov_b32 s46, %[k0]\n\t" \
    "s_and_b32 s40, s48, 63\n\t" \
    "s_lshl_b32 s40, s40, 12\n\t" \
    "s_add_u32 s44, %[srclo], s40\n\t" \
    "s_addc_u32 s45, %[srchi], 0\n\t" \
    "s_lshr_b64 s[48:49], s[48:49], 6\n\t" \
    "s_add_u32 s46, s46, 1\n\t" \
    "s_mov_b64 vcc, -1\n\t" \
    "s_waitcnt vmcnt(0)\n\t" \
    "ds_read_b64 v[120:121], v173\n\t" \
    "s_waitcnt lgkmcnt(0)\n\t" \
    "1:\n" \
    "s_cmp_eq_u32 %[cnt], 4\n\t" \
    "s_cbranch_scc1 5f\n\t" \
    "s_waitcnt vmcnt(0)\n\t" \
    "5:\n" \
    "s_waitcnt vmcnt(9)\n\t" \
    "s_barrier\n\t" \
    "s_bitcmp1_b32 %[act], %[cnt]\n\t" \
    "s_cbranch_scc1 30f\n\t" \
    "2:\n" \
    "s_setprio 3\n\t" \
    "s_waitcnt lgkmcnt(0)\n\t" \
    "v_mfma_f32_32x32x16_bf16 v[48:63], v[80:83], v[64:67], v[16:31]\n\t" \
    "v_exp_f32_e32 v32, v32\n\t" \
    "v_exp_f32_e32 v33, v33\n\t" \
    "v_exp_f32_e32 v34, v34\n\t" \
    "v_mfma_f32_32x32x16_bf16 v[48:63], v[84:87], v[68:71], v[48:63]\n\t" \
    "ds_read_b128 v[80:83], v113 offset:0\n\t" \
    "ds_read_b128 v[84:87], v113 offset:1024\n\t" \
    "v_exp_f32_e32 v35, v35\n\t" \
    "v_exp_f32_e32 v36, v36\n\t" \
    "v_cvt_pk_bf16_f32 v104, v32, v33\n\t" \
    "v_cvt_pk_bf16_f32 v105, v34, v35\n\t" \
    "v_mfma_f32_32x32x16_bf16 v[0:15], v[92:95], v[108:111], v[0:15]\n\t" \
    "ds_read_b128 v[100:103], v112 offset:27648\n\t" \
    "ds_read_b128 v[96:99], v112 offset:30720\n\t" \
    "v_exp_f32_e32 v37, v37\n\t" \
    "v_exp_f32_e32 v38, v38\n\t" \
    "v_exp_f32_e32 v39, v39\n\t" \
    "v_mfma_f32_16x16x32_bf16 v[76:79], v[72:75], v[108:111], v[76:79]\n\t" \
    "v_exp_f32_e32 v40, v40\n\t" \
    "v_cvt_pk_bf16_f32 v106, v36, v37\n\t" \
    "v_cvt_pk_bf16_f32 v107, v38, v39\n\t" \
    "v_exp_f32_e32 v41, v41\n\t" \
    "v_exp_f32_e32 v42, v42\n\t" \
    "v_mfma_f32_32x32x16_bf16 v[0:15], v[88:91], v[104:107], v[0:15]\n\t" \
    "s_cbranch_vccz 20f\n\t" \
    "v_cmp_eq_u64_e64 s[42:43], v[120:121], v[140:141]\n\t" \
    "s_mov_b32 m0, %[dst]\n\t" \
    "s_nop 3\n\t" \
    "s_bitcmp1_b64 s[42:43], s46\n\t" \
    "s_cbranch_scc0 9f\n\t" \
    "12:\n" \
    "global_load_lds_dwordx4 v114, s[44:45] offset:0 sc1\n\t" \
    "20:\n" \
    "v_exp_f32_e32 v43, v43\n\t" \
    "v_exp_f32_e32 v44, v44\n\t" \
    "v_exp_f32_e32 v45, v45\n\t" \
    "v_mfma_f32_16x16x32_bf16 v[76:79], v[72:75], v[104:107], v[76:79]\n\t" \
    "v_exp_f32_e32 v46, v46\n\t" \
    "v_exp_f32_e32 v47, v47\n\t" \
    "v_cvt_pk_bf16_f32 v108, v40, v41\n\t" \
    "v_cvt_pk_bf16_f32 v109, v42, v43\n\t" \
    "v_cvt_pk_bf16_f32 v110, v44, v45\n\t" \
    "v_cvt_pk_bf16_f32 v111, v46, v47\n\t" \
    "s_waitcnt lgkmcnt(0)\n\t" \
    "v_mfma_f32_32x32x16_bf16 v[32:47], v[80:83], v[64:67], v[16:31]\n\t" \
    "v_exp_f32_e32 v48, v48\n\t" \
    "v_exp_f32_e32 v49, v49\n\t" \
    "v_exp_f32_e32 v50, v50\n\t" \
    "v_mfma_f32_32x32x16_bf16 v[32:47], v[84:87], v[68:71], v[32:47]\n\t" \
    "ds_read_b128 v[80:83], v113 offset:4096\n\t" \
    "ds_read_b128 v[84:87], v113 offset:5120\n\t" \
    "v_exp_f32_e32 v51, v51\n\t" \
    "v_exp_f32_e32 v52, v52\n\t" \
    "v_cvt_pk_bf16_f32 v104, v48, v49\n\t" \
    "v_cvt_pk_bf16_f32 v105, v50, v51\n\t" \
    "v_mfma_f32_32x32x16_bf16 v[0:15], v[100:103], v[108:111], v[0:15]\n\t" \
    "ds_read_b128 v[92:95], v112 offset:31744\n\t" \
    "ds_read_b128 v[88:91], v113 offset:2048\n\t" \
    "v_exp_f32_e32 v53, v53\n\t" \
    "v_exp_f32_e32 v54, v54\n\t" \
    "v_exp_f32_e32 v55, v55\n\t" \
    "v_mfma_f32_16x16x32_bf16 v[76:79], v[72:75], v[108:111], v[76:79]\n\t" \
    "v_exp_f32_e32 v56, v56\n\t" \
    "v_cvt_pk_bf16_f32 v106, v52, v53\n\t" \
    "v_cvt_pk_bf16_f32 v107, v54, v55\n\t" \
    "v_exp_f32_e32 v57, v57\n\t" \
    "v_exp_f32_e32 v58, v58\n\t" \
    "v_mfma_f32_32x32x16_bf16 v[0:15], v[96:99], v[104:107], v[0:15]\n\t" \
    "s_cbranch_vccz 21f\n\t" \
    "global_load_lds_dwordx4 v114, s[44:45] offset:1024 sc1\n\t" \
    "21:\n" \
    "v_exp_f32_e32 v59, v59\n\t" \
    "v_exp_f32_e32 v60, v60\n\t" \
    "v_exp_f32_e32 v61, v61\n\t" \
    "v_mfma_f32_16x16x32_bf16 v[76:79], v[72:75], v[104:107], v[76:79]\n\t" \
    "v_exp_f32_e32 v62, v62\n\t" \
    "v_exp_f32_e32 v63, v63\n\t" \
    "v_cvt_pk_bf16_f32 v108, v56, v57\n\t" \
    "v_cvt_pk_bf16_f32 v109, v58, v59\n\t" \
    "v_cvt_pk_bf16_f32 v110, v60, v61\n\t" \
    "v_cvt_pk_bf16_f32 v111, v62, v63\n\t" \
    "s_setprio 2\n\t" \
    "s_waitcnt lgkmcnt(0)\n\t" \
    "v_mfma_f32_32x32x16_bf16 v[48:63], v[80:83], v[64:67], v[16:31]\n\t" \
    "v_exp_f32_e32 v32, v32\n\t" \
    "v_exp_f32_e32 v33, v33\n\t" \
    "v_exp_f32_e32 v34, v34\n\t" \
    "v_mfma_f32_32x32x16_bf16 v[48:63], v[84:87], v[68:71], v[48:63]\n\t" \
    "ds_read_b128 v[80:83], v113 offset:8192\n\t" \
    "ds_read_b128 v[84:87], v113 offset:9216\n\t" \
    "v_exp_f32_e32 v35, v35\n\t" \
    "v_exp_f32_e32 v36, v36\n\t" \
    "v_cvt_pk_bf16_f32 v104, v32, v33\n\t" \
    "v_cvt_pk_bf16_f32 v105, v34, v35\n\t" \
    "v_mfma_f32_32x32x16_bf16 v[0:15], v[92:95], v[108:111], v[0:15]\n\t" \
    "ds_read_b128 v[100:103], v113 offset:3072\n\t" \
    "ds_read_b128 v[96:99], v113 offset:6144\n\t" \
    "v_exp_f32_e32 v37, v37\n\t" \
    "v_exp_f32_e32 v38, v38\n\t" \
    "v_exp_f32_e32 v39, v39\n\t" \
    "v_mfma_f32_16x16x32_bf16 v[76:79], v[72:75], v[108:111], v[76:79]\n\t" \
    "v_exp_f32_e32 v40, v40\n\t" \
    "v_cvt_pk_bf16_f32 v106, v36, v37\n\t" \
    "v_cvt_pk_bf16_f32 v107, v38, v39\n\t" \
    "v_exp_f32_e32 v41, v41\n\t" \
    "v_exp_f32_e32 v42, v42\n\t" \
    "v_mfma_f32_32x32x16_bf16 v[0:15], v[88:91], v[104:107], v[0:15]\n\t" \
    "s_cbranch_vccz 22f\n\t" \
    "global_load_lds_dwordx4 v114, s[44:45] offset:2048 sc1\n\t" \
    "22:\n" \
    "v_exp_f32_e32 v43, v43\n\t" \
    "v_exp_f32_e32 v44, v44\n\t" \
    "v_exp_f32_e32 v45, v45\n\t" \
    "v_mfma_f32_16x16x32_bf16 v[76:79], v[72:75], v[104:107], v[76:79]\n\t" \
    "v_exp_f32_e32 v46, v46\n\t" \
    "v_exp_f32_e32 v47, v47\n\t" \
    "v_cvt_pk_bf16_f32 v108, v40, v41\n\t" \
    "v_cvt_pk_bf16_f32 v109, v42, v43\n\t" \
    "v_cvt_pk_bf16_f32 v110, v44, v45\n\t" \
    "v_cvt_pk_bf16_f32 v111, v46, v47\n\t" \
    "s_waitcnt lgkmcnt(0)\n\t" \
    "v_mfma_f32_32x32x16_bf16 v[32:47], v[80:83], v[64:67], v[16:31]\n\t" \
    "v_exp_f32_e32 v48, v48\n\t" \
    "v_exp_f32_e32 v49, v49\n\t" \
    "v_exp_f32_e32 v50, v50\n\t" \
    "v_mfma_f32_32x32x16_bf16 v[32:47], v[84:87], v[68:71], v[32:47]\n\t" \
    "ds_read_b128 v[80:83], v113 offset:12288\n\t" \
    "ds_read_b128 v[84:87], v113 offset:13312\n\t" \
    "v_exp_f32_e32 v51, v51\n\t" \
    "v_exp_f32_e32 v52, v52\n\t" \
    "v_cvt_pk_bf16_f32 v104, v48, v49\n\t" \
    "v_cvt_pk_bf16_f32 v105, v50, v51\n\t" \
    "v_mfma_f32_32x32x16_bf16 v[0:15], v[100:103], v[108:111], v[0:15]\n\t" \
    "ds_read_b128 v[92:95], v113 offset:7168\n\t" \
    "ds_read_b128 v[88:91], v113 offset:10240\n\t" \
    "v_exp_f32_e32 v53, v53\n\t" \
    "v_exp_f32_e32 v54, v54\n\t" \
    "v_exp_f32_e32 v55, v55\n\t" \
    "v_mfma_f32_16x16x32_bf16 v[76:79], v[72:75], v[108:111], v[76:79]\n\t" \
    "v_exp_f32_e32 v56, v56\n\t" \
    "v_cvt_pk_bf16_f32 v106, v52, v53\n\t" \
    "v_cvt_pk_bf16_f32 v107, v54, v55\n\t" \
    "v_exp_f32_e32 v57, v57\n\t" \
    "v_exp_f32_e32 v58, v58\n\t" \
    "v_mfma_f32_32x32x16_bf16 v[0:15], v[96:99], v[104:107], v[0:15]\n\t" \
    "s_cbranch_vccz 23f\n\t" \
    "global_load_lds_dwordx4 v114, s[44:45] offset:3072 sc1\n\t" \
    "global_load_dwordx2 v[120:121], v170, %[ws] sc1\n\t" \
    "23:\n" \
    "v_exp_f32_e32 v59, v59\n\t" \
    "v_exp_f32_e32 v60, v60\n\t" \
    "v_exp_f32_e32 v61, v61\n\t" \
    "v_mfma_f32_16x16x32_bf16 v[76:79], v[72:75], v[104:107], v[76:79]\n\t" \
    "v_exp_f32_e32 v62, v62\n\t" \
    "v_exp_f32_e32 v63, v63\n\t" \
    "v_cvt_pk_bf16_f32 v108, v56, v57\n\t" \
    "v_cvt_pk_bf16_f32 v109, v58, v59\n\t" \
    "v_cvt_pk_bf16_f32 v110, v60, v61\n\t" \
    "v_cvt_pk_bf16_f32 v111, v62, v63\n\t" \
    "s_setprio 1\n\t" \
    "s_waitcnt lgkmcnt(0)\n\t" \
    "v_mfma_f32_32x32x16_bf16 v[48:63], v[80:83], v[64:67], v[16:31]\n\t" \
    "v_exp_f32_e32 v32, v32\n\t" \
    "v_exp_f32_e32 v33, v33\n\t" \
    "v_exp_f32_e32 v34, v34\n\t" \
    "v_mfma_f32_32x32x16_bf16 v[48:63], v[84:87], v[68:71], v[48:63]\n\t" \
    "ds_read_b128 v[80:83], v113 offset:16384\n\t" \
    "ds_read_b128 v[84:87], v113 offset:17408\n\t" \
    "v_exp_f32_e32 v35, v35\n\t" \
    "v_exp_f32_e32 v36, v36\n\t" \
    "v_cvt_pk_bf16_f32 v104, v32, v33\n\t" \
    "v_cvt_pk_bf16_f32 v105, v34, v35\n\t" \
    "v_mfma_f32_32x32x16_bf16 v[0:15], v[92:95], v[108:111], v[0:15]\n\t" \
    "ds_read_b128 v[100:103], v113 offset:11264\n\t" \
    "ds_read_b128 v[96:99], v113 offset:14336\n\t" \
    "v_exp_f32_e32 v37, v37\n\t" \
    "v_exp_f32_e32 v38, v38\n\t" \
    "v_exp_f32_e32 v39, v39\n\t" \
    "v_mfma_f32_16x16x32_bf16 v[76:79], v[72:75], v[108:111], v[76:79]\n\t" \
    "v_exp_f32_e32 v40, v40\n\t" \
    "v_cvt_pk_bf16_f32 v106, v36, v37\n\t" \
    "v_cvt_pk_bf16_f32 v107, v38, v39\n\t" \
    "v_exp_f32_e32 v41, v41\n\t" \
    "v_exp_f32_e32 v42, v42\n\t" \
    "v_mfma_f32_32x32x16_bf16 v[0:15], v[88:91], v[104:107], v[0:15]\n\t" \
    "s_add_u32 %[dst], %[dst], 0x8000\n\t" \
    "s_cmp_eq_u32 %[dst], %[dend]\n\t" \
    "s_cselect_b32 %[dst], %[dbeg], %[dst]\n\t" \
    "s_add_u32 %[nx], %[nx], 0x8000\n\t" \
    "s_cmp_eq_u32 %[nx], 0x18000\n\t" \
    "s_cselect_b32 %[nx], 0, %[nx]\n\t" \
    "s_cmp_eq_u32 %[cnt], 5\n\t" \
    "s_cbranch_scc1 40f\n\t" \
    "24:\n" \
    "v_exp_f32_e32 v43, v43\n\t" \
    "v_exp_f32_e32 v44, v44\n\t" \
    "v_exp_f32_e32 v45, v45\n\t" \
    "v_mfma_f32_16x16x32_bf16 v[76:79], v[72:75], v[104:107], v[76:79]\n\t" \
    "v_exp_f32_e32 v46, v46\n\t" \
    "v_exp_f32_e32 v47, v47\n\t" \
    "v_cvt_pk_bf16_f32 v108, v40, v41\n\t" \
    "v_cvt_pk_bf16_f32 v109, v42, v43\n\t" \
    "v_cvt_pk_bf16_f32 v110, v44, v45\n\t" \
    "v_cvt_pk_bf16_f32 v111, v46, v47\n\t" \
    "s_waitcnt lgkmcnt(0)\n\t" \
    "v_mfma_f32_32x32x16_bf16 v[32:47], v[80:83], v[64:67], v[16:31]\n\t" \
    "v_exp_f32_e32 v48, v48\n\t" \
    "v_exp_f32_e32 v49, v49\n\t" \
    "v_exp_f32_e32 v50, v50\n\t" \
    "v_mfma_f32_32x32x16_bf16 v[32:47], v[84:87], v[68:71], v[32:47]\n\t" \
    "ds_read_b128 v[80:83], v113 offset:20480\n\t" \
    "ds_read_b128 v[84:87], v113 offset:21504\n\t" \
    "v_exp_f32_e32 v51, v51\n\t" \
    "v_exp_f32_e32 v52, v52\n\t" \
    "v_cvt_pk_bf16_f32 v104, v48, v49\n\t" \
    "v_cvt_pk_bf16_f32 v105, v50, v51\n\t" \
    "v_mfma_f32_32x32x16_bf16 v[0:15], v[100:103], v[108:111], v[0:15]\n\t" \
    "ds_read_b128 v[92:95], v113 offset:15360\n\t" \
    "ds_read_b128 v[88:91], v113 offset:18432\n\t" \
    "v_exp_f32_e32 v53, v53\n\t" \
    "v_exp_f32_e32 v54, v54\n\t" \
    "v_exp_f32_e32 v55, v55\n\t" \
    "v_mfma_f32_16x16x32_bf16 v[76:79], v[72:75], v[108:111], v[76:79]\n\t" \
    "v_exp_f32_e32 v56, v56\n\t" \
    "v_cvt_pk_bf16_f32 v106, v52, v53\n\t" \
    "v_cvt_pk_bf16_f32 v107, v54, v55\n\t" \
    "v_exp_f32_e32 v57, v57\n\t" \
    "v_exp_f32_e32 v58, v58\n\t" \
    "v_mfma_f32_32x32x16_bf16 v[0:15], v[96:99], v[104:107], v[0:15]\n\t" \
    "s_cmp_eq_u32 %[cnt], 5\n\t" \
    "s_cbranch_scc1 41f\n\t" \
    "25:\n" \
    "v_exp_f32_e32 v59, v59\n\t" \
    "v_exp_f32_e32 v60, v60\n\t" \
    "v_exp_f32_e32 v61, v61\n\t" \
    "v_mfma_f32_16x16x32_bf16 v[76:79], v[72:75], v[104:107], v[76:79]\n\t" \
    "v_exp_f32_e32 v62, v62\n\t" \
    "v_exp_f32_e32 v63, v63\n\t" \
    "v_cvt_pk_bf16_f32 v108, v56, v57\n\t" \
    "v_cvt_pk_bf16_f32 v109, v58, v59\n\t" \
    "v_cvt_pk_bf16_f32 v110, v60, v61\n\t" \
    "v_cvt_pk_bf16_f32 v111, v62, v63\n\t" \
    "s_setprio 0\n\t" \
    "s_waitcnt lgkmcnt(0)\n\t" \
    "v_mfma_f32_32x32x16_bf16 v[48:63], v[80:83], v[64:67], v[16:31]\n\t" \
    "v_exp_f32_e32 v32, v32\n\t" \
    "v_exp_f32_e32 v33, v33\n\t" \
    "v_exp_f32_e32 v34, v34\n\t" \
    "v_mfma_f32_32x32x16_bf16 v[48:63], v[84:87], v[68:71], v[48:63]\n\t" \
    "ds_read_b128 v[80:83], v113 offset:24576\n\t" \
    "ds_read_b128 v[84:87], v113 offset:25600\n\t" \
    "v_exp_f32_e32 v35, v35\n\t" \
    "v_exp_f32_e32 v36, v36\n\t" \
    "v_cvt_pk_bf16_f32 v104, v32, v33\n\t" \
    "v_cvt_pk_bf16_f32 v105, v34, v35\n\t" \
    "v_mfma_f32_32x32x16_bf16 v[0:15], v[92:95], v[108:111], v[0:15]\n\t" \
    "ds_read_b128 v[100:103], v113 offset:19456\n\t" \
    "ds_read_b128 v[96:99], v113 offset:22528\n\t" \
    "v_exp_f32_e32 v37, v37\n\t" \
    "v_exp_f32_e32 v38, v38\n\t" \
    "v_exp_f32_e32 v39, v39\n\t" \
    "v_mfma_f32_16x16x32_bf16 v[76:79], v[72:75], v[108:111], v[76:79]\n\t" \
    "v_exp_f32_e32 v40, v40\n\t" \
    "v_cvt_pk_bf16_f32 v106, v36, v37\n\t" \
    "v_cvt_pk_bf16_f32 v107, v38, v39\n\t" \
    "v_exp_f32_e32 v41, v41\n\t" \
    "v_exp_f32_e32 v42, v42\n\t" \
    "v_mfma_f32_32x32x16_bf16 v[0:15], v[88:91], v[104:107], v[0:15]\n\t" \
    "s_and_b32 s40, s48, 63\n\t" \
    "s_lshl_b32 s40, s40, 12\n\t" \
    "s_add_u32 s44, %[srclo], s40\n\t" \
    "s_addc_u32 s45, %[srchi], 0\n\t" \
    "s_lshr_b64 s[48:49], s[48:49], 6\n\t" \
    "s_add_u32 s46, s46, 1\n\t" \
    "s_cmp_eq_u32 %[cnt], 5\n\t" \
    "s_cbranch_scc1 42f\n\t" \
    "26:\n" \
    "v_exp_f32_e32 v43, v43\n\t" \
    "v_exp_f32_e32 v44, v44\n\t" \
    "v_exp_f32_e32 v45, v45\n\t" \
    "v_mfma_f32_16x16x32_bf16 v[76:79], v[72:75], v[104:107], v[76:79]\n\t" \
    "v_exp_f32_e32 v46, v46\n\t" \
    "v_exp_f32_e32 v47, v47\n\t" \
    "v_cvt_pk_bf16_f32 v108, v40, v41\n\t" \
    "v_cvt_pk_bf16_f32 v109, v42, v43\n\t" \
    "v_cvt_pk_bf16_f32 v110, v44, v45\n\t" \
    "v_cvt_pk_bf16_f32 v111, v46, v47\n\t" \
    "s_waitcnt lgkmcnt(0)\n\t" \
    "v_mfma_f32_32x32x16_bf16 v[32:47], v[80:83], v[64:67], v[16:31]\n\t" \
    "v_exp_f32_e32 v48, v48\n\t" \
    "v_exp_f32_e32 v49, v49\n\t" \
    "v_exp_f32_e32 v50, v50\n\t" \
    "v_mfma_f32_32x32x16_bf16 v[32:47], v[84:87], v[68:71], v[32:47]\n\t" \
    "ds_read_b128 v[80:83], v113 offset:28672\n\t" \
    "ds_read_b128 v[84:87], v113 offset:29696\n\t" \
    "v_exp_f32_e32 v51, v51\n\t" \
    "v_exp_f32_e32 v52, v52\n\t" \
    "v_cvt_pk_bf16_f32 v104, v48, v49\n\t" \
    "v_cvt_pk_bf16_f32 v105, v50, v51\n\t" \
    "v_mfma_f32_32x32x16_bf16 v[0:15], v[100:103], v[108:111], v[0:15]\n\t" \
    "ds_read_b128 v[92:95], v113 offset:23552\n\t" \
    "ds_read_b128 v[88:91], v113 offset:26624\n\t" \
    "v_exp_f32_e32 v53, v53\n\t" \
    "v_exp_f32_e32 v54, v54\n\t" \
    "v_exp_f32_e32 v55, v55\n\t" \
    "v_mfma_f32_16x16x32_bf16 v[76:79], v[72:75], v[108:111], v[76:79]\n\t" \
    "v_exp_f32_e32 v56, v56\n\t" \
    "v_cvt_pk_bf16_f32 v106, v52, v53\n\t" \
    "v_cvt_pk_bf16_f32 v107, v54, v55\n\t" \
    "v_exp_f32_e32 v57, v57\n\t" \
    "v_exp_f32_e32 v58, v58\n\t" \
    "v_mfma_f32_32x32x16_bf16 v[0:15], v[96:99], v[104:107], v[0:15]\n\t" \
    "s_cmp_eq_u32 %[cnt], 5\n\t" \
    "s_cbranch_scc1 43f\n\t" \
    "27:\n" \
    "v_mov_b32_e32 v112, v113\n\t" \
    "v_add_u32_e32 v113, %[nx], v115\n\t" \
    "s_sub_u32 %[cnt], %[cnt], 1\n\t" \
    "s_cmp_lg_u32 %[cnt], 0\n\t" \
    "v_exp_f32_e32 v59, v59\n\t" \
    "v_exp_f32_e32 v60, v60\n\t" \
    "v_exp_f32_e32 v61, v61\n\t" \
    "v_mfma_f32_16x16x32_bf16 v[76:79], v[72:75], v[104:107], v[76:79]\n\t" \
    "v_exp_f32_e32 v62, v62\n\t" \
    "v_exp_f32_e32 v63, v63\n\t" \
    "v_cvt_pk_bf16_f32 v108, v56, v57\n\t" \
    "v_cvt_pk_bf16_f32 v109, v58, v59\n\t" \
    "v_cvt_pk_bf16_f32 v110, v60, v61\n\t" \
    "v_cvt_pk_bf16_f32 v111, v62, v63\n\t" \
    "s_cbranch_scc1 1b\n\t" \
    "7:\n" \
    "s_setprio 0\n\t" \
    "s_waitcnt lgkmcnt(0)\n\t" \
    "v_mfma_f32_32x32x16_bf16 v[48:63], v[80:83], v[64:67], v[16:31]\n\t" \
    "v_exp_f32_e32 v32, v32\n\t" \
    "v_exp_f32_e32 v33, v33\n\t" \
    "v_exp_f32_e32 v34, v34\n\t" \
    "v_mfma_f32_32x32x16_bf16 v[48:63], v[84:87], v[68:71], v[48:63]\n\t" \
    "v_exp_f32_e32 v35, v35\n\t" \
    "v_exp_f32_e32 v36, v36\n\t" \
    "v_cvt_pk_bf16_f32 v104, v32, v33\n\t" \
    "v_cvt_pk_bf16_f32 v105, v34, v35\n\t" \
    "v_mfma_f32_32x32x16_bf16 v[0:15], v[92:95], v[108:111], v[0:15]\n\t" \
    "ds_read_b128 v[100:103], v112 offset:27648\n\t" \
    "ds_read_b128 v[96:99], v112 offset:30720\n\t" \
    "v_exp_f32_e32 v37, v37\n\t" \
    "v_exp_f32_e32 v38, v38\n\t" \
    "v_exp_f32_e32 v39, v39\n\t" \
    "v_mfma_f32_16x16x32_bf16 v[76:79], v[72:75], v[108:111], v[76:79]\n\t" \
    "v_exp_f32_e32 v40, v40\n\t" \
    "v_cvt_pk_bf16_f32 v106, v36, v37\n\t" \
    "v_cvt_pk_bf16_f32 v107, v38, v39\n\t" \
    "v_exp_f32_e32 v41, v41\n\t" \
    "v_exp_f32_e32 v42, v42\n\t" \
    "v_mfma_f32_32x32x16_bf16 v[0:15], v[88:91], v[104:107], v[0:15]\n\t" \
    "v_exp_f32_e32 v43, v43\n\t" \
    "v_exp_f32_e32 v44, v44\n\t" \
    "v_exp_f32_e32 v45, v45\n\t" \
    "v_mfma_f32_16x16x32_bf16 v[76:79], v[72:75], v[104:107], v[76:79]\n\t" \
    "v_exp_f32_e32 v46, v46\n\t" \
    "v_exp_f32_e32 v47, v47\n\t" \
    "v_cvt_pk_bf16_f32 v108, v40, v41\n\t" \
    "v_cvt_pk_bf16_f32 v109, v42, v43\n\t" \
    "v_cvt_pk_bf16_f32 v110, v44, v45\n\t" \
    "v_cvt_pk_bf16_f32 v111, v46, v47\n\t" \
    "s_waitcnt lgkmcnt(0)\n\t" \
    "v_exp_f32_e32 v48, v48\n\t" \
    "v_exp_f32_e32 v49, v49\n\t" \
    "v_exp_f32_e32 v50, v50\n\t" \
    "v_exp_f32_e32 v51, v51\n\t" \
    "v_exp_f32_e32 v52, v52\n\t" \
    "v_cvt_pk_bf16_f32 v104, v48, v49\n\t" \
    "v_cvt_pk_bf16_f32 v105, v50, v51\n\t" \
    "v_mfma_f32_32x32x16_bf16 v[0:15], v[100:103], v[108:111], v[0:15]\n\t" \
    "ds_read_b128 v[92:95], v112 offset:31744\n\t" \
    "v_exp_f32_e32 v53, v53\n\t" \
    "v_exp_f32_e32 v54, v54\n\t" \
    "v_exp_f32_e32 v55, v55\n\t" \
    "v_mfma_f32_16x16x32_bf16 v[76:79], v[72:75], v[108:111], v[76:79]\n\t" \
    "v_exp_f32_e32 v56, v56\n\t" \
    "v_cvt_pk_bf16_f32 v106, v52, v53\n\t" \
    "v_cvt_pk_bf16_f32 v107, v54, v55\n\t" \
    "v_exp_f32_e32 v57, v57\n\t" \
    "v_exp_f32_e32 v58, v58\n\t" \
    "v_mfma_f32_32x32x16_bf16 v[0:15], v[96:99], v[104:107], v[0:15]\n\t" \
    "v_exp_f32_e32 v59, v59\n\t" \
    "v_exp_f32_e32 v60, v60\n\t" \
    "v_exp_f32_e32 v61, v61\n\t" \
    "v_mfma_f32_16x16x32_bf16 v[76:79], v[72:75], v[104:107], v[76:79]\n\t" \
    "v_exp_f32_e32 v62, v62\n\t" \
    "v_exp_f32_e32 v63, v63\n\t" \
    "v_cvt_pk_bf16_f32 v108, v56, v57\n\t" \
    "v_cvt_pk_bf16_f32 v109, v58, v59\n\t" \
    "v_cvt_pk_bf16_f32 v110, v60, v61\n\t" \
    "v_cvt_pk_bf16_f32 v111, v62, v63\n\t" \
    "s_waitcnt lgkmcnt(0)\n\t" \
    "v_mfma_f32_32x32x16_bf16 v[0:15], v[92:95], v[108:111], v[0:15]\n\t" \
    "v_mfma_f32_16x16x32_bf16 v[76:79], v[72:75], v[108:111], v[76:79]\n\t" \
    "s_nop 15\n\t" \
    "s_branch 99f\n\t" \
    "9:\n" \
    "s_mov_b32 s47, 0x40000\n\t" \
    "10:\n" \
    "s_sub_u32 s47, s47, 1\n\t" \
    "s_cbranch_scc1 12b\n\t" \
    "global_load_dwordx2 v[120:121], v170, %[ws] sc1\n\t" \
    "s_waitcnt vmcnt(0)\n\t" \
    "v_cmp_eq_u64_e64 s[42:43], v[120:121], v[140:141]\n\t" \
    "s_nop 4\n\t" \
    "s_bitcmp1_b64 s[42:43], s46\n\t" \
    "s_cbranch_scc1 12b\n\t" \
    "s_sleep 4\n\t" \
    "s_branch 10b\n\t" \
    "40:\n" \
    "s_add_u32 m0, %[wdst], 0\n\t" \
    "s_nop 0\n\t" \
    "global_load_lds_dwordx4 v116, %[wo01]\n\t" \
    "global_load_lds_dwordx4 v117, %[wo01] offset:1024\n\t" \
    "v_add_u32_e32 v116, 0x20000, v116\n\t" \
    "v_add_u32_e32 v117, 0x20000, v117\n\t" \
    "s_branch 24b\n\t" \
    "41:\n" \
    "s_add_u32 m0, %[wdst], 16384\n\t" \
    "s_nop 0\n\t" \
    "global_load_lds_dwordx4 v116, %[wo01]\n\t" \
    "global_load_lds_dwordx4 v117, %[wo01] offset:1024\n\t" \
    "v_subrev_u32_e32 v116, 0x20000, v116\n\t" \
    "v_subrev_u32_e32 v117, 0x20000, v117\n\t" \
    "s_branch 25b\n\t" \
    "42:\n" \
    "s_add_u32 m0, %[wdst], 32768\n\t" \
    "s_nop 0\n\t" \
    "global_load_lds_dwordx4 v116, %[wo23]\n\t" \
    "global_load_lds_dwordx4 v117, %[wo23] offset:1024\n\t" \
    "v_add_u32_e32 v116, 0x20000, v116\n\t" \
    "v_add_u32_e32 v117, 0x20000, v117\n\t" \
    "s_branch 26b\n\t" \
    "43:\n" \
    "s_add_u32 m0, %[wdst], 49152\n\t" \
    "s_nop 0\n\t" \
    "global_load_lds_dwordx4 v116, %[wo23]\n\t" \
    "global_load_lds_dwordx4 v117, %[wo23] offset:1024\n\t" \
    "global_load_dword v168, v119, %[wo]\n\t" \
    "s_branch 27b\n\t" \
    "30:\n" \
    "s_cmp_eq_u32 %[cnt], 1\n\t" \
    "s_cbranch_scc0 31f\n\t" \
    "s_mov_b64 vcc, 0\n\t" \
    "s_bitcmp1_b32 %[mode], 1\n\t" \
    "s_cbranch_scc1 7b\n\t" \
    "s_branch 2b\n\t" \
    "31:\n" \
    "33:\n" \
    "s_cmp_eq_u32 %[cnt], 5\n\t" \
    "s_cbranch_scc0 34f\n\t" \
    "global_store_dwordx2 v118, v[140:141], %[ws] sc1\n\t" \
    "s_branch 2b\n\t" \
    "34:\n" \
    "s_cmp_eq_u32 %[cnt], 3\n\t" \
    "s_cbranch_scc0 35f\n\t" \
    "global_load_dwordx2 v[168:169], v142, %[ws] sc1\n\t" \
    "s_branch 2b\n\t" \
    "35:\n" \
    "v_cmp_eq_u64_e64 s[42:43], v[168:169], v[140:141]\n\t" \
    "s_nop 4\n\t" \
    "s_cmp_eq_u64 s[42:43], -1\n\t" \
    "s_cbranch_scc0 2b\n\t" \
    "global_load_dwordx4 v[122:125], v143, %[ws] offset:-2048 sc1\n\t" \
    "global_load_dwordx4 v[126:129], v143, %[ws] offset:-1024 sc1\n\t" \
    "global_load_dwordx4 v[130:133], v143, %[ws] offset:0 sc1\n\t" \
    "global_load_dwordx4 v[134:137], v143, %[ws] offset:1024 sc1\n\t" \
    "global_load_dwordx2 v[138:139], v143, %[ws] offset:2048 sc1\n\t" \
    "s_bitset1_b32 %[mode], 2\n\t" \
    "s_branch 2b\n\t" \
    "99:\n"
#define ATT_TMP "v48", "v49", "v50", "v51", "v52", "v53", "v54", "v55", "v56", "v57", "v58", "v59", "v60", "v61", "v62", "v63", "v96", "v97", "v98", "v99", "v100", "v101", "v102", "v103", "v104", "v105", "v106", "v107"

constexpr int K1_X = 0, K1_W = 65536, K1_WB = 24576;
constexpr int K2_SLOT = 32768, K2_QX = 3 * K2_SLOT, K2_FL = 147456;
constexpr int LDS_BYTES = 163840;
constexpr int p3_wslot(int t) { return (t % 6) < 4 ? 98304 + (t % 6) * 16384 : 65536 + ((t % 6) - 4) * 16384; }
constexpr int p3_allow(int t) { return ((t - 2 >= 1 && t + 3 < 16) ? 4 : 0) + (t + 3 < 16 ? 4 : 0) + ((t >= 1 && t + 5 < 16) ? 4 : 0); }

__global__ __launch_bounds__(512, 2) void mk_fused(const u16* __restrict__ xA, const u16* __restrict__ wA, const u16* __restrict__ woA,
                                                   const u16* __restrict__ xB, const u16* __restrict__ wB, const u16* __restrict__ woB, char* ws0, u16* __restrict__ out) {
    char* ws = ws0;
    __shared__ __attribute__((aligned(1024))) char smem[LDS_BYTES];
    const unsigned lds0 = (unsigned)(uintptr_t)(LAS char*)smem;
    const int wid = __builtin_amdgcn_readfirstlane(threadIdx.x >> 6);
    int lane = threadIdx.x & 63;
    const int b = blockIdx.x, xcd = b & 7, j = b >> 3, br = xcd >> 2, qd = xcd & 3;
    const int mt = (qd & 1) * 4 + (j & 3), hp = (qd >> 1) * 8 + (j >> 2);
    const int mt3 = qd * 4 + (j & 3), nt3 = j >> 2;
    u64 tag;
    u32x4 fq[2][2], fk[2][2], fv[2][2];
    STAMP_AT(0, 0);
    {
        const int g = wid >> 2, wi = wid & 3, mw = wi >> 1, hd = wi & 1;
        const char* x = (const char*)(br ? xB : xA) + (size_t)mt * 256 * 2048;
        const char* w = (const char*)(br ? wB : wA) + hp * 128;
        unsigned vx[4], vw[3];
#pragma unroll
        for (int ii = 0; ii < 4; ++ii) {
            const int row = br ? ((wi >> 1) * 64 + (wi & 1) + ii * 16 + 2 * (lane >> 3)) : (wi * 32 + ii * 8 + (lane >> 3));
            vx[ii] = row * 2048 + (((lane & 7) ^ (((lane >> 4) & 1) | ((ii >> 1) << 1) | ((lane >> 5) << 2))) << 4);
        }
#pragma unroll
        for (int jj = 0; jj < 3; ++jj) {
            const int jw = 12 * g + 3 * wi + jj, n = jw >> 3, k0 = (jw & 7) * 8 + (lane >> 3);
            vw[jj] = k0 * 6144 + n * 2048 + (((lane & 7) ^ ((((lane >> 4) & 1) | ((jw & 1) << 1)) << 1)) << 4);
        }
        const unsigned xdst_o = lds0 + K1_X + (1 - g) * 32768 + wi * 4096;
        const unsigned wdst = lds0 + K1_W + (12 * g + 3 * wi) * 1024;
        const char* xo = x + (size_t)(1 - g) * 128 * 2048;
        const int kt0 = 4 * (j & 3);
#define STAGE_X(kt, dst) do { const char* s_ = xo + (((kt) + kt0) & 15) * 128; _Pragma("unroll") for (int ii = 0; ii < 4; ++ii) glds16(s_, vx[ii], (dst) + ii * 1024); } while (0)
#define STAGE_W(kt, buf) do { const char* s_ = w + (size_t)(((kt) + kt0) & 15) * 64 * 6144; _Pragma("unroll") for (int jj = 0; jj < 3; ++jj) glds16(s_, vw[jj], wdst + (buf) * K1_WB + jj * 1024); } while (0)
        if (wid == 7) {
            const char* pc; unsigned len;
            asm volatile("s_getpc_b64 %0\n.Lmk_code_begin:\n\ts_mov_b32 %1, .Lmk_code_end-.Lmk_code_begin" : "=s"(pc), "=s"(len));
            const unsigned off = (unsigned)(j * 64 + lane) * 64u;
            asm volatile("s_mov_b32 m0, %2\n\ts_nop 0\n\tglobal_load_lds_dword %0, %1" :: "v"(off < len ? off : 0u), "s"(pc), "s"(lds0 + 159744) : "memory");
        }
        STAGE_X(0, xdst_o); STAGE_W(0, 0); STAGE_W(1, 1);
        if (g == 1) STAGE_X(1, xdst_o + 16384);
        { u64 e; asm volatile("s_load_dwordx2 %0, %1, 0x0\n\ts_waitcnt lgkmcnt(0)" : "=s"(e) : "s"(ws + WS_EP) : "memory");
          e += 0x9E3779B97F4A7C15ull; e = (e ^ (e >> 30)) * 0xBF58476D1CE4E5B9ull; e = (e ^ (e >> 27)) * 0x94D049BB133111EBull; tag = e ^ (e >> 31); }
        const int c16 = lane & 15, g4 = lane >> 4;
        unsigned ax[2], aw[2];
        ax[0] = lds0 + K1_X + g * 32768 + (mw * 64 + 16 * (c16 >> 3) + (c16 & 7)) * 128 + ((g4 ^ (((c16 >> 1) & 1) | ((c16 >> 3) << 1) | (((c16 >> 2) & 1) << 2))) << 4);
        ax[1] = ax[0] ^ 64;
        { const int q4 = c16 >> 2, p = lane & 3;
          aw[0] = lds0 + K1_W + (8 * g4 + q4) * 128 + (((hd * 4 + (p >> 1)) ^ ((((q4 >> 1) & 1) | ((g4 & 1) << 1)) << 1)) << 4) + (p & 1) * 8; aw[1] = aw[0] ^ 32; }
        if (g == 1) WAITBAR_(7); else WAITBAR_(3);
        SA_(1);
        if (g == 1) asm volatile("s_barrier" ::: "memory");
        f32x4 acc[4][6];
#pragma unroll
        for (int m = 0; m < 4; ++m)
#pragma unroll
            for (int n = 0; n < 6; ++n) acc[m][n] = f32x4{};
        SBAR();
#pragma unroll
        for (int t = 0; t < 16; ++t) {
            bf16x8 xf[4][2], wf[6][2];
            const int tx = t + 1 + g;
            const char* sx = xo + ((tx + kt0) & 15) * 128; const char* sw = w + (size_t)((t + 2 + kt0) & 15) * 64 * 6144;
            const unsigned dx = xdst_o + (tx & 1) * 16384, dw = wdst + ((t + 2) % 3) * K1_WB;
            const bool dox = t < 14 || (t == 14 && g == 0);
#define RDW(n, ks) wf[n][ks] = cat8(ldstr(aw[(n) & 1] + (t % 3) * K1_WB + ((n) >> 1) * 8192 + (ks) * 4096), ldstr(aw[(n) & 1] + (t % 3) * K1_WB + ((n) >> 1) * 8192 + (ks) * 4096 + 512))
#pragma unroll
            for (int s = 0; s < 4; ++s) {
                xf[s][0] = ldsrd128(ax[0] + (t & 1) * 16384 + (s >> 1) * 4096 + (s & 1) * 1024); xf[s][1] = ldsrd128(ax[1] + (t & 1) * 16384 + (s >> 1) * 4096 + (s & 1) * 1024);
                RDW((3 * s) >> 1, (3 * s) & 1);
                if (t < 15 && dox) glds16(sx, vx[s], dx + s * 1024);
                RDW((3 * s + 1) >> 1, (3 * s + 1) & 1); RDW((3 * s + 2) >> 1, (3 * s + 2) & 1);
                if (t + 2 < 16 && s < 3) glds16(sw, vw[s], dw + s * 1024);
            }
#undef RDW
            if (t + 2 < 16) WAITBAR_(7); else if (t == 14 && g == 0) WAITBAR_(4); else WAITBAR_(0);
            SBAR();
            __builtin_amdgcn_s_setprio(1);
#pragma unroll
            for (int m = 0; m < 4; ++m)
#pragma unroll
                for (int n = 0; n < 6; ++n) {
                    if (n < 4) { acc[m][n] = MFMA16(wf[n][0], xf[m][0], acc[m][n]); acc[m][n] = MFMA16(wf[n][1], xf[m][1], acc[m][n]); }
                    else       { acc[m][n] = MFMA16(xf[m][0], wf[n][0], acc[m][n]); acc[m][n] = MFMA16(xf[m][1], wf[n][1], acc[m][n]); }
                }
            __builtin_amdgcn_s_setprio(0);
            SBAR();
            if (t < 15 || g == 0) asm volatile("s_barrier" ::: "memory");
            SBAR();
        }
#undef STAGE_X
#undef STAGE_W
        SA_(2); SC_(1);
        FRESH();
#pragma unroll
        for (int mb = 0; mb < 2; ++mb)
#pragma unroll
            for (int u = 0; u < 2; ++u) {
                const f32x4 a = acc[2 * mb + u][0] * QSCALE, c = acc[2 * mb + u][1] * QSCALE;
                fq[mb][u] = u32x4{cvtpk(a[0], a[1]), cvtpk(a[2], a[3]), cvtpk(c[0], c[1]), cvtpk(c[2], c[3])};
                fk[mb][u] = pack44(acc[2 * mb + u][2], acc[2 * mb + u][3]);
                fv[mb][u] = pack44(acc[2 * mb][4 + u], acc[2 * mb + 1][4 + u]);
            }
    }
    const char* wo = (const char*)(br ? woB : woA) + nt3 * 256;
    const int k30 = 2 * nt3;
    {
        const int g = wid >> 2, wr = wid >> 1, hd = wid & 1;
        const int ph1 = 2 * hp + hd, pb1 = mt * 8 + wr * 2;
        const int head = 2 * hp + br, blk = mt * 8 + wid;
#define RBLK(e) ((((e) % 7) + (((e) % 7) >= mt)) * 8 + (e) / 7)
        const char* kv = ws + WS_KV + (size_t)head * 262144;
        unsigned l16 = lane * 16;
        const unsigned dbeg = lds0 + wid * 4096, lbase = lds0 + l16;
        const unsigned fk0 = (unsigned)WS_FK + head * 32 * FS;
        const unsigned fxo = (unsigned)WS_FX + ((hp * 8 + mt) * 8 + wid) * FS, xrec0 = (unsigned)WS_XP + ((hp * 8 + mt) * 8 + wid) * 5120 + 2048;
        const f32x16 z = {};
        f32x16 sa, negm, o;
        u32x16 qol, kvf; u32x4 ph;

#define FIRST_TILE(base, q0, q1) do { const unsigned one2 = (((lane >> 4) & 1) == ((lane >> 2) & 1)) ? 0x3F803F80u : 0u; const u32x4 ones = {one2, one2, one2, one2}; \
            const bf16x8 k0_ = ldsrd128(base), k1_ = ldsrd128((base) + 1024); kvf = cat16(asu(ldsrd128((base) + 4096)), asu(ldsrd128((base) + 5120)), asu(ldsrd128((base) + 2048)), ones); \
            sa = MFMA32(k0_, q0, z); sa = MFMA32(k1_, q1, sa); float m_ = sa[0]; \
            _Pragma("unroll") for (int r = 1; r < 16; ++r) m_ = __builtin_fmaxf(m_, sa[r]); \
            m_ = -swap_max(m_); _Pragma("unroll") for (int r = 0; r < 16; ++r) { negm[r] = m_; sa[r] += m_; } \
            qol = cat16(asu(q0), asu(q1), ones, ones); } while (0)
#define L6_OUT "={v[0:15]}"(o), "+{v[32:47]}"(sa), "+{v[64:79]}"(qol), "+{v[80:95]}"(kvf), "={v[108:111]}"(ph)
#define L6C(HL, BASE) asm volatile(ATT_L6C : L6_OUT, "={v[144:159]}"(h0), "={v[160:167]}"(h1) \
            : "{v[16:31]}"(negm), "{v112}"(BASE), "{v[140:143]}"(u32x4{0u, 0u, FLADDR, (unsigned)WS_HALO + (mt * 32 + ph1) * 6144 + 3072 + l16}), \
              [ws] "s"(ws), [hl] "s"(HL), [fld] "s"(lds0 + K2_FL + wid * 1024) : ATT_TMP, "scc", "memory")
#define FLADDR (fk0 + ((((lane & 7) == 7) ? blk : RBLK(8 * (lane & 7) + wid)) >> 1) * FS)
        const unsigned own = (ph1 * 32 + mt * 4 + wr) * FS;
        const unsigned pv = (lane >> 5) * 1024 + ((lane & 15) + 32 * ((lane >> 4) & 1)) * 16, pq = pv + (lane & 8) * 16;
        if (br == 0) {
            const char* kp = ws + WS_KV + (size_t)((ph1 * 64 + pb1) * 4) * 1024;
#define PUB_KV() do { st16_dev<0>(kp, pq, fk[0][0]); st16_dev<128>(kp, pq, fk[0][1]); st16_dev<2048>(kp, pv, fv[0][0]); st16_dev<2304>(kp, pv, fv[0][1]); \
            st16_dev<0>(kp + 4096, pq, fk[1][0]); st16_dev<128>(kp + 4096, pq, fk[1][1]); st16_dev<2048>(kp + 4096, pv, fv[1][0]); st16_dev<2304>(kp + 4096, pv, fv[1][1]); } while (0)
            if (hd == 1) {
                const char* qp = ws + WS_QF + (size_t)((ph1 * 64 + pb1) * 2) * 1024;
                st16_dev<0>(qp, pq, fq[0][0]); st16_dev<128>(qp, pq, fq[0][1]); st16_dev<2048>(qp, pq, fq[1][0]); st16_dev<2176>(qp, pq, fq[1][1]);
            }
            if (g == 0) PUB_KV();
#pragma unroll
            for (int mb = 0; mb < 2; ++mb) {
                const unsigned a = lds0 + hd * 2 * K2_SLOT + (wr * 2 + mb) * 4096, aq = lds0 + K2_QX + hd * 16384 + (wr * 2 + mb) * 2048 + pq;
                ldswr128(a + pq, fk[mb][0]); ldswr128(a + pq + 128, fk[mb][1]); ldswr128(a + 2048 + pv, fv[mb][0]); ldswr128(a + 2304 + pv, fv[mb][1]);
                if (hd == 0 || g == 0) { ldswr128(aq, fq[mb][0]); ldswr128(aq + 128, fq[mb][1]); }
            }
            BARRIER_L();
            if (g == 0 || hd == 1) {
                drain();
                if (lane == 0) { if (g == 0) flag_set(ws + WS_FK, own, tag); if (hd == 1) flag_set(ws + WS_FQ, own, tag); }
            }
            const unsigned qa = lbase + K2_QX + (1 - g) * 16384 + wid * 2048, cx = lbase + (1 - g) * 2 * K2_SLOT;
            SA_(3); SC_(2);
            u32x16 h0; u32x8 h1;
            { const bf16x8 q0 = ldsrd128(qa), q1 = ldsrd128(qa + 1024); FIRST_TILE(cx, q0, q1); }
            L6C(0, cx);
            SC_(3);
            if (g == 0) {
                asm volatile(ATT_XT : "+{v[0:15]}"(o), "+{v[32:47]}"(sa), "+{v[64:79]}"(qol), "+{v[80:95]}"(kvf), "+{v[108:111]}"(ph) : "{v[16:31]}"(negm), "{v112}"(cx) : ATT_TMP, "memory");
                SC_(4);
                st16_dev<-2048>(ws, xrec0 + l16, quad(o, 0)); st16_dev<-1024>(ws, xrec0 + l16, quad(o, 1)); st16_dev<0>(ws, xrec0 + l16, quad(o, 2)); st16_dev<1024>(ws, xrec0 + l16, quad(o, 3));
                st16_dev<2048>(ws, xrec0 + l16, u32x4{qol[12], __float_as_uint(negm[0]), 0u, 0u});
                SC_(5);
                const bf16x8 q0 = ldsrd128(lbase + K2_QX + wid * 2048), q1 = ldsrd128(lbase + K2_QX + wid * 2048 + 1024);
                FIRST_TILE(lbase, q0, q1);
                L6C(0, lbase);
            } else {
                PUB_KV();
                SC_(4);
                const int b1 = RBLK(wid), b0 = RBLK(wid - 4);
                flag_wait(ws, fk0 + (((lane & 1) ? b1 : b0) >> 1) * FS, tag);
                if (lane == 0) flag_set(ws + WS_FK, own, tag);
                SC_(5);
                glds4k_dev(kv + b1 * 4096, l16, dbeg + K2_SLOT);
                glds4k_dev(kv + b0 * 4096, l16, dbeg + K2_SLOT - 16384);
                glds16_dev(ws, FLADDR, lds0 + K2_FL + wid * 1024);
            }
#undef PUB_KV
        } else {
            if (wr == 3 && mt < 7) {
                const char* p = ws + WS_HALO + (size_t)(((mt + 1) * 32 + ph1) * 6) * 1024;
                st16_dev<0>(p, pq, fk[0][0]); st16_dev<128>(p, pq, fk[0][1]); st16_dev<3072>(p, pq, fk[1][0]); st16_dev<3200>(p, pq, fk[1][1]);
                if (lane >= 32) { st16_dev<1024>(p, pv, fv[0][0]); st16_dev<1280>(p, pv, fv[0][1]); st16_dev<1024>(p + 3072, pv, fv[1][0]); st16_dev<1280>(p + 3072, pv, fv[1][1]); }
            }
            const unsigned ex = lds0 + 2 * K2_SLOT + wid * 12288;
#pragma unroll
            for (int mb = 0; mb < 2; ++mb) {
                const unsigned a = ex + mb * 6144;
                ldswr128(a + pq, fq[mb][0]); ldswr128(a + pq + 128, fq[mb][1]); ldswr128(a + 2048 + pq, fk[mb][0]); ldswr128(a + 2176 + pq, fk[mb][1]);
                ldswr128(a + 4096 + pv, fv[mb][0]); ldswr128(a + 4352 + pv, fv[mb][1]);
            }
            BARRIER_L();
            if (wr == 3 && mt < 7) { drain(); if (lane == 0) flag_set(ws + WS_FH, (ph1 * 8 + mt + 1) * FS, tag); }
            SC_(2);
            LocalPart lp[2];
            const f32x16 band = band_mask(lane);
            bf16x8 bq[2][2];
#pragma unroll
            for (int mb = 0; mb < 2; ++mb) { bq[mb][0] = ldsrd128(ex + mb * 6144 + l16); bq[mb][1] = ldsrd128(ex + mb * 6144 + 1024 + l16); }
#define LOCAL_BLOCK(mb) do { const unsigned a_ = ex + mb * 6144 + l16; \
                local_own(bq[mb][0], bq[mb][1], ldsrd128(a_ + 2048), ldsrd128(a_ + 3072), ldsrd128(a_ + 4096), ldsrd128(a_ + 5120), band, lp[mb]); \
                if (wr > 0) local_prev(bq[mb][0], bq[mb][1], ldsrd128(a_ - 24576 + 2048), ldsrd128(a_ - 24576 + 3072), ldsrd128(a_ - 24576 + 5120), lane, lp[mb]); \
                if (wr > 0 || mt == 0) local_fin(lp[mb]); } while (0)
            LOCAL_BLOCK(0);
            SC_(3);
            const int hl = (wid < 2) ? mt : 0;
            const unsigned fh = (unsigned)WS_FH + (ph1 * 8 + mt) * FS;
            const int b0 = RBLK(wid), b1 = RBLK(8 + wid);
            const int q = lane & 3;
            const u64 fm = flag_wait_some(ws, q == 0 ? fk0 - 1024 * FS + (blk >> 1) * FS : q == 1 ? fk0 + (b0 >> 1) * FS : q == 2 ? fk0 + (b1 >> 1) * FS : fh, tag, 3);
            SC_(4);
            glds4k_dev(kv + b0 * 4096, l16, dbeg);
            const char* qp = ws + WS_QF + (size_t)((head * 64 + blk) * 2) * 1024;
            glds16_dev(qp, l16, ex); glds16_dev(qp + 1024, l16, ex + 1024);
            if (fm & 4) glds4k_dev(kv + b1 * 4096, l16, dbeg + K2_SLOT);
            LOCAL_BLOCK(1);
#undef LOCAL_BLOCK
            if (fm & 4) WAITBAR_(4);
            else {
                WAITBAR_(0);
                flag_wait(ws, fk0 + (b1 >> 1) * FS, tag);
                glds4k_dev(kv + b1 * 4096, l16, dbeg + K2_SLOT);
            }
            const bf16x8 q0 = ldsrd128(ex + l16), q1 = ldsrd128(ex + 1024 + l16);
            SA_(3); SC_(5);
            if (hl && !(fm & 8)) flag_wait(ws, fh, tag);
            FIRST_TILE(lbase, q0, q1);
            u32x16 h0; u32x8 h1;
            L6C(hl, lbase);
            if (hl) {
                local_prev(bq[0][0], bq[0][1], asbf(u32x4{h0[0], h0[1], h0[2], h0[3]}), asbf(u32x4{h0[4], h0[5], h0[6], h0[7]}), asbf(u32x4{h0[8], h0[9], h0[10], h0[11]}), lane, lp[0]);
                local_prev(bq[1][0], bq[1][1], asbf(u32x4{h0[12], h0[13], h0[14], h0[15]}), asbf(u32x4{h1[0], h1[1], h1[2], h1[3]}), asbf(u32x4{h1[4], h1[5], h1[6], h1[7]}), lane, lp[1]);
                local_fin(lp[0]); local_fin(lp[1]);
            }
            const char* op = ws + WS_OF + (size_t)(((64 + pb1) * 32 + ph1) * 2) * 1024;
            st16_dev<0>(op, l16, quad(lp[0].o, 0)); st16_dev<1024>(op, l16, quad(lp[0].o, 1)); st16_dev<0>(op + 65536, l16, quad(lp[1].o, 0)); st16_dev<1024>(op + 65536, l16, quad(lp[1].o, 1));
        }
#undef FIRST_TILE
#undef L6_OUT
#undef L6C
        FRESH();
        l16 = lane * 16;
        SA_(4); SC_(6);
        const int isx = br & (1 - g);
        unsigned cnt = 7, dst = dbeg + 2 * K2_SLOT, nx = K2_SLOT;
        unsigned mode = isx << 1;
        const unsigned act = 2u | (isx * 12u) | ((br | (1 - g)) << 5);
        u64 tab = (u64)blk << 42;
#pragma unroll
        for (int k = 1; k < 7; ++k) tab |= (u64)RBLK(8 * k + wid) << (6 * k);
        tab >>= 6 + 6 * br;
        u32x8 adr;
        { const int k = 8 * wid + (lane >> 4), c = lane & 15;
          adr = u32x8{lds0 + l16, lds0 + l16 + K2_SLOT, l16, lds0 + l16, (unsigned)(k * 2048 + ((c ^ ((k & 7) << 1)) << 4)), (unsigned)((k + 4) * 2048 + ((c ^ (((k + 4) & 7) << 1)) << 4) - 1024),
                      br ? (unsigned)WS_FO + ((64 + pb1 + (lane & 1)) * 32 + ph1) * FS : fxo, (unsigned)((((j & 3) * 8 + wid) * 32 + (lane >> 1)) * 2048 + (lane & 1) * 128)}; }
        f32x16 xo; f32x2 xl;
#define DBGOUT
        asm volatile(ATT_MAIN
                     : DBGOUT"+{v[0:15]}"(o), "+{v[32:47]}"(sa), "+{v[64:79]}"(qol), "+{v[80:95]}"(kvf), "+{v[108:111]}"(ph), "+{v[112:119]}"(adr), "={v[122:137]}"(xo), "={v[138:139]}"(xl),
                       [cnt] "+s"(cnt), [dst] "+s"(dst), [nx] "+s"(nx), [mode] "+s"(mode)
                     : "{v[16:31]}"(negm), "{v[140:143]}"(u32x4{(unsigned)tag, (unsigned)(tag >> 32), fxo, xrec0 + l16}),
                       "{v[170:173]}"(u32x4{FLADDR, 0u, 0u, lds0 + K2_FL + wid * 1024 + l16}),
                       [srclo] "s"((unsigned)(uintptr_t)kv), [srchi] "s"((unsigned)((uintptr_t)kv >> 32)), [dbeg] "s"(dbeg), [dend] "s"(dbeg + 3 * K2_SLOT),
                       [wo01] "s"(wo + (size_t)k30 * 131072), [wo23] "s"(wo + (size_t)((k30 + 2) & 15) * 131072), [wo] "s"(wo), [wdst] "s"(lds0 + 98304 + wid * 2048), [act] "s"(act), [tab] "s"(tab), [k0] "s"(br), [ws] "s"(ws)
                     : ATT_TMP, "v120", "v121", "v168", "v169", "s40", "s41", "s42", "s43", "s44", "s45", "s46", "s47", "s48", "s49", "s50", "s51", "vcc", "scc", "memory");
        FRESH();
        l16 = lane * 16;
        const unsigned xrec = xrec0 + l16;
        const bf16x8 q0 = asbf(u32x4{qol[0], qol[1], qol[2], qol[3]}), q1 = asbf(u32x4{qol[4], qol[5], qol[6], qol[7]});
        const u32x4 ones = {qol[8], qol[9], qol[10], qol[11]};
        SA_(5); SB_(1);
        float lsum = __uint_as_float(qol[12]);
        bool ok = lsum < 1.0e18f;
        if (isx) {
            if (__builtin_expect(!(mode & 4), 0)) {
                flag_wait(ws, fxo, tag);
                f32x4 a0, a1, a2, a3;
                asm volatile("global_load_dwordx4 %0, %5, %6 offset:-2048 sc1\n\tglobal_load_dwordx4 %1, %5, %6 offset:-1024 sc1\n\tglobal_load_dwordx4 %2, %5, %6 sc1\n\t"
                             "global_load_dwordx4 %3, %5, %6 offset:1024 sc1\n\tglobal_load_dwordx2 %4, %5, %6 offset:2048 sc1\n\ts_waitcnt vmcnt(0)"
                             : "=&v"(a0), "=&v"(a1), "=&v"(a2), "=&v"(a3), "=&v"(xl) : "v"(xrec), "s"(ws) : "memory");
#pragma unroll
                for (int r = 0; r < 4; ++r) { xo[r] = a0[r]; xo[4 + r] = a1[r]; xo[8 + r] = a2[r]; xo[12 + r] = a3[r]; }
            }
            const float nm = __builtin_fminf(negm[0], xl[1]), fa = EXP2(nm - negm[0]), fx = EXP2(nm - xl[1]);
            ok = ok && (xl[0] < 1.0e18f);
            lsum = lsum * fa + xl[0] * fx;
#pragma unroll
            for (int r = 0; r < 16; ++r) o[r] = o[r] * fa + xo[r] * fx;
        }
#pragma unroll
        for (int r = 0; r < 16; ++r) ok = ok && (__builtin_fabsf(o[r]) < 3.0e38f);
        if (__builtin_expect(__any(!ok), 0)) {
            float m = -__builtin_inff();
            f32x4 l;
            for (int pass = 0; pass < 2; ++pass) {
                o = z; l = f32x4{};
                for (int t = 0; t < 64; ++t) {
                    bf16x8 f0, f1, f2, f3;
                    asm volatile("global_load_dwordx4 %0, %4, %5 sc1\n\tglobal_load_dwordx4 %1, %4, %5 offset:1024 sc1\n\tglobal_load_dwordx4 %2, %4, %5 offset:2048 sc1\n\t"
                                 "global_load_dwordx4 %3, %4, %5 offset:3072 sc1\n\ts_waitcnt vmcnt(0)" : "=&v"(f0), "=&v"(f1), "=&v"(f2), "=&v"(f3) : "v"(l16 + t * 4096), "s"(kv) : "memory");
                    f32x16 s = MFMA32(f0, q0, z); s = MFMA32(f1, q1, s);
                    if (pass == 0) {
#pragma unroll
                        for (int r = 0; r < 16; ++r) m = __builtin_fmaxf(m, s[r]);
                    } else {
#pragma unroll
                        for (int r = 0; r < 16; ++r) s[r] = EXP2(s[r] - m);
                        const bf16x8 p0 = asbf(pack8(s, 0)), p1 = asbf(pack8(s, 1));
                        o = MFMA32(f2, p0, o); o = MFMA32(f3, p1, o);
                        l = MFMA16(asbf(ones), p0, l); l = MFMA16(asbf(ones), p1, l);
                    }
                }
                if (pass == 0) m = swap_max(m);
            }
            lsum = l[0];
        }
        const float inv = 1.0f / lsum;
#pragma unroll
        for (int r = 0; r < 16; ++r) o[r] *= inv;
        const char* op = ws + WS_OF + (size_t)((blk * 32 + head) * 2) * 1024;
        st16_dev<0>(op, l16, pack8(o, 0)); st16_dev<1024>(op, l16, pack8(o, 1));
        drain();
        SB_(2);
        if (lane == 0) flag_set(ws + WS_FO, (blk * 32 + head) * FS, tag);
    }
    {
        FRESH();
        const int g = wid >> 2, wi = wid & 3, wm = wi >> 1, wn = wi & 1, g4 = lane >> 4;
        const char* of = ws + WS_OF + (size_t)(br * 64 + mt3 * 4 + wi) * 65536;
        unsigned vw4[2];
#pragma unroll
        for (int ii = 0; ii < 2; ++ii) { const int k = 16 * wi + 4 * ii + g4; vw4[ii] = k * 2048 + (((lane & 15) ^ ((k & 7) << 1)) << 4); }
        const unsigned l16 = lane * 16, sdst = lds0 + wi * 4096;
#define STAGE_O(t) glds4k_dev(of + (((t) + k30) & 15) * 4096, l16, sdst + ((t) & 3) * 16384)
#define STAGE_WO(t, slot) do { const char* s_ = wo + (size_t)(((t) + k30) & 15) * 131072; glds16(s_, vw4[0], sdst + (slot)); glds16(s_, vw4[1], sdst + (slot) + 1024); \
            glds16(s_ + 16384, vw4[0], sdst + (slot) + 2048); glds16(s_ + 16384, vw4[1], sdst + (slot) + 3072); } while (0)
        asm volatile("s_barrier" ::: "memory");
        SB_(3);
        STAGE_WO(5 - g, 65536 + (1 - g) * 16384);
        flag_wait(ws + WS_FO, ((br * 64 + mt3 * 4 + wi) * 32 + 4 * ((lane & 15) >> 1) + 2 * (1 - g) + (lane & 1)) * FS, tag);
        STAGE_O(1 - g);
        if (g == 1) STAGE_O(2);
        SA_(6); SB_(4);
        const unsigned ao = lds0 + g * 16384 + wm * 8192 + (g4 >> 1) * 1024 + ((lane & 15) + 32 * (g4 & 1)) * 16;
        unsigned aw[4];
        { const int q4 = (lane & 15) >> 2, p = lane & 3, k = 16 * (g4 >> 1) + 4 * (g4 & 1) + q4;
          const unsigned a0 = lds0 + g * 16384 + k * 256 + (((8 * wn + (p >> 1)) ^ ((k & 7) << 1)) << 4) + (p & 1) * 8;
#pragma unroll
          for (int nb = 0; nb < 4; ++nb) aw[nb] = a0 ^ (nb << 5); }
        f32x4 acc[4][4];
#pragma unroll
        for (int mb = 0; mb < 4; ++mb)
#pragma unroll
            for (int nb = 0; nb < 4; ++nb) acc[mb][nb] = f32x4{};
        if (g == 1) { WAITBAR_(4); asm volatile("s_barrier" ::: "memory"); } else asm volatile("s_barrier" ::: "memory");
        SB_(5);
        SBAR();
#pragma unroll
        for (int u = 0; u < 8; ++u) {
            bf16x8 of_[4][2], wf[4][2];
            const int to = 2 * u + 3 + g, tw = to + 2;
            const char* so = of + ((to + k30) & 15) * 4096; const char* sw = wo + (size_t)((tw + k30) & 15) * 131072;
            const unsigned dob = sdst + (to & 3) * 16384, dwb = sdst + (g ? p3_wslot(2 * u + 6) : p3_wslot(2 * u + 5));
            const bool doo = u < 6 || (u == 6 && g == 0), dow = (u >= 1 || g == 1) && (u < 5 || (u == 5 && g == 0));
#pragma unroll
            for (int i = 0; i < 8; ++i) {
                const int hh = i >> 2, q = i & 3;
                of_[q][hh] = ldsrd128(ao + ((2 * u) & 3) * 16384 + (q >> 1) * 4096 + (q & 1) * 256 + hh * 2048);
                wf[q][hh] = cat8(ldstr(aw[q] + p3_wslot(2 * u) + hh * 8192), ldstr(aw[q] + p3_wslot(2 * u) + hh * 8192 + 2048));
                if (i < 4) { if (u < 7 && doo) glds16_dev(so + i * 1024, l16, dob + i * 1024); }
                else if (u < 6 && dow) glds16(sw + ((i & 2) ? 16384 : 0), vw4[i & 1], dwb + (i - 4) * 1024);
            }
            if (p3_allow(2 * u) == p3_allow(2 * u + 1)) WAITBAR(p3_allow(2 * u)); else if (g == 0) WAITBAR(p3_allow(2 * u)); else WAITBAR(p3_allow(2 * u + 1));
            SBAR();
            __builtin_amdgcn_s_setprio(1);
#pragma unroll
            for (int mb = 0; mb < 4; ++mb)
#pragma unroll
                for (int nb = 0; nb < 4; ++nb) { acc[mb][nb] = MFMA16(wf[nb][0], of_[mb][0], acc[mb][nb]); acc[mb][nb] = MFMA16(wf[nb][1], of_[mb][1], acc[mb][nb]); }
            __builtin_amdgcn_s_setprio(0);
            SBAR();
            if (u < 7 || g == 0) asm volatile("s_barrier" ::: "memory");
            SBAR();
        }
        SB_(6);
        FRESH();
        const unsigned xw = lds0 + wid * 8192 + l16, xr = lds0 + (wid ^ 4) * 8192 + l16;
#pragma unroll
        for (int i = 0; i < 8; ++i) { const f32x4 v = g ? acc[i >> 2][i & 3] : acc[2 + (i >> 2)][i & 3]; ldswr128(xw + i * 1024, __builtin_bit_cast(u32x4, v)); }
        BARRIER_L();
        const unsigned st = lds0 + 65536 + wid * 4096;
#pragma unroll
        for (int i = 0; i < 8; ++i) {
            const f32x4 v = (g ? acc[2 + (i >> 2)][i & 3] : acc[i >> 2][i & 3]) + __builtin_bit_cast(f32x4, ldsrd128(xr + i * 1024));
            const int row = 16 * (i >> 2) + (lane & 15), c = 2 * (i & 3) + (g4 >> 1);
            *(LAS u32x2*)(uintptr_t)(st + row * 128 + ((c ^ (row & 7)) << 4) + (g4 & 1) * 8) = u32x2{cvtpk(v[0], v[1]), cvtpk(v[2], v[3])};
        }
        const int blk = mt3 * 4 + 2 * wm + g;
        char* op = (char*)out + br * 2048 + nt3 * 256 + wn * 128 + (lane & 7) * 16;
#pragma unroll
        for (int i = 0; i < 4; ++i) {
            const int r = 8 * i + (lane >> 3), row = br ? ((blk >> 1) * 64 + 2 * r + (blk & 1)) : (blk * 32 + r);
            *(u32x4*)(op + (size_t)row * 4096) = __builtin_bit_cast(u32x4, ldsrd128(st + r * 128 + (((lane & 7) ^ (r & 7)) << 4)));
        }
#undef STAGE_O
#undef STAGE_WO
    }
    STAMP_AT(0, 7);
    if (b == 0 && wid == 0) {
#pragma unroll
        for (int i = 0; i < 2; ++i) { const int u = i * 64 + lane;
            flag_wait(ws0, (unsigned)WS_FK + ((2 * (u >> 3)) * 32 + (u & 7) * 4) * FS, tag);
            flag_wait(ws0, (unsigned)WS_FO + ((64 + (u & 7) * 8) * 32 + 2 * (u >> 3)) * FS, tag); }
        if (lane == 0) flag_set(ws0, (unsigned)WS_EP, tag);
    }
    asm volatile(".Lmk_code_end:");
}

extern "C" void kernel_launch(void* const* d_in, const int* in_sizes, int n_in, void* d_out, int out_size, void* d_ws, size_t ws_size, hipStream_t stream) {
    mk_fused<<<256, 512, 0, stream>>>((const u16*)d_in[0], (const u16*)d_in[1], (const u16*)d_in[2], (const u16*)d_in[3], (const u16*)d_in[4], (const u16*)d_in[5], (char*)d_ws, (u16*)d_out);
}
```

```cpp
#include <hip/hip_runtime.h>
#include <stdint.h>

typedef unsigned short u16;
typedef unsigned long long u64;
typedef __bf16 bf16x8 __attribute__((ext_vector_type(8)));
typedef __bf16 bf16x2 __attribute__((ext_vector_type(2)));
typedef short s16x4 __attribute__((ext_vector_type(4)));
typedef float f32x16 __attribute__((ext_vector_type(16)));
typedef float f32x4 __attribute__((ext_vector_type(4)));
typedef float f32x2 __attribute__((ext_vector_type(2)));
typedef unsigned u32x4 __attribute__((ext_vector_type(4)));
typedef unsigned u32x2 __attribute__((ext_vector_type(2)));
typedef unsigned u32x8 __attribute__((ext_vector_type(8)));
typedef unsigned u32x16 __attribute__((ext_vector_type(16)));
#define LAS __attribute__((address_space(3)))
#define DI __device__ __forceinline__
#define SBAR() __builtin_amdgcn_sched_barrier(0)
#define MFMA32(a, b, c) __builtin_amdgcn_mfma_f32_32x32x16_bf16((a), (b), (c), 0, 0, 0)
#define MFMA16(a, b, c) __builtin_amdgcn_mfma_f32_16x16x32_bf16((a), (b), (c), 0, 0, 0)
#define CHAIN() SBAR()
#define EXP2(x) __builtin_amdgcn_exp2f(x)

constexpr float QSCALE = 0.17677669529663687f * 1.4426950408889634f;

constexpr size_t WS_QF = 0;
constexpr size_t WS_KV = WS_QF + (4u << 20);
constexpr size_t WS_OF = WS_KV + (8u << 20);
constexpr size_t WS_HALO = WS_OF + (8u << 20);
constexpr size_t WS_XP = WS_HALO + (2u << 20);
constexpr size_t WS_FX = WS_XP + (5u << 20);
#define FS 64
constexpr size_t WS_FQ = WS_FX + 1024 * FS;
constexpr size_t WS_FK = WS_FQ + 1024 * FS;
constexpr size_t WS_FO = WS_FK + 1024 * FS;
constexpr size_t WS_FH = WS_FO + 4096 * FS;
constexpr size_t WS_EP = WS_FH + 256 * FS;

#define SLP 1
#define STAMP_AT(K, I)
#define SA_(I)
#define SB_(I)

#define SC_(I)
DI unsigned cvtpk(float lo, float hi) { f32x2 v = {lo, hi}; bf16x2 b = __builtin_convertvector(v, bf16x2); return __builtin_bit_cast(unsigned, b); }
DI u32x4 pack8(const f32x16& a, int s) { u32x4 r; r[0] = cvtpk(a[8 * s], a[8 * s + 1]); r[1] = cvtpk(a[8 * s + 2], a[8 * s + 3]); r[2] = cvtpk(a[8 * s + 4], a[8 * s + 5]); r[3] = cvtpk(a[8 * s + 6], a[8 * s + 7]); return r; }
DI u32x4 pack44(f32x4 a, f32x4 c) { return u32x4{cvtpk(a[0], a[1]), cvtpk(a[2], a[3]), cvtpk(c[0], c[1]), cvtpk(c[2], c[3])}; }
DI u32x4 asu(bf16x8 v) { return __builtin_bit_cast(u32x4, v); }
DI u32x16 cat16(u32x4 a, u32x4 b, u32x4 c, u32x4 d) { return u32x16{a[0], a[1], a[2], a[3], b[0], b[1], b[2], b[3], c[0], c[1], c[2], c[3], d[0], d[1], d[2], d[3]}; }
DI bf16x8 asbf(u32x4 v) { return __builtin_bit_cast(bf16x8, v); }
DI void glds16(const void* sbase, unsigned voff, unsigned lds) {
    asm volatile("s_mov_b32 m0, %2\n\ts_nop 0\n\tglobal_load_lds_dwordx4 %0, %1" :: "v"(voff), "s"(sbase), "s"(lds) : "memory");
}
DI void glds16_dev(const void* sbase, unsigned voff, unsigned lds) {
    asm volatile("s_mov_b32 m0, %2\n\ts_nop 0\n\tglobal_load_lds_dwordx4 %0, %1 sc1" :: "v"(voff), "s"(sbase), "s"(lds) : "memory");
}
DI void glds4k_dev(const void* sbase, unsigned voff, unsigned lds) {
    asm volatile("s_mov_b32 m0, %2\n\ts_nop 0\n\tglobal_load_lds_dwordx4 %0, %1 sc1\n\tglobal_load_lds_dwordx4 %0, %1 offset:1024 sc1\n\t"
                 "global_load_lds_dwordx4 %0, %1 offset:2048 sc1\n\tglobal_load_lds_dwordx4 %0, %1 offset:3072 sc1" :: "v"(voff), "s"(sbase), "s"(lds) : "memory");
}
template <int OFF> DI void st16_dev(const void* sbase, unsigned voff, u32x4 v) {
    asm volatile("global_store_dwordx4 %0, %1, %2 offset:%3 sc1\n\ts_nop 1" :: "v"(voff), "v"(v), "s"(sbase), "i"(OFF) : "memory");
}
DI void drain() { asm volatile("s_waitcnt vmcnt(0)" ::: "memory"); }
DI void flag_set(const void* sbase, unsigned voff, u64 tag) {
    asm volatile("global_store_dwordx2 %0, %1, %2 sc1" :: "v"(voff), "v"(tag), "s"(sbase) : "memory");
}
DI void flag_wait(const void* sbase, unsigned voff, u64 tag) {
    const u64 t0 = __builtin_amdgcn_s_memrealtime();
    for (;;) {
        u64 v;
        asm volatile("global_load_dwordx2 %0, %1, %2 sc1\n\ts_waitcnt vmcnt(0)" : "=v"(v) : "v"(voff), "s"(sbase) : "memory");
        if (__all(v == tag)) break;
        if (__builtin_amdgcn_s_memrealtime() - t0 > 2000000ull) break;
        __builtin_amdgcn_s_sleep(SLP);
    }
}
DI u64 flag_wait_some(const void* sbase, unsigned voff, u64 tag, u64 need) {
    const u64 t0 = __builtin_amdgcn_s_memrealtime();
    for (;;) {
        u64 v;
        asm volatile("global_load_dwordx2 %0, %1, %2 sc1\n\ts_waitcnt vmcnt(0)" : "=v"(v) : "v"(voff), "s"(sbase) : "memory");
        const u64 m = __ballot(v == tag);
        if ((m & need) == need || __builtin_amdgcn_s_memrealtime() - t0 > 2000000ull) return m;
        __builtin_amdgcn_s_sleep(SLP);
    }
}
DI bf16x8 ldsrd128(unsigned addr) { return *(const LAS bf16x8*)(uintptr_t)addr; }
DI void ldswr128(unsigned addr, u32x4 v) { *(LAS u32x4*)(uintptr_t)addr = v; }
DI s16x4 ldstr(unsigned addr) { return __builtin_amdgcn_ds_read_tr16_b64_v4i16((LAS s16x4*)(uintptr_t)addr); }
DI bf16x8 cat8(s16x4 lo, s16x4 hi) { typedef short s16x8 __attribute__((ext_vector_type(8))); s16x8 v = {lo[0], lo[1], lo[2], lo[3], hi[0], hi[1], hi[2], hi[3]}; return __builtin_bit_cast(bf16x8, v); }
DI float swap_max(float m) { auto r = __builtin_amdgcn_permlane32_swap(__float_as_uint(m), __float_as_uint(m), false, false); return __builtin_fmaxf(__uint_as_float(r[0]), __uint_as_float(r[1])); }
DI float swap_add(float m) { auto r = __builtin_amdgcn_permlane32_swap(__float_as_uint(m), __float_as_uint(m), false, false); return __uint_as_float(r[0]) + __uint_as_float(r[1]); }
#define FRESH() asm volatile("" : "+v"(lane), "+s"(ws))
#define BARRIER_L() asm volatile("s_waitcnt lgkmcnt(0)\n\ts_barrier" ::: "memory")
#define WAITBAR_(n) asm volatile("s_waitcnt vmcnt(%0) lgkmcnt(0)\n\ts_barrier" :: "i"(n) : "memory")
#define WAITBAR(n) do { switch (n) { case 0: WAITBAR_(0); break; case 2: WAITBAR_(2); break; case 3: WAITBAR_(3); break; case 4: WAITBAR_(4); break; case 6: WAITBAR_(6); break; \
    case 7: WAITBAR_(7); break; case 8: WAITBAR_(8); break; case 10: WAITBAR_(10); break; case 12: WAITBAR_(12); break; default: WAITBAR_(0); } } while (0)

struct LocalPart { f32x16 o; float m, l; };
DI f32x16 band_mask(int lane) {
    const int i = lane & 31, h = lane >> 5;
    f32x16 c;
#pragma unroll
    for (int r = 0; r < 16; ++r) { const int j = (r & 3) + 8 * (r >> 2) + 4 * h; c[r] = ((unsigned)(i - j) <= 7u) ? 0.f : -__builtin_inff(); }
    return c;
}
DI float max3(float a, float b, float c) { float r; asm("v_max3_f32 %0, %1, %2, %3" : "=v"(r) : "v"(a), "v"(b), "v"(c)); return r; }
DI void local_own(bf16x8 q0, bf16x8 q1, bf16x8 k0, bf16x8 k1, bf16x8 v0, bf16x8 v1, const f32x16& band, LocalPart& P) {
    const f32x16 z = {};
    f32x16 so = MFMA32(k0, q0, band); so = MFMA32(k1, q1, so);
    float m = __builtin_fmaxf(so[0], so[1]);
#pragma unroll
    for (int r = 2; r < 16; r += 2) m = __builtin_fmaxf(__builtin_fmaxf(m, so[r]), so[r + 1]);
    m = swap_max(m);
    float l = 0.f;
#pragma unroll
    for (int r = 0; r < 16; ++r) { so[r] = EXP2(so[r] - m); l += so[r]; }
    P.o = MFMA32(v0, asbf(pack8(so, 0)), z); P.o = MFMA32(v1, asbf(pack8(so, 1)), P.o);
    P.m = m; P.l = l;
}
DI void local_prev(bf16x8 q0, bf16x8 q1, bf16x8 kp0, bf16x8 kp1, bf16x8 vp1, int lane, LocalPart& P) {
    const int i = lane & 31, h = lane >> 5;
    const f32x16 z = {};
    f32x16 sp = MFMA32(kp0, q0, z); sp = MFMA32(kp1, q1, sp);
    const float NEG = -__builtin_inff();
    float m = P.m;
#pragma unroll
    for (int r = 12; r < 16; ++r) { const int j = (r & 3) + 24 + 4 * h; sp[r] = (j >= i + 25) ? sp[r] : NEG; m = __builtin_fmaxf(m, sp[r]); }
    m = swap_max(m);
    const float f = EXP2(P.m - m);
    float l = P.l * f;
#pragma unroll
    for (int r = 12; r < 16; ++r) { sp[r] = EXP2(sp[r] - m); l += sp[r]; }
#pragma unroll
    for (int r = 0; r < 16; ++r) P.o[r] *= f;
    P.o = MFMA32(vp1, asbf(u32x4{0u, 0u, cvtpk(sp[12], sp[13]), cvtpk(sp[14], sp[15])}), P.o);
    P.m = m; P.l = l;
}
DI void local_fin(LocalPart& P) {
    const float inv = 1.0f / swap_add(P.l);
#pragma unroll
    for (int r = 0; r < 16; ++r) P.o[r] *= inv;
    const u32x4 o0 = pack8(P.o, 0), o1 = pack8(P.o, 1);
#pragma unroll
    for (int r = 0; r < 4; ++r) { P.o[r] = __uint_as_float(o0[r]); P.o[4 + r] = __uint_as_float(o1[r]); }
}
DI u32x4 quad(const f32x16& a, int i) { return __builtin_bit_cast(u32x4, f32x4{a[4 * i], a[4 * i + 1], a[4 * i + 2], a[4 * i + 3]}); }

#define ATT_L6C \
    "s_nop 1\n\t" \
    "s_cmp_eq_u32 %[hl], 0\n\t" \
    "s_cbranch_scc1 10f\n\t" \
    "global_load_dwordx4 v[144:147], v143, %[ws] offset:-3072 sc1\n\t" \
    "global_load_dwordx4 v[148:151], v143, %[ws] offset:-2048 sc1\n\t" \
    "global_load_dwordx4 v[152:155], v143, %[ws] offset:-1024 sc1\n\t" \
    "global_load_dwordx4 v[156:159], v143, %[ws] offset:0 sc1\n\t" \
    "global_load_dwordx4 v[160:163], v143, %[ws] offset:1024 sc1\n\t" \
    "global_load_dwordx4 v[164:167], v143, %[ws] offset:2048 sc1\n\t" \
    "10:\n" \
    "s_setprio 3\n\t" \
    "s_waitcnt lgkmcnt(0)\n\t" \
    "v_mfma_f32_32x32x16_bf16 v[48:63], v[80:83], v[64:67], v[16:31]\n\t" \
    "v_exp_f32_e32 v32, v32\n\t" \
    "v_exp_f32_e32 v33, v33\n\t" \
    "v_exp_f32_e32 v34, v34\n\t" \
    "v_mfma_f32_32x32x16_bf16 v[48:63], v[84:87], v[68:71], v[48:63]\n\t" \
    "ds_read_b128 v[80:83], v112 offset:8192\n\t" \
    "ds_read_b128 v[84:87], v112 offset:9216\n\t" \
    "v_exp_f32_e32 v35, v35\n\t" \
    "v_exp_f32_e32 v36, v36\n\t" \
    "v_cvt_pk_bf16_f32 v104, v32, v33\n\t" \
    "v_cvt_pk_bf16_f32 v105, v34, v35\n\t" \
    "ds_read_b128 v[100:103], v112 offset:3072\n\t" \
    "ds_read_b128 v[96:99], v112 offset:6144\n\t" \
    "v_exp_f32_e32 v37, v37\n\t" \
    "v_exp_f32_e32 v38, v38\n\t" \
    "v_exp_f32_e32 v39, v39\n\t" \
    "v_exp_f32_e32 v40, v40\n\t" \
    "v_cvt_pk_bf16_f32 v106, v36, v37\n\t" \
    "v_cvt_pk_bf16_f32 v107, v38, v39\n\t" \
    "v_exp_f32_e32 v41, v41\n\t" \
    "v_exp_f32_e32 v42, v42\n\t" \
    "v_mfma_f32_32x32x16_bf16 v[0:15], v[88:91], v[104:107], 0\n\t" \
    "v_exp_f32_e32 v43, v43\n\t" \
    "v_exp_f32_e32 v44, v44\n\t" \
    "v_exp_f32_e32 v45, v45\n\t" \
    "v_mfma_f32_16x16x32_bf16 v[76:79], v[72:75], v[104:107], 0\n\t" \
    "v_exp_f32_e32 v46, v46\n\t" \
    "v_exp_f32_e32 v47, v47\n\t" \
    "v_cvt_pk_bf16_f32 v108, v40, v41\n\t" \
    "v_cvt_pk_bf16_f32 v109, v42, v43\n\t" \
    "v_cvt_pk_bf16_f32 v110, v44, v45\n\t" \
    "v_cvt_pk_bf16_f32 v111, v46, v47\n\t" \
    "s_waitcnt lgkmcnt(0)\n\t" \
    "v_mfma_f32_32x32x16_bf16 v[32:47], v[80:83], v[64:67], v[16:31]\n\t" \
    "v_exp_f32_e32 v48, v48\n\t" \
    "v_exp_f32_e32 v49, v49\n\t" \
    "v_exp_f32_e32 v50, v50\n\t" \
    "v_mfma_f32_32x32x16_bf16 v[32:47], v[84:87], v[68:71], v[32:47]\n\t" \
    "ds_read_b128 v[80:83], v112 offset:12288\n\t" \
    "ds_read_b128 v[84:87], v112 offset:13312\n\t" \
    "v_exp_f32_e32 v51, v51\n\t" \
    "v_exp_f32_e32 v52, v52\n\t" \
    "v_cvt_pk_bf16_f32 v104, v48, v49\n\t" \
    "v_cvt_pk_bf16_f32 v105, v50, v51\n\t" \
    "v_mfma_f32_32x32x16_bf16 v[0:15], v[100:103], v[108:111], v[0:15]\n\t" \
    "ds_read_b128 v[92:95], v112 offset:7168\n\t" \
    "ds_read_b128 v[88:91], v112 offset:10240\n\t" \
    "v_exp_f32_e32 v53, v53\n\t" \
    "v_exp_f32_e32 v54, v54\n\t" \
    "v_exp_f32_e32 v55, v55\n\t" \
    "v_mfma_f32_16x16x32_bf16 v[76:79], v[72:75], v[108:111], v[76:79]\n\t" \
    "v_exp_f32_e32 v56, v56\n\t" \
    "v_cvt_pk_bf16_f32 v106, v52, v53\n\t" \
    "v_cvt_pk_bf16_f32 v107, v54, v55\n\t" \
    "v_exp_f32_e32 v57, v57\n\t" \
    "v_exp_f32_e32 v58, v58\n\t" \
    "v_mfma_f32_32x32x16_bf16 v[0:15], v[96:99], v[104:107], v[0:15]\n\t" \
    "v_exp_f32_e32 v59, v59\n\t" \
    "v_exp_f32_e32 v60, v60\n\t" \
    "v_exp_f32_e32 v61, v61\n\t" \
    "v_mfma_f32_16x16x32_bf16 v[76:79], v[72:75], v[104:107], v[76:79]\n\t" \
    "v_exp_f32_e32 v62, v62\n\t" \
    "v_exp_f32_e32 v63, v63\n\t" \
    "v_cvt_pk_bf16_f32 v108, v56, v57\n\t" \
    "v_cvt_pk_bf16_f32 v109, v58, v59\n\t" \
    "v_cvt_pk_bf16_f32 v110, v60, v61\n\t" \
    "v_cvt_pk_bf16_f32 v111, v62, v63\n\t" \
    "s_setprio 2\n\t" \
    "s_waitcnt lgkmcnt(0)\n\t" \
    "v_mfma_f32_32x32x16_bf16 v[48:63], v[80:83], v[64:67], v[16:31]\n\t" \
    "v_exp_f32_e32 v32, v32\n\t" \
    "v_exp_f32_e32 v33, v33\n\t" \
    "v_exp_f32_e32 v34, v34\n\t" \
    "v_mfma_f32_32x32x16_bf16 v[48:63], v[84:87], v[68:71], v[48:63]\n\t" \
    "ds_read_b128 v[80:83], v112 offset:16384\n\t" \
    "ds_read_b128 v[84:87], v112 offset:17408\n\t" \
    "v_exp_f32_e32 v35, v35\n\t" \
    "v_exp_f32_e32 v36, v36\n\t" \
    "v_cvt_pk_bf16_f32 v104, v32, v33\n\t" \
    "v_cvt_pk_bf16_f32 v105, v34, v35\n\t" \
    "v_mfma_f32_32x32x16_bf16 v[0:15], v[92:95], v[108:111], v[0:15]\n\t" \
    "ds_read_b128 v[100:103], v112 offset:11264\n\t" \
    "ds_read_b128 v[96:99], v112 offset:14336\n\t" \
    "v_exp_f32_e32 v37, v37\n\t" \
    "v_exp_f32_e32 v38, v38\n\t" \
    "v_exp_f32_e32 v39, v39\n\t" \
    "v_mfma_f32_16x16x32_bf16 v[76:79], v[72:75], v[108:111], v[76:79]\n\t" \
    "v_exp_f32_e32 v40, v40\n\t" \
    "v_cvt_pk_bf16_f32 v106, v36, v37\n\t" \
    "v_cvt_pk_bf16_f32 v107, v38, v39\n\t" \
    "v_exp_f32_e32 v41, v41\n\t" \
    "v_exp_f32_e32 v42, v42\n\t" \
    "v_mfma_f32_32x32x16_bf16 v[0:15], v[88:91], v[104:107], v[0:15]\n\t" \
    "v_exp_f32_e32 v43, v43\n\t" \
    "v_exp_f32_e32 v44, v44\n\t" \
    "v_exp_f32_e32 v45, v45\n\t" \
    "v_mfma_f32_16x16x32_bf16 v[76:79], v[72:75], v[104:107], v[76:79]\n\t" \
    "v_exp_f32_e32 v46, v46\n\t" \
    "v_exp_f32_e32 v47, v47\n\t" \
    "v_cvt_pk_bf16_f32 v108, v40, v41\n\t" \
    "v_cvt_pk_bf16_f32 v109, v42, v43\n\t" \
    "v_cvt_pk_bf16_f32 v110, v44, v45\n\t" \
    "v_cvt_pk_bf16_f32 v111, v46, v47\n\t" \
    "s_waitcnt lgkmcnt(0)\n\t" \
    "v_mfma_f32_32x32x16_bf16 v[32:47], v[80:83], v[64:67], v[16:31]\n\t" \
    "v_exp_f32_e32 v48, v48\n\t" \
    "v_exp_f32_e32 v49, v49\n\t" \
    "v_exp_f32_e32 v50, v50\n\t" \
    "v_mfma_f32_32x32x16_bf16 v[32:47], v[84:87], v[68:71], v[32:47]\n\t" \
    "ds_read_b128 v[80:83], v112 offset:20480\n\t" \
    "ds_read_b128 v[84:87], v112 offset:21504\n\t" \
    "v_exp_f32_e32 v51, v51\n\t" \
    "v_exp_f32_e32 v52, v52\n\t" \
    "v_cvt_pk_bf16_f32 v104, v48, v49\n\t" \
    "v_cvt_pk_bf16_f32 v105, v50, v51\n\t" \
    "v_mfma_f32_32x32x16_bf16 v[0:15], v[100:103], v[108:111], v[0:15]\n\t" \
    "ds_read_b128 v[92:95], v112 offset:15360\n\t" \
    "ds_read_b128 v[88:91], v112 offset:18432\n\t" \
    "v_exp_f32_e32 v53, v53\n\t" \
    "v_exp_f32_e32 v54, v54\n\t" \
    "v_exp_f32_e32 v55, v55\n\t" \
    "v_mfma_f32_16x16x32_bf16 v[76:79], v[72:75], v[108:111], v[76:79]\n\t" \
    "v_exp_f32_e32 v56, v56\n\t" \
    "v_cvt_pk_bf16_f32 v106, v52, v53\n\t" \
    "v_cvt_pk_bf16_f32 v107, v54, v55\n\t" \
    "v_exp_f32_e32 v57, v57\n\t" \
    "v_exp_f32_e32 v58, v58\n\t" \
    "v_mfma_f32_32x32x16_bf16 v[0:15], v[96:99], v[104:107], v[0:15]\n\t" \
    "v_exp_f32_e32 v59, v59\n\t" \
    "v_exp_f32_e32 v60, v60\n\t" \
    "v_exp_f32_e32 v61, v61\n\t" \
    "v_mfma_f32_16x16x32_bf16 v[76:79], v[72:75], v[104:107], v[76:79]\n\t" \
    "v_exp_f32_e32 v62, v62\n\t" \
    "v_exp_f32_e32 v63, v63\n\t" \
    "v_cvt_pk_bf16_f32 v108, v56, v57\n\t" \
    "v_cvt_pk_bf16_f32 v109, v58, v59\n\t" \
    "v_cvt_pk_bf16_f32 v110, v60, v61\n\t" \
    "v_cvt_pk_bf16_f32 v111, v62, v63\n\t" \
    "s_mov_b32 m0, %[fld]\n\t" \
    "s_nop 0\n\t" \
    "global_load_lds_dwordx4 v142, %[ws] sc1\n\t" \
    "s_setprio 1\n\t" \
    "s_waitcnt lgkmcnt(0)\n\t" \
    "v_mfma_f32_32x32x16_bf16 v[48:63], v[80:83], v[64:67], v[16:31]\n\t" \
    "v_exp_f32_e32 v32, v32\n\t" \
    "v_exp_f32_e32 v33, v33\n\t" \
    "v_exp_f32_e32 v34, v34\n\t" \
    "v_mfma_f32_32x32x16_bf16 v[48:63], v[84:87], v[68:71], v[48:63]\n\t" \
    "ds_read_b128 v[80:83], v112 offset:24576\n\t" \
    "ds_read_b128 v[84:87], v112 offset:25600\n\t" \
    "v_exp_f32_e32 v35, v35\n\t" \
    "v_exp_f32_e32 v36, v36\n\t" \
    "v_cvt_pk_bf16_f32 v104, v32, v33\n\t" \
    "v_cvt_pk_bf16_f32 v105, v34, v35\n\t" \
    "v_mfma_f32_32x32x16_bf16 v[0:15], v[92:95], v[108:111], v[0:15]\n\t" \
    "ds_read_b128 v[100:103], v112 offset:19456\n\t" \
    "ds_read_b128 v[96:99], v112 offset:22528\n\t" \
    "v_exp_f32_e32 v37, v37\n\t" \
    "v_exp_f32_e32 v38, v38\n\t" \
    "v_exp_f32_e32 v39, v39\n\t" \
    "v_mfma_f32_16x16x32_bf16 v[76:79], v[72:75], v[108:111], v[76:79]\n\t" \
    "v_exp_f32_e32 v40, v40\n\t" \
    "v_cvt_pk_bf16_f32 v106, v36, v37\n\t" \
    "v_cvt_pk_bf16_f32 v107, v38, v39\n\t" \
    "v_exp_f32_e32 v41, v41\n\t" \
    "v_exp_f32_e32 v42, v42\n\t" \
    "v_mfma_f32_32x32x16_bf16 v[0:15], v[88:91], v[104:107], v[0:15]\n\t" \
    "v_exp_f32_e32 v43, v43\n\t" \
    "v_exp_f32_e32 v44, v44\n\t" \
    "v_exp_f32_e32 v45, v45\n\t" \
    "v_mfma_f32_16x16x32_bf16 v[76:79], v[72:75], v[104:107], v[76:79]\n\t" \
    "v_exp_f32_e32 v46, v46\n\t" \
    "v_exp_f32_e32 v47, v47\n\t" \
    "v_cvt_pk_bf16_f32 v108, v40, v41\n\t" \
    "v_cvt_pk_bf16_f32 v109, v42, v43\n\t" \
    "v_cvt_pk_bf16_f32 v110, v44, v45\n\t" \
    "v_cvt_pk_bf16_f32 v111, v46, v47\n\t" \
    "s_waitcnt lgkmcnt(0)\n\t" \
    "v_mfma_f32_32x32x16_bf16 v[32:47], v[80:83], v[64:67], v[16:31]\n\t" \
    "v_exp_f32_e32 v48, v48\n\t" \
    "v_exp_f32_e32 v49, v49\n\t" \
    "v_exp_f32_e32 v50, v50\n\t" \
    "v_mfma_f32_32x32x16_bf16 v[32:47], v[84:87], v[68:71], v[32:47]\n\t" \
    "ds_read_b128 v[80:83], v112 offset:28672\n\t" \
    "ds_read_b128 v[84:87], v112 offset:29696\n\t" \
    "v_exp_f32_e32 v51, v51\n\t" \
    "v_exp_f32_e32 v52, v52\n\t" \
    "v_cvt_pk_bf16_f32 v104, v48, v49\n\t" \
    "v_cvt_pk_bf16_f32 v105, v50, v51\n\t" \
    "v_mfma_f32_32x32x16_bf16 v[0:15], v[100:103], v[108:111], v[0:15]\n\t" \
    "ds_read_b128 v[92:95], v112 offset:23552\n\t" \
    "ds_read_b128 v[88:91], v112 offset:26624\n\t" \
    "v_exp_f32_e32 v53, v53\n\t" \
    "v_exp_f32_e32 v54, v54\n\t" \
    "v_exp_f32_e32 v55, v55\n\t" \
    "v_mfma_f32_16x16x32_bf16 v[76:79], v[72:75], v[108:111], v[76:79]\n\t" \
    "v_exp_f32_e32 v56, v56\n\t" \
    "v_cvt_pk_bf16_f32 v106, v52, v53\n\t" \
    "v_cvt_pk_bf16_f32 v107, v54, v55\n\t" \
    "v_exp_f32_e32 v57, v57\n\t" \
    "v_exp_f32_e32 v58, v58\n\t" \
    "v_mfma_f32_32x32x16_bf16 v[0:15], v[96:99], v[104:107], v[0:15]\n\t" \
    "v_exp_f32_e32 v59, v59\n\t" \
    "v_exp_f32_e32 v60, v60\n\t" \
    "v_exp_f32_e32 v61, v61\n\t" \
    "v_mfma_f32_16x16x32_bf16 v[76:79], v[72:75], v[104:107], v[76:79]\n\t" \
    "v_exp_f32_e32 v62, v62\n\t" \
    "v_exp_f32_e32 v63, v63\n\t" \
    "v_cvt_pk_bf16_f32 v108, v56, v57\n\t" \
    "v_cvt_pk_bf16_f32 v109, v58, v59\n\t" \
    "v_cvt_pk_bf16_f32 v110, v60, v61\n\t" \
    "v_cvt_pk_bf16_f32 v111, v62, v63\n\t" \
    "s_setprio 0\n\t" \
    "s_cmp_eq_u32 %[hl], 0\n\t" \
    "s_cbranch_scc1 11f\n\t" \
    "s_waitcnt vmcnt(1)\n\t" \
    "11:\n" \
    "s_waitcnt lgkmcnt(0)\n\t" \
    "s_nop 15\n\t"
#define ATT_XT \
    "s_nop 1\n\t" \
    "s_waitcnt lgkmcnt(0)\n\t" \
    "v_mfma_f32_32x32x16_bf16 v[48:63], v[80:83], v[64:67], v[16:31]\n\t" \
    "v_exp_f32_e32 v32, v32\n\t" \
    "v_exp_f32_e32 v33, v33\n\t" \
    "v_exp_f32_e32 v34, v34\n\t" \
    "v_mfma_f32_32x32x16_bf16 v[48:63], v[84:87], v[68:71], v[48:63]\n\t" \
    "v_exp_f32_e32 v35, v35\n\t" \
    "v_exp_f32_e32 v36, v36\n\t" \
    "v_cvt_pk_bf16_f32 v104, v32, v33\n\t" \
    "v_cvt_pk_bf16_f32 v105, v34, v35\n\t" \
    "v_mfma_f32_32x32x16_bf16 v[0:15], v[92:95], v[108:111], v[0:15]\n\t" \
    "ds_read_b128 v[100:103], v112 offset:27648\n\t" \
    "ds_read_b128 v[96:99], v112 offset:30720\n\t" \
    "v_exp_f32_e32 v37, v37\n\t" \
    "v_exp_f32_e32 v38, v38\n\t" \
    "v_exp_f32_e32 v39, v39\n\t" \
    "v_mfma_f32_16x16x32_bf16 v[76:79], v[72:75], v[108:111], v[76:79]\n\t" \
    "v_exp_f32_e32 v40, v40\n\t" \
    "v_cvt_pk_bf16_f32 v106, v36, v37\n\t" \
    "v_cvt_pk_bf16_f32 v107, v38, v39\n\t" \
    "v_exp_f32_e32 v41, v41\n\t" \
    "v_exp_f32_e32 v42, v42\n\t" \
    "v_mfma_f32_32x32x16_bf16 v[0:15], v[88:91], v[104:107], v[0:15]\n\t" \
    "v_exp_f32_e32 v43, v43\n\t" \
    "v_exp_f32_e32 v44, v44\n\t" \
    "v_exp_f32_e32 v45, v45\n\t" \
    "v_mfma_f32_16x16x32_bf16 v[76:79], v[72:75], v[104:107], v[76:79]\n\t" \
    "v_exp_f32_e32 v46, v46\n\t" \
    "v_exp_f32_e32 v47, v47\n\t" \
    "v_cvt_pk_bf16_f32 v108, v40, v41\n\t" \
    "v_cvt_pk_bf16_f32 v109, v42, v43\n\t" \
    "v_cvt_pk_bf16_f32 v110, v44, v45\n\t" \
    "v_cvt_pk_bf16_f32 v111, v46, v47\n\t" \
    "s_waitcnt lgkmcnt(0)\n\t" \
    "v_exp_f32_e32 v48, v48\n\t" \
    "v_exp_f32_e32 v49, v49\n\t" \
    "v_exp_f32_e32 v50, v50\n\t" \
    "v_exp_f32_e32 v51, v51\n\t" \
    "v_exp_f32_e32 v52, v52\n\t" \
    "v_cvt_pk_bf16_f32 v104, v48, v49\n\t" \
    "v_cvt_pk_bf16_f32 v105, v50, v51\n\t" \
    "v_mfma_f32_32x32x16_bf16 v[0:15], v[100:103], v[108:111], v[0:15]\n\t" \
    "ds_read_b128 v[92:95], v112 offset:31744\n\t" \
    "v_exp_f32_e32 v53, v53\n\t" \
    "v_exp_f32_e32 v54, v54\n\t" \
    "v_exp_f32_e32 v55, v55\n\t" \
    "v_mfma_f32_16x16x32_bf16 v[76:79], v[72:75], v[108:111], v[76:79]\n\t" \
    "v_exp_f32_e32 v56, v56\n\t" \
    "v_cvt_pk_bf16_f32 v106, v52, v53\n\t" \
    "v_cvt_pk_bf16_f32 v107, v54, v55\n\t" \
    "v_exp_f32_e32 v57, v57\n\t" \
    "v_exp_f32_e32 v58, v58\n\t" \
    "v_mfma_f32_32x32x16_bf16 v[0:15], v[96:99], v[104:107], v[0:15]\n\t" \
    "v_exp_f32_e32 v59, v59\n\t" \
    "v_exp_f32_e32 v60, v60\n\t" \
    "v_exp_f32_e32 v61, v61\n\t" \
    "v_mfma_f32_16x16x32_bf16 v[76:79], v[72:75], v[104:107], v[76:79]\n\t" \
    "v_exp_f32_e32 v62, v62\n\t" \
    "v_exp_f32_e32 v63, v63\n\t" \
    "v_cvt_pk_bf16_f32 v108, v56, v57\n\t" \
    "v_cvt_pk_bf16_f32 v109, v58, v59\n\t" \
    "v_cvt_pk_bf16_f32 v110, v60, v61\n\t" \
    "v_cvt_pk_bf16_f32 v111, v62, v63\n\t" \
    "s_waitcnt lgkmcnt(0)\n\t" \
    "v_mfma_f32_32x32x16_bf16 v[0:15], v[92:95], v[108:111], v[0:15]\n\t" \
    "v_mfma_f32_16x16x32_bf16 v[76:79], v[72:75], v[108:111], v[76:79]\n\t" \
    "s_nop 15\n\t"
#define ATT_MAIN \
    "s_mov_b64 s[48:49], %[tab]\n\t" \
    "s_mov_b32 s46, %[k0]\n\t" \
    "s_and_b32 s40, s48, 63\n\t" \
    "s_lshl_b32 s40, s40, 12\n\t" \
    "s_add_u32 s44, %[srclo], s40\n\t" \
    "s_addc_u32 s45, %[srchi], 0\n\t" \
    "s_lshr_b64 s[48:49], s[48:49], 6\n\t" \
    "s_add_u32 s46, s46, 1\n\t" \
    "s_mov_b64 vcc, -1\n\t" \
    "s_waitcnt vmcnt(0)\n\t" \
    "ds_read_b64 v[120:121], v173\n\t" \
    "s_waitcnt lgkmcnt(0)\n\t" \
    "1:\n" \
    "s_cmp_eq_u32 %[cnt], 4\n\t" \
    "s_cbranch_scc1 5f\n\t" \
    "s_waitcnt vmcnt(0)\n\t" \
    "5:\n" \
    "s_waitcnt vmcnt(9)\n\t" \
    "s_barrier\n\t" \
    "s_bitcmp1_b32 %[act], %[cnt]\n\t" \
    "s_cbranch_scc1 30f\n\t" \
    "2:\n" \
    "s_setprio 3\n\t" \
    "s_waitcnt lgkmcnt(0)\n\t" \
    "v_mfma_f32_32x32x16_bf16 v[48:63], v[80:83], v[64:67], v[16:31]\n\t" \
    "v_exp_f32_e32 v32, v32\n\t" \
    "v_exp_f32_e32 v33, v33\n\t" \
    "v_exp_f32_e32 v34, v34\n\t" \
    "v_mfma_f32_32x32x16_bf16 v[48:63], v[84:87], v[68:71], v[48:63]\n\t" \
    "ds_read_b128 v[80:83], v113 offset:0\n\t" \
    "ds_read_b128 v[84:87], v113 offset:1024\n\t" \
    "v_exp_f32_e32 v35, v35\n\t" \
    "v_exp_f32_e32 v36, v36\n\t" \
    "v_cvt_pk_bf16_f32 v104, v32, v33\n\t" \
    "v_cvt_pk_bf16_f32 v105, v34, v35\n\t" \
    "v_mfma_f32_32x32x16_bf16 v[0:15], v[92:95], v[108:111], v[0:15]\n\t" \
    "ds_read_b128 v[100:103], v112 offset:27648\n\t" \
    "ds_read_b128 v[96:99], v112 offset:30720\n\t" \
    "v_exp_f32_e32 v37, v37\n\t" \
    "v_exp_f32_e32 v38, v38\n\t" \
    "v_exp_f32_e32 v39, v39\n\t" \
    "v_mfma_f32_16x16x32_bf16 v[76:79], v[72:75], v[108:111], v[76:79]\n\t" \
    "v_exp_f32_e32 v40, v40\n\t" \
    "v_cvt_pk_bf16_f32 v106, v36, v37\n\t" \
    "v_cvt_pk_bf16_f32 v107, v38, v39\n\t" \
    "v_exp_f32_e32 v41, v41\n\t" \
    "v_exp_f32_e32 v42, v42\n\t" \
    "v_mfma_f32_32x32x16_bf16 v[0:15], v[88:91], v[104:107], v[0:15]\n\t" \
    "s_cbranch_vccz 20f\n\t" \
    "v_cmp_eq_u64_e64 s[42:43], v[120:121], v[140:141]\n\t" \
    "s_mov_b32 m0, %[dst]\n\t" \
    "s_nop 3\n\t" \
    "s_bitcmp1_b64 s[42:43], s46\n\t" \
    "s_cbranch_scc0 9f\n\t" \
    "12:\n" \
    "global_load_lds_dwordx4 v114, s[44:45] offset:0 sc1\n\t" \
    "20:\n" \
    "v_exp_f32_e32 v43, v43\n\t" \
    "v_exp_f32_e32 v44, v44\n\t" \
    "v_exp_f32_e32 v45, v45\n\t" \
    "v_mfma_f32_16x16x32_bf16 v[76:79], v[72:75], v[104:107], v[76:79]\n\t" \
    "v_exp_f32_e32 v46, v46\n\t" \
    "v_exp_f32_e32 v47, v47\n\t" \
    "v_cvt_pk_bf16_f32 v108, v40, v41\n\t" \
    "v_cvt_pk_bf16_f32 v109, v42, v43\n\t" \
    "v_cvt_pk_bf16_f32 v110, v44, v45\n\t" \
    "v_cvt_pk_bf16_f32 v111, v46, v47\n\t" \
    "s_waitcnt lgkmcnt(0)\n\t" \
    "v_mfma_f32_32x32x16_bf16 v[32:47], v[80:83], v[64:67], v[16:31]\n\t" \
    "v_exp_f32_e32 v48, v48\n\t" \
    "v_exp_f32_e32 v49, v49\n\t" \
    "v_exp_f32_e32 v50, v50\n\t" \
    "v_mfma_f32_32x32x16_bf16 v[32:47], v[84:87], v[68:71], v[32:47]\n\t" \
    "ds_read_b128 v[80:83], v113 offset:4096\n\t" \
    "ds_read_b128 v[84:87], v113 offset:5120\n\t" \
    "v_exp_f32_e32 v51, v51\n\t" \
    "v_exp_f32_e32 v52, v52\n\t" \
    "v_cvt_pk_bf16_f32 v104, v48, v49\n\t" \
    "v_cvt_pk_bf16_f32 v105, v50, v51\n\t" \
    "v_mfma_f32_32x32x16_bf16 v[0:15], v[100:103], v[108:111], v[0:15]\n\t" \
    "ds_read_b128 v[92:95], v112 offset:31744\n\t" \
    "ds_read_b128 v[88:91], v113 offset:2048\n\t" \
    "v_exp_f32_e32 v53, v53\n\t" \
    "v_exp_f32_e32 v54, v54\n\t" \
    "v_exp_f32_e32 v55, v55\n\t" \
    "v_mfma_f32_16x16x32_bf16 v[76:79], v[72:75], v[108:111], v[76:79]\n\t" \
    "v_exp_f32_e32 v56, v56\n\t" \
    "v_cvt_pk_bf16_f32 v106, v52, v53\n\t" \
    "v_cvt_pk_bf16_f32 v107, v54, v55\n\t" \
    "v_exp_f32_e32 v57, v57\n\t" \
    "v_exp_f32_e32 v58, v58\n\t" \
    "v_mfma_f32_32x32x16_bf16 v[0:15], v[96:99], v[104:107], v[0:15]\n\t" \
    "s_cbranch_vccz 21f\n\t" \
    "global_load_lds_dwordx4 v114, s[44:45] offset:1024 sc1\n\t" \
    "21:\n" \
    "v_exp_f32_e32 v59, v59\n\t" \
    "v_exp_f32_e32 v60, v60\n\t" \
    "v_exp_f32_e32 v61, v61\n\t" \
    "v_mfma_f32_16x16x32_bf16 v[76:79], v[72:75], v[104:107], v[76:79]\n\t" \
    "v_exp_f32_e32 v62, v62\n\t" \
    "v_exp_f32_e32 v63, v63\n\t" \
    "v_cvt_pk_bf16_f32 v108, v56, v57\n\t" \
    "v_cvt_pk_bf16_f32 v109, v58, v59\n\t" \
    "v_cvt_pk_bf16_f32 v110, v60, v61\n\t" \
    "v_cvt_pk_bf16_f32 v111, v62, v63\n\t" \
    "s_setprio 2\n\t" \
    "s_waitcnt lgkmcnt(0)\n\t" \
    "v_mfma_f32_32x32x16_bf16 v[48:63], v[80:83], v[64:67], v[16:31]\n\t" \
    "v_exp_f32_e32 v32, v32\n\t" \
    "v_exp_f32_e32 v33, v33\n\t" \
    "v_exp_f32_e32 v34, v34\n\t" \
    "v_mfma_f32_32x32x16_bf16 v[48:63], v[84:87], v[68:71], v[48:63]\n\t" \
    "ds_read_b128 v[80:83], v113 offset:8192\n\t" \
    "ds_read_b128 v[84:87], v113 offset:9216\n\t" \
    "v_exp_f32_e32 v35, v35\n\t" \
    "v_exp_f32_e32 v36, v36\n\t" \
    "v_cvt_pk_bf16_f32 v104, v32, v33\n\t" \
    "v_cvt_pk_bf16_f32 v105, v34, v35\n\t" \
    "v_mfma_f32_32x32x16_bf16 v[0:15], v[92:95], v[108:111], v[0:15]\n\t" \
    "ds_read_b128 v[100:103], v113 offset:3072\n\t" \
    "ds_read_b128 v[96:99], v113 offset:6144\n\t" \
    "v_exp_f32_e32 v37, v37\n\t" \
    "v_exp_f32_e32 v38, v38\n\t" \
    "v_exp_f32_e32 v39, v39\n\t" \
    "v_mfma_f32_16x16x32_bf16 v[76:79], v[72:75], v[108:111], v[76:79]\n\t" \
    "v_exp_f32_e32 v40, v40\n\t" \
    "v_cvt_pk_bf16_f32 v106, v36, v37\n\t" \
    "v_cvt_pk_bf16_f32 v107, v38, v39\n\t" \
    "v_exp_f32_e32 v41, v41\n\t" \
    "v_exp_f32_e32 v42, v42\n\t" \
    "v_mfma_f32_32x32x16_bf16 v[0:15], v[88:91], v[104:107], v[0:15]\n\t" \
    "s_cbranch_vccz 22f\n\t" \
    "global_load_lds_dwordx4 v114, s[44:45] offset:2048 sc1\n\t" \
    "22:\n" \
    "v_exp_f32_e32 v43, v43\n\t" \
    "v_exp_f32_e32 v44, v44\n\t" \
    "v_exp_f32_e32 v45, v45\n\t" \
    "v_mfma_f32_16x16x32_bf16 v[76:79], v[72:75], v[104:107], v[76:79]\n\t" \
    "v_exp_f32_e32 v46, v46\n\t" \
    "v_exp_f32_e32 v47, v47\n\t" \
    "v_cvt_pk_bf16_f32 v108, v40, v41\n\t" \
    "v_cvt_pk_bf16_f32 v109, v42, v43\n\t" \
    "v_cvt_pk_bf16_f32 v110, v44, v45\n\t" \
    "v_cvt_pk_bf16_f32 v111, v46, v47\n\t" \
    "s_waitcnt lgkmcnt(0)\n\t" \
    "v_mfma_f32_32x32x16_bf16 v[32:47], v[80:83], v[64:67], v[16:31]\n\t" \
    "v_exp_f32_e32 v48, v48\n\t" \
    "v_exp_f32_e32 v49, v49\n\t" \
    "v_exp_f32_e32 v50, v50\n\t" \
    "v_mfma_f32_32x32x16_bf16 v[32:47], v[84:87], v[68:71], v[32:47]\n\t" \
    "ds_read_b128 v[80:83], v113 offset:12288\n\t" \
    "ds_read_b128 v[84:87], v113 offset:13312\n\t" \
    "v_exp_f32_e32 v51, v51\n\t" \
    "v_exp_f32_e32 v52, v52\n\t" \
    "v_cvt_pk_bf16_f32 v104, v48, v49\n\t" \
    "v_cvt_pk_bf16_f32 v105, v50, v51\n\t" \
    "v_mfma_f32_32x32x16_bf16 v[0:15], v[100:103], v[108:111], v[0:15]\n\t" \
    "ds_read_b128 v[92:95], v113 offset:7168\n\t" \
    "ds_read_b128 v[88:91], v113 offset:10240\n\t" \
    "v_exp_f32_e32 v53, v53\n\t" \
    "v_exp_f32_e32 v54, v54\n\t" \
    "v_exp_f32_e32 v55, v55\n\t" \
    "v_mfma_f32_16x16x32_bf16 v[76:79], v[72:75], v[108:111], v[76:79]\n\t" \
    "v_exp_f32_e32 v56, v56\n\t" \
    "v_cvt_pk_bf16_f32 v106, v52, v53\n\t" \
    "v_cvt_pk_bf16_f32 v107, v54, v55\n\t" \
    "v_exp_f32_e32 v57, v57\n\t" \
    "v_exp_f32_e32 v58, v58\n\t" \
    "v_mfma_f32_32x32x16_bf16 v[0:15], v[96:99], v[104:107], v[0:15]\n\t" \
    "s_cbranch_vccz 23f\n\t" \
    "global_load_lds_dwordx4 v114, s[44:45] offset:3072 sc1\n\t" \
    "global_load_dwordx2 v[120:121], v170, %[ws] sc1\n\t" \
    "23:\n" \
    "v_exp_f32_e32 v59, v59\n\t" \
    "v_exp_f32_e32 v60, v60\n\t" \
    "v_exp_f32_e32 v61, v61\n\t" \
    "v_mfma_f32_16x16x32_bf16 v[76:79], v[72:75], v[104:107], v[76:79]\n\t" \
    "v_exp_f32_e32 v62, v62\n\t" \
    "v_exp_f32_e32 v63, v63\n\t" \
    "v_cvt_pk_bf16_f32 v108, v56, v57\n\t" \
    "v_cvt_pk_bf16_f32 v109, v58, v59\n\t" \
    "v_cvt_pk_bf16_f32 v110, v60, v61\n\t" \
    "v_cvt_pk_bf16_f32 v111, v62, v63\n\t" \
    "s_setprio 1\n\t" \
    "s_waitcnt lgkmcnt(0)\n\t" \
    "v_mfma_f32_32x32x16_bf16 v[48:63], v[80:83], v[64:67], v[16:31]\n\t" \
    "v_exp_f32_e32 v32, v32\n\t" \
    "v_exp_f32_e32 v33, v33\n\t" \
    "v_exp_f32_e32 v34, v34\n\t" \
    "v_mfma_f32_32x32x16_bf16 v[48:63], v[84:87], v[68:71], v[48:63]\n\t" \
    "ds_read_b128 v[80:83], v113 offset:16384\n\t" \
    "ds_read_b128 v[84:87], v113 offset:17408\n\t" \
    "v_exp_f32_e32 v35, v35\n\t" \
    "v_exp_f32_e32 v36, v36\n\t" \
    "v_cvt_pk_bf16_f32 v104, v32, v33\n\t" \
    "v_cvt_pk_bf16_f32 v105, v34, v35\n\t" \
    "v_mfma_f32_32x32x16_bf16 v[0:15], v[92:95], v[108:111], v[0:15]\n\t" \
    "ds_read_b128 v[100:103], v113 offset:11264\n\t" \
    "ds_read_b128 v[96:99], v113 offset:14336\n\t" \
    "v_exp_f32_e32 v37, v37\n\t" \
    "v_exp_f32_e32 v38, v38\n\t" \
    "v_exp_f32_e32 v39, v39\n\t" \
    "v_mfma_f32_16x16x32_bf16 v[76:79], v[72:75], v[108:111], v[76:79]\n\t" \
    "v_exp_f32_e32 v40, v40\n\t" \
    "v_cvt_pk_bf16_f32 v106, v36, v37\n\t" \
    "v_cvt_pk_bf16_f32 v107, v38, v39\n\t" \
    "v_exp_f32_e32 v41, v41\n\t" \
    "v_exp_f32_e32 v42, v42\n\t" \
    "v_mfma_f32_32x32x16_bf16 v[0:15], v[88:91], v[104:107], v[0:15]\n\t" \
    "s_add_u32 %[dst], %[dst], 0x8000\n\t" \
    "s_cmp_eq_u32 %[dst], %[dend]\n\t" \
    "s_cselect_b32 %[dst], %[dbeg], %[dst]\n\t" \
    "s_add_u32 %[nx], %[nx], 0x8000\n\t" \
    "s_cmp_eq_u32 %[nx], 0x18000\n\t" \
    "s_cselect_b32 %[nx], 0, %[nx]\n\t" \
    "s_cmp_eq_u32 %[cnt], 5\n\t" \
    "s_cbranch_scc1 40f\n\t" \
    "24:\n" \
    "v_exp_f32_e32 v43, v43\n\t" \
    "v_exp_f32_e32 v44, v44\n\t" \
    "v_exp_f32_e32 v45, v45\n\t" \
    "v_mfma_f32_16x16x32_bf16 v[76:79], v[72:75], v[104:107], v[76:79]\n\t" \
    "v_exp_f32_e32 v46, v46\n\t" \
    "v_exp_f32_e32 v47, v47\n\t" \
    "v_cvt_pk_bf16_f32 v108, v40, v41\n\t" \
    "v_cvt_pk_bf16_f32 v109, v42, v43\n\t" \
    "v_cvt_pk_bf16_f32 v110, v44, v45\n\t" \
    "v_cvt_pk_bf16_f32 v111, v46, v47\n\t" \
    "s_waitcnt lgkmcnt(0)\n\t" \
    "v_mfma_f32_32x32x16_bf16 v[32:47], v[80:83], v[64:67], v[16:31]\n\t" \
    "v_exp_f32_e32 v48, v48\n\t" \
    "v_exp_f32_e32 v49, v49\n\t" \
    "v_exp_f32_e32 v50, v50\n\t" \
    "v_mfma_f32_32x32x16_bf16 v[32:47], v[84:87], v[68:71], v[32:47]\n\t" \
    "ds_read_b128 v[80:83], v113 offset:20480\n\t" \
    "ds_read_b128 v[84:87], v113 offset:21504\n\t" \
    "v_exp_f32_e32 v51, v51\n\t" \
    "v_exp_f32_e32 v52, v52\n\t" \
    "v_cvt_pk_bf16_f32 v104, v48, v49\n\t" \
    "v_cvt_pk_bf16_f32 v105, v50, v51\n\t" \
    "v_mfma_f32_32x32x16_bf16 v[0:15], v[100:103], v[108:111], v[0:15]\n\t" \
    "ds_read_b128 v[92:95], v113 offset:15360\n\t" \
    "ds_read_b128 v[88:91], v113 offset:18432\n\t" \
    "v_exp_f32_e32 v53, v53\n\t" \
    "v_exp_f32_e32 v54, v54\n\t" \
    "v_exp_f32_e32 v55, v55\n\t" \
    "v_mfma_f32_16x16x32_bf16 v[76:79], v[72:75], v[108:111], v[76:79]\n\t" \
    "v_exp_f32_e32 v56, v56\n\t" \
    "v_cvt_pk_bf16_f32 v106, v52, v53\n\t" \
    "v_cvt_pk_bf16_f32 v107, v54, v55\n\t" \
    "v_exp_f32_e32 v57, v57\n\t" \
    "v_exp_f32_e32 v58, v58\n\t" \
    "v_mfma_f32_32x32x16_bf16 v[0:15], v[96:99], v[104:107], v[0:15]\n\t" \
    "s_cmp_eq_u32 %[cnt], 5\n\t" \
    "s_cbranch_scc1 41f\n\t" \
    "25:\n" \
    "v_exp_f32_e32 v59, v59\n\t" \
    "v_exp_f32_e32 v60, v60\n\t" \
    "v_exp_f32_e32 v61, v61\n\t" \
    "v_mfma_f32_16x16x32_bf16 v[76:79], v[72:75], v[104:107], v[76:79]\n\t" \
    "v_exp_f32_e32 v62, v62\n\t" \
    "v_exp_f32_e32 v63, v63\n\t" \
    "v_cvt_pk_bf16_f32 v108, v56, v57\n\t" \
    "v_cvt_pk_bf16_f32 v109, v58, v59\n\t" \
    "v_cvt_pk_bf16_f32 v110, v60, v61\n\t" \
    "v_cvt_pk_bf16_f32 v111, v62, v63\n\t" \
    "s_setprio 0\n\t" \
    "s_waitcnt lgkmcnt(0)\n\t" \
    "v_mfma_f32_32x32x16_bf16 v[48:63], v[80:83], v[64:67], v[16:31]\n\t" \
    "v_exp_f32_e32 v32, v32\n\t" \
    "v_exp_f32_e32 v33, v33\n\t" \
    "v_exp_f32_e32 v34, v34\n\t" \
    "v_mfma_f32_32x32x16_bf16 v[48:63], v[84:87], v[68:71], v[48:63]\n\t" \
    "ds_read_b128 v[80:83], v113 offset:24576\n\t" \
    "ds_read_b128 v[84:87], v113 offset:25600\n\t" \
    "v_exp_f32_e32 v35, v35\n\t" \
    "v_exp_f32_e32 v36, v36\n\t" \
    "v_cvt_pk_bf16_f32 v104, v32, v33\n\t" \
    "v_cvt_pk_bf16_f32 v105, v34, v35\n\t" \
    "v_mfma_f32_32x32x16_bf16 v[0:15], v[92:95], v[108:111], v[0:15]\n\t" \
    "ds_read_b128 v[100:103], v113 offset:19456\n\t" \
    "ds_read_b128 v[96:99], v113 offset:22528\n\t" \
    "v_exp_f32_e32 v37, v37\n\t" \
    "v_exp_f32_e32 v38, v38\n\t" \
    "v_exp_f32_e32 v39, v39\n\t" \
    "v_mfma_f32_16x16x32_bf16 v[76:79], v[72:75], v[108:111], v[76:79]\n\t" \
    "v_exp_f32_e32 v40, v40\n\t" \
    "v_cvt_pk_bf16_f32 v106, v36, v37\n\t" \
    "v_cvt_pk_bf16_f32 v107, v38, v39\n\t" \
    "v_exp_f32_e32 v41, v41\n\t" \
    "v_exp_f32_e32 v42, v42\n\t" \
    "v_mfma_f32_32x32x16_bf16 v[0:15], v[88:91], v[104:107], v[0:15]\n\t" \
    "s_and_b32 s40, s48, 63\n\t" \
    "s_lshl_b32 s40, s40, 12\n\t" \
    "s_add_u32 s44, %[srclo], s40\n\t" \
    "s_addc_u32 s45, %[srchi], 0\n\t" \
    "s_lshr_b64 s[48:49], s[48:49], 6\n\t" \
    "s_add_u32 s46, s46, 1\n\t" \
    "s_cmp_eq_u32 %[cnt], 5\n\t" \
    "s_cbranch_scc1 42f\n\t" \
    "26:\n" \
    "v_exp_f32_e32 v43, v43\n\t" \
    "v_exp_f32_e32 v44, v44\n\t" \
    "v_exp_f32_e32 v45, v45\n\t" \
    "v_mfma_f32_16x16x32_bf16 v[76:79], v[72:75], v[104:107], v[76:79]\n\t" \
    "v_exp_f32_e32 v46, v46\n\t" \
    "v_exp_f32_e32 v47, v47\n\t" \
    "v_cvt_pk_bf16_f32 v108, v40, v41\n\t" \
    "v_cvt_pk_bf16_f32 v109, v42, v43\n\t" \
    "v_cvt_pk_bf16_f32 v110, v44, v45\n\t" \
    "v_cvt_pk_bf16_f32 v111, v46, v47\n\t" \
    "s_waitcnt lgkmcnt(0)\n\t" \
    "v_mfma_f32_32x32x16_bf16 v[32:47], v[80:83], v[64:67], v[16:31]\n\t" \
    "v_exp_f32_e32 v48, v48\n\t" \
    "v_exp_f32_e32 v49, v49\n\t" \
    "v_exp_f32_e32 v50, v50\n\t" \
    "v_mfma_f32_32x32x16_bf16 v[32:47], v[84:87], v[68:71], v[32:47]\n\t" \
    "ds_read_b128 v[80:83], v113 offset:28672\n\t" \
    "ds_read_b128 v[84:87], v113 offset:29696\n\t" \
    "v_exp_f32_e32 v51, v51\n\t" \
    "v_exp_f32_e32 v52, v52\n\t" \
    "v_cvt_pk_bf16_f32 v104, v48, v49\n\t" \
    "v_cvt_pk_bf16_f32 v105, v50, v51\n\t" \
    "v_mfma_f32_32x32x16_bf16 v[0:15], v[100:103], v[108:111], v[0:15]\n\t" \
    "ds_read_b128 v[92:95], v113 offset:23552\n\t" \
    "ds_read_b128 v[88:91], v113 offset:26624\n\t" \
    "v_exp_f32_e32 v53, v53\n\t" \
    "v_exp_f32_e32 v54, v54\n\t" \
    "v_exp_f32_e32 v55, v55\n\t" \
    "v_mfma_f32_16x16x32_bf16 v[76:79], v[72:75], v[108:111], v[76:79]\n\t" \
    "v_exp_f32_e32 v56, v56\n\t" \
    "v_cvt_pk_bf16_f32 v106, v52, v53\n\t" \
    "v_cvt_pk_bf16_f32 v107, v54, v55\n\t" \
    "v_exp_f32_e32 v57, v57\n\t" \
    "v_exp_f32_e32 v58, v58\n\t" \
    "v_mfma_f32_32x32x16_bf16 v[0:15], v[96:99], v[104:107], v[0:15]\n\t" \
    "s_cmp_eq_u32 %[cnt], 5\n\t" \
    "s_cbranch_scc1 43f\n\t" \
    "27:\n" \
    "v_mov_b32_e32 v112, v113\n\t" \
    "v_add_u32_e32 v113, %[nx], v115\n\t" \
    "s_sub_u32 %[cnt], %[cnt], 1\n\t" \
    "s_cmp_lg_u32 %[cnt], 0\n\t" \
    "v_exp_f32_e32 v59, v59\n\t" \
    "v_exp_f32_e32 v60, v60\n\t" \
    "v_exp_f32_e32 v61, v61\n\t" \
    "v_mfma_f32_16x16x32_bf16 v[76:79], v[72:75], v[104:107], v[76:79]\n\t" \
    "v_exp_f32_e32 v62, v62\n\t" \
    "v_exp_f32_e32 v63, v63\n\t" \
    "v_cvt_pk_bf16_f32 v108, v56, v57\n\t" \
    "v_cvt_pk_bf16_f32 v109, v58, v59\n\t" \
    "v_cvt_pk_bf16_f32 v110, v60, v61\n\t" \
    "v_cvt_pk_bf16_f32 v111, v62, v63\n\t" \
    "s_cbranch_scc1 1b\n\t" \
    "7:\n" \
    "s_setprio 0\n\t" \
    "s_waitcnt lgkmcnt(0)\n\t" \
    "v_mfma_f32_32x32x16_bf16 v[48:63], v[80:83], v[64:67], v[16:31]\n\t" \
    "v_exp_f32_e32 v32, v32\n\t" \
    "v_exp_f32_e32 v33, v33\n\t" \
    "v_exp_f32_e32 v34, v34\n\t" \
    "v_mfma_f32_32x32x16_bf16 v[48:63], v[84:87], v[68:71], v[48:63]\n\t" \
    "v_exp_f32_e32 v35, v35\n\t" \
    "v_exp_f32_e32 v36, v36\n\t" \
    "v_cvt_pk_bf16_f32 v104, v32, v33\n\t" \
    "v_cvt_pk_bf16_f32 v105, v34, v35\n\t" \
    "v_mfma_f32_32x32x16_bf16 v[0:15], v[92:95], v[108:111], v[0:15]\n\t" \
    "ds_read_b128 v[100:103], v112 offset:27648\n\t" \
    "ds_read_b128 v[96:99], v112 offset:30720\n\t" \
    "v_exp_f32_e32 v37, v37\n\t" \
    "v_exp_f32_e32 v38, v38\n\t" \
    "v_exp_f32_e32 v39, v39\n\t" \
    "v_mfma_f32_16x16x32_bf16 v[76:79], v[72:75], v[108:111], v[76:79]\n\t" \
    "v_exp_f32_e32 v40, v40\n\t" \
    "v_cvt_pk_bf16_f32 v106, v36, v37\n\t" \
    "v_cvt_pk_bf16_f32 v107, v38, v39\n\t" \
    "v_exp_f32_e32 v41, v41\n\t" \
    "v_exp_f32_e32 v42, v42\n\t" \
    "v_mfma_f32_32x32x16_bf16 v[0:15], v[88:91], v[104:107], v[0:15]\n\t" \
    "v_exp_f32_e32 v43, v43\n\t" \
    "v_exp_f32_e32 v44, v44\n\t" \
    "v_exp_f32_e32 v45, v45\n\t" \
    "v_mfma_f32_16x16x32_bf16 v[76:79], v[72:75], v[104:107], v[76:79]\n\t" \
    "v_exp_f32_e32 v46, v46\n\t" \
    "v_exp_f32_e32 v47, v47\n\t" \
    "v_cvt_pk_bf16_f32 v108, v40, v41\n\t" \
    "v_cvt_pk_bf16_f32 v109, v42, v43\n\t" \
    "v_cvt_pk_bf16_f32 v110, v44, v45\n\t" \
    "v_cvt_pk_bf16_f32 v111, v46, v47\n\t" \
    "s_waitcnt lgkmcnt(0)\n\t" \
    "v_exp_f32_e32 v48, v48\n\t" \
    "v_exp_f32_e32 v49, v49\n\t" \
    "v_exp_f32_e32 v50, v50\n\t" \
    "v_exp_f32_e32 v51, v51\n\t" \
    "v_exp_f32_e32 v52, v52\n\t" \
    "v_cvt_pk_bf16_f32 v104, v48, v49\n\t" \
    "v_cvt_pk_bf16_f32 v105, v50, v51\n\t" \
    "v_mfma_f32_32x32x16_bf16 v[0:15], v[100:103], v[108:111], v[0:15]\n\t" \
    "ds_read_b128 v[92:95], v112 offset:31744\n\t" \
    "v_exp_f32_e32 v53, v53\n\t" \
    "v_exp_f32_e32 v54, v54\n\t" \
    "v_exp_f32_e32 v55, v55\n\t" \
    "v_mfma_f32_16x16x32_bf16 v[76:79], v[72:75], v[108:111], v[76:79]\n\t" \
    "v_exp_f32_e32 v56, v56\n\t" \
    "v_cvt_pk_bf16_f32 v106, v52, v53\n\t" \
    "v_cvt_pk_bf16_f32 v107, v54, v55\n\t" \
    "v_exp_f32_e32 v57, v57\n\t" \
    "v_exp_f32_e32 v58, v58\n\t" \
    "v_mfma_f32_32x32x16_bf16 v[0:15], v[96:99], v[104:107], v[0:15]\n\t" \
    "v_exp_f32_e32 v59, v59\n\t" \
    "v_exp_f32_e32 v60, v60\n\t" \
    "v_exp_f32_e32 v61, v61\n\t" \
    "v_mfma_f32_16x16x32_bf16 v[76:79], v[72:75], v[104:107], v[76:79]\n\t" \
    "v_exp_f32_e32 v62, v62\n\t" \
    "v_exp_f32_e32 v63, v63\n\t" \
    "v_cvt_pk_bf16_f32 v108, v56, v57\n\t" \
    "v_cvt_pk_bf16_f32 v109, v58, v59\n\t" \
    "v_cvt_pk_bf16_f32 v110, v60, v61\n\t" \
    "v_cvt_pk_bf16_f32 v111, v62, v63\n\t" \
    "s_waitcnt lgkmcnt(0)\n\t" \
    "v_mfma_f32_32x32x16_bf16 v[0:15], v[92:95], v[108:111], v[0:15]\n\t" \
    "v_mfma_f32_16x16x32_bf16 v[76:79], v[72:75], v[108:111], v[76:79]\n\t" \
    "s_nop 15\n\t" \
    "s_branch 99f\n\t" \
    "9:\n" \
    "s_mov_b32 s47, 0x40000\n\t" \
    "10:\n" \
    "s_sub_u32 s47, s47, 1\n\t" \
    "s_cbranch_scc1 12b\n\t" \
    "global_load_dwordx2 v[120:121], v170, %[ws] sc1\n\t" \
    "s_waitcnt vmcnt(0)\n\t" \
    "v_cmp_eq_u64_e64 s[42:43], v[120:121], v[140:141]\n\t" \
    "s_nop 4\n\t" \
    "s_bitcmp1_b64 s[42:43], s46\n\t" \
    "s_cbranch_scc1 12b\n\t" \
    "s_sleep 4\n\t" \
    "s_branch 10b\n\t" \
    "40:\n" \
    "s_add_u32 m0, %[wdst], 0\n\t" \
    "s_nop 0\n\t" \
    "global_load_lds_dwordx4 v116, %[wo01]\n\t" \
    "global_load_lds_dwordx4 v117, %[wo01] offset:1024\n\t" \
    "v_add_u32_e32 v116, 0x20000, v116\n\t" \
    "v_add_u32_e32 v117, 0x20000, v117\n\t" \
    "s_branch 24b\n\t" \
    "41:\n" \
    "s_add_u32 m0, %[wdst], 16384\n\t" \
    "s_nop 0\n\t" \
    "global_load_lds_dwordx4 v116, %[wo01]\n\t" \
    "global_load_lds_dwordx4 v117, %[wo01] offset:1024\n\t" \
    "v_subrev_u32_e32 v116, 0x20000, v116\n\t" \
    "v_subrev_u32_e32 v117, 0x20000, v117\n\t" \
    "s_branch 25b\n\t" \
    "42:\n" \
    "s_add_u32 m0, %[wdst], 32768\n\t" \
    "s_nop 0\n\t" \
    "global_load_lds_dwordx4 v116, %[wo23]\n\t" \
    "global_load_lds_dwordx4 v117, %[wo23] offset:1024\n\t" \
    "v_add_u32_e32 v116, 0x20000, v116\n\t" \
    "v_add_u32_e32 v117, 0x20000, v117\n\t" \
    "s_branch 26b\n\t" \
    "43:\n" \
    "s_add_u32 m0, %[wdst], 49152\n\t" \
    "s_nop 0\n\t" \
    "global_load_lds_dwordx4 v116, %[wo23]\n\t" \
    "global_load_lds_dwordx4 v117, %[wo23] offset:1024\n\t" \
    "global_load_dword v168, v119, %[wo]\n\t" \
    "s_branch 27b\n\t" \
    "30:\n" \
    "s_cmp_eq_u32 %[cnt], 1\n\t" \
    "s_cbranch_scc0 31f\n\t" \
    "s_mov_b64 vcc, 0\n\t" \
    "s_bitcmp1_b32 %[mode], 1\n\t" \
    "s_cbranch_scc1 7b\n\t" \
    "s_branch 2b\n\t" \
    "31:\n" \
    "33:\n" \
    "s_cmp_eq_u32 %[cnt], 5\n\t" \
    "s_cbranch_scc0 34f\n\t" \
    "global_store_dwordx2 v118, v[140:141], %[ws] sc1\n\t" \
    "s_branch 2b\n\t" \
    "34:\n" \
    "s_cmp_eq_u32 %[cnt], 3\n\t" \
    "s_cbranch_scc0 35f\n\t" \
    "global_load_dwordx2 v[168:169], v142, %[ws] sc1\n\t" \
    "s_branch 2b\n\t" \
    "35:\n" \
    "v_cmp_eq_u64_e64 s[42:43], v[168:169], v[140:141]\n\t" \
    "s_nop 4\n\t" \
    "s_cmp_eq_u64 s[42:43], -1\n\t" \
    "s_cbranch_scc0 2b\n\t" \
    "global_load_dwordx4 v[122:125], v143, %[ws] offset:-2048 sc1\n\t" \
    "global_load_dwordx4 v[126:129], v143, %[ws] offset:-1024 sc1\n\t" \
    "global_load_dwordx4 v[130:133], v143, %[ws] offset:0 sc1\n\t" \
    "global_load_dwordx4 v[134:137], v143, %[ws] offset:1024 sc1\n\t" \
    "global_load_dwordx2 v[138:139], v143, %[ws] offset:2048 sc1\n\t" \
    "s_bitset1_b32 %[mode], 2\n\t" \
    "s_branch 2b\n\t" \
    "99:\n"
#define ATT_TMP "v48", "v49", "v50", "v51", "v52", "v53", "v54", "v55", "v56", "v57", "v58", "v59", "v60", "v61", "v62", "v63", "v96", "v97", "v98", "v99", "v100", "v101", "v102", "v103", "v104", "v105", "v106", "v107"

constexpr int K1_X = 0, K1_W = 65536, K1_WB = 24576;
constexpr int K2_SLOT = 32768, K2_QX = 3 * K2_SLOT, K2_FL = 147456;
constexpr int LDS_BYTES = 163840;
constexpr int p3_wslot(int t) { return (t % 6) < 4 ? 98304 + (t % 6) * 16384 : 65536 + ((t % 6) - 4) * 16384; }
constexpr int p3_allow(int t) { return ((t - 2 >= 1 && t + 3 < 16) ? 4 : 0) + (t + 3 < 16 ? 4 : 0) + ((t >= 1 && t + 5 < 16) ? 4 : 0); }

__global__ __launch_bounds__(512, 2) void mk_fused(const u16* __restrict__ xA, const u16* __restrict__ wA, const u16* __restrict__ woA,
                                                   const u16* __restrict__ xB, const u16* __restrict__ wB, const u16* __restrict__ woB, char* ws0, u16* __restrict__ out) {
    char* ws = ws0;
    __shared__ __attribute__((aligned(1024))) char smem[LDS_BYTES];
    const unsigned lds0 = (unsigned)(uintptr_t)(LAS char*)smem;
    const int wid = __builtin_amdgcn_readfirstlane(threadIdx.x >> 6);
    int lane = threadIdx.x & 63;
#define BRX 0
    const int b = blockIdx.x, xcd = b & 7, j = b >> 3, br = (xcd >> 2) ^ BRX, qd = xcd & 3;
    const int mt = (qd & 1) * 4 + (j & 3), hp = (qd >> 1) * 8 + (j >> 2);
    const int mt3 = qd * 4 + (j & 3), nt3 = j >> 2;
    u64 tag;
    u32x4 fq[2][2], fk[2][2], fv[2][2];
    STAMP_AT(0, 0);
    {
        const int g = wid >> 2, wi = wid & 3, mw = wi >> 1, hd = wi & 1;
        const char* x = (const char*)(br ? xB : xA) + (size_t)mt * 256 * 2048;
        const char* w = (const char*)(br ? wB : wA) + hp * 128;
        unsigned vx[4], vw[3];
#pragma unroll
        for (int ii = 0; ii < 4; ++ii) {
            const int row = br ? ((wi >> 1) * 64 + (wi & 1) + ii * 16 + 2 * (lane >> 3)) : (wi * 32 + ii * 8 + (lane >> 3));
            vx[ii] = row * 2048 + (((lane & 7) ^ (((lane >> 4) & 1) | ((ii >> 1) << 1) | ((lane >> 5) << 2))) << 4);
        }
#pragma unroll
        for (int jj = 0; jj < 3; ++jj) {
            const int jw = 12 * g + 3 * wi + jj, n = jw >> 3, k0 = (jw & 7) * 8 + (lane >> 3);
            vw[jj] = k0 * 6144 + n * 2048 + (((lane & 7) ^ ((((lane >> 4) & 1) | ((jw & 1) << 1)) << 1)) << 4);
        }
        const unsigned xdst_o = lds0 + K1_X + (1 - g) * 32768 + wi * 4096;
        const unsigned wdst = lds0 + K1_W + (12 * g + 3 * wi) * 1024;
        const char* xo = x + (size_t)(1 - g) * 128 * 2048;
#define ROTM 4
        const int kt0 = ROTM * (j & 3);
#define STAGE_X(kt, dst) do { const char* s_ = xo + (((kt) + kt0) & 15) * 128; _Pragma("unroll") for (int ii = 0; ii < 4; ++ii) glds16(s_, vx[ii], (dst) + ii * 1024); } while (0)
#define STAGE_W(kt, buf) do { const char* s_ = w + (size_t)(((kt) + kt0) & 15) * 64 * 6144; _Pragma("unroll") for (int jj = 0; jj < 3; ++jj) glds16(s_, vw[jj], wdst + (buf) * K1_WB + jj * 1024); } while (0)
        if (wid == 7) {
            const char* pc; unsigned len;
            asm volatile("s_getpc_b64 %0\n.Lmk_code_begin:\n\ts_mov_b32 %1, .Lmk_code_end-.Lmk_code_begin" : "=s"(pc), "=s"(len));
            const unsigned off = (unsigned)(j * 64 + lane) * 64u;
            asm volatile("s_mov_b32 m0, %2\n\ts_nop 0\n\tglobal_load_lds_dword %0, %1" :: "v"(off < len ? off : 0u), "s"(pc), "s"(lds0 + 159744) : "memory");
        }
        STAGE_X(0, xdst_o); STAGE_W(0, 0); STAGE_W(1, 1);
        if (g == 1) STAGE_X(1, xdst_o + 16384);
        { u64 e; asm volatile("s_load_dwordx2 %0, %1, 0x0\n\ts_waitcnt lgkmcnt(0)" : "=s"(e) : "s"(ws + WS_EP) : "memory");
          e += 0x9E3779B97F4A7C15ull; e = (e ^ (e >> 30)) * 0xBF58476D1CE4E5B9ull; e = (e ^ (e >> 27)) * 0x94D049BB133111EBull; tag = e ^ (e >> 31); }
        const int c16 = lane & 15, g4 = lane >> 4;
        unsigned ax[2], aw[2];
        ax[0] = lds0 + K1_X + g * 32768 + (mw * 64 + 16 * (c16 >> 3) + (c16 & 7)) * 128 + ((g4 ^ (((c16 >> 1) & 1) | ((c16 >> 3) << 1) | (((c16 >> 2) & 1) << 2))) << 4);
        ax[1] = ax[0] ^ 64;
        { const int q4 = c16 >> 2, p = lane & 3;
          aw[0] = lds0 + K1_W + (8 * g4 + q4) * 128 + (((hd * 4 + (p >> 1)) ^ ((((q4 >> 1) & 1) | ((g4 & 1) << 1)) << 1)) << 4) + (p & 1) * 8; aw[1] = aw[0] ^ 32; }
        if (g == 1) WAITBAR_(7); else WAITBAR_(3);
        SA_(1);
        if (g == 1) asm volatile("s_barrier" ::: "memory");
        f32x4 acc[4][6];
#pragma unroll
        for (int m = 0; m < 4; ++m)
#pragma unroll
            for (int n = 0; n < 6; ++n) acc[m][n] = f32x4{};
        SBAR();
        unsigned xp = 0, wr_ = 0, ws_ = 2 * K1_WB;
        int tx = (1 + g + kt0) & 15, tw = (2 + kt0) & 15;
        unsigned dxp = (1 - g) * 16384;
#define RDW(n, ks) wf[n][ks] = cat8(ldstr(awc[(n) & 1] + ((n) >> 1) * 8192 + (ks) * 4096), ldstr(awc[(n) & 1] + ((n) >> 1) * 8192 + (ks) * 4096 + 512))
#define P1_TILE(DOX, DOW, WAIT, LASTBAR) do { \
            bf16x8 xf[4][2], wf[6][2]; \
            unsigned awc[2] = {aw[0] + wr_, aw[1] + wr_}; asm volatile("" : "+v"(awc[0]), "+v"(awc[1])); \
            const char* sx = xo + tx * 128; const char* sw = w + (size_t)tw * 64 * 6144; \
            _Pragma("unroll") for (int s = 0; s < 4; ++s) { \
                xf[s][0] = ldsrd128(ax[0] + xp + (s >> 1) * 4096 + (s & 1) * 1024); xf[s][1] = ldsrd128(ax[1] + xp + (s >> 1) * 4096 + (s & 1) * 1024); \
                RDW((3 * s) >> 1, (3 * s) & 1); \
                if (DOX) glds16(sx, vx[s], xdst_o + dxp + s * 1024); \
                RDW((3 * s + 1) >> 1, (3 * s + 1) & 1); RDW((3 * s + 2) >> 1, (3 * s + 2) & 1); \
                if ((DOW) && s < 3) glds16(sw, vw[s], wdst + ws_ + s * 1024); \
            } \
            WAIT; \
            SBAR(); \
            __builtin_amdgcn_s_setprio(1); \
            _Pragma("unroll") for (int m = 0; m < 4; ++m) \
                _Pragma("unroll") for (int n = 0; n < 6; ++n) { \
                    if (n < 4) { acc[m][n] = MFMA16(wf[n][0], xf[m][0], acc[m][n]); acc[m][n] = MFMA16(wf[n][1], xf[m][1], acc[m][n]); } \
                    else       { acc[m][n] = MFMA16(xf[m][0], wf[n][0], acc[m][n]); acc[m][n] = MFMA16(xf[m][1], wf[n][1], acc[m][n]); } \
                    CHAIN(); \
                } \
            __builtin_amdgcn_s_setprio(0); \
            xp ^= 16384; dxp ^= 16384; ws_ = wr_; wr_ = (wr_ == 2 * K1_WB) ? 0 : wr_ + K1_WB; tx = (tx + 1) & 15; tw = (tw + 1) & 15; \
            SBAR(); \
            if (LASTBAR) asm volatile("s_barrier" ::: "memory"); \
            SBAR(); \
        } while (0)
#pragma unroll 1
        for (int t = 0; t < 14; ++t) P1_TILE(true, true, WAITBAR_(7), true);
        P1_TILE(g == 0, false, if (g == 0) WAITBAR_(4); else WAITBAR_(0), true);
        P1_TILE(false, false, WAITBAR_(0), g == 0);
#undef P1_TILE
#undef RDW
#undef STAGE_X
#undef STAGE_W
        SA_(2); SC_(1);
        FRESH();
#pragma unroll
        for (int mb = 0; mb < 2; ++mb)
#pragma unroll
            for (int u = 0; u < 2; ++u) {
                const f32x4 a = acc[2 * mb + u][0] * QSCALE, c = acc[2 * mb + u][1] * QSCALE;
                fq[mb][u] = u32x4{cvtpk(a[0], a[1]), cvtpk(a[2], a[3]), cvtpk(c[0], c[1]), cvtpk(c[2], c[3])};
                fk[mb][u] = pack44(acc[2 * mb + u][2], acc[2 * mb + u][3]);
                fv[mb][u] = pack44(acc[2 * mb][4 + u], acc[2 * mb + 1][4 + u]);
            }
    }
    const char* wo = (const char*)(br ? woB : woA) + nt3 * 256;
    const int k30 = 2 * nt3;
    {
        const int g = wid >> 2, wr = wid >> 1, hd = wid & 1;
        const int ph1 = 2 * hp + hd, pb1 = mt * 8 + wr * 2;
        const int head = 2 * hp + br, blk = mt * 8 + wid;
#define RBLK(e) ((((e) % 7) + (((e) % 7) >= mt)) * 8 + (e) / 7)
        const char* kv = ws + WS_KV + (size_t)head * 262144;
        unsigned l16 = lane * 16;
        const unsigned dbeg = lds0 + wid * 4096, lbase = lds0 + l16;
        const unsigned fk0 = (unsigned)WS_FK + head * 32 * FS;
        const unsigned fxo = (unsigned)WS_FX + ((hp * 8 + mt) * 8 + wid) * FS, xrec0 = (unsigned)WS_XP + ((hp * 8 + mt) * 8 + wid) * 5120 + 2048;
        const f32x16 z = {};
        f32x16 sa, negm, o;
        u32x16 qol, kvf; u32x4 ph;

#define FIRST_TILE(base, q0, q1) do { const unsigned one2 = (((lane >> 4) & 1) == ((lane >> 2) & 1)) ? 0x3F803F80u : 0u; const u32x4 ones = {one2, one2, one2, one2}; \
            const bf16x8 k0_ = ldsrd128(base), k1_ = ldsrd128((base) + 1024); kvf = cat16(asu(ldsrd128((base) + 4096)), asu(ldsrd128((base) + 5120)), asu(ldsrd128((base) + 2048)), ones); \
            sa = MFMA32(k0_, q0, z); sa = MFMA32(k1_, q1, sa); float m_ = sa[0]; \
            _Pragma("unroll") for (int r = 1; r < 16; ++r) m_ = __builtin_fmaxf(m_, sa[r]); \
            m_ = -swap_max(m_); _Pragma("unroll") for (int r = 0; r < 16; ++r) { negm[r] = m_; sa[r] += m_; } \
            qol = cat16(asu(q0), asu(q1), ones, ones); } while (0)
#define L6_OUT "={v[0:15]}"(o), "+{v[32:47]}"(sa), "+{v[64:79]}"(qol), "+{v[80:95]}"(kvf), "={v[108:111]}"(ph)
#define L6C(HL, BASE) asm volatile(ATT_L6C : L6_OUT, "={v[144:159]}"(h0), "={v[160:167]}"(h1) \
            : "{v[16:31]}"(negm), "{v112}"(BASE), "{v[140:143]}"(u32x4{0u, 0u, FLADDR, (unsigned)WS_HALO + (mt * 32 + ph1) * 6144 + 3072 + l16}), \
              [ws] "s"(ws), [hl] "s"(HL), [fld] "s"(lds0 + K2_FL + wid * 1024) : ATT_TMP, "scc", "memory")
#define FLADDR (fk0 + ((((lane & 7) == 7) ? blk : RBLK(8 * (lane & 7) + wid)) >> 1) * FS)
        const unsigned own = (ph1 * 32 + mt * 4 + wr) * FS;
        const unsigned pv = (lane >> 5) * 1024 + ((lane & 15) + 32 * ((lane >> 4) & 1)) * 16, pq = pv + (lane & 8) * 16;
        if (br == 0) {
            const char* kp = ws + WS_KV + (size_t)((ph1 * 64 + pb1) * 4) * 1024;
#define PUB_KV() do { st16_dev<0>(kp, pq, fk[0][0]); st16_dev<128>(kp, pq, fk[0][1]); st16_dev<2048>(kp, pv, fv[0][0]); st16_dev<2304>(kp, pv, fv[0][1]); \
            st16_dev<0>(kp + 4096, pq, fk[1][0]); st16_dev<128>(kp + 4096, pq, fk[1][1]); st16_dev<2048>(kp + 4096, pv, fv[1][0]); st16_dev<2304>(kp + 4096, pv, fv[1][1]); } while (0)
            if (hd == 1) {
                const char* qp = ws + WS_QF + (size_t)((ph1 * 64 + pb1) * 2) * 1024;
                st16_dev<0>(qp, pq, fq[0][0]); st16_dev<128>(qp, pq, fq[0][1]); st16_dev<2048>(qp, pq, fq[1][0]); st16_dev<2176>(qp, pq, fq[1][1]);
            }
            if (g == 0) PUB_KV();
#pragma unroll
            for (int mb = 0; mb < 2; ++mb) {
                const unsigned a = lds0 + hd * 2 * K2_SLOT + (wr * 2 + mb) * 4096, aq = lds0 + K2_QX + hd * 16384 + (wr * 2 + mb) * 2048 + pq;
                ldswr128(a + pq, fk[mb][0]); ldswr128(a + pq + 128, fk[mb][1]); ldswr128(a + 2048 + pv, fv[mb][0]); ldswr128(a + 2304 + pv, fv[mb][1]);
                if (hd == 0 || g == 0) { ldswr128(aq, fq[mb][0]); ldswr128(aq + 128, fq[mb][1]); }
            }
            BARRIER_L();
            if (g == 0 || hd == 1) {
                drain();
                if (lane == 0) { if (g == 0) flag_set(ws + WS_FK, own, tag); if (hd == 1) flag_set(ws + WS_FQ, own, tag); }
            }
            const unsigned qa = lbase + K2_QX + (1 - g) * 16384 + wid * 2048, cx = lbase + (1 - g) * 2 * K2_SLOT;
            SA_(3); SC_(2);
            u32x16 h0; u32x8 h1;
            { const bf16x8 q0 = ldsrd128(qa), q1 = ldsrd128(qa + 1024); FIRST_TILE(cx, q0, q1); }
            L6C(0, cx);
            SC_(3);
            if (g == 0) {
                asm volatile(ATT_XT : "+{v[0:15]}"(o), "+{v[32:47]}"(sa), "+{v[64:79]}"(qol), "+{v[80:95]}"(kvf), "+{v[108:111]}"(ph) : "{v[16:31]}"(negm), "{v112}"(cx) : ATT_TMP, "memory");
                SC_(4);
                st16_dev<-2048>(ws, xrec0 + l16, quad(o, 0)); st16_dev<-1024>(ws, xrec0 + l16, quad(o, 1)); st16_dev<0>(ws, xrec0 + l16, quad(o, 2)); st16_dev<1024>(ws, xrec0 + l16, quad(o, 3));
                st16_dev<2048>(ws, xrec0 + l16, u32x4{qol[12], __float_as_uint(negm[0]), 0u, 0u});
                SC_(5);
                const bf16x8 q0 = ldsrd128(lbase + K2_QX + wid * 2048), q1 = ldsrd128(lbase + K2_QX + wid * 2048 + 1024);
                FIRST_TILE(lbase, q0, q1);
                L6C(0, lbase);
            } else {
                PUB_KV();
                SC_(4);
                const int b1 = RBLK(wid), b0 = RBLK(wid - 4);
                flag_wait(ws, fk0 + (((lane & 1) ? b1 : b0) >> 1) * FS, tag);
                if (lane == 0) flag_set(ws + WS_FK, own, tag);
                SC_(5);
                glds4k_dev(kv + b1 * 4096, l16, dbeg + K2_SLOT);
                glds4k_dev(kv + b0 * 4096, l16, dbeg + K2_SLOT - 16384);
                glds16_dev(ws, FLADDR, lds0 + K2_FL + wid * 1024);
            }
#undef PUB_KV
        } else {
            if (wr == 3 && mt < 7) {
                const char* p = ws + WS_HALO + (size_t)(((mt + 1) * 32 + ph1) * 6) * 1024;
                st16_dev<0>(p, pq, fk[0][0]); st16_dev<128>(p, pq, fk[0][1]); st16_dev<3072>(p, pq, fk[1][0]); st16_dev<3200>(p, pq, fk[1][1]);
                if (lane >= 32) { st16_dev<1024>(p, pv, fv[0][0]); st16_dev<1280>(p, pv, fv[0][1]); st16_dev<1024>(p + 3072, pv, fv[1][0]); st16_dev<1280>(p + 3072, pv, fv[1][1]); }
            }
            const unsigned ex = lds0 + 2 * K2_SLOT + wid * 12288;
#pragma unroll
            for (int mb = 0; mb < 2; ++mb) {
                const unsigned a = ex + mb * 6144;
                ldswr128(a + pq, fq[mb][0]); ldswr128(a + pq + 128, fq[mb][1]); ldswr128(a + 2048 + pq, fk[mb][0]); ldswr128(a + 2176 + pq, fk[mb][1]);
                ldswr128(a + 4096 + pv, fv[mb][0]); ldswr128(a + 4352 + pv, fv[mb][1]);
            }
            BARRIER_L();
            if (wr == 3 && mt < 7) { drain(); if (lane == 0) flag_set(ws + WS_FH, (ph1 * 8 + mt + 1) * FS, tag); }
            SC_(2);
            LocalPart lp[2];
            const f32x16 band = band_mask(lane);
            bf16x8 bq[2][2];
#pragma unroll
            for (int mb = 0; mb < 2; ++mb) { bq[mb][0] = ldsrd128(ex + mb * 6144 + l16); bq[mb][1] = ldsrd128(ex + mb * 6144 + 1024 + l16); }
#define LOCAL_BLOCK(mb) do { const unsigned a_ = ex + mb * 6144 + l16; \
                local_own(bq[mb][0], bq[mb][1], ldsrd128(a_ + 2048), ldsrd128(a_ + 3072), ldsrd128(a_ + 4096), ldsrd128(a_ + 5120), band, lp[mb]); \
                if (wr > 0) local_prev(bq[mb][0], bq[mb][1], ldsrd128(a_ - 24576 + 2048), ldsrd128(a_ - 24576 + 3072), ldsrd128(a_ - 24576 + 5120), lane, lp[mb]); \
                if (wr > 0 || mt == 0) local_fin(lp[mb]); } while (0)
            LOCAL_BLOCK(0);
            SC_(3);
            const int hl = (wid < 2) ? mt : 0;
            const unsigned fh = (unsigned)WS_FH + (ph1 * 8 + mt) * FS;
            const int b0 = RBLK(wid), b1 = RBLK(8 + wid);
            const int q = lane & 3;
            const u64 fm = flag_wait_some(ws, q == 0 ? fk0 - 1024 * FS + (blk >> 1) * FS : q == 1 ? fk0 + (b0 >> 1) * FS : q == 2 ? fk0 + (b1 >> 1) * FS : fh, tag, 3);
            SC_(4);
            glds4k_dev(kv + b0 * 4096, l16, dbeg);
            const char* qp = ws + WS_QF + (size_t)((head * 64 + blk) * 2) * 1024;
            glds16_dev(qp, l16, ex); glds16_dev(qp + 1024, l16, ex + 1024);
            if (fm & 4) glds4k_dev(kv + b1 * 4096, l16, dbeg + K2_SLOT);
            LOCAL_BLOCK(1);
#undef LOCAL_BLOCK
            if (fm & 4) WAITBAR_(4);
            else {
                WAITBAR_(0);
                flag_wait(ws, fk0 + (b1 >> 1) * FS, tag);
                glds4k_dev(kv + b1 * 4096, l16, dbeg + K2_SLOT);
            }
            const bf16x8 q0 = ldsrd128(ex + l16), q1 = ldsrd128(ex + 1024 + l16);
            SA_(3); SC_(5);
            if (hl && !(fm & 8)) flag_wait(ws, fh, tag);
            FIRST_TILE(lbase, q0, q1);
            u32x16 h0; u32x8 h1;
            L6C(hl, lbase);
            if (hl) {
                local_prev(bq[0][0], bq[0][1], asbf(u32x4{h0[0], h0[1], h0[2], h0[3]}), asbf(u32x4{h0[4], h0[5], h0[6], h0[7]}), asbf(u32x4{h0[8], h0[9], h0[10], h0[11]}), lane, lp[0]);
                local_prev(bq[1][0], bq[1][1], asbf(u32x4{h0[12], h0[13], h0[14], h0[15]}), asbf(u32x4{h1[0], h1[1], h1[2], h1[3]}), asbf(u32x4{h1[4], h1[5], h1[6], h1[7]}), lane, lp[1]);
                local_fin(lp[0]); local_fin(lp[1]);
            }
            const char* op = ws + WS_OF + (size_t)(((64 + pb1) * 32 + ph1) * 2) * 1024;
            st16_dev<0>(op, l16, quad(lp[0].o, 0)); st16_dev<1024>(op, l16, quad(lp[0].o, 1)); st16_dev<0>(op + 65536, l16, quad(lp[1].o, 0)); st16_dev<1024>(op + 65536, l16, quad(lp[1].o, 1));
        }
#undef FIRST_TILE
#undef L6_OUT
#undef L6C
        FRESH();
        l16 = lane * 16;
        SA_(4); SC_(6);
        const int isx = br & (1 - g);
        unsigned cnt = 7, dst = dbeg + 2 * K2_SLOT, nx = K2_SLOT;
        unsigned mode = isx << 1;
        const unsigned act = 2u | (isx * 12u) | ((br | (1 - g)) << 5);
        u64 tab = (u64)blk << 42;
#pragma unroll
        for (int k = 1; k < 7; ++k) tab |= (u64)RBLK(8 * k + wid) << (6 * k);
        tab >>= 6 + 6 * br;
        u32x8 adr;
        { const int k = 8 * wid + (lane >> 4), c = lane & 15;
          adr = u32x8{lds0 + l16, lds0 + l16 + K2_SLOT, l16, lds0 + l16, (unsigned)(k * 2048 + ((c ^ ((k & 7) << 1)) << 4)), (unsigned)((k + 4) * 2048 + ((c ^ (((k + 4) & 7) << 1)) << 4) - 1024),
                      br ? (unsigned)WS_FO + ((64 + pb1 + (lane & 1)) * 32 + ph1) * FS : fxo, 0u};
          const int i = (mt3 * 8 + wid) * 12 + ((lane * 3) >> 4); adr[7] = ((((4 + (i >> 7) + k30) & 15) * 64 + ((i & 127) >> 1)) * 2048 + (i & 1) * 128); }
        f32x16 xo; f32x2 xl;
#define DBGOUT
        asm volatile(ATT_MAIN
                     : DBGOUT"+{v[0:15]}"(o), "+{v[32:47]}"(sa), "+{v[64:79]}"(qol), "+{v[80:95]}"(kvf), "+{v[108:111]}"(ph), "+{v[112:119]}"(adr), "={v[122:137]}"(xo), "={v[138:139]}"(xl),
                       [cnt] "+s"(cnt), [dst] "+s"(dst), [nx] "+s"(nx), [mode] "+s"(mode)
                     : "{v[16:31]}"(negm), "{v[140:143]}"(u32x4{(unsigned)tag, (unsigned)(tag >> 32), fxo, xrec0 + l16}),
                       "{v[170:173]}"(u32x4{FLADDR, 0u, 0u, lds0 + K2_FL + wid * 1024 + l16}),
                       [srclo] "s"((unsigned)(uintptr_t)kv), [srchi] "s"((unsigned)((uintptr_t)kv >> 32)), [dbeg] "s"(dbeg), [dend] "s"(dbeg + 3 * K2_SLOT),
                       [wo01] "s"(wo + (size_t)k30 * 131072), [wo23] "s"(wo + (size_t)((k30 + 2) & 15) * 131072), [wo] "s"(wo), [wdst] "s"(lds0 + 98304 + wid * 2048), [act] "s"(act), [tab] "s"(tab), [k0] "s"(br), [ws] "s"(ws)
                     : ATT_TMP, "v120", "v121", "v168", "v169", "s40", "s41", "s42", "s43", "s44", "s45", "s46", "s47", "s48", "s49", "s50", "s51", "vcc", "scc", "memory");
        FRESH();
        l16 = lane * 16;
        const unsigned xrec = xrec0 + l16;
        const bf16x8 q0 = asbf(u32x4{qol[0], qol[1], qol[2], qol[3]}), q1 = asbf(u32x4{qol[4], qol[5], qol[6], qol[7]});
        const u32x4 ones = {qol[8], qol[9], qol[10], qol[11]};
        SA_(5); SB_(1);
        float lsum = __uint_as_float(qol[12]);
        bool ok = lsum < 1.0e18f;
        if (isx) {
            if (__builtin_expect(!(mode & 4), 0)) {
                flag_wait(ws, fxo, tag);
                f32x4 a0, a1, a2, a3;
                asm volatile("global_load_dwordx4 %0, %5, %6 offset:-2048 sc1\n\tglobal_load_dwordx4 %1, %5, %6 offset:-1024 sc1\n\tglobal_load_dwordx4 %2, %5, %6 sc1\n\t"
                             "global_load_dwordx4 %3, %5, %6 offset:1024 sc1\n\tglobal_load_dwordx2 %4, %5, %6 offset:2048 sc1\n\ts_waitcnt vmcnt(0)"
                             : "=&v"(a0), "=&v"(a1), "=&v"(a2), "=&v"(a3), "=&v"(xl) : "v"(xrec), "s"(ws) : "memory");
#pragma unroll
                for (int r = 0; r < 4; ++r) { xo[r] = a0[r]; xo[4 + r] = a1[r]; xo[8 + r] = a2[r]; xo[12 + r] = a3[r]; }
            }
            const float nm = __builtin_fminf(negm[0], xl[1]), fa = EXP2(nm - negm[0]), fx = EXP2(nm - xl[1]);
            ok = ok && (xl[0] < 1.0e18f);
            lsum = lsum * fa + xl[0] * fx;
#pragma unroll
            for (int r = 0; r < 16; ++r) o[r] = o[r] * fa + xo[r] * fx;
        }
#pragma unroll
        for (int r = 0; r < 16; ++r) ok = ok && (__builtin_fabsf(o[r]) < 3.0e38f);
        if (__builtin_expect(__any(!ok), 0)) {
            float m = -__builtin_inff();
            f32x4 l;
            for (int pass = 0; pass < 2; ++pass) {
                o = z; l = f32x4{};
                for (int t = 0; t < 64; ++t) {
                    bf16x8 f0, f1, f2, f3;
                    asm volatile("global_load_dwordx4 %0, %4, %5 sc1\n\tglobal_load_dwordx4 %1, %4, %5 offset:1024 sc1\n\tglobal_load_dwordx4 %2, %4, %5 offset:2048 sc1\n\t"
                                 "global_load_dwordx4 %3, %4, %5 offset:3072 sc1\n\ts_waitcnt vmcnt(0)" : "=&v"(f0), "=&v"(f1), "=&v"(f2), "=&v"(f3) : "v"(l16 + t * 4096), "s"(kv) : "memory");
                    f32x16 s = MFMA32(f0, q0, z); s = MFMA32(f1, q1, s);
                    if (pass == 0) {
#pragma unroll
                        for (int r = 0; r < 16; ++r) m = __builtin_fmaxf(m, s[r]);
                    } else {
#pragma unroll
                        for (int r = 0; r < 16; ++r) s[r] = EXP2(s[r] - m);
                        const bf16x8 p0 = asbf(pack8(s, 0)), p1 = asbf(pack8(s, 1));
                        o = MFMA32(f2, p0, o); o = MFMA32(f3, p1, o);
                        l = MFMA16(asbf(ones), p0, l); l = MFMA16(asbf(ones), p1, l);
                    }
                }
                if (pass == 0) m = swap_max(m);
            }
            lsum = l[0];
        }
        const float inv = 1.0f / lsum;
#pragma unroll
        for (int r = 0; r < 16; ++r) o[r] *= inv;
        const char* op = ws + WS_OF + (size_t)((blk * 32 + head) * 2) * 1024;
        st16_dev<0>(op, l16, pack8(o, 0)); st16_dev<1024>(op, l16, pack8(o, 1));
        drain();
        SB_(2);
        if (lane == 0) flag_set(ws + WS_FO, (blk * 32 + head) * FS, tag);
    }
    {
        FRESH();
        const int g = wid >> 2, wi = wid & 3, wm = wi >> 1, wn = wi & 1, g4 = lane >> 4;
        const char* of = ws + WS_OF + (size_t)(br * 64 + mt3 * 4 + wi) * 65536;
        unsigned vw4[2];
#pragma unroll
        for (int ii = 0; ii < 2; ++ii) { const int k = 16 * wi + 4 * ii + g4; vw4[ii] = k * 2048 + (((lane & 15) ^ ((k & 7) << 1)) << 4); }
        const unsigned l16 = lane * 16, sdst = lds0 + wi * 4096;
#define STAGE_O(t) glds4k_dev(of + (((t) + k30) & 15) * 4096, l16, sdst + ((t) & 3) * 16384)
#define STAGE_WO(t, slot) do { const char* s_ = wo + (size_t)(((t) + k30) & 15) * 131072; glds16(s_, vw4[0], sdst + (slot)); glds16(s_, vw4[1], sdst + (slot) + 1024); \
            glds16(s_ + 16384, vw4[0], sdst + (slot) + 2048); glds16(s_ + 16384, vw4[1], sdst + (slot) + 3072); } while (0)
        asm volatile("s_barrier" ::: "memory");
        SB_(3);
        STAGE_WO(5 - g, 65536 + (1 - g) * 16384);
        flag_wait(ws + WS_FO, ((br * 64 + mt3 * 4 + wi) * 32 + 4 * ((lane & 15) >> 1) + 2 * (1 - g) + (lane & 1)) * FS, tag);
        STAGE_O(1 - g);
        if (g == 1) STAGE_O(2);
        SA_(6); SB_(4);
        const unsigned ao = lds0 + g * 16384 + wm * 8192 + (g4 >> 1) * 1024 + ((lane & 15) + 32 * (g4 & 1)) * 16;
        unsigned aw[4];
        { const int q4 = (lane & 15) >> 2, p = lane & 3, k = 16 * (g4 >> 1) + 4 * (g4 & 1) + q4;
          const unsigned a0 = lds0 + g * 16384 + k * 256 + (((8 * wn + (p >> 1)) ^ ((k & 7) << 1)) << 4) + (p & 1) * 8;
#pragma unroll
          for (int nb = 0; nb < 4; ++nb) aw[nb] = a0 ^ (nb << 5); }
        f32x4 acc[4][4];
#pragma unroll
        for (int mb = 0; mb < 4; ++mb)
#pragma unroll
            for (int nb = 0; nb < 4; ++nb) acc[mb][nb] = f32x4{};
        if (g == 1) { WAITBAR_(4); asm volatile("s_barrier" ::: "memory"); } else asm volatile("s_barrier" ::: "memory");
        SB_(5);
        SBAR();
#pragma unroll
        for (int u = 0; u < 8; ++u) {
            bf16x8 of_[4][2], wf[4][2];
            const int to = 2 * u + 3 + g, tw = to + 2;
            const char* so = of + ((to + k30) & 15) * 4096; const char* sw = wo + (size_t)((tw + k30) & 15) * 131072;
            const unsigned dob = sdst + (to & 3) * 16384, dwb = sdst + (g ? p3_wslot(2 * u + 6) : p3_wslot(2 * u + 5));
            const bool doo = u < 6 || (u == 6 && g == 0), dow = (u >= 1 || g == 1) && (u < 5 || (u == 5 && g == 0));
#pragma unroll
            for (int i = 0; i < 8; ++i) {
                const int hh = i >> 2, q = i & 3;
                of_[q][hh] = ldsrd128(ao + ((2 * u) & 3) * 16384 + (q >> 1) * 4096 + (q & 1) * 256 + hh * 2048);
                wf[q][hh] = cat8(ldstr(aw[q] + p3_wslot(2 * u) + hh * 8192), ldstr(aw[q] + p3_wslot(2 * u) + hh * 8192 + 2048));
                if (i < 4) { if (u < 7 && doo) glds16_dev(so + i * 1024, l16, dob + i * 1024); }
                else if (u < 6 && dow) glds16(sw + ((i & 2) ? 16384 : 0), vw4[i & 1], dwb + (i - 4) * 1024);
            }
            if (p3_allow(2 * u) == p3_allow(2 * u + 1)) WAITBAR(p3_allow(2 * u)); else if (g == 0) WAITBAR(p3_allow(2 * u)); else WAITBAR(p3_allow(2 * u + 1));
            SBAR();
            __builtin_amdgcn_s_setprio(1);
#pragma unroll
            for (int mb = 0; mb < 4; ++mb)
#pragma unroll
                for (int nb = 0; nb < 4; ++nb) { acc[mb][nb] = MFMA16(wf[nb][0], of_[mb][0], acc[mb][nb]); acc[mb][nb] = MFMA16(wf[nb][1], of_[mb][1], acc[mb][nb]); CHAIN(); }
            __builtin_amdgcn_s_setprio(0);
            SBAR();
            if (u < 7 || g == 0) asm volatile("s_barrier" ::: "memory");
            SBAR();
        }
        SB_(6);
        FRESH();
        const unsigned xw = lds0 + wid * 8192 + l16, xr = lds0 + (wid ^ 4) * 8192 + l16;
#pragma unroll
        for (int i = 0; i < 8; ++i) { const f32x4 v = g ? acc[i >> 2][i & 3] : acc[2 + (i >> 2)][i & 3]; ldswr128(xw + i * 1024, __builtin_bit_cast(u32x4, v)); }
        BARRIER_L();
        const unsigned st = lds0 + 65536 + wid * 4096;
#pragma unroll
        for (int i = 0; i < 8; ++i) {
            const f32x4 v = (g ? acc[2 + (i >> 2)][i & 3] : acc[i >> 2][i & 3]) + __builtin_bit_cast(f32x4, ldsrd128(xr + i * 1024));
            const int row = 16 * (i >> 2) + (lane & 15), c = 2 * (i & 3) + (g4 >> 1);
            *(LAS u32x2*)(uintptr_t)(st + row * 128 + ((c ^ (row & 7)) << 4) + (g4 & 1) * 8) = u32x2{cvtpk(v[0], v[1]), cvtpk(v[2], v[3])};
        }
        const int blk = mt3 * 4 + 2 * wm + g;
        char* op = (char*)out + br * 2048 + nt3 * 256 + wn * 128 + (lane & 7) * 16;
#pragma unroll
        for (int i = 0; i < 4; ++i) {
            const int r = 8 * i + (lane >> 3), row = br ? ((blk >> 1) * 64 + 2 * r + (blk & 1)) : (blk * 32 + r);
            *(u32x4*)(op + (size_t)row * 4096) = __builtin_bit_cast(u32x4, ldsrd128(st + r * 128 + (((lane & 7) ^ (r & 7)) << 4)));
        }
#undef STAGE_O
#undef STAGE_WO
    }
    STAMP_AT(0, 7);
    if (b == 0 && wid == 0) {
#pragma unroll
        for (int i = 0; i < 2; ++i) { const int u = i * 64 + lane;
            flag_wait(ws0, (unsigned)WS_FK + ((2 * (u >> 3)) * 32 + (u & 7) * 4) * FS, tag);
            flag_wait(ws0, (unsigned)WS_FO + ((64 + (u & 7) * 8) * 32 + 2 * (u >> 3)) * FS, tag); }
        if (lane == 0) flag_set(ws0, (unsigned)WS_EP, tag);
    }
    asm volatile(".Lmk_code_end:");
}

extern "C" void kernel_launch(void* const* d_in, const int* in_sizes, int n_in, void* d_out, int out_size, void* d_ws, size_t ws_size, hipStream_t stream) {
    mk_fused<<<256, 512, 0, stream>>>((const u16*)d_in[0], (const u16*)d_in[1], (const u16*)d_in[2], (const u16*)d_in[3], (const u16*)d_in[4], (const u16*)d_in[5], (char*)d_ws, (u16*)d_out);
}
```

```cpp
#define PROBE 8
#include <hip/hip_runtime.h>
#include <stdint.h>

typedef unsigned short u16;
typedef unsigned long long u64;
typedef __bf16 bf16x8 __attribute__((ext_vector_type(8)));
typedef __bf16 bf16x2 __attribute__((ext_vector_type(2)));
typedef short s16x4 __attribute__((ext_vector_type(4)));
typedef float f32x16 __attribute__((ext_vector_type(16)));
typedef float f32x4 __attribute__((ext_vector_type(4)));
typedef float f32x2 __attribute__((ext_vector_type(2)));
typedef unsigned u32x4 __attribute__((ext_vector_type(4)));
typedef unsigned u32x2 __attribute__((ext_vector_type(2)));
typedef unsigned u32x8 __attribute__((ext_vector_type(8)));
typedef unsigned u32x16 __attribute__((ext_vector_type(16)));
#define LAS __attribute__((address_space(3)))
#define DI __device__ __forceinline__
#define SBAR() __builtin_amdgcn_sched_barrier(0)
#define MFMA32(a, b, c) __builtin_amdgcn_mfma_f32_32x32x16_bf16((a), (b), (c), 0, 0, 0)
#define MFMA16(a, b, c) __builtin_amdgcn_mfma_f32_16x16x32_bf16((a), (b), (c), 0, 0, 0)
#define CHAIN() SBAR()
#define EXP2(x) __builtin_amdgcn_exp2f(x)

constexpr float QSCALE = 0.17677669529663687f * 1.4426950408889634f;

constexpr size_t WS_QF = 0;
constexpr size_t WS_KV = WS_QF + (4u << 20);
constexpr size_t WS_OF = WS_KV + (8u << 20);
constexpr size_t WS_HALO = WS_OF + (8u << 20);
constexpr size_t WS_XP = WS_HALO + (2u << 20);
constexpr size_t WS_FX = WS_XP + (5u << 20);
#define FS 64
constexpr size_t WS_FQ = WS_FX + 1024 * FS;
constexpr size_t WS_FK = WS_FQ + 1024 * FS;
constexpr size_t WS_FO = WS_FK + 1024 * FS;
constexpr size_t WS_FH = WS_FO + 4096 * FS;
constexpr size_t WS_EP = WS_FH + 256 * FS;

#define SLP 1
#define STAMP_AT(K, I)
#define SA_(I)
#define SB_(I)

#define SC_(I)
#undef STAMP_AT
#undef SA_
#define STAMP_AT(K, I) do { pst[I] = (unsigned)__builtin_amdgcn_s_memrealtime(); } while (0)
#define SA_(I) STAMP_AT(0, I)
DI unsigned cvtpk(float lo, float hi) { f32x2 v = {lo, hi}; bf16x2 b = __builtin_convertvector(v, bf16x2); return __builtin_bit_cast(unsigned, b); }
DI u32x4 pack8(const f32x16& a, int s) { u32x4 r; r[0] = cvtpk(a[8 * s], a[8 * s + 1]); r[1] = cvtpk(a[8 * s + 2], a[8 * s + 3]); r[2] = cvtpk(a[8 * s + 4], a[8 * s + 5]); r[3] = cvtpk(a[8 * s + 6], a[8 * s + 7]); return r; }
DI u32x4 pack44(f32x4 a, f32x4 c) { return u32x4{cvtpk(a[0], a[1]), cvtpk(a[2], a[3]), cvtpk(c[0], c[1]), cvtpk(c[2], c[3])}; }
DI u32x4 asu(bf16x8 v) { return __builtin_bit_cast(u32x4, v); }
DI u32x16 cat16(u32x4 a, u32x4 b, u32x4 c, u32x4 d) { return u32x16{a[0], a[1], a[2], a[3], b[0], b[1], b[2], b[3], c[0], c[1], c[2], c[3], d[0], d[1], d[2], d[3]}; }
DI bf16x8 asbf(u32x4 v) { return __builtin_bit_cast(bf16x8, v); }
DI void glds16(const void* sbase, unsigned voff, unsigned lds) {
    asm volatile("s_mov_b32 m0, %2\n\ts_nop 0\n\tglobal_load_lds_dwordx4 %0, %1" :: "v"(voff), "s"(sbase), "s"(lds) : "memory");
}
DI void glds16_dev(const void* sbase, unsigned voff, unsigned lds) {
    asm volatile("s_mov_b32 m0, %2\n\ts_nop 0\n\tglobal_load_lds_dwordx4 %0, %1 sc1" :: "v"(voff), "s"(sbase), "s"(lds) : "memory");
}
DI void glds4k_dev(const void* sbase, unsigned voff, unsigned lds) {
    asm volatile("s_mov_b32 m0, %2\n\ts_nop 0\n\tglobal_load_lds_dwordx4 %0, %1 sc1\n\tglobal_load_lds_dwordx4 %0, %1 offset:1024 sc1\n\t"
                 "global_load_lds_dwordx4 %0, %1 offset:2048 sc1\n\tglobal_load_lds_dwordx4 %0, %1 offset:3072 sc1" :: "v"(voff), "s"(sbase), "s"(lds) : "memory");
}
template <int OFF> DI void st16_dev(const void* sbase, unsigned voff, u32x4 v) {
    asm volatile("global_store_dwordx4 %0, %1, %2 offset:%3 sc1\n\ts_nop 1" :: "v"(voff), "v"(v), "s"(sbase), "i"(OFF) : "memory");
}
DI void drain() { asm volatile("s_waitcnt vmcnt(0)" ::: "memory"); }
DI void flag_set(const void* sbase, unsigned voff, u64 tag) {
    asm volatile("global_store_dwordx2 %0, %1, %2 sc1" :: "v"(voff), "v"(tag), "s"(sbase) : "memory");
}
DI void flag_wait(const void* sbase, unsigned voff, u64 tag) {
    const u64 t0 = __builtin_amdgcn_s_memrealtime();
    for (;;) {
        u64 v;
        asm volatile("global_load_dwordx2 %0, %1, %2 sc1\n\ts_waitcnt vmcnt(0)" : "=v"(v) : "v"(voff), "s"(sbase) : "memory");
        if (__all(v == tag)) break;
        if (__builtin_amdgcn_s_memrealtime() - t0 > 2000000ull) break;
        __builtin_amdgcn_s_sleep(SLP);
    }
}
DI u64 flag_wait_some(const void* sbase, unsigned voff, u64 tag, u64 need) {
    const u64 t0 = __builtin_amdgcn_s_memrealtime();
    for (;;) {
        u64 v;
        asm volatile("global_load_dwordx2 %0, %1, %2 sc1\n\ts_waitcnt vmcnt(0)" : "=v"(v) : "v"(voff), "s"(sbase) : "memory");
        const u64 m = __ballot(v == tag);
        if ((m & need) == need || __builtin_amdgcn_s_memrealtime() - t0 > 2000000ull) return m;
        __builtin_amdgcn_s_sleep(SLP);
    }
}
DI bf16x8 ldsrd128(unsigned addr) { return *(const LAS bf16x8*)(uintptr_t)addr; }
DI void ldswr128(unsigned addr, u32x4 v) { *(LAS u32x4*)(uintptr_t)addr = v; }
DI s16x4 ldstr(unsigned addr) { return __builtin_amdgcn_ds_read_tr16_b64_v4i16((LAS s16x4*)(uintptr_t)addr); }
DI bf16x8 cat8(s16x4 lo, s16x4 hi) { typedef short s16x8 __attribute__((ext_vector_type(8))); s16x8 v = {lo[0], lo[1], lo[2], lo[3], hi[0], hi[1], hi[2], hi[3]}; return __builtin_bit_cast(bf16x8, v); }
DI float swap_max(float m) { auto r = __builtin_amdgcn_permlane32_swap(__float_as_uint(m), __float_as_uint(m), false, false); return __builtin_fmaxf(__uint_as_float(r[0]), __uint_as_float(r[1])); }
DI float swap_add(float m) { auto r = __builtin_amdgcn_permlane32_swap(__float_as_uint(m), __float_as_uint(m), false, false); return __uint_as_float(r[0]) + __uint_as_float(r[1]); }
#define FRESH() asm volatile("" : "+v"(lane), "+s"(ws))
#define BARRIER_L() asm volatile("s_waitcnt lgkmcnt(0)\n\ts_barrier" ::: "memory")
#define WAITBAR_(n) asm volatile("s_waitcnt vmcnt(%0) lgkmcnt(0)\n\ts_barrier" :: "i"(n) : "memory")
#define WAITBAR(n) do { switch (n) { case 0: WAITBAR_(0); break; case 2: WAITBAR_(2); break; case 3: WAITBAR_(3); break; case 4: WAITBAR_(4); break; case 6: WAITBAR_(6); break; \
    case 7: WAITBAR_(7); break; case 8: WAITBAR_(8); break; case 10: WAITBAR_(10); break; case 12: WAITBAR_(12); break; default: WAITBAR_(0); } } while (0)

struct LocalPart { f32x16 o; float m, l; };
DI f32x16 band_mask(int lane) {
    const int i = lane & 31, h = lane >> 5;
    f32x16 c;
#pragma unroll
    for (int r = 0; r < 16; ++r) { const int j = (r & 3) + 8 * (r >> 2) + 4 * h; c[r] = ((unsigned)(i - j) <= 7u) ? 0.f : -__builtin_inff(); }
    return c;
}
DI float max3(float a, float b, float c) { float r; asm("v_max3_f32 %0, %1, %2, %3" : "=v"(r) : "v"(a), "v"(b), "v"(c)); return r; }
template <bool PREV> DI void local_own(bf16x8 q0, bf16x8 q1, bf16x8 k0, bf16x8 k1, bf16x8 v0, bf16x8 v1, bf16x8 kp0, bf16x8 kp1, bf16x8 vp1, const f32x16& band, int lane, LocalPart& P) {
    const f32x16 z = {};
    f32x16 so = MFMA32(k0, q0, band), sp = z;
    if (PREV) sp = MFMA32(kp0, q0, z);
    so = MFMA32(k1, q1, so);
    if (PREV) sp = MFMA32(kp1, q1, sp);
    float m = __builtin_fmaxf(so[0], so[1]);
#pragma unroll
    for (int r = 2; r < 16; r += 2) m = __builtin_fmaxf(__builtin_fmaxf(m, so[r]), so[r + 1]);
    if (PREV) {
        const int d = 4 * (lane >> 5) - (lane & 31);
#pragma unroll
        for (int r = 12; r < 16; ++r) { sp[r] = (d + (r & 3) >= 1) ? sp[r] : -__builtin_inff(); m = __builtin_fmaxf(m, sp[r]); }
    }
    m = swap_max(m);
    float l = 0.f;
#pragma unroll
    for (int r = 0; r < 16; ++r) { so[r] = EXP2(so[r] - m); l += so[r]; }
    P.o = MFMA32(v0, asbf(pack8(so, 0)), z); P.o = MFMA32(v1, asbf(pack8(so, 1)), P.o);
    if (PREV) {
#pragma unroll
        for (int r = 12; r < 16; ++r) { sp[r] = EXP2(sp[r] - m); l += sp[r]; }
        P.o = MFMA32(vp1, asbf(u32x4{0u, 0u, cvtpk(sp[12], sp[13]), cvtpk(sp[14], sp[15])}), P.o);
    }
    P.m = m; P.l = l;
}
DI void local_prev(bf16x8 q0, bf16x8 q1, bf16x8 kp0, bf16x8 kp1, bf16x8 vp1, int lane, LocalPart& P) {
    const int i = lane & 31, h = lane >> 5;
    const f32x16 z = {};
    f32x16 sp = MFMA32(kp0, q0, z); sp = MFMA32(kp1, q1, sp);
    const float NEG = -__builtin_inff();
    float m = P.m;
#pragma unroll
    for (int r = 12; r < 16; ++r) { const int j = (r & 3) + 24 + 4 * h; sp[r] = (j >= i + 25) ? sp[r] : NEG; m = __builtin_fmaxf(m, sp[r]); }
    m = swap_max(m);
    const float f = EXP2(P.m - m);
    float l = P.l * f;
#pragma unroll
    for (int r = 12; r < 16; ++r) { sp[r] = EXP2(sp[r] - m); l += sp[r]; }
#pragma unroll
    for (int r = 0; r < 16; ++r) P.o[r] *= f;
    P.o = MFMA32(vp1, asbf(u32x4{0u, 0u, cvtpk(sp[12], sp[13]), cvtpk(sp[14], sp[15])}), P.o);
    P.m = m; P.l = l;
}
DI void local_fin(LocalPart& P) {
    const float inv = __builtin_amdgcn_rcpf(swap_add(P.l));
#pragma unroll
    for (int r = 0; r < 16; ++r) P.o[r] *= inv;
    const u32x4 o0 = pack8(P.o, 0), o1 = pack8(P.o, 1);
#pragma unroll
    for (int r = 0; r < 4; ++r) { P.o[r] = __uint_as_float(o0[r]); P.o[4 + r] = __uint_as_float(o1[r]); }
}
DI u32x4 quad(const f32x16& a, int i) { return __builtin_bit_cast(u32x4, f32x4{a[4 * i], a[4 * i + 1], a[4 * i + 2], a[4 * i + 3]}); }

#define ATT_L6C \
    "s_nop 1\n\t" \
    "s_cmp_eq_u32 %[hl], 0\n\t" \
    "s_cbranch_scc1 10f\n\t" \
    "global_load_dwordx4 v[144:147], v143, %[ws] offset:-3072 sc1\n\t" \
    "global_load_dwordx4 v[148:151], v143, %[ws] offset:-2048 sc1\n\t" \
    "global_load_dwordx4 v[152:155], v143, %[ws] offset:-1024 sc1\n\t" \
    "global_load_dwordx4 v[156:159], v143, %[ws] offset:0 sc1\n\t" \
    "global_load_dwordx4 v[160:163], v143, %[ws] offset:1024 sc1\n\t" \
    "global_load_dwordx4 v[164:167], v143, %[ws] offset:2048 sc1\n\t" \
    "10:\n" \
    "s_setprio 3\n\t" \
    "s_waitcnt lgkmcnt(0)\n\t" \
    "v_mfma_f32_32x32x16_bf16 v[48:63], v[80:83], v[64:67], v[16:31]\n\t" \
    "v_exp_f32_e32 v32, v32\n\t" \
    "v_exp_f32_e32 v33, v33\n\t" \
    "v_exp_f32_e32 v34, v34\n\t" \
    "v_mfma_f32_32x32x16_bf16 v[48:63], v[84:87], v[68:71], v[48:63]\n\t" \
    "ds_read_b128 v[80:83], v112 offset:8192\n\t" \
    "ds_read_b128 v[84:87], v112 offset:9216\n\t" \
    "v_exp_f32_e32 v35, v35\n\t" \
    "v_exp_f32_e32 v36, v36\n\t" \
    "v_cvt_pk_bf16_f32 v104, v32, v33\n\t" \
    "v_cvt_pk_bf16_f32 v105, v34, v35\n\t" \
    "ds_read_b128 v[100:103], v112 offset:3072\n\t" \
    "ds_read_b128 v[96:99], v112 offset:6144\n\t" \
    "v_exp_f32_e32 v37, v37\n\t" \
    "v_exp_f32_e32 v38, v38\n\t" \
    "v_exp_f32_e32 v39, v39\n\t" \
    "v_exp_f32_e32 v40, v40\n\t" \
    "v_cvt_pk_bf16_f32 v106, v36, v37\n\t" \
    "v_cvt_pk_bf16_f32 v107, v38, v39\n\t" \
    "v_exp_f32_e32 v41, v41\n\t" \
    "v_exp_f32_e32 v42, v42\n\t" \
    "v_mfma_f32_32x32x16_bf16 v[0:15], v[88:91], v[104:107], 0\n\t" \
    "v_exp_f32_e32 v43, v43\n\t" \
    "v_exp_f32_e32 v44, v44\n\t" \
    "v_exp_f32_e32 v45, v45\n\t" \
    "v_mfma_f32_16x16x32_bf16 v[76:79], v[72:75], v[104:107], 0\n\t" \
    "v_exp_f32_e32 v46, v46\n\t" \
    "v_exp_f32_e32 v47, v47\n\t" \
    "v_cvt_pk_bf16_f32 v108, v40, v41\n\t" \
    "v_cvt_pk_bf16_f32 v109, v42, v43\n\t" \
    "v_cvt_pk_bf16_f32 v110, v44, v45\n\t" \
    "v_cvt_pk_bf16_f32 v111, v46, v47\n\t" \
    "s_mov_b32 m0, %[fld]\n\t" \
    "s_nop 0\n\t" \
    "global_load_lds_dwordx4 v142, %[ws] sc1\n\t" \
    "s_waitcnt lgkmcnt(0)\n\t" \
    "v_mfma_f32_32x32x16_bf16 v[32:47], v[80:83], v[64:67], v[16:31]\n\t" \
    "v_exp_f32_e32 v48, v48\n\t" \
    "v_exp_f32_e32 v49, v49\n\t" \
    "v_exp_f32_e32 v50, v50\n\t" \
    "v_mfma_f32_32x32x16_bf16 v[32:47], v[84:87], v[68:71], v[32:47]\n\t" \
    "ds_read_b128 v[80:83], v112 offset:12288\n\t" \
    "ds_read_b128 v[84:87], v112 offset:13312\n\t" \
    "v_exp_f32_e32 v51, v51\n\t" \
    "v_exp_f32_e32 v52, v52\n\t" \
    "v_cvt_pk_bf16_f32 v104, v48, v49\n\t" \
    "v_cvt_pk_bf16_f32 v105, v50, v51\n\t" \
    "v_mfma_f32_32x32x16_bf16 v[0:15], v[100:103], v[108:111], v[0:15]\n\t" \
    "ds_read_b128 v[92:95], v112 offset:7168\n\t" \
    "ds_read_b128 v[88:91], v112 offset:10240\n\t" \
    "v_exp_f32_e32 v53, v53\n\t" \
    "v_exp_f32_e32 v54, v54\n\t" \
    "v_exp_f32_e32 v55, v55\n\t" \
    "v_mfma_f32_16x16x32_bf16 v[76:79], v[72:75], v[108:111], v[76:79]\n\t" \
    "v_exp_f32_e32 v56, v56\n\t" \
    "v_cvt_pk_bf16_f32 v106, v52, v53\n\t" \
    "v_cvt_pk_bf16_f32 v107, v54, v55\n\t" \
    "v_exp_f32_e32 v57, v57\n\t" \
    "v_exp_f32_e32 v58, v58\n\t" \
    "v_mfma_f32_32x32x16_bf16 v[0:15], v[96:99], v[104:107], v[0:15]\n\t" \
    "v_exp_f32_e32 v59, v59\n\t" \
    "v_exp_f32_e32 v60, v60\n\t" \
    "v_exp_f32_e32 v61, v61\n\t" \
    "v_mfma_f32_16x16x32_bf16 v[76:79], v[72:75], v[104:107], v[76:79]\n\t" \
    "v_exp_f32_e32 v62, v62\n\t" \
    "v_exp_f32_e32 v63, v63\n\t" \
    "v_cvt_pk_bf16_f32 v108, v56, v57\n\t" \
    "v_cvt_pk_bf16_f32 v109, v58, v59\n\t" \
    "v_cvt_pk_bf16_f32 v110, v60, v61\n\t" \
    "v_cvt_pk_bf16_f32 v111, v62, v63\n\t" \
    "s_setprio 2\n\t" \
    "s_waitcnt lgkmcnt(0)\n\t" \
    "v_mfma_f32_32x32x16_bf16 v[48:63], v[80:83], v[64:67], v[16:31]\n\t" \
    "v_exp_f32_e32 v32, v32\n\t" \
    "v_exp_f32_e32 v33, v33\n\t" \
    "v_exp_f32_e32 v34, v34\n\t" \
    "v_mfma_f32_32x32x16_bf16 v[48:63], v[84:87], v[68:71], v[48:63]\n\t" \
    "ds_read_b128 v[80:83], v112 offset:16384\n\t" \
    "ds_read_b128 v[84:87], v112 offset:17408\n\t" \
    "v_exp_f32_e32 v35, v35\n\t" \
    "v_exp_f32_e32 v36, v36\n\t" \
    "v_cvt_pk_bf16_f32 v104, v32, v33\n\t" \
    "v_cvt_pk_bf16_f32 v105, v34, v35\n\t" \
    "v_mfma_f32_32x32x16_bf16 v[0:15], v[92:95], v[108:111], v[0:15]\n\t" \
    "ds_read_b128 v[100:103], v112 offset:11264\n\t" \
    "ds_read_b128 v[96:99], v112 offset:14336\n\t" \
    "v_exp_f32_e32 v37, v37\n\t" \
    "v_exp_f32_e32 v38, v38\n\t" \
    "v_exp_f32_e32 v39, v39\n\t" \
    "v_mfma_f32_16x16x32_bf16 v[76:79], v[72:75], v[108:111], v[76:79]\n\t" \
    "v_exp_f32_e32 v40, v40\n\t" \
    "v_cvt_pk_bf16_f32 v106, v36, v37\n\t" \
    "v_cvt_pk_bf16_f32 v107, v38, v39\n\t" \
    "v_exp_f32_e32 v41, v41\n\t" \
    "v_exp_f32_e32 v42, v42\n\t" \
    "v_mfma_f32_32x32x16_bf16 v[0:15], v[88:91], v[104:107], v[0:15]\n\t" \
    "v_exp_f32_e32 v43, v43\n\t" \
    "v_exp_f32_e32 v44, v44\n\t" \
    "v_exp_f32_e32 v45, v45\n\t" \
    "v_mfma_f32_16x16x32_bf16 v[76:79], v[72:75], v[104:107], v[76:79]\n\t" \
    "v_exp_f32_e32 v46, v46\n\t" \
    "v_exp_f32_e32 v47, v47\n\t" \
    "v_cvt_pk_bf16_f32 v108, v40, v41\n\t" \
    "v_cvt_pk_bf16_f32 v109, v42, v43\n\t" \
    "v_cvt_pk_bf16_f32 v110, v44, v45\n\t" \
    "v_cvt_pk_bf16_f32 v111, v46, v47\n\t" \
    "s_waitcnt lgkmcnt(0)\n\t" \
    "v_mfma_f32_32x32x16_bf16 v[32:47], v[80:83], v[64:67], v[16:31]\n\t" \
    "v_exp_f32_e32 v48, v48\n\t" \
    "v_exp_f32_e32 v49, v49\n\t" \
    "v_exp_f32_e32 v50, v50\n\t" \
    "v_mfma_f32_32x32x16_bf16 v[32:47], v[84:87], v[68:71], v[32:47]\n\t" \
    "ds_read_b128 v[80:83], v112 offset:20480\n\t" \
    "ds_read_b128 v[84:87], v112 offset:21504\n\t" \
    "v_exp_f32_e32 v51, v51\n\t" \
    "v_exp_f32_e32 v52, v52\n\t" \
    "v_cvt_pk_bf16_f32 v104, v48, v49\n\t" \
    "v_cvt_pk_bf16_f32 v105, v50, v51\n\t" \
    "v_mfma_f32_32x32x16_bf16 v[0:15], v[100:103], v[108:111], v[0:15]\n\t" \
    "ds_read_b128 v[92:95], v112 offset:15360\n\t" \
    "ds_read_b128 v[88:91], v112 offset:18432\n\t" \
    "v_exp_f32_e32 v53, v53\n\t" \
    "v_exp_f32_e32 v54, v54\n\t" \
    "v_exp_f32_e32 v55, v55\n\t" \
    "v_mfma_f32_16x16x32_bf16 v[76:79], v[72:75], v[108:111], v[76:79]\n\t" \
    "v_exp_f32_e32 v56, v56\n\t" \
    "v_cvt_pk_bf16_f32 v106, v52, v53\n\t" \
    "v_cvt_pk_bf16_f32 v107, v54, v55\n\t" \
    "v_exp_f32_e32 v57, v57\n\t" \
    "v_exp_f32_e32 v58, v58\n\t" \
    "v_mfma_f32_32x32x16_bf16 v[0:15], v[96:99], v[104:107], v[0:15]\n\t" \
    "v_exp_f32_e32 v59, v59\n\t" \
    "v_exp_f32_e32 v60, v60\n\t" \
    "v_exp_f32_e32 v61, v61\n\t" \
    "v_mfma_f32_16x16x32_bf16 v[76:79], v[72:75], v[104:107], v[76:79]\n\t" \
    "v_exp_f32_e32 v62, v62\n\t" \
    "v_exp_f32_e32 v63, v63\n\t" \
    "v_cvt_pk_bf16_f32 v108, v56, v57\n\t" \
    "v_cvt_pk_bf16_f32 v109, v58, v59\n\t" \
    "v_cvt_pk_bf16_f32 v110, v60, v61\n\t" \
    "v_cvt_pk_bf16_f32 v111, v62, v63\n\t" \
    "s_setprio 1\n\t" \
    "s_waitcnt lgkmcnt(0)\n\t" \
    "v_mfma_f32_32x32x16_bf16 v[48:63], v[80:83], v[64:67], v[16:31]\n\t" \
    "v_exp_f32_e32 v32, v32\n\t" \
    "v_exp_f32_e32 v33, v33\n\t" \
    "v_exp_f32_e32 v34, v34\n\t" \
    "v_mfma_f32_32x32x16_bf16 v[48:63], v[84:87], v[68:71], v[48:63]\n\t" \
    "ds_read_b128 v[80:83], v112 offset:24576\n\t" \
    "ds_read_b128 v[84:87], v112 offset:25600\n\t" \
    "v_exp_f32_e32 v35, v35\n\t" \
    "v_exp_f32_e32 v36, v36\n\t" \
    "v_cvt_pk_bf16_f32 v104, v32, v33\n\t" \
    "v_cvt_pk_bf16_f32 v105, v34, v35\n\t" \
    "v_mfma_f32_32x32x16_bf16 v[0:15], v[92:95], v[108:111], v[0:15]\n\t" \
    "ds_read_b128 v[100:103], v112 offset:19456\n\t" \
    "ds_read_b128 v[96:99], v112 offset:22528\n\t" \
    "v_exp_f32_e32 v37, v37\n\t" \
    "v_exp_f32_e32 v38, v38\n\t" \
    "v_exp_f32_e32 v39, v39\n\t" \
    "v_mfma_f32_16x16x32_bf16 v[76:79], v[72:75], v[108:111], v[76:79]\n\t" \
    "v_exp_f32_e32 v40, v40\n\t" \
    "v_cvt_pk_bf16_f32 v106, v36, v37\n\t" \
    "v_cvt_pk_bf16_f32 v107, v38, v39\n\t" \
    "v_exp_f32_e32 v41, v41\n\t" \
    "v_exp_f32_e32 v42, v42\n\t" \
    "v_mfma_f32_32x32x16_bf16 v[0:15], v[88:91], v[104:107], v[0:15]\n\t" \
    "v_exp_f32_e32 v43, v43\n\t" \
    "v_exp_f32_e32 v44, v44\n\t" \
    "v_exp_f32_e32 v45, v45\n\t" \
    "v_mfma_f32_16x16x32_bf16 v[76:79], v[72:75], v[104:107], v[76:79]\n\t" \
    "v_exp_f32_e32 v46, v46\n\t" \
    "v_exp_f32_e32 v47, v47\n\t" \
    "v_cvt_pk_bf16_f32 v108, v40, v41\n\t" \
    "v_cvt_pk_bf16_f32 v109, v42, v43\n\t" \
    "v_cvt_pk_bf16_f32 v110, v44, v45\n\t" \
    "v_cvt_pk_bf16_f32 v111, v46, v47\n\t" \
    "s_waitcnt lgkmcnt(0)\n\t" \
    "v_mfma_f32_32x32x16_bf16 v[32:47], v[80:83], v[64:67], v[16:31]\n\t" \
    "v_exp_f32_e32 v48, v48\n\t" \
    "v_exp_f32_e32 v49, v49\n\t" \
    "v_exp_f32_e32 v50, v50\n\t" \
    "v_mfma_f32_32x32x16_bf16 v[32:47], v[84:87], v[68:71], v[32:47]\n\t" \
    "ds_read_b128 v[80:83], v112 offset:28672\n\t" \
    "ds_read_b128 v[84:87], v112 offset:29696\n\t" \
    "v_exp_f32_e32 v51, v51\n\t" \
    "v_exp_f32_e32 v52, v52\n\t" \
    "v_cvt_pk_bf16_f32 v104, v48, v49\n\t" \
    "v_cvt_pk_bf16_f32 v105, v50, v51\n\t" \
    "v_mfma_f32_32x32x16_bf16 v[0:15], v[100:103], v[108:111], v[0:15]\n\t" \
    "ds_read_b128 v[92:95], v112 offset:23552\n\t" \
    "ds_read_b128 v[88:91], v112 offset:26624\n\t" \
    "v_exp_f32_e32 v53, v53\n\t" \
    "v_exp_f32_e32 v54, v54\n\t" \
    "v_exp_f32_e32 v55, v55\n\t" \
    "v_mfma_f32_16x16x32_bf16 v[76:79], v[72:75], v[108:111], v[76:79]\n\t" \
    "v_exp_f32_e32 v56, v56\n\t" \
    "v_cvt_pk_bf16_f32 v106, v52, v53\n\t" \
    "v_cvt_pk_bf16_f32 v107, v54, v55\n\t" \
    "v_exp_f32_e32 v57, v57\n\t" \
    "v_exp_f32_e32 v58, v58\n\t" \
    "v_mfma_f32_32x32x16_bf16 v[0:15], v[96:99], v[104:107], v[0:15]\n\t" \
    "v_exp_f32_e32 v59, v59\n\t" \
    "v_exp_f32_e32 v60, v60\n\t" \
    "v_exp_f32_e32 v61, v61\n\t" \
    "v_mfma_f32_16x16x32_bf16 v[76:79], v[72:75], v[104:107], v[76:79]\n\t" \
    "v_exp_f32_e32 v62, v62\n\t" \
    "v_exp_f32_e32 v63, v63\n\t" \
    "v_cvt_pk_bf16_f32 v108, v56, v57\n\t" \
    "v_cvt_pk_bf16_f32 v109, v58, v59\n\t" \
    "v_cvt_pk_bf16_f32 v110, v60, v61\n\t" \
    "v_cvt_pk_bf16_f32 v111, v62, v63\n\t" \
    "s_setprio 0\n\t" \
    "s_cmp_eq_u32 %[hl], 0\n\t" \
    "s_cbranch_scc1 11f\n\t" \
    "s_waitcnt vmcnt(1)\n\t" \
    "11:\n" \
    "s_waitcnt lgkmcnt(0)\n\t" \
    "s_nop 15\n\t"
#define ATT_L4 \
    "s_nop 1\n\t" \
    "s_setprio 3\n\t" \
    "s_waitcnt lgkmcnt(0)\n\t" \
    "v_mfma_f32_32x32x16_bf16 v[48:63], v[80:83], v[64:67], v[16:31]\n\t" \
    "v_exp_f32_e32 v32, v32\n\t" \
    "v_exp_f32_e32 v33, v33\n\t" \
    "v_exp_f32_e32 v34, v34\n\t" \
    "v_mfma_f32_32x32x16_bf16 v[48:63], v[84:87], v[68:71], v[48:63]\n\t" \
    "ds_read_b128 v[80:83], v112 offset:8192\n\t" \
    "ds_read_b128 v[84:87], v112 offset:9216\n\t" \
    "v_exp_f32_e32 v35, v35\n\t" \
    "v_exp_f32_e32 v36, v36\n\t" \
    "v_cvt_pk_bf16_f32 v104, v32, v33\n\t" \
    "v_cvt_pk_bf16_f32 v105, v34, v35\n\t" \
    "ds_read_b128 v[100:103], v112 offset:3072\n\t" \
    "ds_read_b128 v[96:99], v112 offset:6144\n\t" \
    "v_exp_f32_e32 v37, v37\n\t" \
    "v_exp_f32_e32 v38, v38\n\t" \
    "v_exp_f32_e32 v39, v39\n\t" \
    "v_exp_f32_e32 v40, v40\n\t" \
    "v_cvt_pk_bf16_f32 v106, v36, v37\n\t" \
    "v_cvt_pk_bf16_f32 v107, v38, v39\n\t" \
    "v_exp_f32_e32 v41, v41\n\t" \
    "v_exp_f32_e32 v42, v42\n\t" \
    "v_mfma_f32_32x32x16_bf16 v[0:15], v[88:91], v[104:107], 0\n\t" \
    "v_exp_f32_e32 v43, v43\n\t" \
    "v_exp_f32_e32 v44, v44\n\t" \
    "v_exp_f32_e32 v45, v45\n\t" \
    "v_mfma_f32_16x16x32_bf16 v[76:79], v[72:75], v[104:107], 0\n\t" \
    "v_exp_f32_e32 v46, v46\n\t" \
    "v_exp_f32_e32 v47, v47\n\t" \
    "v_cvt_pk_bf16_f32 v108, v40, v41\n\t" \
    "v_cvt_pk_bf16_f32 v109, v42, v43\n\t" \
    "v_cvt_pk_bf16_f32 v110, v44, v45\n\t" \
    "v_cvt_pk_bf16_f32 v111, v46, v47\n\t" \
    "s_waitcnt lgkmcnt(0)\n\t" \
    "v_mfma_f32_32x32x16_bf16 v[32:47], v[80:83], v[64:67], v[16:31]\n\t" \
    "v_exp_f32_e32 v48, v48\n\t" \
    "v_exp_f32_e32 v49, v49\n\t" \
    "v_exp_f32_e32 v50, v50\n\t" \
    "v_mfma_f32_32x32x16_bf16 v[32:47], v[84:87], v[68:71], v[32:47]\n\t" \
    "ds_read_b128 v[80:83], v112 offset:12288\n\t" \
    "ds_read_b128 v[84:87], v112 offset:13312\n\t" \
    "v_exp_f32_e32 v51, v51\n\t" \
    "v_exp_f32_e32 v52, v52\n\t" \
    "v_cvt_pk_bf16_f32 v104, v48, v49\n\t" \
    "v_cvt_pk_bf16_f32 v105, v50, v51\n\t" \
    "v_mfma_f32_32x32x16_bf16 v[0:15], v[100:103], v[108:111], v[0:15]\n\t" \
    "ds_read_b128 v[92:95], v112 offset:7168\n\t" \
    "ds_read_b128 v[88:91], v112 offset:10240\n\t" \
    "v_exp_f32_e32 v53, v53\n\t" \
    "v_exp_f32_e32 v54, v54\n\t" \
    "v_exp_f32_e32 v55, v55\n\t" \
    "v_mfma_f32_16x16x32_bf16 v[76:79], v[72:75], v[108:111], v[76:79]\n\t" \
    "v_exp_f32_e32 v56, v56\n\t" \
    "v_cvt_pk_bf16_f32 v106, v52, v53\n\t" \
    "v_cvt_pk_bf16_f32 v107, v54, v55\n\t" \
    "v_exp_f32_e32 v57, v57\n\t" \
    "v_exp_f32_e32 v58, v58\n\t" \
    "v_mfma_f32_32x32x16_bf16 v[0:15], v[96:99], v[104:107], v[0:15]\n\t" \
    "v_exp_f32_e32 v59, v59\n\t" \
    "v_exp_f32_e32 v60, v60\n\t" \
    "v_exp_f32_e32 v61, v61\n\t" \
    "v_mfma_f32_16x16x32_bf16 v[76:79], v[72:75], v[104:107], v[76:79]\n\t" \
    "v_exp_f32_e32 v62, v62\n\t" \
    "v_exp_f32_e32 v63, v63\n\t" \
    "v_cvt_pk_bf16_f32 v108, v56, v57\n\t" \
    "v_cvt_pk_bf16_f32 v109, v58, v59\n\t" \
    "v_cvt_pk_bf16_f32 v110, v60, v61\n\t" \
    "v_cvt_pk_bf16_f32 v111, v62, v63\n\t" \
    "s_setprio 2\n\t" \
    "s_waitcnt lgkmcnt(0)\n\t" \
    "v_mfma_f32_32x32x16_bf16 v[48:63], v[80:83], v[64:67], v[16:31]\n\t" \
    "v_exp_f32_e32 v32, v32\n\t" \
    "v_exp_f32_e32 v33, v33\n\t" \
    "v_exp_f32_e32 v34, v34\n\t" \
    "v_mfma_f32_32x32x16_bf16 v[48:63], v[84:87], v[68:71], v[48:63]\n\t" \
    "ds_read_b128 v[80:83], v112 offset:16384\n\t" \
    "ds_read_b128 v[84:87], v112 offset:17408\n\t" \
    "v_exp_f32_e32 v35, v35\n\t" \
    "v_exp_f32_e32 v36, v36\n\t" \
    "v_cvt_pk_bf16_f32 v104, v32, v33\n\t" \
    "v_cvt_pk_bf16_f32 v105, v34, v35\n\t" \
    "v_mfma_f32_32x32x16_bf16 v[0:15], v[92:95], v[108:111], v[0:15]\n\t" \
    "ds_read_b128 v[100:103], v112 offset:11264\n\t" \
    "ds_read_b128 v[96:99], v112 offset:14336\n\t" \
    "v_exp_f32_e32 v37, v37\n\t" \
    "v_exp_f32_e32 v38, v38\n\t" \
    "v_exp_f32_e32 v39, v39\n\t" \
    "v_mfma_f32_16x16x32_bf16 v[76:79], v[72:75], v[108:111], v[76:79]\n\t" \
    "v_exp_f32_e32 v40, v40\n\t" \
    "v_cvt_pk_bf16_f32 v106, v36, v37\n\t" \
    "v_cvt_pk_bf16_f32 v107, v38, v39\n\t" \
    "v_exp_f32_e32 v41, v41\n\t" \
    "v_exp_f32_e32 v42, v42\n\t" \
    "v_mfma_f32_32x32x16_bf16 v[0:15], v[88:91], v[104:107], v[0:15]\n\t" \
    "v_exp_f32_e32 v43, v43\n\t" \
    "v_exp_f32_e32 v44, v44\n\t" \
    "v_exp_f32_e32 v45, v45\n\t" \
    "v_mfma_f32_16x16x32_bf16 v[76:79], v[72:75], v[104:107], v[76:79]\n\t" \
    "v_exp_f32_e32 v46, v46\n\t" \
    "v_exp_f32_e32 v47, v47\n\t" \
    "v_cvt_pk_bf16_f32 v108, v40, v41\n\t" \
    "v_cvt_pk_bf16_f32 v109, v42, v43\n\t" \
    "v_cvt_pk_bf16_f32 v110, v44, v45\n\t" \
    "v_cvt_pk_bf16_f32 v111, v46, v47\n\t" \
    "s_waitcnt lgkmcnt(0)\n\t" \
    "v_mfma_f32_32x32x16_bf16 v[32:47], v[80:83], v[64:67], v[16:31]\n\t" \
    "v_exp_f32_e32 v48, v48\n\t" \
    "v_exp_f32_e32 v49, v49\n\t" \
    "v_exp_f32_e32 v50, v50\n\t" \
    "v_mfma_f32_32x32x16_bf16 v[32:47], v[84:87], v[68:71], v[32:47]\n\t" \
    "ds_read_b128 v[80:83], v112 offset:20480\n\t" \
    "ds_read_b128 v[84:87], v112 offset:21504\n\t" \
    "v_exp_f32_e32 v51, v51\n\t" \
    "v_exp_f32_e32 v52, v52\n\t" \
    "v_cvt_pk_bf16_f32 v104, v48, v49\n\t" \
    "v_cvt_pk_bf16_f32 v105, v50, v51\n\t" \
    "v_mfma_f32_32x32x16_bf16 v[0:15], v[100:103], v[108:111], v[0:15]\n\t" \
    "ds_read_b128 v[92:95], v112 offset:15360\n\t" \
    "ds_read_b128 v[88:91], v112 offset:18432\n\t" \
    "v_exp_f32_e32 v53, v53\n\t" \
    "v_exp_f32_e32 v54, v54\n\t" \
    "v_exp_f32_e32 v55, v55\n\t" \
    "v_mfma_f32_16x16x32_bf16 v[76:79], v[72:75], v[108:111], v[76:79]\n\t" \
    "v_exp_f32_e32 v56, v56\n\t" \
    "v_cvt_pk_bf16_f32 v106, v52, v53\n\t" \
    "v_cvt_pk_bf16_f32 v107, v54, v55\n\t" \
    "v_exp_f32_e32 v57, v57\n\t" \
    "v_exp_f32_e32 v58, v58\n\t" \
    "v_mfma_f32_32x32x16_bf16 v[0:15], v[96:99], v[104:107], v[0:15]\n\t" \
    "v_exp_f32_e32 v59, v59\n\t" \
    "v_exp_f32_e32 v60, v60\n\t" \
    "v_exp_f32_e32 v61, v61\n\t" \
    "v_mfma_f32_16x16x32_bf16 v[76:79], v[72:75], v[104:107], v[76:79]\n\t" \
    "v_exp_f32_e32 v62, v62\n\t" \
    "v_exp_f32_e32 v63, v63\n\t" \
    "v_cvt_pk_bf16_f32 v108, v56, v57\n\t" \
    "v_cvt_pk_bf16_f32 v109, v58, v59\n\t" \
    "v_cvt_pk_bf16_f32 v110, v60, v61\n\t" \
    "v_cvt_pk_bf16_f32 v111, v62, v63\n\t" \
    "s_setprio 0\n\t" \
    "s_waitcnt lgkmcnt(0)\n\t" \
    "s_nop 15\n\t"
#define ATT_L2 \
    "s_nop 1\n\t" \
    "s_setprio 1\n\t" \
    "s_waitcnt lgkmcnt(0)\n\t" \
    "v_mfma_f32_32x32x16_bf16 v[48:63], v[80:83], v[64:67], v[16:31]\n\t" \
    "v_exp_f32_e32 v32, v32\n\t" \
    "v_exp_f32_e32 v33, v33\n\t" \
    "v_exp_f32_e32 v34, v34\n\t" \
    "v_mfma_f32_32x32x16_bf16 v[48:63], v[84:87], v[68:71], v[48:63]\n\t" \
    "ds_read_b128 v[80:83], v112 offset:24576\n\t" \
    "ds_read_b128 v[84:87], v112 offset:25600\n\t" \
    "v_exp_f32_e32 v35, v35\n\t" \
    "v_exp_f32_e32 v36, v36\n\t" \
    "v_cvt_pk_bf16_f32 v104, v32, v33\n\t" \
    "v_cvt_pk_bf16_f32 v105, v34, v35\n\t" \
    "v_mfma_f32_32x32x16_bf16 v[0:15], v[92:95], v[108:111], v[0:15]\n\t" \
    "ds_read_b128 v[100:103], v112 offset:19456\n\t" \
    "ds_read_b128 v[96:99], v112 offset:22528\n\t" \
    "v_exp_f32_e32 v37, v37\n\t" \
    "v_exp_f32_e32 v38, v38\n\t" \
    "v_exp_f32_e32 v39, v39\n\t" \
    "v_mfma_f32_16x16x32_bf16 v[76:79], v[72:75], v[108:111], v[76:79]\n\t" \
    "v_exp_f32_e32 v40, v40\n\t" \
    "v_cvt_pk_bf16_f32 v106, v36, v37\n\t" \
    "v_cvt_pk_bf16_f32 v107, v38, v39\n\t" \
    "v_exp_f32_e32 v41, v41\n\t" \
    "v_exp_f32_e32 v42, v42\n\t" \
    "v_mfma_f32_32x32x16_bf16 v[0:15], v[88:91], v[104:107], v[0:15]\n\t" \
    "v_exp_f32_e32 v43, v43\n\t" \
    "v_exp_f32_e32 v44, v44\n\t" \
    "v_exp_f32_e32 v45, v45\n\t" \
    "v_mfma_f32_16x16x32_bf16 v[76:79], v[72:75], v[104:107], v[76:79]\n\t" \
    "v_exp_f32_e32 v46, v46\n\t" \
    "v_exp_f32_e32 v47, v47\n\t" \
    "v_cvt_pk_bf16_f32 v108, v40, v41\n\t" \
    "v_cvt_pk_bf16_f32 v109, v42, v43\n\t" \
    "v_cvt_pk_bf16_f32 v110, v44, v45\n\t" \
    "v_cvt_pk_bf16_f32 v111, v46, v47\n\t" \
    "s_waitcnt lgkmcnt(0)\n\t" \
    "v_mfma_f32_32x32x16_bf16 v[32:47], v[80:83], v[64:67], v[16:31]\n\t" \
    "v_exp_f32_e32 v48, v48\n\t" \
    "v_exp_f32_e32 v49, v49\n\t" \
    "v_exp_f32_e32 v50, v50\n\t" \
    "v_mfma_f32_32x32x16_bf16 v[32:47], v[84:87], v[68:71], v[32:47]\n\t" \
    "ds_read_b128 v[80:83], v112 offset:28672\n\t" \
    "ds_read_b128 v[84:87], v112 offset:29696\n\t" \
    "v_exp_f32_e32 v51, v51\n\t" \
    "v_exp_f32_e32 v52, v52\n\t" \
    "v_cvt_pk_bf16_f32 v104, v48, v49\n\t" \
    "v_cvt_pk_bf16_f32 v105, v50, v51\n\t" \
    "v_mfma_f32_32x32x16_bf16 v[0:15], v[100:103], v[108:111], v[0:15]\n\t" \
    "ds_read_b128 v[92:95], v112 offset:23552\n\t" \
    "ds_read_b128 v[88:91], v112 offset:26624\n\t" \
    "v_exp_f32_e32 v53, v53\n\t" \
    "v_exp_f32_e32 v54, v54\n\t" \
    "v_exp_f32_e32 v55, v55\n\t" \
    "v_mfma_f32_16x16x32_bf16 v[76:79], v[72:75], v[108:111], v[76:79]\n\t" \
    "v_exp_f32_e32 v56, v56\n\t" \
    "v_cvt_pk_bf16_f32 v106, v52, v53\n\t" \
    "v_cvt_pk_bf16_f32 v107, v54, v55\n\t" \
    "v_exp_f32_e32 v57, v57\n\t" \
    "v_exp_f32_e32 v58, v58\n\t" \
    "v_mfma_f32_32x32x16_bf16 v[0:15], v[96:99], v[104:107], v[0:15]\n\t" \
    "v_exp_f32_e32 v59, v59\n\t" \
    "v_exp_f32_e32 v60, v60\n\t" \
    "v_exp_f32_e32 v61, v61\n\t" \
    "v_mfma_f32_16x16x32_bf16 v[76:79], v[72:75], v[104:107], v[76:79]\n\t" \
    "v_exp_f32_e32 v62, v62\n\t" \
    "v_exp_f32_e32 v63, v63\n\t" \
    "v_cvt_pk_bf16_f32 v108, v56, v57\n\t" \
    "v_cvt_pk_bf16_f32 v109, v58, v59\n\t" \
    "v_cvt_pk_bf16_f32 v110, v60, v61\n\t" \
    "v_cvt_pk_bf16_f32 v111, v62, v63\n\t" \
    "s_setprio 0\n\t" \
    "s_waitcnt lgkmcnt(0)\n\t" \
    "s_nop 15\n\t"
#define ATT_XT \
    "s_nop 1\n\t" \
    "s_waitcnt lgkmcnt(0)\n\t" \
    "v_mfma_f32_32x32x16_bf16 v[48:63], v[80:83], v[64:67], v[16:31]\n\t" \
    "v_exp_f32_e32 v32, v32\n\t" \
    "v_exp_f32_e32 v33, v33\n\t" \
    "v_exp_f32_e32 v34, v34\n\t" \
    "v_mfma_f32_32x32x16_bf16 v[48:63], v[84:87], v[68:71], v[48:63]\n\t" \
    "v_exp_f32_e32 v35, v35\n\t" \
    "v_exp_f32_e32 v36, v36\n\t" \
    "v_cvt_pk_bf16_f32 v104, v32, v33\n\t" \
    "v_cvt_pk_bf16_f32 v105, v34, v35\n\t" \
    "v_mfma_f32_32x32x16_bf16 v[0:15], v[92:95], v[108:111], v[0:15]\n\t" \
    "ds_read_b128 v[100:103], v112 offset:27648\n\t" \
    "ds_read_b128 v[96:99], v112 offset:30720\n\t" \
    "v_exp_f32_e32 v37, v37\n\t" \
    "v_exp_f32_e32 v38, v38\n\t" \
    "v_exp_f32_e32 v39, v39\n\t" \
    "v_mfma_f32_16x16x32_bf16 v[76:79], v[72:75], v[108:111], v[76:79]\n\t" \
    "v_exp_f32_e32 v40, v40\n\t" \
    "v_cvt_pk_bf16_f32 v106, v36, v37\n\t" \
    "v_cvt_pk_bf16_f32 v107, v38, v39\n\t" \
    "v_exp_f32_e32 v41, v41\n\t" \
    "v_exp_f32_e32 v42, v42\n\t" \
    "v_mfma_f32_32x32x16_bf16 v[0:15], v[88:91], v[104:107], v[0:15]\n\t" \
    "v_exp_f32_e32 v43, v43\n\t" \
    "v_exp_f32_e32 v44, v44\n\t" \
    "v_exp_f32_e32 v45, v45\n\t" \
    "v_mfma_f32_16x16x32_bf16 v[76:79], v[72:75], v[104:107], v[76:79]\n\t" \
    "v_exp_f32_e32 v46, v46\n\t" \
    "v_exp_f32_e32 v47, v47\n\t" \
    "v_cvt_pk_bf16_f32 v108, v40, v41\n\t" \
    "v_cvt_pk_bf16_f32 v109, v42, v43\n\t" \
    "v_cvt_pk_bf16_f32 v110, v44, v45\n\t" \
    "v_cvt_pk_bf16_f32 v111, v46, v47\n\t" \
    "s_waitcnt lgkmcnt(0)\n\t" \
    "v_exp_f32_e32 v48, v48\n\t" \
    "v_exp_f32_e32 v49, v49\n\t" \
    "v_exp_f32_e32 v50, v50\n\t" \
    "v_exp_f32_e32 v51, v51\n\t" \
    "v_exp_f32_e32 v52, v52\n\t" \
    "v_cvt_pk_bf16_f32 v104, v48, v49\n\t" \
    "v_cvt_pk_bf16_f32 v105, v50, v51\n\t" \
    "v_mfma_f32_32x32x16_bf16 v[0:15], v[100:103], v[108:111], v[0:15]\n\t" \
    "ds_read_b128 v[92:95], v112 offset:31744\n\t" \
    "v_exp_f32_e32 v53, v53\n\t" \
    "v_exp_f32_e32 v54, v54\n\t" \
    "v_exp_f32_e32 v55, v55\n\t" \
    "v_mfma_f32_16x16x32_bf16 v[76:79], v[72:75], v[108:111], v[76:79]\n\t" \
    "v_exp_f32_e32 v56, v56\n\t" \
    "v_cvt_pk_bf16_f32 v106, v52, v53\n\t" \
    "v_cvt_pk_bf16_f32 v107, v54, v55\n\t" \
    "v_exp_f32_e32 v57, v57\n\t" \
    "v_exp_f32_e32 v58, v58\n\t" \
    "v_mfma_f32_32x32x16_bf16 v[0:15], v[96:99], v[104:107], v[0:15]\n\t" \
    "v_exp_f32_e32 v59, v59\n\t" \
    "v_exp_f32_e32 v60, v60\n\t" \
    "v_exp_f32_e32 v61, v61\n\t" \
    "v_mfma_f32_16x16x32_bf16 v[76:79], v[72:75], v[104:107], v[76:79]\n\t" \
    "v_exp_f32_e32 v62, v62\n\t" \
    "v_exp_f32_e32 v63, v63\n\t" \
    "v_cvt_pk_bf16_f32 v108, v56, v57\n\t" \
    "v_cvt_pk_bf16_f32 v109, v58, v59\n\t" \
    "v_cvt_pk_bf16_f32 v110, v60, v61\n\t" \
    "v_cvt_pk_bf16_f32 v111, v62, v63\n\t" \
    "s_waitcnt lgkmcnt(0)\n\t" \
    "v_mfma_f32_32x32x16_bf16 v[0:15], v[92:95], v[108:111], v[0:15]\n\t" \
    "v_mfma_f32_16x16x32_bf16 v[76:79], v[72:75], v[108:111], v[76:79]\n\t" \
    "s_nop 15\n\t"
#define ATT_MAIN \
    "s_mov_b64 s[48:49], %[tab]\n\t" \
    "s_mov_b32 s46, %[k0]\n\t" \
    "s_and_b32 s40, s48, 63\n\t" \
    "s_lshl_b32 s40, s40, 12\n\t" \
    "s_add_u32 s44, %[srclo], s40\n\t" \
    "s_addc_u32 s45, %[srchi], 0\n\t" \
    "s_lshr_b64 s[48:49], s[48:49], 6\n\t" \
    "s_add_u32 s46, s46, 1\n\t" \
    "s_mov_b64 vcc, -1\n\t" \
    "s_bitcmp1_b32 %[mode], 3\n\t" \
    "s_cbranch_scc1 16f\n\t" \
    "s_waitcnt vmcnt(0)\n\t" \
    "16:\n" \
    "s_waitcnt vmcnt(8)\n\t" \
    "ds_read_b64 v[120:121], v173\n\t" \
    "s_waitcnt lgkmcnt(0)\n\t" \
    "s_branch 17f\n\t" \
    "1:\n" \
    "s_cmp_eq_u32 %[cnt], 4\n\t" \
    "s_cbranch_scc1 5f\n\t" \
    "s_waitcnt vmcnt(0)\n\t" \
    "5:\n" \
    "s_waitcnt vmcnt(9)\n\t" \
    "17:\n" \
    "s_barrier\n\t" \
    "s_bitcmp1_b32 %[act], %[cnt]\n\t" \
    "s_cbranch_scc1 30f\n\t" \
    "2:\n" \
    "s_setprio 3\n\t" \
    "s_waitcnt lgkmcnt(0)\n\t" \
    "v_mfma_f32_32x32x16_bf16 v[48:63], v[80:83], v[64:67], v[16:31]\n\t" \
    "v_exp_f32_e32 v32, v32\n\t" \
    "v_exp_f32_e32 v33, v33\n\t" \
    "v_exp_f32_e32 v34, v34\n\t" \
    "v_mfma_f32_32x32x16_bf16 v[48:63], v[84:87], v[68:71], v[48:63]\n\t" \
    "ds_read_b128 v[80:83], v113 offset:0\n\t" \
    "ds_read_b128 v[84:87], v113 offset:1024\n\t" \
    "v_exp_f32_e32 v35, v35\n\t" \
    "v_exp_f32_e32 v36, v36\n\t" \
    "v_cvt_pk_bf16_f32 v104, v32, v33\n\t" \
    "v_cvt_pk_bf16_f32 v105, v34, v35\n\t" \
    "v_mfma_f32_32x32x16_bf16 v[0:15], v[92:95], v[108:111], v[0:15]\n\t" \
    "ds_read_b128 v[100:103], v112 offset:27648\n\t" \
    "ds_read_b128 v[96:99], v112 offset:30720\n\t" \
    "v_exp_f32_e32 v37, v37\n\t" \
    "v_exp_f32_e32 v38, v38\n\t" \
    "v_exp_f32_e32 v39, v39\n\t" \
    "v_mfma_f32_16x16x32_bf16 v[76:79], v[72:75], v[108:111], v[76:79]\n\t" \
    "v_exp_f32_e32 v40, v40\n\t" \
    "v_cvt_pk_bf16_f32 v106, v36, v37\n\t" \
    "v_cvt_pk_bf16_f32 v107, v38, v39\n\t" \
    "v_exp_f32_e32 v41, v41\n\t" \
    "v_exp_f32_e32 v42, v42\n\t" \
    "v_mfma_f32_32x32x16_bf16 v[0:15], v[88:91], v[104:107], v[0:15]\n\t" \
    "s_cbranch_vccz 20f\n\t" \
    "v_cmp_eq_u64_e64 s[42:43], v[120:121], v[140:141]\n\t" \
    "s_mov_b32 m0, %[dst]\n\t" \
    "s_nop 3\n\t" \
    "s_bitcmp1_b64 s[42:43], s46\n\t" \
    "s_cbranch_scc0 9f\n\t" \
    "12:\n" \
    "global_load_lds_dwordx4 v114, s[44:45] offset:0 sc1\n\t" \
    "20:\n" \
    "v_exp_f32_e32 v43, v43\n\t" \
    "v_exp_f32_e32 v44, v44\n\t" \
    "v_exp_f32_e32 v45, v45\n\t" \
    "v_mfma_f32_16x16x32_bf16 v[76:79], v[72:75], v[104:107], v[76:79]\n\t" \
    "v_exp_f32_e32 v46, v46\n\t" \
    "v_exp_f32_e32 v47, v47\n\t" \
    "v_cvt_pk_bf16_f32 v108, v40, v41\n\t" \
    "v_cvt_pk_bf16_f32 v109, v42, v43\n\t" \
    "v_cvt_pk_bf16_f32 v110, v44, v45\n\t" \
    "v_cvt_pk_bf16_f32 v111, v46, v47\n\t" \
    "s_waitcnt lgkmcnt(0)\n\t" \
    "v_mfma_f32_32x32x16_bf16 v[32:47], v[80:83], v[64:67], v[16:31]\n\t" \
    "v_exp_f32_e32 v48, v48\n\t" \
    "v_exp_f32_e32 v49, v49\n\t" \
    "v_exp_f32_e32 v50, v50\n\t" \
    "v_mfma_f32_32x32x16_bf16 v[32:47], v[84:87], v[68:71], v[32:47]\n\t" \
    "ds_read_b128 v[80:83], v113 offset:4096\n\t" \
    "ds_read_b128 v[84:87], v113 offset:5120\n\t" \
    "v_exp_f32_e32 v51, v51\n\t" \
    "v_exp_f32_e32 v52, v52\n\t" \
    "v_cvt_pk_bf16_f32 v104, v48, v49\n\t" \
    "v_cvt_pk_bf16_f32 v105, v50, v51\n\t" \
    "v_mfma_f32_32x32x16_bf16 v[0:15], v[100:103], v[108:111], v[0:15]\n\t" \
    "ds_read_b128 v[92:95], v112 offset:31744\n\t" \
    "ds_read_b128 v[88:91], v113 offset:2048\n\t" \
    "v_exp_f32_e32 v53, v53\n\t" \
    "v_exp_f32_e32 v54, v54\n\t" \
    "v_exp_f32_e32 v55, v55\n\t" \
    "v_mfma_f32_16x16x32_bf16 v[76:79], v[72:75], v[108:111], v[76:79]\n\t" \
    "v_exp_f32_e32 v56, v56\n\t" \
    "v_cvt_pk_bf16_f32 v106, v52, v53\n\t" \
    "v_cvt_pk_bf16_f32 v107, v54, v55\n\t" \
    "v_exp_f32_e32 v57, v57\n\t" \
    "v_exp_f32_e32 v58, v58\n\t" \
    "v_mfma_f32_32x32x16_bf16 v[0:15], v[96:99], v[104:107], v[0:15]\n\t" \
    "s_cbranch_vccz 21f\n\t" \
    "global_load_lds_dwordx4 v114, s[44:45] offset:1024 sc1\n\t" \
    "21:\n" \
    "v_exp_f32_e32 v59, v59\n\t" \
    "v_exp_f32_e32 v60, v60\n\t" \
    "v_exp_f32_e32 v61, v61\n\t" \
    "v_mfma_f32_16x16x32_bf16 v[76:79], v[72:75], v[104:107], v[76:79]\n\t" \
    "v_exp_f32_e32 v62, v62\n\t" \
    "v_exp_f32_e32 v63, v63\n\t" \
    "v_cvt_pk_bf16_f32 v108, v56, v57\n\t" \
    "v_cvt_pk_bf16_f32 v109, v58, v59\n\t" \
    "v_cvt_pk_bf16_f32 v110, v60, v61\n\t" \
    "v_cvt_pk_bf16_f32 v111, v62, v63\n\t" \
    "s_setprio 2\n\t" \
    "s_waitcnt lgkmcnt(0)\n\t" \
    "v_mfma_f32_32x32x16_bf16 v[48:63], v[80:83], v[64:67], v[16:31]\n\t" \
    "v_exp_f32_e32 v32, v32\n\t" \
    "v_exp_f32_e32 v33, v33\n\t" \
    "v_exp_f32_e32 v34, v34\n\t" \
    "v_mfma_f32_32x32x16_bf16 v[48:63], v[84:87], v[68:71], v[48:63]\n\t" \
    "ds_read_b128 v[80:83], v113 offset:8192\n\t" \
    "ds_read_b128 v[84:87], v113 offset:9216\n\t" \
    "v_exp_f32_e32 v35, v35\n\t" \
    "v_exp_f32_e32 v36, v36\n\t" \
    "v_cvt_pk_bf16_f32 v104, v32, v33\n\t" \
    "v_cvt_pk_bf16_f32 v105, v34, v35\n\t" \
    "v_mfma_f32_32x32x16_bf16 v[0:15], v[92:95], v[108:111], v[0:15]\n\t" \
    "ds_read_b128 v[100:103], v113 offset:3072\n\t" \
    "ds_read_b128 v[96:99], v113 offset:6144\n\t" \
    "v_exp_f32_e32 v37, v37\n\t" \
    "v_exp_f32_e32 v38, v38\n\t" \
    "v_exp_f32_e32 v39, v39\n\t" \
    "v_mfma_f32_16x16x32_bf16 v[76:79], v[72:75], v[108:111], v[76:79]\n\t" \
    "v_exp_f32_e32 v40, v40\n\t" \
    "v_cvt_pk_bf16_f32 v106, v36, v37\n\t" \
    "v_cvt_pk_bf16_f32 v107, v38, v39\n\t" \
    "v_exp_f32_e32 v41, v41\n\t" \
    "v_exp_f32_e32 v42, v42\n\t" \
    "v_mfma_f32_32x32x16_bf16 v[0:15], v[88:91], v[104:107], v[0:15]\n\t" \
    "s_cbranch_vccz 22f\n\t" \
    "global_load_lds_dwordx4 v114, s[44:45] offset:2048 sc1\n\t" \
    "22:\n" \
    "v_exp_f32_e32 v43, v43\n\t" \
    "v_exp_f32_e32 v44, v44\n\t" \
    "v_exp_f32_e32 v45, v45\n\t" \
    "v_mfma_f32_16x16x32_bf16 v[76:79], v[72:75], v[104:107], v[76:79]\n\t" \
    "v_exp_f32_e32 v46, v46\n\t" \
    "v_exp_f32_e32 v47, v47\n\t" \
    "v_cvt_pk_bf16_f32 v108, v40, v41\n\t" \
    "v_cvt_pk_bf16_f32 v109, v42, v43\n\t" \
    "v_cvt_pk_bf16_f32 v110, v44, v45\n\t" \
    "v_cvt_pk_bf16_f32 v111, v46, v47\n\t" \
    "s_waitcnt lgkmcnt(0)\n\t" \
    "v_mfma_f32_32x32x16_bf16 v[32:47], v[80:83], v[64:67], v[16:31]\n\t" \
    "v_exp_f32_e32 v48, v48\n\t" \
    "v_exp_f32_e32 v49, v49\n\t" \
    "v_exp_f32_e32 v50, v50\n\t" \
    "v_mfma_f32_32x32x16_bf16 v[32:47], v[84:87], v[68:71], v[32:47]\n\t" \
    "ds_read_b128 v[80:83], v113 offset:12288\n\t" \
    "ds_read_b128 v[84:87], v113 offset:13312\n\t" \
    "v_exp_f32_e32 v51, v51\n\t" \
    "v_exp_f32_e32 v52, v52\n\t" \
    "v_cvt_pk_bf16_f32 v104, v48, v49\n\t" \
    "v_cvt_pk_bf16_f32 v105, v50, v51\n\t" \
    "v_mfma_f32_32x32x16_bf16 v[0:15], v[100:103], v[108:111], v[0:15]\n\t" \
    "ds_read_b128 v[92:95], v113 offset:7168\n\t" \
    "ds_read_b128 v[88:91], v113 offset:10240\n\t" \
    "v_exp_f32_e32 v53, v53\n\t" \
    "v_exp_f32_e32 v54, v54\n\t" \
    "v_exp_f32_e32 v55, v55\n\t" \
    "v_mfma_f32_16x16x32_bf16 v[76:79], v[72:75], v[108:111], v[76:79]\n\t" \
    "v_exp_f32_e32 v56, v56\n\t" \
    "v_cvt_pk_bf16_f32 v106, v52, v53\n\t" \
    "v_cvt_pk_bf16_f32 v107, v54, v55\n\t" \
    "v_exp_f32_e32 v57, v57\n\t" \
    "v_exp_f32_e32 v58, v58\n\t" \
    "v_mfma_f32_32x32x16_bf16 v[0:15], v[96:99], v[104:107], v[0:15]\n\t" \
    "s_cbranch_vccz 23f\n\t" \
    "global_load_lds_dwordx4 v114, s[44:45] offset:3072 sc1\n\t" \
    "global_load_dwordx2 v[120:121], v170, %[ws] sc1\n\t" \
    "23:\n" \
    "v_exp_f32_e32 v59, v59\n\t" \
    "v_exp_f32_e32 v60, v60\n\t" \
    "v_exp_f32_e32 v61, v61\n\t" \
    "v_mfma_f32_16x16x32_bf16 v[76:79], v[72:75], v[104:107], v[76:79]\n\t" \
    "v_exp_f32_e32 v62, v62\n\t" \
    "v_exp_f32_e32 v63, v63\n\t" \
    "v_cvt_pk_bf16_f32 v108, v56, v57\n\t" \
    "v_cvt_pk_bf16_f32 v109, v58, v59\n\t" \
    "v_cvt_pk_bf16_f32 v110, v60, v61\n\t" \
    "v_cvt_pk_bf16_f32 v111, v62, v63\n\t" \
    "s_setprio 1\n\t" \
    "s_waitcnt lgkmcnt(0)\n\t" \
    "v_mfma_f32_32x32x16_bf16 v[48:63], v[80:83], v[64:67], v[16:31]\n\t" \
    "v_exp_f32_e32 v32, v32\n\t" \
    "v_exp_f32_e32 v33, v33\n\t" \
    "v_exp_f32_e32 v34, v34\n\t" \
    "v_mfma_f32_32x32x16_bf16 v[48:63], v[84:87], v[68:71], v[48:63]\n\t" \
    "ds_read_b128 v[80:83], v113 offset:16384\n\t" \
    "ds_read_b128 v[84:87], v113 offset:17408\n\t" \
    "v_exp_f32_e32 v35, v35\n\t" \
    "v_exp_f32_e32 v36, v36\n\t" \
    "v_cvt_pk_bf16_f32 v104, v32, v33\n\t" \
    "v_cvt_pk_bf16_f32 v105, v34, v35\n\t" \
    "v_mfma_f32_32x32x16_bf16 v[0:15], v[92:95], v[108:111], v[0:15]\n\t" \
    "ds_read_b128 v[100:103], v113 offset:11264\n\t" \
    "ds_read_b128 v[96:99], v113 offset:14336\n\t" \
    "v_exp_f32_e32 v37, v37\n\t" \
    "v_exp_f32_e32 v38, v38\n\t" \
    "v_exp_f32_e32 v39, v39\n\t" \
    "v_mfma_f32_16x16x32_bf16 v[76:79], v[72:75], v[108:111], v[76:79]\n\t" \
    "v_exp_f32_e32 v40, v40\n\t" \
    "v_cvt_pk_bf16_f32 v106, v36, v37\n\t" \
    "v_cvt_pk_bf16_f32 v107, v38, v39\n\t" \
    "v_exp_f32_e32 v41, v41\n\t" \
    "v_exp_f32_e32 v42, v42\n\t" \
    "v_mfma_f32_32x32x16_bf16 v[0:15], v[88:91], v[104:107], v[0:15]\n\t" \
    "s_add_u32 %[dst], %[dst], 0x8000\n\t" \
    "s_cmp_eq_u32 %[dst], %[dend]\n\t" \
    "s_cselect_b32 %[dst], %[dbeg], %[dst]\n\t" \
    "s_add_u32 %[nx], %[nx], 0x8000\n\t" \
    "s_cmp_eq_u32 %[nx], 0x18000\n\t" \
    "s_cselect_b32 %[nx], 0, %[nx]\n\t" \
    "s_cmp_eq_u32 %[cnt], 5\n\t" \
    "s_cbranch_scc1 40f\n\t" \
    "24:\n" \
    "v_exp_f32_e32 v43, v43\n\t" \
    "v_exp_f32_e32 v44, v44\n\t" \
    "v_exp_f32_e32 v45, v45\n\t" \
    "v_mfma_f32_16x16x32_bf16 v[76:79], v[72:75], v[104:107], v[76:79]\n\t" \
    "v_exp_f32_e32 v46, v46\n\t" \
    "v_exp_f32_e32 v47, v47\n\t" \
    "v_cvt_pk_bf16_f32 v108, v40, v41\n\t" \
    "v_cvt_pk_bf16_f32 v109, v42, v43\n\t" \
    "v_cvt_pk_bf16_f32 v110, v44, v45\n\t" \
    "v_cvt_pk_bf16_f32 v111, v46, v47\n\t" \
    "s_waitcnt lgkmcnt(0)\n\t" \
    "v_mfma_f32_32x32x16_bf16 v[32:47], v[80:83], v[64:67], v[16:31]\n\t" \
    "v_exp_f32_e32 v48, v48\n\t" \
    "v_exp_f32_e32 v49, v49\n\t" \
    "v_exp_f32_e32 v50, v50\n\t" \
    "v_mfma_f32_32x32x16_bf16 v[32:47], v[84:87], v[68:71], v[32:47]\n\t" \
    "ds_read_b128 v[80:83], v113 offset:20480\n\t" \
    "ds_read_b128 v[84:87], v113 offset:21504\n\t" \
    "v_exp_f32_e32 v51, v51\n\t" \
    "v_exp_f32_e32 v52, v52\n\t" \
    "v_cvt_pk_bf16_f32 v104, v48, v49\n\t" \
    "v_cvt_pk_bf16_f32 v105, v50, v51\n\t" \
    "v_mfma_f32_32x32x16_bf16 v[0:15], v[100:103], v[108:111], v[0:15]\n\t" \
    "ds_read_b128 v[92:95], v113 offset:15360\n\t" \
    "ds_read_b128 v[88:91], v113 offset:18432\n\t" \
    "v_exp_f32_e32 v53, v53\n\t" \
    "v_exp_f32_e32 v54, v54\n\t" \
    "v_exp_f32_e32 v55, v55\n\t" \
    "v_mfma_f32_16x16x32_bf16 v[76:79], v[72:75], v[108:111], v[76:79]\n\t" \
    "v_exp_f32_e32 v56, v56\n\t" \
    "v_cvt_pk_bf16_f32 v106, v52, v53\n\t" \
    "v_cvt_pk_bf16_f32 v107, v54, v55\n\t" \
    "v_exp_f32_e32 v57, v57\n\t" \
    "v_exp_f32_e32 v58, v58\n\t" \
    "v_mfma_f32_32x32x16_bf16 v[0:15], v[96:99], v[104:107], v[0:15]\n\t" \
    "s_cmp_eq_u32 %[cnt], 5\n\t" \
    "s_cbranch_scc1 41f\n\t" \
    "25:\n" \
    "v_exp_f32_e32 v59, v59\n\t" \
    "v_exp_f32_e32 v60, v60\n\t" \
    "v_exp_f32_e32 v61, v61\n\t" \
    "v_mfma_f32_16x16x32_bf16 v[76:79], v[72:75], v[104:107], v[76:79]\n\t" \
    "v_exp_f32_e32 v62, v62\n\t" \
    "v_exp_f32_e32 v63, v63\n\t" \
    "v_cvt_pk_bf16_f32 v108, v56, v57\n\t" \
    "v_cvt_pk_bf16_f32 v109, v58, v59\n\t" \
    "v_cvt_pk_bf16_f32 v110, v60, v61\n\t" \
    "v_cvt_pk_bf16_f32 v111, v62, v63\n\t" \
    "s_setprio 0\n\t" \
    "s_waitcnt lgkmcnt(0)\n\t" \
    "v_mfma_f32_32x32x16_bf16 v[48:63], v[80:83], v[64:67], v[16:31]\n\t" \
    "v_exp_f32_e32 v32, v32\n\t" \
    "v_exp_f32_e32 v33, v33\n\t" \
    "v_exp_f32_e32 v34, v34\n\t" \
    "v_mfma_f32_32x32x16_bf16 v[48:63], v[84:87], v[68:71], v[48:63]\n\t" \
    "ds_read_b128 v[80:83], v113 offset:24576\n\t" \
    "ds_read_b128 v[84:87], v113 offset:25600\n\t" \
    "v_exp_f32_e32 v35, v35\n\t" \
    "v_exp_f32_e32 v36, v36\n\t" \
    "v_cvt_pk_bf16_f32 v104, v32, v33\n\t" \
    "v_cvt_pk_bf16_f32 v105, v34, v35\n\t" \
    "v_mfma_f32_32x32x16_bf16 v[0:15], v[92:95], v[108:111], v[0:15]\n\t" \
    "ds_read_b128 v[100:103], v113 offset:19456\n\t" \
    "ds_read_b128 v[96:99], v113 offset:22528\n\t" \
    "v_exp_f32_e32 v37, v37\n\t" \
    "v_exp_f32_e32 v38, v38\n\t" \
    "v_exp_f32_e32 v39, v39\n\t" \
    "v_mfma_f32_16x16x32_bf16 v[76:79], v[72:75], v[108:111], v[76:79]\n\t" \
    "v_exp_f32_e32 v40, v40\n\t" \
    "v_cvt_pk_bf16_f32 v106, v36, v37\n\t" \
    "v_cvt_pk_bf16_f32 v107, v38, v39\n\t" \
    "v_exp_f32_e32 v41, v41\n\t" \
    "v_exp_f32_e32 v42, v42\n\t" \
    "v_mfma_f32_32x32x16_bf16 v[0:15], v[88:91], v[104:107], v[0:15]\n\t" \
    "s_and_b32 s40, s48, 63\n\t" \
    "s_lshl_b32 s40, s40, 12\n\t" \
    "s_add_u32 s44, %[srclo], s40\n\t" \
    "s_addc_u32 s45, %[srchi], 0\n\t" \
    "s_lshr_b64 s[48:49], s[48:49], 6\n\t" \
    "s_add_u32 s46, s46, 1\n\t" \
    "s_bitcmp1_b32 %[mode], 3\n\t" \
    "s_cbranch_scc1 18f\n\t" \
    "19:\n" \
    "s_cmp_eq_u32 %[cnt], 5\n\t" \
    "s_cbranch_scc1 42f\n\t" \
    "26:\n" \
    "v_exp_f32_e32 v43, v43\n\t" \
    "v_exp_f32_e32 v44, v44\n\t" \
    "v_exp_f32_e32 v45, v45\n\t" \
    "v_mfma_f32_16x16x32_bf16 v[76:79], v[72:75], v[104:107], v[76:79]\n\t" \
    "v_exp_f32_e32 v46, v46\n\t" \
    "v_exp_f32_e32 v47, v47\n\t" \
    "v_cvt_pk_bf16_f32 v108, v40, v41\n\t" \
    "v_cvt_pk_bf16_f32 v109, v42, v43\n\t" \
    "v_cvt_pk_bf16_f32 v110, v44, v45\n\t" \
    "v_cvt_pk_bf16_f32 v111, v46, v47\n\t" \
    "s_waitcnt lgkmcnt(0)\n\t" \
    "v_mfma_f32_32x32x16_bf16 v[32:47], v[80:83], v[64:67], v[16:31]\n\t" \
    "v_exp_f32_e32 v48, v48\n\t" \
    "v_exp_f32_e32 v49, v49\n\t" \
    "v_exp_f32_e32 v50, v50\n\t" \
    "v_mfma_f32_32x32x16_bf16 v[32:47], v[84:87], v[68:71], v[32:47]\n\t" \
    "ds_read_b128 v[80:83], v113 offset:28672\n\t" \
    "ds_read_b128 v[84:87], v113 offset:29696\n\t" \
    "v_exp_f32_e32 v51, v51\n\t" \
    "v_exp_f32_e32 v52, v52\n\t" \
    "v_cvt_pk_bf16_f32 v104, v48, v49\n\t" \
    "v_cvt_pk_bf16_f32 v105, v50, v51\n\t" \
    "v_mfma_f32_32x32x16_bf16 v[0:15], v[100:103], v[108:111], v[0:15]\n\t" \
    "ds_read_b128 v[92:95], v113 offset:23552\n\t" \
    "ds_read_b128 v[88:91], v113 offset:26624\n\t" \
    "v_exp_f32_e32 v53, v53\n\t" \
    "v_exp_f32_e32 v54, v54\n\t" \
    "v_exp_f32_e32 v55, v55\n\t" \
    "v_mfma_f32_16x16x32_bf16 v[76:79], v[72:75], v[108:111], v[76:79]\n\t" \
    "v_exp_f32_e32 v56, v56\n\t" \
    "v_cvt_pk_bf16_f32 v106, v52, v53\n\t" \
    "v_cvt_pk_bf16_f32 v107, v54, v55\n\t" \
    "v_exp_f32_e32 v57, v57\n\t" \
    "v_exp_f32_e32 v58, v58\n\t" \
    "v_mfma_f32_32x32x16_bf16 v[0:15], v[96:99], v[104:107], v[0:15]\n\t" \
    "s_cmp_eq_u32 %[cnt], 5\n\t" \
    "s_cbranch_scc1 43f\n\t" \
    "27:\n" \
    "v_mov_b32_e32 v112, v113\n\t" \
    "v_add_u32_e32 v113, %[nx], v115\n\t" \
    "s_sub_u32 %[cnt], %[cnt], 1\n\t" \
    "s_cmp_lg_u32 %[cnt], 0\n\t" \
    "v_exp_f32_e32 v59, v59\n\t" \
    "v_exp_f32_e32 v60, v60\n\t" \
    "v_exp_f32_e32 v61, v61\n\t" \
    "v_mfma_f32_16x16x32_bf16 v[76:79], v[72:75], v[104:107], v[76:79]\n\t" \
    "v_exp_f32_e32 v62, v62\n\t" \
    "v_exp_f32_e32 v63, v63\n\t" \
    "v_cvt_pk_bf16_f32 v108, v56, v57\n\t" \
    "v_cvt_pk_bf16_f32 v109, v58, v59\n\t" \
    "v_cvt_pk_bf16_f32 v110, v60, v61\n\t" \
    "v_cvt_pk_bf16_f32 v111, v62, v63\n\t" \
    "s_cbranch_scc1 1b\n\t" \
    "7:\n" \
    "s_setprio 0\n\t" \
    "s_waitcnt lgkmcnt(0)\n\t" \
    "v_mfma_f32_32x32x16_bf16 v[48:63], v[80:83], v[64:67], v[16:31]\n\t" \
    "v_exp_f32_e32 v32, v32\n\t" \
    "v_exp_f32_e32 v33, v33\n\t" \
    "v_exp_f32_e32 v34, v34\n\t" \
    "v_mfma_f32_32x32x16_bf16 v[48:63], v[84:87], v[68:71], v[48:63]\n\t" \
    "v_exp_f32_e32 v35, v35\n\t" \
    "v_exp_f32_e32 v36, v36\n\t" \
    "v_cvt_pk_bf16_f32 v104, v32, v33\n\t" \
    "v_cvt_pk_bf16_f32 v105, v34, v35\n\t" \
    "v_mfma_f32_32x32x16_bf16 v[0:15], v[92:95], v[108:111], v[0:15]\n\t" \
    "ds_read_b128 v[100:103], v112 offset:27648\n\t" \
    "ds_read_b128 v[96:99], v112 offset:30720\n\t" \
    "v_exp_f32_e32 v37, v37\n\t" \
    "v_exp_f32_e32 v38, v38\n\t" \
    "v_exp_f32_e32 v39, v39\n\t" \
    "v_mfma_f32_16x16x32_bf16 v[76:79], v[72:75], v[108:111], v[76:79]\n\t" \
    "v_exp_f32_e32 v40, v40\n\t" \
    "v_cvt_pk_bf16_f32 v106, v36, v37\n\t" \
    "v_cvt_pk_bf16_f32 v107, v38, v39\n\t" \
    "v_exp_f32_e32 v41, v41\n\t" \
    "v_exp_f32_e32 v42, v42\n\t" \
    "v_mfma_f32_32x32x16_bf16 v[0:15], v[88:91], v[104:107], v[0:15]\n\t" \
    "v_exp_f32_e32 v43, v43\n\t" \
    "v_exp_f32_e32 v44, v44\n\t" \
    "v_exp_f32_e32 v45, v45\n\t" \
    "v_mfma_f32_16x16x32_bf16 v[76:79], v[72:75], v[104:107], v[76:79]\n\t" \
    "v_exp_f32_e32 v46, v46\n\t" \
    "v_exp_f32_e32 v47, v47\n\t" \
    "v_cvt_pk_bf16_f32 v108, v40, v41\n\t" \
    "v_cvt_pk_bf16_f32 v109, v42, v43\n\t" \
    "v_cvt_pk_bf16_f32 v110, v44, v45\n\t" \
    "v_cvt_pk_bf16_f32 v111, v46, v47\n\t" \
    "s_waitcnt lgkmcnt(0)\n\t" \
    "v_exp_f32_e32 v48, v48\n\t" \
    "v_exp_f32_e32 v49, v49\n\t" \
    "v_exp_f32_e32 v50, v50\n\t" \
    "v_exp_f32_e32 v51, v51\n\t" \
    "v_exp_f32_e32 v52, v52\n\t" \
    "v_cvt_pk_bf16_f32 v104, v48, v49\n\t" \
    "v_cvt_pk_bf16_f32 v105, v50, v51\n\t" \
    "v_mfma_f32_32x32x16_bf16 v[0:15], v[100:103], v[108:111], v[0:15]\n\t" \
    "ds_read_b128 v[92:95], v112 offset:31744\n\t" \
    "v_exp_f32_e32 v53, v53\n\t" \
    "v_exp_f32_e32 v54, v54\n\t" \
    "v_exp_f32_e32 v55, v55\n\t" \
    "v_mfma_f32_16x16x32_bf16 v[76:79], v[72:75], v[108:111], v[76:79]\n\t" \
    "v_exp_f32_e32 v56, v56\n\t" \
    "v_cvt_pk_bf16_f32 v106, v52, v53\n\t" \
    "v_cvt_pk_bf16_f32 v107, v54, v55\n\t" \
    "v_exp_f32_e32 v57, v57\n\t" \
    "v_exp_f32_e32 v58, v58\n\t" \
    "v_mfma_f32_32x32x16_bf16 v[0:15], v[96:99], v[104:107], v[0:15]\n\t" \
    "v_exp_f32_e32 v59, v59\n\t" \
    "v_exp_f32_e32 v60, v60\n\t" \
    "v_exp_f32_e32 v61, v61\n\t" \
    "v_mfma_f32_16x16x32_bf16 v[76:79], v[72:75], v[104:107], v[76:79]\n\t" \
    "v_exp_f32_e32 v62, v62\n\t" \
    "v_exp_f32_e32 v63, v63\n\t" \
    "v_cvt_pk_bf16_f32 v108, v56, v57\n\t" \
    "v_cvt_pk_bf16_f32 v109, v58, v59\n\t" \
    "v_cvt_pk_bf16_f32 v110, v60, v61\n\t" \
    "v_cvt_pk_bf16_f32 v111, v62, v63\n\t" \
    "s_waitcnt lgkmcnt(0)\n\t" \
    "v_mfma_f32_32x32x16_bf16 v[0:15], v[92:95], v[108:111], v[0:15]\n\t" \
    "v_mfma_f32_16x16x32_bf16 v[76:79], v[72:75], v[108:111], v[76:79]\n\t" \
    "s_nop 15\n\t" \
    "s_branch 99f\n\t" \
    "9:\n" \
    "s_mov_b32 s47, 0x40000\n\t" \
    "10:\n" \
    "s_sub_u32 s47, s47, 1\n\t" \
    "s_cbranch_scc1 12b\n\t" \
    "global_load_dwordx2 v[120:121], v170, %[ws] sc1\n\t" \
    "s_waitcnt vmcnt(0)\n\t" \
    "v_cmp_eq_u64_e64 s[42:43], v[120:121], v[140:141]\n\t" \
    "s_nop 4\n\t" \
    "s_bitcmp1_b64 s[42:43], s46\n\t" \
    "s_cbranch_scc1 12b\n\t" \
    "s_sleep 4\n\t" \
    "s_branch 10b\n\t" \
    "18:\n" \
    "s_waitcnt vmcnt(0)\n\t" \
    "global_store_dwordx2 v171, v[140:141], %[ws] sc1\n\t" \
    "s_bitset0_b32 %[mode], 3\n\t" \
    "s_branch 19b\n\t" \
    "40:\n" \
    "s_add_u32 m0, %[wdst], 0\n\t" \
    "s_nop 0\n\t" \
    "global_load_lds_dwordx4 v116, %[wo01]\n\t" \
    "global_load_lds_dwordx4 v117, %[wo01] offset:1024\n\t" \
    "v_add_u32_e32 v116, 0x20000, v116\n\t" \
    "v_add_u32_e32 v117, 0x20000, v117\n\t" \
    "s_branch 24b\n\t" \
    "41:\n" \
    "s_add_u32 m0, %[wdst], 16384\n\t" \
    "s_nop 0\n\t" \
    "global_load_lds_dwordx4 v116, %[wo01]\n\t" \
    "global_load_lds_dwordx4 v117, %[wo01] offset:1024\n\t" \
    "v_subrev_u32_e32 v116, 0x20000, v116\n\t" \
    "v_subrev_u32_e32 v117, 0x20000, v117\n\t" \
    "s_branch 25b\n\t" \
    "42:\n" \
    "s_add_u32 m0, %[wdst], 32768\n\t" \
    "s_nop 0\n\t" \
    "global_load_lds_dwordx4 v116, %[wo23]\n\t" \
    "global_load_lds_dwordx4 v117, %[wo23] offset:1024\n\t" \
    "v_add_u32_e32 v116, 0x20000, v116\n\t" \
    "v_add_u32_e32 v117, 0x20000, v117\n\t" \
    "s_branch 26b\n\t" \
    "43:\n" \
    "s_add_u32 m0, %[wdst], 49152\n\t" \
    "s_nop 0\n\t" \
    "global_load_lds_dwordx4 v116, %[wo23]\n\t" \
    "global_load_lds_dwordx4 v117, %[wo23] offset:1024\n\t" \
    "global_load_dword v168, v119, %[wo]\n\t" \
    "s_branch 27b\n\t" \
    "30:\n" \
    "s_cmp_eq_u32 %[cnt], 1\n\t" \
    "s_cbranch_scc0 31f\n\t" \
    "s_mov_b64 vcc, 0\n\t" \
    "s_bitcmp1_b32 %[mode], 1\n\t" \
    "s_cbranch_scc1 7b\n\t" \
    "s_branch 2b\n\t" \
    "31:\n" \
    "33:\n" \
    "s_cmp_eq_u32 %[cnt], 5\n\t" \
    "s_cbranch_scc0 34f\n\t" \
    "global_store_dwordx2 v118, v[140:141], %[ws] sc1\n\t" \
    "s_branch 2b\n\t" \
    "34:\n" \
    "s_cmp_eq_u32 %[cnt], 3\n\t" \
    "s_cbranch_scc0 35f\n\t" \
    "global_load_dwordx2 v[168:169], v142, %[ws] sc1\n\t" \
    "s_branch 2b\n\t" \
    "35:\n" \
    "v_cmp_eq_u64_e64 s[42:43], v[168:169], v[140:141]\n\t" \
    "s_nop 4\n\t" \
    "s_cmp_eq_u64 s[42:43], -1\n\t" \
    "s_cbranch_scc0 2b\n\t" \
    "global_load_dwordx4 v[122:125], v143, %[ws] offset:-2048 sc1\n\t" \
    "global_load_dwordx4 v[126:129], v143, %[ws] offset:-1024 sc1\n\t" \
    "global_load_dwordx4 v[130:133], v143, %[ws] offset:0 sc1\n\t" \
    "global_load_dwordx4 v[134:137], v143, %[ws] offset:1024 sc1\n\t" \
    "global_load_dwordx2 v[138:139], v143, %[ws] offset:2048 sc1\n\t" \
    "s_bitset1_b32 %[mode], 2\n\t" \
    "s_branch 2b\n\t" \
    "99:\n"
#define ATT_TMP "v48", "v49", "v50", "v51", "v52", "v53", "v54", "v55", "v56", "v57", "v58", "v59", "v60", "v61", "v62", "v63", "v96", "v97", "v98", "v99", "v100", "v101", "v102", "v103", "v104", "v105", "v106", "v107"

constexpr int K1_X = 0, K1_W = 65536, K1_WB = 24576;
constexpr int K2_SLOT = 32768, K2_QX = 3 * K2_SLOT, K2_FL = 147456;
constexpr int LDS_BYTES = 163840;
constexpr int p3_wslot(int t) { return (t % 6) < 4 ? 98304 + (t % 6) * 16384 : 65536 + ((t % 6) - 4) * 16384; }
constexpr int p3_allow(int t) { return (((t < 2 || t - 2 >= 1) && t + 3 < 16) ? 4 : 0) + (t + 3 < 16 ? 4 : 0) + ((t >= 1 && t + 5 < 16) ? 4 : 0); }

__global__ __launch_bounds__(512, 2) void mk_fused(const u16* __restrict__ xA, const u16* __restrict__ wA, const u16* __restrict__ woA,
                                                   const u16* __restrict__ xB, const u16* __restrict__ wB, const u16* __restrict__ woB, char* ws0, u16* __restrict__ out) {
    char* ws = ws0;
    __shared__ __attribute__((aligned(1024))) char smem[LDS_BYTES];
    const unsigned lds0 = (unsigned)(uintptr_t)(LAS char*)smem;
    const int wid = __builtin_amdgcn_readfirstlane(threadIdx.x >> 6);
    int lane = threadIdx.x & 63;
#define BRX 1
    const int b = blockIdx.x, xcd = b & 7, j = b >> 3, br = (xcd >> 2) ^ BRX, qd = xcd & 3;
    const int mt = (qd & 1) * 4 + (j & 3), hp = (qd >> 1) * 8 + (j >> 2);
    const int mt3 = qd * 4 + (j & 3), nt3 = j >> 2;
    u64 tag;
    unsigned pst[8];
    u32x4 fq[2][2], fk[2][2], fv[2][2];
    f32x16 band;
    STAMP_AT(0, 0);
    {
        const int g = wid >> 2, wi = wid & 3, mw = wi >> 1, hd = wi & 1;
        const char* x = (const char*)(br ? xB : xA) + (size_t)mt * 256 * 2048;
        const char* w = (const char*)(br ? wB : wA) + hp * 128;
        unsigned vx[4], vw[3];
#pragma unroll
        for (int ii = 0; ii < 4; ++ii) {
            const int row = br ? ((wi >> 1) * 64 + (wi & 1) + ii * 16 + 2 * (lane >> 3)) : (wi * 32 + ii * 8 + (lane >> 3));
            vx[ii] = row * 2048 + (((lane & 7) ^ (((lane >> 4) & 1) | ((ii >> 1) << 1) | ((lane >> 5) << 2))) << 4);
        }
#pragma unroll
        for (int jj = 0; jj < 3; ++jj) {
            const int jw = 12 * g + 3 * wi + jj, n = jw >> 3, k0 = (jw & 7) * 8 + (lane >> 3);
            vw[jj] = k0 * 6144 + n * 2048 + (((lane & 7) ^ ((((lane >> 4) & 1) | ((jw & 1) << 1)) << 1)) << 4);
        }
        const unsigned xdst_o = lds0 + K1_X + (1 - g) * 32768 + wi * 4096;
        const unsigned wdst = lds0 + K1_W + (12 * g + 3 * wi) * 1024;
        const char* xo = x + (size_t)(1 - g) * 128 * 2048;
#define ROTM 4
        const int kt0 = ROTM * (j & 3);
#define STAGE_X(kt, dst) do { const char* s_ = xo + (((kt) + kt0) & 15) * 128; _Pragma("unroll") for (int ii = 0; ii < 4; ++ii) glds16(s_, vx[ii], (dst) + ii * 1024); } while (0)
#define STAGE_W(kt, buf) do { const char* s_ = w + (size_t)(((kt) + kt0) & 15) * 64 * 6144; _Pragma("unroll") for (int jj = 0; jj < 3; ++jj) glds16(s_, vw[jj], wdst + (buf) * K1_WB + jj * 1024); } while (0)
        if (wid == 7) {
            const char* pc; unsigned len;
            asm volatile("s_getpc_b64 %0\n.Lmk_code_begin:\n\ts_mov_b32 %1, .Lmk_code_end-.Lmk_code_begin" : "=s"(pc), "=s"(len));
            const unsigned off = (unsigned)(j * 64 + lane) * 64u;
            asm volatile("s_mov_b32 m0, %2\n\ts_nop 0\n\tglobal_load_lds_dword %0, %1" :: "v"(off < len ? off : 0u), "s"(pc), "s"(lds0 + 159744) : "memory");
        }
        STAGE_X(0, xdst_o); STAGE_W(0, 0); STAGE_W(1, 1);
        if (g == 1) STAGE_X(1, xdst_o + 16384);
        { u64 e; asm volatile("s_load_dwordx2 %0, %1, 0x0\n\ts_waitcnt lgkmcnt(0)" : "=s"(e) : "s"(ws + WS_EP) : "memory");
          e += 0x9E3779B97F4A7C15ull; e = (e ^ (e >> 30)) * 0xBF58476D1CE4E5B9ull; e = (e ^ (e >> 27)) * 0x94D049BB133111EBull; tag = e ^ (e >> 31); }
        const int c16 = lane & 15, g4 = lane >> 4;
        unsigned ax[2], aw[2];
        ax[0] = lds0 + K1_X + g * 32768 + (mw * 64 + 16 * (c16 >> 3) + (c16 & 7)) * 128 + ((g4 ^ (((c16 >> 1) & 1) | ((c16 >> 3) << 1) | (((c16 >> 2) & 1) << 2))) << 4);
        ax[1] = ax[0] ^ 64;
        { const int q4 = c16 >> 2, p = lane & 3;
          aw[0] = lds0 + K1_W + (8 * g4 + q4) * 128 + (((hd * 4 + (p >> 1)) ^ ((((q4 >> 1) & 1) | ((g4 & 1) << 1)) << 1)) << 4) + (p & 1) * 8; aw[1] = aw[0] ^ 32; }
        band = band_mask(lane); asm volatile("" : "+v"(band));
        if (g == 1) WAITBAR_(7); else WAITBAR_(3);
        SA_(1);
        if (g == 1) asm volatile("s_barrier" ::: "memory");
        f32x4 acc[4][6];
#pragma unroll
        for (int m = 0; m < 4; ++m)
#pragma unroll
            for (int n = 0; n < 6; ++n) acc[m][n] = f32x4{};
        SBAR();
        unsigned xp = 0, wr_ = 0, ws_ = 2 * K1_WB;
        int tx = (1 + g + kt0) & 15, tw = (2 + kt0) & 15;
        unsigned dxp = (1 - g) * 16384;
#define RDW(n, ks) wf[n][ks] = cat8(ldstr(awc[(n) & 1] + ((n) >> 1) * 8192 + (ks) * 4096), ldstr(awc[(n) & 1] + ((n) >> 1) * 8192 + (ks) * 4096 + 512))
#define P1_TILE(DOX, DOW, WAIT, LASTBAR) do { \
            bf16x8 xf[4][2], wf[6][2]; \
            unsigned awc[2] = {aw[0] + wr_, aw[1] + wr_}; asm volatile("" : "+v"(awc[0]), "+v"(awc[1])); \
            const char* sx = xo + tx * 128; const char* sw = w + (size_t)tw * 64 * 6144; \
            _Pragma("unroll") for (int s = 0; s < 4; ++s) { \
                xf[s][0] = ldsrd128(ax[0] + xp + (s >> 1) * 4096 + (s & 1) * 1024); xf[s][1] = ldsrd128(ax[1] + xp + (s >> 1) * 4096 + (s & 1) * 1024); \
                RDW((3 * s) >> 1, (3 * s) & 1); \
                if (DOX) glds16(sx, vx[s], xdst_o + dxp + s * 1024); \
                RDW((3 * s + 1) >> 1, (3 * s + 1) & 1); RDW((3 * s + 2) >> 1, (3 * s + 2) & 1); \
                if ((DOW) && s < 3) glds16(sw, vw[s], wdst + ws_ + s * 1024); \
            } \
            WAIT; \
            SBAR(); \
            __builtin_amdgcn_s_setprio(1); \
            _Pragma("unroll") for (int m = 0; m < 4; ++m) \
                _Pragma("unroll") for (int n = 0; n < 6; ++n) { \
                    if (n < 4) { acc[m][n] = MFMA16(wf[n][0], xf[m][0], acc[m][n]); acc[m][n] = MFMA16(wf[n][1], xf[m][1], acc[m][n]); } \
                    else       { acc[m][n] = MFMA16(xf[m][0], wf[n][0], acc[m][n]); acc[m][n] = MFMA16(xf[m][1], wf[n][1], acc[m][n]); } \
                    CHAIN(); \
                } \
            __builtin_amdgcn_s_setprio(0); \
            xp ^= 16384; dxp ^= 16384; ws_ = wr_; wr_ = (wr_ == 2 * K1_WB) ? 0 : wr_ + K1_WB; tx = (tx + 1) & 15; tw = (tw + 1) & 15; \
            SBAR(); \
            if (LASTBAR) asm volatile("s_barrier" ::: "memory"); \
            SBAR(); \
        } while (0)
#pragma unroll 1
        for (int t = 0; t < 14; ++t) P1_TILE(true, true, WAITBAR_(7), true);
        P1_TILE(g == 0, false, if (g == 0) WAITBAR_(4); else WAITBAR_(0), true);
        P1_TILE(false, false, WAITBAR_(0), g == 0);
#undef P1_TILE
#undef RDW
#undef STAGE_X
#undef STAGE_W
        SA_(2); SC_(1);
        FRESH();
#pragma unroll
        for (int mb = 0; mb < 2; ++mb)
#pragma unroll
            for (int u = 0; u < 2; ++u) {
                const f32x4 a = acc[2 * mb + u][0] * QSCALE, c = acc[2 * mb + u][1] * QSCALE;
                fq[mb][u] = u32x4{cvtpk(a[0], a[1]), cvtpk(a[2], a[3]), cvtpk(c[0], c[1]), cvtpk(c[2], c[3])};
                fk[mb][u] = pack44(acc[2 * mb + u][2], acc[2 * mb + u][3]);
                fv[mb][u] = pack44(acc[2 * mb][4 + u], acc[2 * mb + 1][4 + u]);
            }
    }
    const char* wo = (const char*)(br ? woB : woA) + nt3 * 256;
    const int k30 = 2 * nt3;
    {
        const int g = wid >> 2, wr = wid >> 1, hd = wid & 1;
        const int ph1 = 2 * hp + hd, pb1 = mt * 8 + wr * 2;
        const int head = 2 * hp + br, blk = mt * 8 + wid;
#define RBLK(e) ((((e) % 7) + (((e) % 7) >= mt)) * 8 + (e) / 7)
        const char* kv = ws + WS_KV + (size_t)head * 262144;
        unsigned l16 = lane * 16;
        const unsigned dbeg = lds0 + wid * 4096, lbase = lds0 + l16;
        const unsigned fk0 = (unsigned)WS_FK + head * 32 * FS;
        const unsigned fxo = (unsigned)WS_FX + ((hp * 8 + mt) * 8 + wid) * FS, xrec0 = (unsigned)WS_XP + ((hp * 8 + mt) * 8 + wid) * 5120 + 2048;
        const f32x16 z = {};
        f32x16 sa, negm, o;
        u32x16 qol, kvf; u32x4 ph;

#define FIRST_TILE(base, q0, q1) do { const unsigned one2 = (((lane >> 4) & 1) == ((lane >> 2) & 1)) ? 0x3F803F80u : 0u; const u32x4 ones = {one2, one2, one2, one2}; \
            const bf16x8 k0_ = ldsrd128(base), k1_ = ldsrd128((base) + 1024); kvf = cat16(asu(ldsrd128((base) + 4096)), asu(ldsrd128((base) + 5120)), asu(ldsrd128((base) + 2048)), ones); \
            sa = MFMA32(k0_, q0, z); sa = MFMA32(k1_, q1, sa); float m_ = sa[0]; \
            _Pragma("unroll") for (int r = 1; r < 16; ++r) m_ = __builtin_fmaxf(m_, sa[r]); \
            m_ = -swap_max(m_); _Pragma("unroll") for (int r = 0; r < 16; ++r) { negm[r] = m_; sa[r] += m_; } \
            qol = cat16(asu(q0), asu(q1), ones, ones); } while (0)
#define L6_OUT "={v[0:15]}"(o), "+{v[32:47]}"(sa), "+{v[64:79]}"(qol), "+{v[80:95]}"(kvf), "={v[108:111]}"(ph)
#define L6C(HL, BASE) asm volatile(ATT_L6C : L6_OUT, "={v[144:159]}"(h0), "={v[160:167]}"(h1) \
            : "{v[16:31]}"(negm), "{v112}"(BASE), "{v[140:143]}"(u32x4{0u, 0u, FLADDR, (unsigned)WS_HALO + (mt * 32 + ph1) * 6144 + 3072 + l16}), \
              [ws] "s"(ws), [hl] "s"(HL), [fld] "s"(lds0 + K2_FL + wid * 1024) : ATT_TMP, "scc", "memory")
#define FLADDR (fk0 + ((((lane & 7) == 7) ? blk : RBLK(8 * (lane & 7) + wid)) >> 1) * FS)
        const unsigned own = (ph1 * 32 + mt * 4 + wr) * FS;
        const unsigned pv = (lane >> 5) * 1024 + ((lane & 15) + 32 * ((lane >> 4) & 1)) * 16, pq = pv + (lane & 8) * 16;
        if (br == 0) {
            const char* kp = ws + WS_KV + (size_t)((ph1 * 64 + pb1) * 4) * 1024;
#define PUB_KV() do { st16_dev<0>(kp, pq, fk[0][0]); st16_dev<128>(kp, pq, fk[0][1]); st16_dev<2048>(kp, pv, fv[0][0]); st16_dev<2304>(kp, pv, fv[0][1]); \
            st16_dev<0>(kp + 4096, pq, fk[1][0]); st16_dev<128>(kp + 4096, pq, fk[1][1]); st16_dev<2048>(kp + 4096, pv, fv[1][0]); st16_dev<2304>(kp + 4096, pv, fv[1][1]); } while (0)
            if (hd == 1) {
                const char* qp = ws + WS_QF + (size_t)((ph1 * 64 + pb1) * 2) * 1024;
                st16_dev<0>(qp, pq, fq[0][0]); st16_dev<128>(qp, pq, fq[0][1]); st16_dev<2048>(qp, pq, fq[1][0]); st16_dev<2176>(qp, pq, fq[1][1]);
            }
            if (wr == 0) PUB_KV();
#pragma unroll
            for (int mb = 0; mb < 2; ++mb) {
                const unsigned a = lds0 + hd * 2 * K2_SLOT + (wr * 2 + mb) * 4096, aq = lds0 + K2_QX + hd * 16384 + (wr * 2 + mb) * 2048 + pq;
                ldswr128(a + pq, fk[mb][0]); ldswr128(a + pq + 128, fk[mb][1]); ldswr128(a + 2048 + pv, fv[mb][0]); ldswr128(a + 2304 + pv, fv[mb][1]);
                if (hd == 0 || g == 0) { ldswr128(aq, fq[mb][0]); ldswr128(aq + 128, fq[mb][1]); }
            }
            BARRIER_L();
            if (wr == 0 || hd == 1) {
                drain();
                if (lane == 0) { if (wr == 0) flag_set(ws + WS_FK, own, tag); if (hd == 1) flag_set(ws + WS_FQ, own, tag); }
            }
            if (wr == 1) PUB_KV();
            const unsigned qa = lbase + K2_QX + (1 - g) * 16384 + wid * 2048, cx = lbase + (1 - g) * 2 * K2_SLOT;
            SA_(3); SC_(2);
            { const bf16x8 q0 = ldsrd128(qa), q1 = ldsrd128(qa + 1024); FIRST_TILE(cx, q0, q1); }
            asm volatile(ATT_L4 : L6_OUT : "{v[16:31]}"(negm), "{v112}"(cx) : ATT_TMP, "memory");
            SC_(3);
            if (g == 1) {
                const int b1 = RBLK(wid), b0 = RBLK(wid - 4);
                flag_wait(ws, fk0 + (((lane & 1) ? b1 : b0) >> 1) * FS, tag);
                glds4k_dev(kv + b1 * 4096, l16, dbeg + K2_SLOT);
                glds4k_dev(kv + b0 * 4096, l16, dbeg + K2_SLOT - 16384);
                glds16_dev(ws, FLADDR, lds0 + K2_FL + wid * 1024);
                PUB_KV();
            } else if (wr == 1) { drain(); if (lane == 0) flag_set(ws + WS_FK, own, tag); }
            SC_(4);
            asm volatile(ATT_L2 : "+{v[0:15]}"(o), "+{v[32:47]}"(sa), "+{v[64:79]}"(qol), "+{v[80:95]}"(kvf), "+{v[108:111]}"(ph) : "{v[16:31]}"(negm), "{v112}"(cx) : ATT_TMP, "memory");
            if (g == 0) {
                asm volatile(ATT_XT : "+{v[0:15]}"(o), "+{v[32:47]}"(sa), "+{v[64:79]}"(qol), "+{v[80:95]}"(kvf), "+{v[108:111]}"(ph) : "{v[16:31]}"(negm), "{v112}"(cx) : ATT_TMP, "memory");
                st16_dev<-2048>(ws, xrec0 + l16, quad(o, 0)); st16_dev<-1024>(ws, xrec0 + l16, quad(o, 1)); st16_dev<0>(ws, xrec0 + l16, quad(o, 2)); st16_dev<1024>(ws, xrec0 + l16, quad(o, 3));
                st16_dev<2048>(ws, xrec0 + l16, u32x4{qol[12], __float_as_uint(negm[0]), 0u, 0u});
                SC_(5);
                const bf16x8 q0 = ldsrd128(lbase + K2_QX + wid * 2048), q1 = ldsrd128(lbase + K2_QX + wid * 2048 + 1024);
                FIRST_TILE(lbase, q0, q1);
                u32x16 h0; u32x8 h1;
                L6C(0, lbase);
            }
#undef PUB_KV
        } else {
            if (wr == 3 && mt < 7) {
                const char* p = ws + WS_HALO + (size_t)(((mt + 1) * 32 + ph1) * 6) * 1024;
                st16_dev<0>(p, pq, fk[0][0]); st16_dev<128>(p, pq, fk[0][1]); st16_dev<3072>(p, pq, fk[1][0]); st16_dev<3200>(p, pq, fk[1][1]);
                if (lane >= 32) { st16_dev<1024>(p, pv, fv[0][0]); st16_dev<1280>(p, pv, fv[0][1]); st16_dev<1024>(p + 3072, pv, fv[1][0]); st16_dev<1280>(p + 3072, pv, fv[1][1]); }
            }
            const unsigned ex = lds0 + 2 * K2_SLOT + wid * 12288;
#pragma unroll
            for (int mb = 0; mb < 2; ++mb) {
                const unsigned a = ex + mb * 6144;
                ldswr128(a + pq, fq[mb][0]); ldswr128(a + pq + 128, fq[mb][1]); ldswr128(a + 2048 + pq, fk[mb][0]); ldswr128(a + 2176 + pq, fk[mb][1]);
                ldswr128(a + 4096 + pv, fv[mb][0]); ldswr128(a + 4352 + pv, fv[mb][1]);
            }
            BARRIER_L();
            if (wr == 3 && mt < 7) { drain(); if (lane == 0) flag_set(ws + WS_FH, (ph1 * 8 + mt + 1) * FS, tag); }
            SC_(2);
            LocalPart lp[2];
            bf16x8 bq[2][2];
#pragma unroll
            for (int mb = 0; mb < 2; ++mb) { bq[mb][0] = ldsrd128(ex + mb * 6144 + l16); bq[mb][1] = ldsrd128(ex + mb * 6144 + 1024 + l16); }
#define LOCAL_BLOCK(mb) do { const unsigned a_ = ex + mb * 6144 + l16; \
                const bf16x8 k0_ = ldsrd128(a_ + 2048), k1_ = ldsrd128(a_ + 3072), v0_ = ldsrd128(a_ + 4096), v1_ = ldsrd128(a_ + 5120); \
                if (wr > 0) local_own<true>(bq[mb][0], bq[mb][1], k0_, k1_, v0_, v1_, ldsrd128(a_ - 24576 + 2048), ldsrd128(a_ - 24576 + 3072), ldsrd128(a_ - 24576 + 5120), band, lane, lp[mb]); \
                else local_own<false>(bq[mb][0], bq[mb][1], k0_, k1_, v0_, v1_, k0_, k0_, k0_, band, lane, lp[mb]); \
                if (wr > 0 || mt == 0) local_fin(lp[mb]); } while (0)
            LOCAL_BLOCK(0);
            SC_(3);
            const int hl = (wid < 2) ? mt : 0;
            const unsigned fh = (unsigned)WS_FH + (ph1 * 8 + mt) * FS;
            const int b0 = RBLK(wid), b1 = RBLK(8 + wid);
            const int q = lane & 3;
            unsigned fa0 = fk0 - 1024 * FS + (blk >> 1) * FS, fa1 = fk0 + (b0 >> 1) * FS, fa2 = fk0 + (b1 >> 1) * FS, fa3 = fh;
            asm volatile("" : "+s"(fa0), "+s"(fa1), "+s"(fa2), "+s"(fa3));
            const unsigned lo_ = (q & 1) ? fa1 : fa0, hi_ = (q & 1) ? fa3 : fa2;
            const u64 fm = flag_wait_some(ws, (q & 2) ? hi_ : lo_, tag, 3);
            SC_(4);
            glds4k_dev(kv + b0 * 4096, l16, dbeg);
            const char* qp = ws + WS_QF + (size_t)((head * 64 + blk) * 2) * 1024;
            glds16_dev(qp, l16, ex); glds16_dev(qp + 1024, l16, ex + 1024);
            if (fm & 4) glds4k_dev(kv + b1 * 4096, l16, dbeg + K2_SLOT);
            LOCAL_BLOCK(1);
#undef LOCAL_BLOCK
            if (fm & 4) WAITBAR_(4);
            else {
                WAITBAR_(0);
                flag_wait(ws, fk0 + (b1 >> 1) * FS, tag);
                glds4k_dev(kv + b1 * 4096, l16, dbeg + K2_SLOT);
            }
            const bf16x8 q0 = ldsrd128(ex + l16), q1 = ldsrd128(ex + 1024 + l16);
            SA_(3); SC_(5);
            if (hl && !(fm & 8)) flag_wait(ws, fh, tag);
            FIRST_TILE(lbase, q0, q1);
            u32x16 h0; u32x8 h1;
            L6C(hl, lbase);
            if (hl) {
                local_prev(bq[0][0], bq[0][1], asbf(u32x4{h0[0], h0[1], h0[2], h0[3]}), asbf(u32x4{h0[4], h0[5], h0[6], h0[7]}), asbf(u32x4{h0[8], h0[9], h0[10], h0[11]}), lane, lp[0]);
                local_prev(bq[1][0], bq[1][1], asbf(u32x4{h0[12], h0[13], h0[14], h0[15]}), asbf(u32x4{h1[0], h1[1], h1[2], h1[3]}), asbf(u32x4{h1[4], h1[5], h1[6], h1[7]}), lane, lp[1]);
                local_fin(lp[0]); local_fin(lp[1]);
            }
            const char* op = ws + WS_OF + (size_t)(((64 + pb1) * 32 + ph1) * 2) * 1024;
            st16_dev<0>(op, l16, quad(lp[0].o, 0)); st16_dev<1024>(op, l16, quad(lp[0].o, 1)); st16_dev<0>(op + 65536, l16, quad(lp[1].o, 0)); st16_dev<1024>(op + 65536, l16, quad(lp[1].o, 1));
        }
#undef FIRST_TILE
#undef L6_OUT
#undef L6C
        FRESH();
        l16 = lane * 16;
        SA_(4); SC_(6);
        const int isx = br & (1 - g);
        unsigned cnt = 7, dst = dbeg + 2 * K2_SLOT, nx = K2_SLOT;
        unsigned mode = (isx << 1) | (((1 - br) & g) << 3);
        const unsigned act = 2u | (isx * 12u) | ((br | (1 - g)) << 5);
        u64 tab = (u64)blk << 42;
#pragma unroll
        for (int k = 1; k < 7; ++k) tab |= (u64)RBLK(8 * k + wid) << (6 * k);
        tab >>= 6 + 6 * br;
        u32x8 adr;
        { const int k = 8 * wid + (lane >> 4), c = lane & 15;
          adr = u32x8{lds0 + l16, lds0 + l16 + K2_SLOT, l16, lds0 + l16, (unsigned)(k * 2048 + ((c ^ ((k & 7) << 1)) << 4)), (unsigned)((k + 4) * 2048 + ((c ^ (((k + 4) & 7) << 1)) << 4) - 1024),
                      br ? (unsigned)WS_FO + ((64 + pb1 + (lane & 1)) * 32 + ph1) * FS : fxo, 0u};
          const int i = (mt3 * 8 + wid) * 12 + ((lane * 3) >> 4); adr[7] = ((((4 + (i >> 7) + k30) & 15) * 64 + ((i & 127) >> 1)) * 2048 + (i & 1) * 128); }
        f32x16 xo; f32x2 xl;
#define DBGOUT
        asm volatile(ATT_MAIN
                     : DBGOUT"+{v[0:15]}"(o), "+{v[32:47]}"(sa), "+{v[64:79]}"(qol), "+{v[80:95]}"(kvf), "+{v[108:111]}"(ph), "+{v[112:119]}"(adr), "={v[122:137]}"(xo), "={v[138:139]}"(xl),
                       [cnt] "+s"(cnt), [dst] "+s"(dst), [nx] "+s"(nx), [mode] "+s"(mode)
                     : "{v[16:31]}"(negm), "{v[140:143]}"(u32x4{(unsigned)tag, (unsigned)(tag >> 32), fxo, xrec0 + l16}),
                       "{v[170:173]}"(u32x4{FLADDR, (unsigned)WS_FK + own, 0u, lds0 + K2_FL + wid * 1024 + l16}),
                       [srclo] "s"((unsigned)(uintptr_t)kv), [srchi] "s"((unsigned)((uintptr_t)kv >> 32)), [dbeg] "s"(dbeg), [dend] "s"(dbeg + 3 * K2_SLOT),
                       [wo01] "s"(wo + (size_t)k30 * 131072), [wo23] "s"(wo + (size_t)((k30 + 2) & 15) * 131072), [wo] "s"(wo), [wdst] "s"(lds0 + 98304 + wid * 2048), [act] "s"(act), [tab] "s"(tab), [k0] "s"(br), [ws] "s"(ws)
                     : ATT_TMP, "v120", "v121", "v168", "v169", "s40", "s41", "s42", "s43", "s44", "s45", "s46", "s47", "s48", "s49", "s50", "s51", "vcc", "scc", "memory");
        FRESH();
        l16 = lane * 16;
        const unsigned xrec = xrec0 + l16;
        const bf16x8 q0 = asbf(u32x4{qol[0], qol[1], qol[2], qol[3]}), q1 = asbf(u32x4{qol[4], qol[5], qol[6], qol[7]});
        const u32x4 ones = {qol[8], qol[9], qol[10], qol[11]};
        SA_(5); SB_(1);
        float lsum = __uint_as_float(qol[12]);
        bool ok = lsum < 1.0e18f;
        if (isx) {
            if (__builtin_expect(!(mode & 4), 0)) {
                flag_wait(ws, fxo, tag);
                f32x4 a0, a1, a2, a3;
                asm volatile("global_load_dwordx4 %0, %5, %6 offset:-2048 sc1\n\tglobal_load_dwordx4 %1, %5, %6 offset:-1024 sc1\n\tglobal_load_dwordx4 %2, %5, %6 sc1\n\t"
                             "global_load_dwordx4 %3, %5, %6 offset:1024 sc1\n\tglobal_load_dwordx2 %4, %5, %6 offset:2048 sc1\n\ts_waitcnt vmcnt(0)"
                             : "=&v"(a0), "=&v"(a1), "=&v"(a2), "=&v"(a3), "=&v"(xl) : "v"(xrec), "s"(ws) : "memory");
#pragma unroll
                for (int r = 0; r < 4; ++r) { xo[r] = a0[r]; xo[4 + r] = a1[r]; xo[8 + r] = a2[r]; xo[12 + r] = a3[r]; }
            }
            const float nm = __builtin_fminf(negm[0], xl[1]), fa = EXP2(nm - negm[0]), fx = EXP2(nm - xl[1]);
            ok = ok && (xl[0] < 1.0e18f);
            lsum = lsum * fa + xl[0] * fx;
#pragma unroll
            for (int r = 0; r < 16; ++r) o[r] = o[r] * fa + xo[r] * fx;
        }
#pragma unroll
        for (int r = 0; r < 16; ++r) ok = ok && (__builtin_fabsf(o[r]) < 3.0e38f);
        if (__builtin_expect(__any(!ok), 0)) {
            float m = -__builtin_inff();
            f32x4 l;
            for (int pass = 0; pass < 2; ++pass) {
                o = z; l = f32x4{};
                for (int t = 0; t < 64; ++t) {
                    bf16x8 f0, f1, f2, f3;
                    asm volatile("global_load_dwordx4 %0, %4, %5 sc1\n\tglobal_load_dwordx4 %1, %4, %5 offset:1024 sc1\n\tglobal_load_dwordx4 %2, %4, %5 offset:2048 sc1\n\t"
                                 "global_load_dwordx4 %3, %4, %5 offset:3072 sc1\n\ts_waitcnt vmcnt(0)" : "=&v"(f0), "=&v"(f1), "=&v"(f2), "=&v"(f3) : "v"(l16 + t * 4096), "s"(kv) : "memory");
                    f32x16 s = MFMA32(f0, q0, z); s = MFMA32(f1, q1, s);
                    if (pass == 0) {
#pragma unroll
                        for (int r = 0; r < 16; ++r) m = __builtin_fmaxf(m, s[r]);
                    } else {
#pragma unroll
                        for (int r = 0; r < 16; ++r) s[r] = EXP2(s[r] - m);
                        const bf16x8 p0 = asbf(pack8(s, 0)), p1 = asbf(pack8(s, 1));
                        o = MFMA32(f2, p0, o); o = MFMA32(f3, p1, o);
                        l = MFMA16(asbf(ones), p0, l); l = MFMA16(asbf(ones), p1, l);
                    }
                }
                if (pass == 0) m = swap_max(m);
            }
            lsum = l[0];
        }
        const float inv = __builtin_amdgcn_rcpf(lsum);
#pragma unroll
        for (int r = 0; r < 16; ++r) o[r] *= inv;
        const char* op = ws + WS_OF + (size_t)((blk * 32 + head) * 2) * 1024;
        st16_dev<0>(op, l16, pack8(o, 0)); st16_dev<1024>(op, l16, pack8(o, 1));
    }
    {
        FRESH();
        const int g = wid >> 2, wi = wid & 3, wm = wi >> 1, wn = wi & 1, g4 = lane >> 4;
        const char* of = ws + WS_OF + (size_t)(br * 64 + mt3 * 4 + wi) * 65536;
        unsigned vw4[2];
#pragma unroll
        for (int ii = 0; ii < 2; ++ii) { const int k = 16 * wi + 4 * ii + g4; vw4[ii] = k * 2048 + (((lane & 15) ^ ((k & 7) << 1)) << 4); }
        const unsigned l16 = lane * 16, sdst = lds0 + wi * 4096;
#define STAGE_O(t) glds4k_dev(of + (((t) + k30) & 15) * 4096, l16, sdst + ((t) & 3) * 16384)
#define STAGE_WO(t, slot) do { const char* s_ = wo + (size_t)(((t) + k30) & 15) * 131072; glds16(s_, vw4[0], sdst + (slot)); glds16(s_, vw4[1], sdst + (slot) + 1024); \
            glds16(s_ + 16384, vw4[0], sdst + (slot) + 2048); glds16(s_ + 16384, vw4[1], sdst + (slot) + 3072); } while (0)
        const unsigned fo = (unsigned)WS_FO + ((br * 64 + mt3 * 4 + wi) * 32 + 4 * ((lane & 15) >> 1) + 2 * (1 - g) + (lane & 1)) * FS;
        drain();
        if (lane == 0) flag_set(ws + WS_FO, ((mt * 8 + wid) * 32 + 2 * hp + br) * FS, tag);
        SB_(2);
        asm volatile("s_barrier" ::: "memory");
        SB_(3);
        flag_wait(ws, fo, tag);
        STAGE_O(1 - g);
        if (g == 1) STAGE_O(2);
        STAGE_WO(5 - g, 65536 + (1 - g) * 16384);
        SA_(6); SB_(4);
        const unsigned ao = lds0 + g * 16384 + wm * 8192 + (g4 >> 1) * 1024 + ((lane & 15) + 32 * (g4 & 1)) * 16;
        unsigned aw[4];
        { const int q4 = (lane & 15) >> 2, p = lane & 3, k = 16 * (g4 >> 1) + 4 * (g4 & 1) + q4;
          const unsigned a0 = lds0 + g * 16384 + k * 256 + (((8 * wn + (p >> 1)) ^ ((k & 7) << 1)) << 4) + (p & 1) * 8;
#pragma unroll
          for (int nb = 0; nb < 4; ++nb) aw[nb] = a0 ^ (nb << 5); }
        f32x4 acc[4][4];
#pragma unroll
        for (int mb = 0; mb < 4; ++mb)
#pragma unroll
            for (int nb = 0; nb < 4; ++nb) acc[mb][nb] = f32x4{};
        if (g == 1) { WAITBAR_(8); asm volatile("s_barrier" ::: "memory"); } else asm volatile("s_barrier" ::: "memory");
        SB_(5);
        SBAR();
#pragma unroll
        for (int u = 0; u < 8; ++u) {
            bf16x8 of_[4][2], wf[4][2];
            const int to = 2 * u + 3 + g, tw = to + 2;
            const char* so = of + ((to + k30) & 15) * 4096; const char* sw = wo + (size_t)((tw + k30) & 15) * 131072;
            const unsigned dob = sdst + (to & 3) * 16384, dwb = sdst + (g ? p3_wslot(2 * u + 6) : p3_wslot(2 * u + 5));
            const bool doo = u < 6 || (u == 6 && g == 0), dow = (u >= 1 || g == 1) && (u < 5 || (u == 5 && g == 0));
#pragma unroll
            for (int i = 0; i < 8; ++i) {
                const int hh = i >> 2, q = i & 3;
                of_[q][hh] = ldsrd128(ao + ((2 * u) & 3) * 16384 + (q >> 1) * 4096 + (q & 1) * 256 + hh * 2048);
                wf[q][hh] = cat8(ldstr(aw[q] + p3_wslot(2 * u) + hh * 8192), ldstr(aw[q] + p3_wslot(2 * u) + hh * 8192 + 2048));
                if (i < 4) { if (u < 7 && doo) glds16_dev(so + i * 1024, l16, dob + i * 1024); }
                else if (u < 6 && dow) glds16(sw + ((i & 2) ? 16384 : 0), vw4[i & 1], dwb + (i - 4) * 1024);
            }
            if (p3_allow(2 * u) == p3_allow(2 * u + 1)) WAITBAR(p3_allow(2 * u)); else if (g == 0) WAITBAR(p3_allow(2 * u)); else WAITBAR(p3_allow(2 * u + 1));
            SBAR();
            __builtin_amdgcn_s_setprio(1);
#pragma unroll
            for (int mb = 0; mb < 4; ++mb)
#pragma unroll
                for (int nb = 0; nb < 4; ++nb) { acc[mb][nb] = MFMA16(wf[nb][0], of_[mb][0], acc[mb][nb]); acc[mb][nb] = MFMA16(wf[nb][1], of_[mb][1], acc[mb][nb]); CHAIN(); }
            __builtin_amdgcn_s_setprio(0);
            SBAR();
            if (u < 7 || g == 0) asm volatile("s_barrier" ::: "memory");
            SBAR();
        }
        SB_(6);
        FRESH();
        const unsigned xw = lds0 + wid * 8192 + l16, xr = lds0 + (wid ^ 4) * 8192 + l16;
#pragma unroll
        for (int i = 0; i < 8; ++i) { const f32x4 v = g ? acc[i >> 2][i & 3] : acc[2 + (i >> 2)][i & 3]; ldswr128(xw + i * 1024, __builtin_bit_cast(u32x4, v)); }
        BARRIER_L();
        const unsigned st = lds0 + 65536 + wid * 4096;
#pragma unroll
        for (int i = 0; i < 8; ++i) {
            const f32x4 v = (g ? acc[2 + (i >> 2)][i & 3] : acc[i >> 2][i & 3]) + __builtin_bit_cast(f32x4, ldsrd128(xr + i * 1024));
            const int row = 16 * (i >> 2) + (lane & 15), c = 2 * (i & 3) + (g4 >> 1);
            *(LAS u32x2*)(uintptr_t)(st + row * 128 + ((c ^ (row & 7)) << 4) + (g4 & 1) * 8) = u32x2{cvtpk(v[0], v[1]), cvtpk(v[2], v[3])};
        }
        const int blk = mt3 * 4 + 2 * wm + g;
        char* op = (char*)out + br * 2048 + nt3 * 256 + wn * 128 + (lane & 7) * 16;
#pragma unroll
        for (int i = 0; i < 4; ++i) {
            const int r = 8 * i + (lane >> 3), row = br ? ((blk >> 1) * 64 + 2 * r + (blk & 1)) : (blk * 32 + r);
            *(u32x4*)(op + (size_t)row * 4096) = __builtin_bit_cast(u32x4, ldsrd128(st + r * 128 + (((lane & 7) ^ (r & 7)) << 4)));
        }
#undef STAGE_O
#undef STAGE_WO
    }
    STAMP_AT(0, 7);
    if (wid == 4) {
        const unsigned rec = (100u << 20) + b * 128;
        if (lane == 0) { st16_dev<0>(ws0, rec, u32x4{pst[0], pst[1], pst[2], pst[3]}); st16_dev<16>(ws0, rec, u32x4{pst[4], pst[5], pst[6], pst[7]}); }
        drain();
        if (lane == 0) flag_set(ws0, rec + 64, tag);
    }
    if (b == 0 && wid == 1) {
        unsigned mn = 0xffffffffu, base = 0, mx = 0;
        for (int pass = 0; pass < 2; ++pass)
        for (int i = 0; i < 4; ++i) { const unsigned rec = (100u << 20) + (i * 64 + lane) * 128;
            flag_wait(ws0, rec + 64, tag);
            unsigned t0v, txv;
            asm volatile("global_load_dword %0, %2, %3 sc1\n\tglobal_load_dword %1, %2, %3 offset:%4 sc1\n\ts_waitcnt vmcnt(0)" : "=&v"(t0v), "=&v"(txv) : "v"(rec), "s"(ws0), "i"((PROBE & 7) * 4) : "memory");
            if (pass == 0) { if (i == 0) base = __builtin_amdgcn_readfirstlane(t0v) - 100000u; mn = min(mn, t0v - base); } else mx = max(mx, txv - base - mn);
            if (pass == 0 && i == 3) { for (int o = 32; o; o >>= 1) mn = min(mn, (unsigned)__shfl_xor((int)mn, o)); } }
        for (int o = 32; o; o >>= 1) mx = max(mx, (unsigned)__shfl_xor((int)mx, o));
        const unsigned until = base + mn + 15000u + ((PROBE & 8) ? 0u : mx);
        while ((int)((unsigned)__builtin_amdgcn_s_memrealtime() - until) < 0) __builtin_amdgcn_s_sleep(1);
    }
    if (b == 0 && wid == 0) {
#pragma unroll
        for (int i = 0; i < 2; ++i) { const int u = i * 64 + lane;
            flag_wait(ws0, (unsigned)WS_FK + ((2 * (u >> 3)) * 32 + (u & 7) * 4) * FS, tag);
            flag_wait(ws0, (unsigned)WS_FO + ((64 + (u & 7) * 8) * 32 + 2 * (u >> 3)) * FS, tag); }
        if (lane == 0) flag_set(ws0, (unsigned)WS_EP, tag);
    }
    asm volatile(".Lmk_code_end:");
}

extern "C" void kernel_launch(void* const* d_in, const int* in_sizes, int n_in, void* d_out, int out_size, void* d_ws, size_t ws_size, hipStream_t stream) {
    mk_fused<<<256, 512, 0, stream>>>((const u16*)d_in[0], (const u16*)d_in[1], (const u16*)d_in[2], (const u16*)d_in[3], (const u16*)d_in[4], (const u16*)d_in[5], (char*)d_ws, (u16*)d_out);
}
```

```cpp
#define OUTST 1
#include <hip/hip_runtime.h>
#include <stdint.h>

typedef unsigned short u16;
typedef unsigned long long u64;
typedef __bf16 bf16x8 __attribute__((ext_vector_type(8)));
typedef __bf16 bf16x2 __attribute__((ext_vector_type(2)));
typedef short s16x4 __attribute__((ext_vector_type(4)));
typedef float f32x16 __attribute__((ext_vector_type(16)));
typedef float f32x4 __attribute__((ext_vector_type(4)));
typedef float f32x2 __attribute__((ext_vector_type(2)));
typedef unsigned u32x4 __attribute__((ext_vector_type(4)));
typedef unsigned u32x2 __attribute__((ext_vector_type(2)));
typedef unsigned u32x8 __attribute__((ext_vector_type(8)));
typedef unsigned u32x16 __attribute__((ext_vector_type(16)));
#define LAS __attribute__((address_space(3)))
#define DI __device__ __forceinline__
#define SBAR() __builtin_amdgcn_sched_barrier(0)
#define MFMA32(a, b, c) __builtin_amdgcn_mfma_f32_32x32x16_bf16((a), (b), (c), 0, 0, 0)
#define MFMA16(a, b, c) __builtin_amdgcn_mfma_f32_16x16x32_bf16((a), (b), (c), 0, 0, 0)
#define CHAIN() SBAR()
#define EXP2(x) __builtin_amdgcn_exp2f(x)

constexpr float QSCALE = 0.17677669529663687f * 1.4426950408889634f;

constexpr size_t WS_QF = 0;
constexpr size_t WS_KV = WS_QF + (4u << 20);
constexpr size_t WS_OF = WS_KV + (8u << 20);
constexpr size_t WS_HALO = WS_OF + (8u << 20);
constexpr size_t WS_XP = WS_HALO + (2u << 20);
constexpr size_t WS_FX = WS_XP + (5u << 20);
#define FS 64
constexpr size_t WS_FQ = WS_FX + 1024 * FS;
constexpr size_t WS_FK = WS_FQ + 1024 * FS;
constexpr size_t WS_FO = WS_FK + 1024 * FS;
constexpr size_t WS_FH = WS_FO + 4096 * FS;
constexpr size_t WS_EP = WS_FH + 256 * FS;

#define SLP 1
#define STAMP_AT(K, I)
#define SA_(I)
#define SB_(I)

#define SC_(I)
DI unsigned cvtpk(float lo, float hi) { f32x2 v = {lo, hi}; bf16x2 b = __builtin_convertvector(v, bf16x2); return __builtin_bit_cast(unsigned, b); }
DI u32x4 pack8(const f32x16& a, int s) { u32x4 r; r[0] = cvtpk(a[8 * s], a[8 * s + 1]); r[1] = cvtpk(a[8 * s + 2], a[8 * s + 3]); r[2] = cvtpk(a[8 * s + 4], a[8 * s + 5]); r[3] = cvtpk(a[8 * s + 6], a[8 * s + 7]); return r; }
DI u32x4 pack44(f32x4 a, f32x4 c) { return u32x4{cvtpk(a[0], a[1]), cvtpk(a[2], a[3]), cvtpk(c[0], c[1]), cvtpk(c[2], c[3])}; }
DI u32x4 asu(bf16x8 v) { return __builtin_bit_cast(u32x4, v); }
DI u32x16 cat16(u32x4 a, u32x4 b, u32x4 c, u32x4 d) { return u32x16{a[0], a[1], a[2], a[3], b[0], b[1], b[2], b[3], c[0], c[1], c[2], c[3], d[0], d[1], d[2], d[3]}; }
DI bf16x8 asbf(u32x4 v) { return __builtin_bit_cast(bf16x8, v); }
DI void glds16(const void* sbase, unsigned voff, unsigned lds) {
    asm volatile("s_mov_b32 m0, %2\n\ts_nop 0\n\tglobal_load_lds_dwordx4 %0, %1" :: "v"(voff), "s"(sbase), "s"(lds) : "memory");
}
DI void glds16_dev(const void* sbase, unsigned voff, unsigned lds) {
    asm volatile("s_mov_b32 m0, %2\n\ts_nop 0\n\tglobal_load_lds_dwordx4 %0, %1 sc1" :: "v"(voff), "s"(sbase), "s"(lds) : "memory");
}
DI void glds4k_dev(const void* sbase, unsigned voff, unsigned lds) {
    asm volatile("s_mov_b32 m0, %2\n\ts_nop 0\n\tglobal_load_lds_dwordx4 %0, %1 sc1\n\tglobal_load_lds_dwordx4 %0, %1 offset:1024 sc1\n\t"
                 "global_load_lds_dwordx4 %0, %1 offset:2048 sc1\n\tglobal_load_lds_dwordx4 %0, %1 offset:3072 sc1" :: "v"(voff), "s"(sbase), "s"(lds) : "memory");
}
template <int OFF> DI void st16_dev(const void* sbase, unsigned voff, u32x4 v) {
    asm volatile("global_store_dwordx4 %0, %1, %2 offset:%3 sc1\n\ts_nop 1" :: "v"(voff), "v"(v), "s"(sbase), "i"(OFF) : "memory");
}
DI void drain() { asm volatile("s_waitcnt vmcnt(0)" ::: "memory"); }
DI void flag_set(const void* sbase, unsigned voff, u64 tag) {
    asm volatile("global_store_dwordx2 %0, %1, %2 sc1" :: "v"(voff), "v"(tag), "s"(sbase) : "memory");
}
DI void flag_wait(const void* sbase, unsigned voff, u64 tag) {
    const u64 t0 = __builtin_amdgcn_s_memrealtime();
    for (;;) {
        u64 v;
        asm volatile("global_load_dwordx2 %0, %1, %2 sc1\n\ts_waitcnt vmcnt(0)" : "=v"(v) : "v"(voff), "s"(sbase) : "memory");
        if (__all(v == tag)) break;
        if (__builtin_amdgcn_s_memrealtime() - t0 > 2000000ull) break;
        __builtin_amdgcn_s_sleep(SLP);
    }
}
DI u64 flag_wait_some(const void* sbase, unsigned voff, u64 tag, u64 need) {
    const u64 t0 = __builtin_amdgcn_s_memrealtime();
    for (;;) {
        u64 v;
        asm volatile("global_load_dwordx2 %0, %1, %2 sc1\n\ts_waitcnt vmcnt(0)" : "=v"(v) : "v"(voff), "s"(sbase) : "memory");
        const u64 m = __ballot(v == tag);
        if ((m & need) == need || __builtin_amdgcn_s_memrealtime() - t0 > 2000000ull) return m;
        __builtin_amdgcn_s_sleep(SLP);
    }
}
DI bf16x8 ldsrd128(unsigned addr) { return *(const LAS bf16x8*)(uintptr_t)addr; }
DI void ldswr128(unsigned addr, u32x4 v) { *(LAS u32x4*)(uintptr_t)addr = v; }
DI s16x4 ldstr(unsigned addr) { return __builtin_amdgcn_ds_read_tr16_b64_v4i16((LAS s16x4*)(uintptr_t)addr); }
DI bf16x8 cat8(s16x4 lo, s16x4 hi) { typedef short s16x8 __attribute__((ext_vector_type(8))); s16x8 v = {lo[0], lo[1], lo[2], lo[3], hi[0], hi[1], hi[2], hi[3]}; return __builtin_bit_cast(bf16x8, v); }
DI float swap_max(float m) { auto r = __builtin_amdgcn_permlane32_swap(__float_as_uint(m), __float_as_uint(m), false, false); return __builtin_fmaxf(__uint_as_float(r[0]), __uint_as_float(r[1])); }
DI float swap_add(float m) { auto r = __builtin_amdgcn_permlane32_swap(__float_as_uint(m), __float_as_uint(m), false, false); return __uint_as_float(r[0]) + __uint_as_float(r[1]); }
#define FRESH() asm volatile("" : "+v"(lane), "+s"(ws))
#define BARRIER_L() asm volatile("s_waitcnt lgkmcnt(0)\n\ts_barrier" ::: "memory")
#define WAITBAR_(n) asm volatile("s_waitcnt vmcnt(%0) lgkmcnt(0)\n\ts_barrier" :: "i"(n) : "memory")
#define WAITBAR(n) do { switch (n) { case 0: WAITBAR_(0); break; case 2: WAITBAR_(2); break; case 3: WAITBAR_(3); break; case 4: WAITBAR_(4); break; case 6: WAITBAR_(6); break; \
    case 7: WAITBAR_(7); break; case 8: WAITBAR_(8); break; case 10: WAITBAR_(10); break; case 12: WAITBAR_(12); break; default: WAITBAR_(0); } } while (0)

struct LocalPart { f32x16 o; float m, l; };
DI f32x16 band_mask(int lane) {
    const int i = lane & 31, h = lane >> 5;
    f32x16 c;
#pragma unroll
    for (int r = 0; r < 16; ++r) { const int j = (r & 3) + 8 * (r >> 2) + 4 * h; c[r] = ((unsigned)(i - j) <= 7u) ? 0.f : -__builtin_inff(); }
    return c;
}
DI float max3(float a, float b, float c) { float r; asm("v_max3_f32 %0, %1, %2, %3" : "=v"(r) : "v"(a), "v"(b), "v"(c)); return r; }
template <bool PREV> DI void local_own(bf16x8 q0, bf16x8 q1, bf16x8 k0, bf16x8 k1, bf16x8 v0, bf16x8 v1, bf16x8 kp0, bf16x8 kp1, bf16x8 vp1, const f32x16& band, int lane, LocalPart& P) {
    const f32x16 z = {};
    f32x16 so = MFMA32(k0, q0, band), sp = z;
    if (PREV) sp = MFMA32(kp0, q0, z);
    so = MFMA32(k1, q1, so);
    if (PREV) sp = MFMA32(kp1, q1, sp);
    float m = __builtin_fmaxf(so[0], so[1]);
#pragma unroll
    for (int r = 2; r < 16; r += 2) m = __builtin_fmaxf(__builtin_fmaxf(m, so[r]), so[r + 1]);
    if (PREV) {
        const int d = 4 * (lane >> 5) - (lane & 31);
#pragma unroll
        for (int r = 12; r < 16; ++r) { sp[r] = (d + (r & 3) >= 1) ? sp[r] : -__builtin_inff(); m = __builtin_fmaxf(m, sp[r]); }
    }
    m = swap_max(m);
    float l = 0.f;
#pragma unroll
    for (int r = 0; r < 16; ++r) { so[r] = EXP2(so[r] - m); l += so[r]; }
    P.o = MFMA32(v0, asbf(pack8(so, 0)), z); P.o = MFMA32(v1, asbf(pack8(so, 1)), P.o);
    if (PREV) {
#pragma unroll
        for (int r = 12; r < 16; ++r) { sp[r] = EXP2(sp[r] - m); l += sp[r]; }
        P.o = MFMA32(vp1, asbf(u32x4{0u, 0u, cvtpk(sp[12], sp[13]), cvtpk(sp[14], sp[15])}), P.o);
    }
    P.m = m; P.l = l;
}
DI void local_prev(bf16x8 q0, bf16x8 q1, bf16x8 kp0, bf16x8 kp1, bf16x8 vp1, int lane, LocalPart& P) {
    const int i = lane & 31, h = lane >> 5;
    const f32x16 z = {};
    f32x16 sp = MFMA32(kp0, q0, z); sp = MFMA32(kp1, q1, sp);
    const float NEG = -__builtin_inff();
    float m = P.m;
#pragma unroll
    for (int r = 12; r < 16; ++r) { const int j = (r & 3) + 24 + 4 * h; sp[r] = (j >= i + 25) ? sp[r] : NEG; m = __builtin_fmaxf(m, sp[r]); }
    m = swap_max(m);
    const float f = EXP2(P.m - m);
    float l = P.l * f;
#pragma unroll
    for (int r = 12; r < 16; ++r) { sp[r] = EXP2(sp[r] - m); l += sp[r]; }
#pragma unroll
    for (int r = 0; r < 16; ++r) P.o[r] *= f;
    P.o = MFMA32(vp1, asbf(u32x4{0u, 0u, cvtpk(sp[12], sp[13]), cvtpk(sp[14], sp[15])}), P.o);
    P.m = m; P.l = l;
}
DI void local_fin(LocalPart& P) {
    const float inv = __builtin_amdgcn_rcpf(swap_add(P.l));
#pragma unroll
    for (int r = 0; r < 16; ++r) P.o[r] *= inv;
    const u32x4 o0 = pack8(P.o, 0), o1 = pack8(P.o, 1);
#pragma unroll
    for (int r = 0; r < 4; ++r) { P.o[r] = __uint_as_float(o0[r]); P.o[4 + r] = __uint_as_float(o1[r]); }
}
DI u32x4 quad(const f32x16& a, int i) { return __builtin_bit_cast(u32x4, f32x4{a[4 * i], a[4 * i + 1], a[4 * i + 2], a[4 * i + 3]}); }

#define ATT_L6C \
    "s_nop 1\n\t" \
    "s_cmp_eq_u32 %[hl], 0\n\t" \
    "s_cbranch_scc1 10f\n\t" \
    "global_load_dwordx4 v[144:147], v143, %[ws] offset:-3072 sc1\n\t" \
    "global_load_dwordx4 v[148:151], v143, %[ws] offset:-2048 sc1\n\t" \
    "global_load_dwordx4 v[152:155], v143, %[ws] offset:-1024 sc1\n\t" \
    "global_load_dwordx4 v[156:159], v143, %[ws] offset:0 sc1\n\t" \
    "global_load_dwordx4 v[160:163], v143, %[ws] offset:1024 sc1\n\t" \
    "global_load_dwordx4 v[164:167], v143, %[ws] offset:2048 sc1\n\t" \
    "10:\n" \
    "s_setprio 3\n\t" \
    "s_waitcnt lgkmcnt(0)\n\t" \
    "v_mfma_f32_32x32x16_bf16 v[48:63], v[80:83], v[64:67], v[16:31]\n\t" \
    "v_exp_f32_e32 v32, v32\n\t" \
    "v_exp_f32_e32 v33, v33\n\t" \
    "v_exp_f32_e32 v34, v34\n\t" \
    "v_mfma_f32_32x32x16_bf16 v[48:63], v[84:87], v[68:71], v[48:63]\n\t" \
    "ds_read_b128 v[80:83], v112 offset:8192\n\t" \
    "ds_read_b128 v[84:87], v112 offset:9216\n\t" \
    "v_exp_f32_e32 v35, v35\n\t" \
    "v_exp_f32_e32 v36, v36\n\t" \
    "v_cvt_pk_bf16_f32 v104, v32, v33\n\t" \
    "v_cvt_pk_bf16_f32 v105, v34, v35\n\t" \
    "ds_read_b128 v[100:103], v112 offset:3072\n\t" \
    "ds_read_b128 v[96:99], v112 offset:6144\n\t" \
    "v_exp_f32_e32 v37, v37\n\t" \
    "v_exp_f32_e32 v38, v38\n\t" \
    "v_exp_f32_e32 v39, v39\n\t" \
    "v_exp_f32_e32 v40, v40\n\t" \
    "v_cvt_pk_bf16_f32 v106, v36, v37\n\t" \
    "v_cvt_pk_bf16_f32 v107, v38, v39\n\t" \
    "v_exp_f32_e32 v41, v41\n\t" \
    "v_exp_f32_e32 v42, v42\n\t" \
    "v_mfma_f32_32x32x16_bf16 v[0:15], v[88:91], v[104:107], 0\n\t" \
    "v_exp_f32_e32 v43, v43\n\t" \
    "v_exp_f32_e32 v44, v44\n\t" \
    "v_exp_f32_e32 v45, v45\n\t" \
    "v_mfma_f32_16x16x32_bf16 v[76:79], v[72:75], v[104:107], 0\n\t" \
    "v_exp_f32_e32 v46, v46\n\t" \
    "v_exp_f32_e32 v47, v47\n\t" \
    "v_cvt_pk_bf16_f32 v108, v40, v41\n\t" \
    "v_cvt_pk_bf16_f32 v109, v42, v43\n\t" \
    "v_cvt_pk_bf16_f32 v110, v44, v45\n\t" \
    "v_cvt_pk_bf16_f32 v111, v46, v47\n\t" \
    "s_mov_b32 m0, %[fld]\n\t" \
    "s_nop 0\n\t" \
    "global_load_lds_dwordx4 v142, %[ws] sc1\n\t" \
    "s_waitcnt lgkmcnt(0)\n\t" \
    "v_mfma_f32_32x32x16_bf16 v[32:47], v[80:83], v[64:67], v[16:31]\n\t" \
    "v_exp_f32_e32 v48, v48\n\t" \
    "v_exp_f32_e32 v49, v49\n\t" \
    "v_exp_f32_e32 v50, v50\n\t" \
    "v_mfma_f32_32x32x16_bf16 v[32:47], v[84:87], v[68:71], v[32:47]\n\t" \
    "ds_read_b128 v[80:83], v112 offset:12288\n\t" \
    "ds_read_b128 v[84:87], v112 offset:13312\n\t" \
    "v_exp_f32_e32 v51, v51\n\t" \
    "v_exp_f32_e32 v52, v52\n\t" \
    "v_cvt_pk_bf16_f32 v104, v48, v49\n\t" \
    "v_cvt_pk_bf16_f32 v105, v50, v51\n\t" \
    "v_mfma_f32_32x32x16_bf16 v[0:15], v[100:103], v[108:111], v[0:15]\n\t" \
    "ds_read_b128 v[92:95], v112 offset:7168\n\t" \
    "ds_read_b128 v[88:91], v112 offset:10240\n\t" \
    "v_exp_f32_e32 v53, v53\n\t" \
    "v_exp_f32_e32 v54, v54\n\t" \
    "v_exp_f32_e32 v55, v55\n\t" \
    "v_mfma_f32_16x16x32_bf16 v[76:79], v[72:75], v[108:111], v[76:79]\n\t" \
    "v_exp_f32_e32 v56, v56\n\t" \
    "v_cvt_pk_bf16_f32 v106, v52, v53\n\t" \
    "v_cvt_pk_bf16_f32 v107, v54, v55\n\t" \
    "v_exp_f32_e32 v57, v57\n\t" \
    "v_exp_f32_e32 v58, v58\n\t" \
    "v_mfma_f32_32x32x16_bf16 v[0:15], v[96:99], v[104:107], v[0:15]\n\t" \
    "v_exp_f32_e32 v59, v59\n\t" \
    "v_exp_f32_e32 v60, v60\n\t" \
    "v_exp_f32_e32 v61, v61\n\t" \
    "v_mfma_f32_16x16x32_bf16 v[76:79], v[72:75], v[104:107], v[76:79]\n\t" \
    "v_exp_f32_e32 v62, v62\n\t" \
    "v_exp_f32_e32 v63, v63\n\t" \
    "v_cvt_pk_bf16_f32 v108, v56, v57\n\t" \
    "v_cvt_pk_bf16_f32 v109, v58, v59\n\t" \
    "v_cvt_pk_bf16_f32 v110, v60, v61\n\t" \
    "v_cvt_pk_bf16_f32 v111, v62, v63\n\t" \
    "s_setprio 2\n\t" \
    "s_waitcnt lgkmcnt(0)\n\t" \
    "v_mfma_f32_32x32x16_bf16 v[48:63], v[80:83], v[64:67], v[16:31]\n\t" \
    "v_exp_f32_e32 v32, v32\n\t" \
    "v_exp_f32_e32 v33, v33\n\t" \
    "v_exp_f32_e32 v34, v34\n\t" \
    "v_mfma_f32_32x32x16_bf16 v[48:63], v[84:87], v[68:71], v[48:63]\n\t" \
    "ds_read_b128 v[80:83], v112 offset:16384\n\t" \
    "ds_read_b128 v[84:87], v112 offset:17408\n\t" \
    "v_exp_f32_e32 v35, v35\n\t" \
    "v_exp_f32_e32 v36, v36\n\t" \
    "v_cvt_pk_bf16_f32 v104, v32, v33\n\t" \
    "v_cvt_pk_bf16_f32 v105, v34, v35\n\t" \
    "v_mfma_f32_32x32x16_bf16 v[0:15], v[92:95], v[108:111], v[0:15]\n\t" \
    "ds_read_b128 v[100:103], v112 offset:11264\n\t" \
    "ds_read_b128 v[96:99], v112 offset:14336\n\t" \
    "v_exp_f32_e32 v37, v37\n\t" \
    "v_exp_f32_e32 v38, v38\n\t" \
    "v_exp_f32_e32 v39, v39\n\t" \
    "v_mfma_f32_16x16x32_bf16 v[76:79], v[72:75], v[108:111], v[76:79]\n\t" \
    "v_exp_f32_e32 v40, v40\n\t" \
    "v_cvt_pk_bf16_f32 v106, v36, v37\n\t" \
    "v_cvt_pk_bf16_f32 v107, v38, v39\n\t" \
    "v_exp_f32_e32 v41, v41\n\t" \
    "v_exp_f32_e32 v42, v42\n\t" \
    "v_mfma_f32_32x32x16_bf16 v[0:15], v[88:91], v[104:107], v[0:15]\n\t" \
    "v_exp_f32_e32 v43, v43\n\t" \
    "v_exp_f32_e32 v44, v44\n\t" \
    "v_exp_f32_e32 v45, v45\n\t" \
    "v_mfma_f32_16x16x32_bf16 v[76:79], v[72:75], v[104:107], v[76:79]\n\t" \
    "v_exp_f32_e32 v46, v46\n\t" \
    "v_exp_f32_e32 v47, v47\n\t" \
    "v_cvt_pk_bf16_f32 v108, v40, v41\n\t" \
    "v_cvt_pk_bf16_f32 v109, v42, v43\n\t" \
    "v_cvt_pk_bf16_f32 v110, v44, v45\n\t" \
    "v_cvt_pk_bf16_f32 v111, v46, v47\n\t" \
    "s_waitcnt lgkmcnt(0)\n\t" \
    "v_mfma_f32_32x32x16_bf16 v[32:47], v[80:83], v[64:67], v[16:31]\n\t" \
    "v_exp_f32_e32 v48, v48\n\t" \
    "v_exp_f32_e32 v49, v49\n\t" \
    "v_exp_f32_e32 v50, v50\n\t" \
    "v_mfma_f32_32x32x16_bf16 v[32:47], v[84:87], v[68:71], v[32:47]\n\t" \
    "ds_read_b128 v[80:83], v112 offset:20480\n\t" \
    "ds_read_b128 v[84:87], v112 offset:21504\n\t" \
    "v_exp_f32_e32 v51, v51\n\t" \
    "v_exp_f32_e32 v52, v52\n\t" \
    "v_cvt_pk_bf16_f32 v104, v48, v49\n\t" \
    "v_cvt_pk_bf16_f32 v105, v50, v51\n\t" \
    "v_mfma_f32_32x32x16_bf16 v[0:15], v[100:103], v[108:111], v[0:15]\n\t" \
    "ds_read_b128 v[92:95], v112 offset:15360\n\t" \
    "ds_read_b128 v[88:91], v112 offset:18432\n\t" \
    "v_exp_f32_e32 v53, v53\n\t" \
    "v_exp_f32_e32 v54, v54\n\t" \
    "v_exp_f32_e32 v55, v55\n\t" \
    "v_mfma_f32_16x16x32_bf16 v[76:79], v[72:75], v[108:111], v[76:79]\n\t" \
    "v_exp_f32_e32 v56, v56\n\t" \
    "v_cvt_pk_bf16_f32 v106, v52, v53\n\t" \
    "v_cvt_pk_bf16_f32 v107, v54, v55\n\t" \
    "v_exp_f32_e32 v57, v57\n\t" \
    "v_exp_f32_e32 v58, v58\n\t" \
    "v_mfma_f32_32x32x16_bf16 v[0:15], v[96:99], v[104:107], v[0:15]\n\t" \
    "v_exp_f32_e32 v59, v59\n\t" \
    "v_exp_f32_e32 v60, v60\n\t" \
    "v_exp_f32_e32 v61, v61\n\t" \
    "v_mfma_f32_16x16x32_bf16 v[76:79], v[72:75], v[104:107], v[76:79]\n\t" \
    "v_exp_f32_e32 v62, v62\n\t" \
    "v_exp_f32_e32 v63, v63\n\t" \
    "v_cvt_pk_bf16_f32 v108, v56, v57\n\t" \
    "v_cvt_pk_bf16_f32 v109, v58, v59\n\t" \
    "v_cvt_pk_bf16_f32 v110, v60, v61\n\t" \
    "v_cvt_pk_bf16_f32 v111, v62, v63\n\t" \
    "s_setprio 1\n\t" \
    "s_waitcnt lgkmcnt(0)\n\t" \
    "v_mfma_f32_32x32x16_bf16 v[48:63], v[80:83], v[64:67], v[16:31]\n\t" \
    "v_exp_f32_e32 v32, v32\n\t" \
    "v_exp_f32_e32 v33, v33\n\t" \
    "v_exp_f32_e32 v34, v34\n\t" \
    "v_mfma_f32_32x32x16_bf16 v[48:63], v[84:87], v[68:71], v[48:63]\n\t" \
    "ds_read_b128 v[80:83], v112 offset:24576\n\t" \
    "ds_read_b128 v[84:87], v112 offset:25600\n\t" \
    "v_exp_f32_e32 v35, v35\n\t" \
    "v_exp_f32_e32 v36, v36\n\t" \
    "v_cvt_pk_bf16_f32 v104, v32, v33\n\t" \
    "v_cvt_pk_bf16_f32 v105, v34, v35\n\t" \
    "v_mfma_f32_32x32x16_bf16 v[0:15], v[92:95], v[108:111], v[0:15]\n\t" \
    "ds_read_b128 v[100:103], v112 offset:19456\n\t" \
    "ds_read_b128 v[96:99], v112 offset:22528\n\t" \
    "v_exp_f32_e32 v37, v37\n\t" \
    "v_exp_f32_e32 v38, v38\n\t" \
    "v_exp_f32_e32 v39, v39\n\t" \
    "v_mfma_f32_16x16x32_bf16 v[76:79], v[72:75], v[108:111], v[76:79]\n\t" \
    "v_exp_f32_e32 v40, v40\n\t" \
    "v_cvt_pk_bf16_f32 v106, v36, v37\n\t" \
    "v_cvt_pk_bf16_f32 v107, v38, v39\n\t" \
    "v_exp_f32_e32 v41, v41\n\t" \
    "v_exp_f32_e32 v42, v42\n\t" \
    "v_mfma_f32_32x32x16_bf16 v[0:15], v[88:91], v[104:107], v[0:15]\n\t" \
    "v_exp_f32_e32 v43, v43\n\t" \
    "v_exp_f32_e32 v44, v44\n\t" \
    "v_exp_f32_e32 v45, v45\n\t" \
    "v_mfma_f32_16x16x32_bf16 v[76:79], v[72:75], v[104:107], v[76:79]\n\t" \
    "v_exp_f32_e32 v46, v46\n\t" \
    "v_exp_f32_e32 v47, v47\n\t" \
    "v_cvt_pk_bf16_f32 v108, v40, v41\n\t" \
    "v_cvt_pk_bf16_f32 v109, v42, v43\n\t" \
    "v_cvt_pk_bf16_f32 v110, v44, v45\n\t" \
    "v_cvt_pk_bf16_f32 v111, v46, v47\n\t" \
    "s_waitcnt lgkmcnt(0)\n\t" \
    "v_mfma_f32_32x32x16_bf16 v[32:47], v[80:83], v[64:67], v[16:31]\n\t" \
    "v_exp_f32_e32 v48, v48\n\t" \
    "v_exp_f32_e32 v49, v49\n\t" \
    "v_exp_f32_e32 v50, v50\n\t" \
    "v_mfma_f32_32x32x16_bf16 v[32:47], v[84:87], v[68:71], v[32:47]\n\t" \
    "ds_read_b128 v[80:83], v112 offset:28672\n\t" \
    "ds_read_b128 v[84:87], v112 offset:29696\n\t" \
    "v_exp_f32_e32 v51, v51\n\t" \
    "v_exp_f32_e32 v52, v52\n\t" \
    "v_cvt_pk_bf16_f32 v104, v48, v49\n\t" \
    "v_cvt_pk_bf16_f32 v105, v50, v51\n\t" \
    "v_mfma_f32_32x32x16_bf16 v[0:15], v[100:103], v[108:111], v[0:15]\n\t" \
    "ds_read_b128 v[92:95], v112 offset:23552\n\t" \
    "ds_read_b128 v[88:91], v112 offset:26624\n\t" \
    "v_exp_f32_e32 v53, v53\n\t" \
    "v_exp_f32_e32 v54, v54\n\t" \
    "v_exp_f32_e32 v55, v55\n\t" \
    "v_mfma_f32_16x16x32_bf16 v[76:79], v[72:75], v[108:111], v[76:79]\n\t" \
    "v_exp_f32_e32 v56, v56\n\t" \
    "v_cvt_pk_bf16_f32 v106, v52, v53\n\t" \
    "v_cvt_pk_bf16_f32 v107, v54, v55\n\t" \
    "v_exp_f32_e32 v57, v57\n\t" \
    "v_exp_f32_e32 v58, v58\n\t" \
    "v_mfma_f32_32x32x16_bf16 v[0:15], v[96:99], v[104:107], v[0:15]\n\t" \
    "v_exp_f32_e32 v59, v59\n\t" \
    "v_exp_f32_e32 v60, v60\n\t" \
    "v_exp_f32_e32 v61, v61\n\t" \
    "v_mfma_f32_16x16x32_bf16 v[76:79], v[72:75], v[104:107], v[76:79]\n\t" \
    "v_exp_f32_e32 v62, v62\n\t" \
    "v_exp_f32_e32 v63, v63\n\t" \
    "v_cvt_pk_bf16_f32 v108, v56, v57\n\t" \
    "v_cvt_pk_bf16_f32 v109, v58, v59\n\t" \
    "v_cvt_pk_bf16_f32 v110, v60, v61\n\t" \
    "v_cvt_pk_bf16_f32 v111, v62, v63\n\t" \
    "s_setprio 0\n\t" \
    "s_cmp_eq_u32 %[hl], 0\n\t" \
    "s_cbranch_scc1 11f\n\t" \
    "s_waitcnt vmcnt(1)\n\t" \
    "11:\n" \
    "s_waitcnt lgkmcnt(0)\n\t" \
    "s_nop 15\n\t"
#define ATT_L4 \
    "s_nop 1\n\t" \
    "s_setprio 3\n\t" \
    "s_waitcnt lgkmcnt(0)\n\t" \
    "v_mfma_f32_32x32x16_bf16 v[48:63], v[80:83], v[64:67], v[16:31]\n\t" \
    "v_exp_f32_e32 v32, v32\n\t" \
    "v_exp_f32_e32 v33, v33\n\t" \
    "v_exp_f32_e32 v34, v34\n\t" \
    "v_mfma_f32_32x32x16_bf16 v[48:63], v[84:87], v[68:71], v[48:63]\n\t" \
    "ds_read_b128 v[80:83], v112 offset:8192\n\t" \
    "ds_read_b128 v[84:87], v112 offset:9216\n\t" \
    "v_exp_f32_e32 v35, v35\n\t" \
    "v_exp_f32_e32 v36, v36\n\t" \
    "v_cvt_pk_bf16_f32 v104, v32, v33\n\t" \
    "v_cvt_pk_bf16_f32 v105, v34, v35\n\t" \
    "ds_read_b128 v[100:103], v112 offset:3072\n\t" \
    "ds_read_b128 v[96:99], v112 offset:6144\n\t" \
    "v_exp_f32_e32 v37, v37\n\t" \
    "v_exp_f32_e32 v38, v38\n\t" \
    "v_exp_f32_e32 v39, v39\n\t" \
    "v_exp_f32_e32 v40, v40\n\t" \
    "v_cvt_pk_bf16_f32 v106, v36, v37\n\t" \
    "v_cvt_pk_bf16_f32 v107, v38, v39\n\t" \
    "v_exp_f32_e32 v41, v41\n\t" \
    "v_exp_f32_e32 v42, v42\n\t" \
    "v_mfma_f32_32x32x16_bf16 v[0:15], v[88:91], v[104:107], 0\n\t" \
    "v_exp_f32_e32 v43, v43\n\t" \
    "v_exp_f32_e32 v44, v44\n\t" \
    "v_exp_f32_e32 v45, v45\n\t" \
    "v_mfma_f32_16x16x32_bf16 v[76:79], v[72:75], v[104:107], 0\n\t" \
    "v_exp_f32_e32 v46, v46\n\t" \
    "v_exp_f32_e32 v47, v47\n\t" \
    "v_cvt_pk_bf16_f32 v108, v40, v41\n\t" \
    "v_cvt_pk_bf16_f32 v109, v42, v43\n\t" \
    "v_cvt_pk_bf16_f32 v110, v44, v45\n\t" \
    "v_cvt_pk_bf16_f32 v111, v46, v47\n\t" \
    "s_waitcnt lgkmcnt(0)\n\t" \
    "v_mfma_f32_32x32x16_bf16 v[32:47], v[80:83], v[64:67], v[16:31]\n\t" \
    "v_exp_f32_e32 v48, v48\n\t" \
    "v_exp_f32_e32 v49, v49\n\t" \
    "v_exp_f32_e32 v50, v50\n\t" \
    "v_mfma_f32_32x32x16_bf16 v[32:47], v[84:87], v[68:71], v[32:47]\n\t" \
    "ds_read_b128 v[80:83], v112 offset:12288\n\t" \
    "ds_read_b128 v[84:87], v112 offset:13312\n\t" \
    "v_exp_f32_e32 v51, v51\n\t" \
    "v_exp_f32_e32 v52, v52\n\t" \
    "v_cvt_pk_bf16_f32 v104, v48, v49\n\t" \
    "v_cvt_pk_bf16_f32 v105, v50, v51\n\t" \
    "v_mfma_f32_32x32x16_bf16 v[0:15], v[100:103], v[108:111], v[0:15]\n\t" \
    "ds_read_b128 v[92:95], v112 offset:7168\n\t" \
    "ds_read_b128 v[88:91], v112 offset:10240\n\t" \
    "v_exp_f32_e32 v53, v53\n\t" \
    "v_exp_f32_e32 v54, v54\n\t" \
    "v_exp_f32_e32 v55, v55\n\t" \
    "v_mfma_f32_16x16x32_bf16 v[76:79], v[72:75], v[108:111], v[76:79]\n\t" \
    "v_exp_f32_e32 v56, v56\n\t" \
    "v_cvt_pk_bf16_f32 v106, v52, v53\n\t" \
    "v_cvt_pk_bf16_f32 v107, v54, v55\n\t" \
    "v_exp_f32_e32 v57, v57\n\t" \
    "v_exp_f32_e32 v58, v58\n\t" \
    "v_mfma_f32_32x32x16_bf16 v[0:15], v[96:99], v[104:107], v[0:15]\n\t" \
    "v_exp_f32_e32 v59, v59\n\t" \
    "v_exp_f32_e32 v60, v60\n\t" \
    "v_exp_f32_e32 v61, v61\n\t" \
    "v_mfma_f32_16x16x32_bf16 v[76:79], v[72:75], v[104:107], v[76:79]\n\t" \
    "v_exp_f32_e32 v62, v62\n\t" \
    "v_exp_f32_e32 v63, v63\n\t" \
    "v_cvt_pk_bf16_f32 v108, v56, v57\n\t" \
    "v_cvt_pk_bf16_f32 v109, v58, v59\n\t" \
    "v_cvt_pk_bf16_f32 v110, v60, v61\n\t" \
    "v_cvt_pk_bf16_f32 v111, v62, v63\n\t" \
    "s_setprio 2\n\t" \
    "s_waitcnt lgkmcnt(0)\n\t" \
    "v_mfma_f32_32x32x16_bf16 v[48:63], v[80:83], v[64:67], v[16:31]\n\t" \
    "v_exp_f32_e32 v32, v32\n\t" \
    "v_exp_f32_e32 v33, v33\n\t" \
    "v_exp_f32_e32 v34, v34\n\t" \
    "v_mfma_f32_32x32x16_bf16 v[48:63], v[84:87], v[68:71], v[48:63]\n\t" \
    "ds_read_b128 v[80:83], v112 offset:16384\n\t" \
    "ds_read_b128 v[84:87], v112 offset:17408\n\t" \
    "v_exp_f32_e32 v35, v35\n\t" \
    "v_exp_f32_e32 v36, v36\n\t" \
    "v_cvt_pk_bf16_f32 v104, v32, v33\n\t" \
    "v_cvt_pk_bf16_f32 v105, v34, v35\n\t" \
    "v_mfma_f32_32x32x16_bf16 v[0:15], v[92:95], v[108:111], v[0:15]\n\t" \
    "ds_read_b128 v[100:103], v112 offset:11264\n\t" \
    "ds_read_b128 v[96:99], v112 offset:14336\n\t" \
    "v_exp_f32_e32 v37, v37\n\t" \
    "v_exp_f32_e32 v38, v38\n\t" \
    "v_exp_f32_e32 v39, v39\n\t" \
    "v_mfma_f32_16x16x32_bf16 v[76:79], v[72:75], v[108:111], v[76:79]\n\t" \
    "v_exp_f32_e32 v40, v40\n\t" \
    "v_cvt_pk_bf16_f32 v106, v36, v37\n\t" \
    "v_cvt_pk_bf16_f32 v107, v38, v39\n\t" \
    "v_exp_f32_e32 v41, v41\n\t" \
    "v_exp_f32_e32 v42, v42\n\t" \
    "v_mfma_f32_32x32x16_bf16 v[0:15], v[88:91], v[104:107], v[0:15]\n\t" \
    "v_exp_f32_e32 v43, v43\n\t" \
    "v_exp_f32_e32 v44, v44\n\t" \
    "v_exp_f32_e32 v45, v45\n\t" \
    "v_mfma_f32_16x16x32_bf16 v[76:79], v[72:75], v[104:107], v[76:79]\n\t" \
    "v_exp_f32_e32 v46, v46\n\t" \
    "v_exp_f32_e32 v47, v47\n\t" \
    "v_cvt_pk_bf16_f32 v108, v40, v41\n\t" \
    "v_cvt_pk_bf16_f32 v109, v42, v43\n\t" \
    "v_cvt_pk_bf16_f32 v110, v44, v45\n\t" \
    "v_cvt_pk_bf16_f32 v111, v46, v47\n\t" \
    "s_waitcnt lgkmcnt(0)\n\t" \
    "v_mfma_f32_32x32x16_bf16 v[32:47], v[80:83], v[64:67], v[16:31]\n\t" \
    "v_exp_f32_e32 v48, v48\n\t" \
    "v_exp_f32_e32 v49, v49\n\t" \
    "v_exp_f32_e32 v50, v50\n\t" \
    "v_mfma_f32_32x32x16_bf16 v[32:47], v[84:87], v[68:71], v[32:47]\n\t" \
    "ds_read_b128 v[80:83], v112 offset:20480\n\t" \
    "ds_read_b128 v[84:87], v112 offset:21504\n\t" \
    "v_exp_f32_e32 v51, v51\n\t" \
    "v_exp_f32_e32 v52, v52\n\t" \
    "v_cvt_pk_bf16_f32 v104, v48, v49\n\t" \
    "v_cvt_pk_bf16_f32 v105, v50, v51\n\t" \
    "v_mfma_f32_32x32x16_bf16 v[0:15], v[100:103], v[108:111], v[0:15]\n\t" \
    "ds_read_b128 v[92:95], v112 offset:15360\n\t" \
    "ds_read_b128 v[88:91], v112 offset:18432\n\t" \
    "v_exp_f32_e32 v53, v53\n\t" \
    "v_exp_f32_e32 v54, v54\n\t" \
    "v_exp_f32_e32 v55, v55\n\t" \
    "v_mfma_f32_16x16x32_bf16 v[76:79], v[72:75], v[108:111], v[76:79]\n\t" \
    "v_exp_f32_e32 v56, v56\n\t" \
    "v_cvt_pk_bf16_f32 v106, v52, v53\n\t" \
    "v_cvt_pk_bf16_f32 v107, v54, v55\n\t" \
    "v_exp_f32_e32 v57, v57\n\t" \
    "v_exp_f32_e32 v58, v58\n\t" \
    "v_mfma_f32_32x32x16_bf16 v[0:15], v[96:99], v[104:107], v[0:15]\n\t" \
    "v_exp_f32_e32 v59, v59\n\t" \
    "v_exp_f32_e32 v60, v60\n\t" \
    "v_exp_f32_e32 v61, v61\n\t" \
    "v_mfma_f32_16x16x32_bf16 v[76:79], v[72:75], v[104:107], v[76:79]\n\t" \
    "v_exp_f32_e32 v62, v62\n\t" \
    "v_exp_f32_e32 v63, v63\n\t" \
    "v_cvt_pk_bf16_f32 v108, v56, v57\n\t" \
    "v_cvt_pk_bf16_f32 v109, v58, v59\n\t" \
    "v_cvt_pk_bf16_f32 v110, v60, v61\n\t" \
    "v_cvt_pk_bf16_f32 v111, v62, v63\n\t" \
    "s_setprio 0\n\t" \
    "s_waitcnt lgkmcnt(0)\n\t" \
    "s_nop 15\n\t"
#define ATT_L2 \
    "s_nop 1\n\t" \
    "s_setprio 1\n\t" \
    "s_waitcnt lgkmcnt(0)\n\t" \
    "v_mfma_f32_32x32x16_bf16 v[48:63], v[80:83], v[64:67], v[16:31]\n\t" \
    "v_exp_f32_e32 v32, v32\n\t" \
    "v_exp_f32_e32 v33, v33\n\t" \
    "v_exp_f32_e32 v34, v34\n\t" \
    "v_mfma_f32_32x32x16_bf16 v[48:63], v[84:87], v[68:71], v[48:63]\n\t" \
    "ds_read_b128 v[80:83], v112 offset:24576\n\t" \
    "ds_read_b128 v[84:87], v112 offset:25600\n\t" \
    "v_exp_f32_e32 v35, v35\n\t" \
    "v_exp_f32_e32 v36, v36\n\t" \
    "v_cvt_pk_bf16_f32 v104, v32, v33\n\t" \
    "v_cvt_pk_bf16_f32 v105, v34, v35\n\t" \
    "v_mfma_f32_32x32x16_bf16 v[0:15], v[92:95], v[108:111], v[0:15]\n\t" \
    "ds_read_b128 v[100:103], v112 offset:19456\n\t" \
    "ds_read_b128 v[96:99], v112 offset:22528\n\t" \
    "v_exp_f32_e32 v37, v37\n\t" \
    "v_exp_f32_e32 v38, v38\n\t" \
    "v_exp_f32_e32 v39, v39\n\t" \
    "v_mfma_f32_16x16x32_bf16 v[76:79], v[72:75], v[108:111], v[76:79]\n\t" \
    "v_exp_f32_e32 v40, v40\n\t" \
    "v_cvt_pk_bf16_f32 v106, v36, v37\n\t" \
    "v_cvt_pk_bf16_f32 v107, v38, v39\n\t" \
    "v_exp_f32_e32 v41, v41\n\t" \
    "v_exp_f32_e32 v42, v42\n\t" \
    "v_mfma_f32_32x32x16_bf16 v[0:15], v[88:91], v[104:107], v[0:15]\n\t" \
    "v_exp_f32_e32 v43, v43\n\t" \
    "v_exp_f32_e32 v44, v44\n\t" \
    "v_exp_f32_e32 v45, v45\n\t" \
    "v_mfma_f32_16x16x32_bf16 v[76:79], v[72:75], v[104:107], v[76:79]\n\t" \
    "v_exp_f32_e32 v46, v46\n\t" \
    "v_exp_f32_e32 v47, v47\n\t" \
    "v_cvt_pk_bf16_f32 v108, v40, v41\n\t" \
    "v_cvt_pk_bf16_f32 v109, v42, v43\n\t" \
    "v_cvt_pk_bf16_f32 v110, v44, v45\n\t" \
    "v_cvt_pk_bf16_f32 v111, v46, v47\n\t" \
    "s_waitcnt lgkmcnt(0)\n\t" \
    "v_mfma_f32_32x32x16_bf16 v[32:47], v[80:83], v[64:67], v[16:31]\n\t" \
    "v_exp_f32_e32 v48, v48\n\t" \
    "v_exp_f32_e32 v49, v49\n\t" \
    "v_exp_f32_e32 v50, v50\n\t" \
    "v_mfma_f32_32x32x16_bf16 v[32:47], v[84:87], v[68:71], v[32:47]\n\t" \
    "ds_read_b128 v[80:83], v112 offset:28672\n\t" \
    "ds_read_b128 v[84:87], v112 offset:29696\n\t" \
    "v_exp_f32_e32 v51, v51\n\t" \
    "v_exp_f32_e32 v52, v52\n\t" \
    "v_cvt_pk_bf16_f32 v104, v48, v49\n\t" \
    "v_cvt_pk_bf16_f32 v105, v50, v51\n\t" \
    "v_mfma_f32_32x32x16_bf16 v[0:15], v[100:103], v[108:111], v[0:15]\n\t" \
    "ds_read_b128 v[92:95], v112 offset:23552\n\t" \
    "ds_read_b128 v[88:91], v112 offset:26624\n\t" \
    "v_exp_f32_e32 v53, v53\n\t" \
    "v_exp_f32_e32 v54, v54\n\t" \
    "v_exp_f32_e32 v55, v55\n\t" \
    "v_mfma_f32_16x16x32_bf16 v[76:79], v[72:75], v[108:111], v[76:79]\n\t" \
    "v_exp_f32_e32 v56, v56\n\t" \
    "v_cvt_pk_bf16_f32 v106, v52, v53\n\t" \
    "v_cvt_pk_bf16_f32 v107, v54, v55\n\t" \
    "v_exp_f32_e32 v57, v57\n\t" \
    "v_exp_f32_e32 v58, v58\n\t" \
    "v_mfma_f32_32x32x16_bf16 v[0:15], v[96:99], v[104:107], v[0:15]\n\t" \
    "v_exp_f32_e32 v59, v59\n\t" \
    "v_exp_f32_e32 v60, v60\n\t" \
    "v_exp_f32_e32 v61, v61\n\t" \
    "v_mfma_f32_16x16x32_bf16 v[76:79], v[72:75], v[104:107], v[76:79]\n\t" \
    "v_exp_f32_e32 v62, v62\n\t" \
    "v_exp_f32_e32 v63, v63\n\t" \
    "v_cvt_pk_bf16_f32 v108, v56, v57\n\t" \
    "v_cvt_pk_bf16_f32 v109, v58, v59\n\t" \
    "v_cvt_pk_bf16_f32 v110, v60, v61\n\t" \
    "v_cvt_pk_bf16_f32 v111, v62, v63\n\t" \
    "s_setprio 0\n\t" \
    "s_waitcnt lgkmcnt(0)\n\t" \
    "s_nop 15\n\t"
#define ATT_XT \
    "s_nop 1\n\t" \
    "s_waitcnt lgkmcnt(0)\n\t" \
    "v_mfma_f32_32x32x16_bf16 v[48:63], v[80:83], v[64:67], v[16:31]\n\t" \
    "v_exp_f32_e32 v32, v32\n\t" \
    "v_exp_f32_e32 v33, v33\n\t" \
    "v_exp_f32_e32 v34, v34\n\t" \
    "v_mfma_f32_32x32x16_bf16 v[48:63], v[84:87], v[68:71], v[48:63]\n\t" \
    "v_exp_f32_e32 v35, v35\n\t" \
    "v_exp_f32_e32 v36, v36\n\t" \
    "v_cvt_pk_bf16_f32 v104, v32, v33\n\t" \
    "v_cvt_pk_bf16_f32 v105, v34, v35\n\t" \
    "v_mfma_f32_32x32x16_bf16 v[0:15], v[92:95], v[108:111], v[0:15]\n\t" \
    "ds_read_b128 v[100:103], v112 offset:27648\n\t" \
    "ds_read_b128 v[96:99], v112 offset:30720\n\t" \
    "v_exp_f32_e32 v37, v37\n\t" \
    "v_exp_f32_e32 v38, v38\n\t" \
    "v_exp_f32_e32 v39, v39\n\t" \
    "v_mfma_f32_16x16x32_bf16 v[76:79], v[72:75], v[108:111], v[76:79]\n\t" \
    "v_exp_f32_e32 v40, v40\n\t" \
    "v_cvt_pk_bf16_f32 v106, v36, v37\n\t" \
    "v_cvt_pk_bf16_f32 v107, v38, v39\n\t" \
    "v_exp_f32_e32 v41, v41\n\t" \
    "v_exp_f32_e32 v42, v42\n\t" \
    "v_mfma_f32_32x32x16_bf16 v[0:15], v[88:91], v[104:107], v[0:15]\n\t" \
    "v_exp_f32_e32 v43, v43\n\t" \
    "v_exp_f32_e32 v44, v44\n\t" \
    "v_exp_f32_e32 v45, v45\n\t" \
    "v_mfma_f32_16x16x32_bf16 v[76:79], v[72:75], v[104:107], v[76:79]\n\t" \
    "v_exp_f32_e32 v46, v46\n\t" \
    "v_exp_f32_e32 v47, v47\n\t" \
    "v_cvt_pk_bf16_f32 v108, v40, v41\n\t" \
    "v_cvt_pk_bf16_f32 v109, v42, v43\n\t" \
    "v_cvt_pk_bf16_f32 v110, v44, v45\n\t" \
    "v_cvt_pk_bf16_f32 v111, v46, v47\n\t" \
    "s_waitcnt lgkmcnt(0)\n\t" \
    "v_exp_f32_e32 v48, v48\n\t" \
    "v_exp_f32_e32 v49, v49\n\t" \
    "v_exp_f32_e32 v50, v50\n\t" \
    "v_exp_f32_e32 v51, v51\n\t" \
    "v_exp_f32_e32 v52, v52\n\t" \
    "v_cvt_pk_bf16_f32 v104, v48, v49\n\t" \
    "v_cvt_pk_bf16_f32 v105, v50, v51\n\t" \
    "v_mfma_f32_32x32x16_bf16 v[0:15], v[100:103], v[108:111], v[0:15]\n\t" \
    "ds_read_b128 v[92:95], v112 offset:31744\n\t" \
    "v_exp_f32_e32 v53, v53\n\t" \
    "v_exp_f32_e32 v54, v54\n\t" \
    "v_exp_f32_e32 v55, v55\n\t" \
    "v_mfma_f32_16x16x32_bf16 v[76:79], v[72:75], v[108:111], v[76:79]\n\t" \
    "v_exp_f32_e32 v56, v56\n\t" \
    "v_cvt_pk_bf16_f32 v106, v52, v53\n\t" \
    "v_cvt_pk_bf16_f32 v107, v54, v55\n\t" \
    "v_exp_f32_e32 v57, v57\n\t" \
    "v_exp_f32_e32 v58, v58\n\t" \
    "v_mfma_f32_32x32x16_bf16 v[0:15], v[96:99], v[104:107], v[0:15]\n\t" \
    "v_exp_f32_e32 v59, v59\n\t" \
    "v_exp_f32_e32 v60, v60\n\t" \
    "v_exp_f32_e32 v61, v61\n\t" \
    "v_mfma_f32_16x16x32_bf16 v[76:79], v[72:75], v[104:107], v[76:79]\n\t" \
    "v_exp_f32_e32 v62, v62\n\t" \
    "v_exp_f32_e32 v63, v63\n\t" \
    "v_cvt_pk_bf16_f32 v108, v56, v57\n\t" \
    "v_cvt_pk_bf16_f32 v109, v58, v59\n\t" \
    "v_cvt_pk_bf16_f32 v110, v60, v61\n\t" \
    "v_cvt_pk_bf16_f32 v111, v62, v63\n\t" \
    "s_waitcnt lgkmcnt(0)\n\t" \
    "v_mfma_f32_32x32x16_bf16 v[0:15], v[92:95], v[108:111], v[0:15]\n\t" \
    "v_mfma_f32_16x16x32_bf16 v[76:79], v[72:75], v[108:111], v[76:79]\n\t" \
    "s_nop 15\n\t"
#define ATT_MAIN \
    "s_mov_b64 s[48:49], %[tab]\n\t" \
    "s_mov_b32 s46, %[k0]\n\t" \
    "s_and_b32 s40, s48, 63\n\t" \
    "s_lshl_b32 s40, s40, 12\n\t" \
    "s_add_u32 s44, %[srclo], s40\n\t" \
    "s_addc_u32 s45, %[srchi], 0\n\t" \
    "s_lshr_b64 s[48:49], s[48:49], 6\n\t" \
    "s_add_u32 s46, s46, 1\n\t" \
    "s_mov_b64 vcc, -1\n\t" \
    "s_waitcnt vmcnt(0)\n\t" \
    "ds_read_b64 v[120:121], v173\n\t" \
    "s_waitcnt lgkmcnt(0)\n\t" \
    "1:\n" \
    "s_cmp_eq_u32 %[cnt], 4\n\t" \
    "s_cbranch_scc1 5f\n\t" \
    "s_waitcnt vmcnt(0)\n\t" \
    "5:\n" \
    "s_waitcnt vmcnt(9)\n\t" \
    "s_barrier\n\t" \
    "s_bitcmp1_b32 %[act], %[cnt]\n\t" \
    "s_cbranch_scc1 30f\n\t" \
    "2:\n" \
    "s_setprio 3\n\t" \
    "s_waitcnt lgkmcnt(0)\n\t" \
    "v_mfma_f32_32x32x16_bf16 v[48:63], v[80:83], v[64:67], v[16:31]\n\t" \
    "v_exp_f32_e32 v32, v32\n\t" \
    "v_exp_f32_e32 v33, v33\n\t" \
    "v_exp_f32_e32 v34, v34\n\t" \
    "v_mfma_f32_32x32x16_bf16 v[48:63], v[84:87], v[68:71], v[48:63]\n\t" \
    "ds_read_b128 v[80:83], v113 offset:0\n\t" \
    "ds_read_b128 v[84:87], v113 offset:1024\n\t" \
    "v_exp_f32_e32 v35, v35\n\t" \
    "v_exp_f32_e32 v36, v36\n\t" \
    "v_cvt_pk_bf16_f32 v104, v32, v33\n\t" \
    "v_cvt_pk_bf16_f32 v105, v34, v35\n\t" \
    "v_mfma_f32_32x32x16_bf16 v[0:15], v[92:95], v[108:111], v[0:15]\n\t" \
    "ds_read_b128 v[100:103], v112 offset:27648\n\t" \
    "ds_read_b128 v[96:99], v112 offset:30720\n\t" \
    "v_exp_f32_e32 v37, v37\n\t" \
    "v_exp_f32_e32 v38, v38\n\t" \
    "v_exp_f32_e32 v39, v39\n\t" \
    "v_mfma_f32_16x16x32_bf16 v[76:79], v[72:75], v[108:111], v[76:79]\n\t" \
    "v_exp_f32_e32 v40, v40\n\t" \
    "v_cvt_pk_bf16_f32 v106, v36, v37\n\t" \
    "v_cvt_pk_bf16_f32 v107, v38, v39\n\t" \
    "v_exp_f32_e32 v41, v41\n\t" \
    "v_exp_f32_e32 v42, v42\n\t" \
    "v_mfma_f32_32x32x16_bf16 v[0:15], v[88:91], v[104:107], v[0:15]\n\t" \
    "s_cbranch_vccz 20f\n\t" \
    "v_cmp_eq_u64_e64 s[42:43], v[120:121], v[140:141]\n\t" \
    "s_mov_b32 m0, %[dst]\n\t" \
    "s_nop 3\n\t" \
    "s_bitcmp1_b64 s[42:43], s46\n\t" \
    "s_cbranch_scc0 9f\n\t" \
    "12:\n" \
    "global_load_lds_dwordx4 v114, s[44:45] offset:0 sc1\n\t" \
    "20:\n" \
    "v_exp_f32_e32 v43, v43\n\t" \
    "v_exp_f32_e32 v44, v44\n\t" \
    "v_exp_f32_e32 v45, v45\n\t" \
    "v_mfma_f32_16x16x32_bf16 v[76:79], v[72:75], v[104:107], v[76:79]\n\t" \
    "v_exp_f32_e32 v46, v46\n\t" \
    "v_exp_f32_e32 v47, v47\n\t" \
    "v_cvt_pk_bf16_f32 v108, v40, v41\n\t" \
    "v_cvt_pk_bf16_f32 v109, v42, v43\n\t" \
    "v_cvt_pk_bf16_f32 v110, v44, v45\n\t" \
    "v_cvt_pk_bf16_f32 v111, v46, v47\n\t" \
    "s_waitcnt lgkmcnt(0)\n\t" \
    "v_mfma_f32_32x32x16_bf16 v[32:47], v[80:83], v[64:67], v[16:31]\n\t" \
    "v_exp_f32_e32 v48, v48\n\t" \
    "v_exp_f32_e32 v49, v49\n\t" \
    "v_exp_f32_e32 v50, v50\n\t" \
    "v_mfma_f32_32x32x16_bf16 v[32:47], v[84:87], v[68:71], v[32:47]\n\t" \
    "ds_read_b128 v[80:83], v113 offset:4096\n\t" \
    "ds_read_b128 v[84:87], v113 offset:5120\n\t" \
    "v_exp_f32_e32 v51, v51\n\t" \
    "v_exp_f32_e32 v52, v52\n\t" \
    "v_cvt_pk_bf16_f32 v104, v48, v49\n\t" \
    "v_cvt_pk_bf16_f32 v105, v50, v51\n\t" \
    "v_mfma_f32_32x32x16_bf16 v[0:15], v[100:103], v[108:111], v[0:15]\n\t" \
    "ds_read_b128 v[92:95], v112 offset:31744\n\t" \
    "ds_read_b128 v[88:91], v113 offset:2048\n\t" \
    "v_exp_f32_e32 v53, v53\n\t" \
    "v_exp_f32_e32 v54, v54\n\t" \
    "v_exp_f32_e32 v55, v55\n\t" \
    "v_mfma_f32_16x16x32_bf16 v[76:79], v[72:75], v[108:111], v[76:79]\n\t" \
    "v_exp_f32_e32 v56, v56\n\t" \
    "v_cvt_pk_bf16_f32 v106, v52, v53\n\t" \
    "v_cvt_pk_bf16_f32 v107, v54, v55\n\t" \
    "v_exp_f32_e32 v57, v57\n\t" \
    "v_exp_f32_e32 v58, v58\n\t" \
    "v_mfma_f32_32x32x16_bf16 v[0:15], v[96:99], v[104:107], v[0:15]\n\t" \
    "s_cbranch_vccz 21f\n\t" \
    "global_load_lds_dwordx4 v114, s[44:45] offset:1024 sc1\n\t" \
    "21:\n" \
    "v_exp_f32_e32 v59, v59\n\t" \
    "v_exp_f32_e32 v60, v60\n\t" \
    "v_exp_f32_e32 v61, v61\n\t" \
    "v_mfma_f32_16x16x32_bf16 v[76:79], v[72:75], v[104:107], v[76:79]\n\t" \
    "v_exp_f32_e32 v62, v62\n\t" \
    "v_exp_f32_e32 v63, v63\n\t" \
    "v_cvt_pk_bf16_f32 v108, v56, v57\n\t" \
    "v_cvt_pk_bf16_f32 v109, v58, v59\n\t" \
    "v_cvt_pk_bf16_f32 v110, v60, v61\n\t" \
    "v_cvt_pk_bf16_f32 v111, v62, v63\n\t" \
    "s_setprio 2\n\t" \
    "s_waitcnt lgkmcnt(0)\n\t" \
    "v_mfma_f32_32x32x16_bf16 v[48:63], v[80:83], v[64:67], v[16:31]\n\t" \
    "v_exp_f32_e32 v32, v32\n\t" \
    "v_exp_f32_e32 v33, v33\n\t" \
    "v_exp_f32_e32 v34, v34\n\t" \
    "v_mfma_f32_32x32x16_bf16 v[48:63], v[84:87], v[68:71], v[48:63]\n\t" \
    "ds_read_b128 v[80:83], v113 offset:8192\n\t" \
    "ds_read_b128 v[84:87], v113 offset:9216\n\t" \
    "v_exp_f32_e32 v35, v35\n\t" \
    "v_exp_f32_e32 v36, v36\n\t" \
    "v_cvt_pk_bf16_f32 v104, v32, v33\n\t" \
    "v_cvt_pk_bf16_f32 v105, v34, v35\n\t" \
    "v_mfma_f32_32x32x16_bf16 v[0:15], v[92:95], v[108:111], v[0:15]\n\t" \
    "ds_read_b128 v[100:103], v113 offset:3072\n\t" \
    "ds_read_b128 v[96:99], v113 offset:6144\n\t" \
    "v_exp_f32_e32 v37, v37\n\t" \
    "v_exp_f32_e32 v38, v38\n\t" \
    "v_exp_f32_e32 v39, v39\n\t" \
    "v_mfma_f32_16x16x32_bf16 v[76:79], v[72:75], v[108:111], v[76:79]\n\t" \
    "v_exp_f32_e32 v40, v40\n\t" \
    "v_cvt_pk_bf16_f32 v106, v36, v37\n\t" \
    "v_cvt_pk_bf16_f32 v107, v38, v39\n\t" \
    "v_exp_f32_e32 v41, v41\n\t" \
    "v_exp_f32_e32 v42, v42\n\t" \
    "v_mfma_f32_32x32x16_bf16 v[0:15], v[88:91], v[104:107], v[0:15]\n\t" \
    "s_cbranch_vccz 22f\n\t" \
    "global_load_lds_dwordx4 v114, s[44:45] offset:2048 sc1\n\t" \
    "22:\n" \
    "v_exp_f32_e32 v43, v43\n\t" \
    "v_exp_f32_e32 v44, v44\n\t" \
    "v_exp_f32_e32 v45, v45\n\t" \
    "v_mfma_f32_16x16x32_bf16 v[76:79], v[72:75], v[104:107], v[76:79]\n\t" \
    "v_exp_f32_e32 v46, v46\n\t" \
    "v_exp_f32_e32 v47, v47\n\t" \
    "v_cvt_pk_bf16_f32 v108, v40, v41\n\t" \
    "v_cvt_pk_bf16_f32 v109, v42, v43\n\t" \
    "v_cvt_pk_bf16_f32 v110, v44, v45\n\t" \
    "v_cvt_pk_bf16_f32 v111, v46, v47\n\t" \
    "s_waitcnt lgkmcnt(0)\n\t" \
    "v_mfma_f32_32x32x16_bf16 v[32:47], v[80:83], v[64:67], v[16:31]\n\t" \
    "v_exp_f32_e32 v48, v48\n\t" \
    "v_exp_f32_e32 v49, v49\n\t" \
    "v_exp_f32_e32 v50, v50\n\t" \
    "v_mfma_f32_32x32x16_bf16 v[32:47], v[84:87], v[68:71], v[32:47]\n\t" \
    "ds_read_b128 v[80:83], v113 offset:12288\n\t" \
    "ds_read_b128 v[84:87], v113 offset:13312\n\t" \
    "v_exp_f32_e32 v51, v51\n\t" \
    "v_exp_f32_e32 v52, v52\n\t" \
    "v_cvt_pk_bf16_f32 v104, v48, v49\n\t" \
    "v_cvt_pk_bf16_f32 v105, v50, v51\n\t" \
    "v_mfma_f32_32x32x16_bf16 v[0:15], v[100:103], v[108:111], v[0:15]\n\t" \
    "ds_read_b128 v[92:95], v113 offset:7168\n\t" \
    "ds_read_b128 v[88:91], v113 offset:10240\n\t" \
    "v_exp_f32_e32 v53, v53\n\t" \
    "v_exp_f32_e32 v54, v54\n\t" \
    "v_exp_f32_e32 v55, v55\n\t" \
    "v_mfma_f32_16x16x32_bf16 v[76:79], v[72:75], v[108:111], v[76:79]\n\t" \
    "v_exp_f32_e32 v56, v56\n\t" \
    "v_cvt_pk_bf16_f32 v106, v52, v53\n\t" \
    "v_cvt_pk_bf16_f32 v107, v54, v55\n\t" \
    "v_exp_f32_e32 v57, v57\n\t" \
    "v_exp_f32_e32 v58, v58\n\t" \
    "v_mfma_f32_32x32x16_bf16 v[0:15], v[96:99], v[104:107], v[0:15]\n\t" \
    "s_cbranch_vccz 23f\n\t" \
    "global_load_lds_dwordx4 v114, s[44:45] offset:3072 sc1\n\t" \
    "global_load_dwordx2 v[120:121], v170, %[ws] sc1\n\t" \
    "23:\n" \
    "v_exp_f32_e32 v59, v59\n\t" \
    "v_exp_f32_e32 v60, v60\n\t" \
    "v_exp_f32_e32 v61, v61\n\t" \
    "v_mfma_f32_16x16x32_bf16 v[76:79], v[72:75], v[104:107], v[76:79]\n\t" \
    "v_exp_f32_e32 v62, v62\n\t" \
    "v_exp_f32_e32 v63, v63\n\t" \
    "v_cvt_pk_bf16_f32 v108, v56, v57\n\t" \
    "v_cvt_pk_bf16_f32 v109, v58, v59\n\t" \
    "v_cvt_pk_bf16_f32 v110, v60, v61\n\t" \
    "v_cvt_pk_bf16_f32 v111, v62, v63\n\t" \
    "s_setprio 1\n\t" \
    "s_waitcnt lgkmcnt(0)\n\t" \
    "v_mfma_f32_32x32x16_bf16 v[48:63], v[80:83], v[64:67], v[16:31]\n\t" \
    "v_exp_f32_e32 v32, v32\n\t" \
    "v_exp_f32_e32 v33, v33\n\t" \
    "v_exp_f32_e32 v34, v34\n\t" \
    "v_mfma_f32_32x32x16_bf16 v[48:63], v[84:87], v[68:71], v[48:63]\n\t" \
    "ds_read_b128 v[80:83], v113 offset:16384\n\t" \
    "ds_read_b128 v[84:87], v113 offset:17408\n\t" \
    "v_exp_f32_e32 v35, v35\n\t" \
    "v_exp_f32_e32 v36, v36\n\t" \
    "v_cvt_pk_bf16_f32 v104, v32, v33\n\t" \
    "v_cvt_pk_bf16_f32 v105, v34, v35\n\t" \
    "v_mfma_f32_32x32x16_bf16 v[0:15], v[92:95], v[108:111], v[0:15]\n\t" \
    "ds_read_b128 v[100:103], v113 offset:11264\n\t" \
    "ds_read_b128 v[96:99], v113 offset:14336\n\t" \
    "v_exp_f32_e32 v37, v37\n\t" \
    "v_exp_f32_e32 v38, v38\n\t" \
    "v_exp_f32_e32 v39, v39\n\t" \
    "v_mfma_f32_16x16x32_bf16 v[76:79], v[72:75], v[108:111], v[76:79]\n\t" \
    "v_exp_f32_e32 v40, v40\n\t" \
    "v_cvt_pk_bf16_f32 v106, v36, v37\n\t" \
    "v_cvt_pk_bf16_f32 v107, v38, v39\n\t" \
    "v_exp_f32_e32 v41, v41\n\t" \
    "v_exp_f32_e32 v42, v42\n\t" \
    "v_mfma_f32_32x32x16_bf16 v[0:15], v[88:91], v[104:107], v[0:15]\n\t" \
    "s_add_u32 %[dst], %[dst], 0x8000\n\t" \
    "s_cmp_eq_u32 %[dst], %[dend]\n\t" \
    "s_cselect_b32 %[dst], %[dbeg], %[dst]\n\t" \
    "s_add_u32 %[nx], %[nx], 0x8000\n\t" \
    "s_cmp_eq_u32 %[nx], 0x18000\n\t" \
    "s_cselect_b32 %[nx], 0, %[nx]\n\t" \
    "s_cmp_eq_u32 %[cnt], 5\n\t" \
    "s_cbranch_scc1 40f\n\t" \
    "24:\n" \
    "v_exp_f32_e32 v43, v43\n\t" \
    "v_exp_f32_e32 v44, v44\n\t" \
    "v_exp_f32_e32 v45, v45\n\t" \
    "v_mfma_f32_16x16x32_bf16 v[76:79], v[72:75], v[104:107], v[76:79]\n\t" \
    "v_exp_f32_e32 v46, v46\n\t" \
    "v_exp_f32_e32 v47, v47\n\t" \
    "v_cvt_pk_bf16_f32 v108, v40, v41\n\t" \
    "v_cvt_pk_bf16_f32 v109, v42, v43\n\t" \
    "v_cvt_pk_bf16_f32 v110, v44, v45\n\t" \
    "v_cvt_pk_bf16_f32 v111, v46, v47\n\t" \
    "s_waitcnt lgkmcnt(0)\n\t" \
    "v_mfma_f32_32x32x16_bf16 v[32:47], v[80:83], v[64:67], v[16:31]\n\t" \
    "v_exp_f32_e32 v48, v48\n\t" \
    "v_exp_f32_e32 v49, v49\n\t" \
    "v_exp_f32_e32 v50, v50\n\t" \
    "v_mfma_f32_32x32x16_bf16 v[32:47], v[84:87], v[68:71], v[32:47]\n\t" \
    "ds_read_b128 v[80:83], v113 offset:20480\n\t" \
    "ds_read_b128 v[84:87], v113 offset:21504\n\t" \
    "v_exp_f32_e32 v51, v51\n\t" \
    "v_exp_f32_e32 v52, v52\n\t" \
    "v_cvt_pk_bf16_f32 v104, v48, v49\n\t" \
    "v_cvt_pk_bf16_f32 v105, v50, v51\n\t" \
    "v_mfma_f32_32x32x16_bf16 v[0:15], v[100:103], v[108:111], v[0:15]\n\t" \
    "ds_read_b128 v[92:95], v113 offset:15360\n\t" \
    "ds_read_b128 v[88:91], v113 offset:18432\n\t" \
    "v_exp_f32_e32 v53, v53\n\t" \
    "v_exp_f32_e32 v54, v54\n\t" \
    "v_exp_f32_e32 v55, v55\n\t" \
    "v_mfma_f32_16x16x32_bf16 v[76:79], v[72:75], v[108:111], v[76:79]\n\t" \
    "v_exp_f32_e32 v56, v56\n\t" \
    "v_cvt_pk_bf16_f32 v106, v52, v53\n\t" \
    "v_cvt_pk_bf16_f32 v107, v54, v55\n\t" \
    "v_exp_f32_e32 v57, v57\n\t" \
    "v_exp_f32_e32 v58, v58\n\t" \
    "v_mfma_f32_32x32x16_bf16 v[0:15], v[96:99], v[104:107], v[0:15]\n\t" \
    "s_cmp_eq_u32 %[cnt], 5\n\t" \
    "s_cbranch_scc1 41f\n\t" \
    "25:\n" \
    "v_exp_f32_e32 v59, v59\n\t" \
    "v_exp_f32_e32 v60, v60\n\t" \
    "v_exp_f32_e32 v61, v61\n\t" \
    "v_mfma_f32_16x16x32_bf16 v[76:79], v[72:75], v[104:107], v[76:79]\n\t" \
    "v_exp_f32_e32 v62, v62\n\t" \
    "v_exp_f32_e32 v63, v63\n\t" \
    "v_cvt_pk_bf16_f32 v108, v56, v57\n\t" \
    "v_cvt_pk_bf16_f32 v109, v58, v59\n\t" \
    "v_cvt_pk_bf16_f32 v110, v60, v61\n\t" \
    "v_cvt_pk_bf16_f32 v111, v62, v63\n\t" \
    "s_setprio 0\n\t" \
    "s_waitcnt lgkmcnt(0)\n\t" \
    "v_mfma_f32_32x32x16_bf16 v[48:63], v[80:83], v[64:67], v[16:31]\n\t" \
    "v_exp_f32_e32 v32, v32\n\t" \
    "v_exp_f32_e32 v33, v33\n\t" \
    "v_exp_f32_e32 v34, v34\n\t" \
    "v_mfma_f32_32x32x16_bf16 v[48:63], v[84:87], v[68:71], v[48:63]\n\t" \
    "ds_read_b128 v[80:83], v113 offset:24576\n\t" \
    "ds_read_b128 v[84:87], v113 offset:25600\n\t" \
    "v_exp_f32_e32 v35, v35\n\t" \
    "v_exp_f32_e32 v36, v36\n\t" \
    "v_cvt_pk_bf16_f32 v104, v32, v33\n\t" \
    "v_cvt_pk_bf16_f32 v105, v34, v35\n\t" \
    "v_mfma_f32_32x32x16_bf16 v[0:15], v[92:95], v[108:111], v[0:15]\n\t" \
    "ds_read_b128 v[100:103], v113 offset:19456\n\t" \
    "ds_read_b128 v[96:99], v113 offset:22528\n\t" \
    "v_exp_f32_e32 v37, v37\n\t" \
    "v_exp_f32_e32 v38, v38\n\t" \
    "v_exp_f32_e32 v39, v39\n\t" \
    "v_mfma_f32_16x16x32_bf16 v[76:79], v[72:75], v[108:111], v[76:79]\n\t" \
    "v_exp_f32_e32 v40, v40\n\t" \
    "v_cvt_pk_bf16_f32 v106, v36, v37\n\t" \
    "v_cvt_pk_bf16_f32 v107, v38, v39\n\t" \
    "v_exp_f32_e32 v41, v41\n\t" \
    "v_exp_f32_e32 v42, v42\n\t" \
    "v_mfma_f32_32x32x16_bf16 v[0:15], v[88:91], v[104:107], v[0:15]\n\t" \
    "s_and_b32 s40, s48, 63\n\t" \
    "s_lshl_b32 s40, s40, 12\n\t" \
    "s_add_u32 s44, %[srclo], s40\n\t" \
    "s_addc_u32 s45, %[srchi], 0\n\t" \
    "s_lshr_b64 s[48:49], s[48:49], 6\n\t" \
    "s_add_u32 s46, s46, 1\n\t" \
    "s_cmp_eq_u32 %[cnt], 5\n\t" \
    "s_cbranch_scc1 42f\n\t" \
    "26:\n" \
    "v_exp_f32_e32 v43, v43\n\t" \
    "v_exp_f32_e32 v44, v44\n\t" \
    "v_exp_f32_e32 v45, v45\n\t" \
    "v_mfma_f32_16x16x32_bf16 v[76:79], v[72:75], v[104:107], v[76:79]\n\t" \
    "v_exp_f32_e32 v46, v46\n\t" \
    "v_exp_f32_e32 v47, v47\n\t" \
    "v_cvt_pk_bf16_f32 v108, v40, v41\n\t" \
    "v_cvt_pk_bf16_f32 v109, v42, v43\n\t" \
    "v_cvt_pk_bf16_f32 v110, v44, v45\n\t" \
    "v_cvt_pk_bf16_f32 v111, v46, v47\n\t" \
    "s_waitcnt lgkmcnt(0)\n\t" \
    "v_mfma_f32_32x32x16_bf16 v[32:47], v[80:83], v[64:67], v[16:31]\n\t" \
    "v_exp_f32_e32 v48, v48\n\t" \
    "v_exp_f32_e32 v49, v49\n\t" \
    "v_exp_f32_e32 v50, v50\n\t" \
    "v_mfma_f32_32x32x16_bf16 v[32:47], v[84:87], v[68:71], v[32:47]\n\t" \
    "ds_read_b128 v[80:83], v113 offset:28672\n\t" \
    "ds_read_b128 v[84:87], v113 offset:29696\n\t" \
    "v_exp_f32_e32 v51, v51\n\t" \
    "v_exp_f32_e32 v52, v52\n\t" \
    "v_cvt_pk_bf16_f32 v104, v48, v49\n\t" \
    "v_cvt_pk_bf16_f32 v105, v50, v51\n\t" \
    "v_mfma_f32_32x32x16_bf16 v[0:15], v[100:103], v[108:111], v[0:15]\n\t" \
    "ds_read_b128 v[92:95], v113 offset:23552\n\t" \
    "ds_read_b128 v[88:91], v113 offset:26624\n\t" \
    "v_exp_f32_e32 v53, v53\n\t" \
    "v_exp_f32_e32 v54, v54\n\t" \
    "v_exp_f32_e32 v55, v55\n\t" \
    "v_mfma_f32_16x16x32_bf16 v[76:79], v[72:75], v[108:111], v[76:79]\n\t" \
    "v_exp_f32_e32 v56, v56\n\t" \
    "v_cvt_pk_bf16_f32 v106, v52, v53\n\t" \
    "v_cvt_pk_bf16_f32 v107, v54, v55\n\t" \
    "v_exp_f32_e32 v57, v57\n\t" \
    "v_exp_f32_e32 v58, v58\n\t" \
    "v_mfma_f32_32x32x16_bf16 v[0:15], v[96:99], v[104:107], v[0:15]\n\t" \
    "s_cmp_eq_u32 %[cnt], 5\n\t" \
    "s_cbranch_scc1 43f\n\t" \
    "27:\n" \
    "v_mov_b32_e32 v112, v113\n\t" \
    "v_add_u32_e32 v113, %[nx], v115\n\t" \
    "s_sub_u32 %[cnt], %[cnt], 1\n\t" \
    "s_cmp_lg_u32 %[cnt], 0\n\t" \
    "v_exp_f32_e32 v59, v59\n\t" \
    "v_exp_f32_e32 v60, v60\n\t" \
    "v_exp_f32_e32 v61, v61\n\t" \
    "v_mfma_f32_16x16x32_bf16 v[76:79], v[72:75], v[104:107], v[76:79]\n\t" \
    "v_exp_f32_e32 v62, v62\n\t" \
    "v_exp_f32_e32 v63, v63\n\t" \
    "v_cvt_pk_bf16_f32 v108, v56, v57\n\t" \
    "v_cvt_pk_bf16_f32 v109, v58, v59\n\t" \
    "v_cvt_pk_bf16_f32 v110, v60, v61\n\t" \
    "v_cvt_pk_bf16_f32 v111, v62, v63\n\t" \
    "s_cbranch_scc1 1b\n\t" \
    "7:\n" \
    "s_setprio 0\n\t" \
    "s_waitcnt lgkmcnt(0)\n\t" \
    "v_mfma_f32_32x32x16_bf16 v[48:63], v[80:83], v[64:67], v[16:31]\n\t" \
    "v_exp_f32_e32 v32, v32\n\t" \
    "v_exp_f32_e32 v33, v33\n\t" \
    "v_exp_f32_e32 v34, v34\n\t" \
    "v_mfma_f32_32x32x16_bf16 v[48:63], v[84:87], v[68:71], v[48:63]\n\t" \
    "v_exp_f32_e32 v35, v35\n\t" \
    "v_exp_f32_e32 v36, v36\n\t" \
    "v_cvt_pk_bf16_f32 v104, v32, v33\n\t" \
    "v_cvt_pk_bf16_f32 v105, v34, v35\n\t" \
    "v_mfma_f32_32x32x16_bf16 v[0:15], v[92:95], v[108:111], v[0:15]\n\t" \
    "ds_read_b128 v[100:103], v112 offset:27648\n\t" \
    "ds_read_b128 v[96:99], v112 offset:30720\n\t" \
    "v_exp_f32_e32 v37, v37\n\t" \
    "v_exp_f32_e32 v38, v38\n\t" \
    "v_exp_f32_e32 v39, v39\n\t" \
    "v_mfma_f32_16x16x32_bf16 v[76:79], v[72:75], v[108:111], v[76:79]\n\t" \
    "v_exp_f32_e32 v40, v40\n\t" \
    "v_cvt_pk_bf16_f32 v106, v36, v37\n\t" \
    "v_cvt_pk_bf16_f32 v107, v38, v39\n\t" \
    "v_exp_f32_e32 v41, v41\n\t" \
    "v_exp_f32_e32 v42, v42\n\t" \
    "v_mfma_f32_32x32x16_bf16 v[0:15], v[88:91], v[104:107], v[0:15]\n\t" \
    "v_exp_f32_e32 v43, v43\n\t" \
    "v_exp_f32_e32 v44, v44\n\t" \
    "v_exp_f32_e32 v45, v45\n\t" \
    "v_mfma_f32_16x16x32_bf16 v[76:79], v[72:75], v[104:107], v[76:79]\n\t" \
    "v_exp_f32_e32 v46, v46\n\t" \
    "v_exp_f32_e32 v47, v47\n\t" \
    "v_cvt_pk_bf16_f32 v108, v40, v41\n\t" \
    "v_cvt_pk_bf16_f32 v109, v42, v43\n\t" \
    "v_cvt_pk_bf16_f32 v110, v44, v45\n\t" \
    "v_cvt_pk_bf16_f32 v111, v46, v47\n\t" \
    "s_waitcnt lgkmcnt(0)\n\t" \
    "v_exp_f32_e32 v48, v48\n\t" \
    "v_exp_f32_e32 v49, v49\n\t" \
    "v_exp_f32_e32 v50, v50\n\t" \
    "v_exp_f32_e32 v51, v51\n\t" \
    "v_exp_f32_e32 v52, v52\n\t" \
    "v_cvt_pk_bf16_f32 v104, v48, v49\n\t" \
    "v_cvt_pk_bf16_f32 v105, v50, v51\n\t" \
    "v_mfma_f32_32x32x16_bf16 v[0:15], v[100:103], v[108:111], v[0:15]\n\t" \
    "ds_read_b128 v[92:95], v112 offset:31744\n\t" \
    "v_exp_f32_e32 v53, v53\n\t" \
    "v_exp_f32_e32 v54, v54\n\t" \
    "v_exp_f32_e32 v55, v55\n\t" \
    "v_mfma_f32_16x16x32_bf16 v[76:79], v[72:75], v[108:111], v[76:79]\n\t" \
    "v_exp_f32_e32 v56, v56\n\t" \
    "v_cvt_pk_bf16_f32 v106, v52, v53\n\t" \
    "v_cvt_pk_bf16_f32 v107, v54, v55\n\t" \
    "v_exp_f32_e32 v57, v57\n\t" \
    "v_exp_f32_e32 v58, v58\n\t" \
    "v_mfma_f32_32x32x16_bf16 v[0:15], v[96:99], v[104:107], v[0:15]\n\t" \
    "v_exp_f32_e32 v59, v59\n\t" \
    "v_exp_f32_e32 v60, v60\n\t" \
    "v_exp_f32_e32 v61, v61\n\t" \
    "v_mfma_f32_16x16x32_bf16 v[76:79], v[72:75], v[104:107], v[76:79]\n\t" \
    "v_exp_f32_e32 v62, v62\n\t" \
    "v_exp_f32_e32 v63, v63\n\t" \
    "v_cvt_pk_bf16_f32 v108, v56, v57\n\t" \
    "v_cvt_pk_bf16_f32 v109, v58, v59\n\t" \
    "v_cvt_pk_bf16_f32 v110, v60, v61\n\t" \
    "v_cvt_pk_bf16_f32 v111, v62, v63\n\t" \
    "s_waitcnt lgkmcnt(0)\n\t" \
    "v_mfma_f32_32x32x16_bf16 v[0:15], v[92:95], v[108:111], v[0:15]\n\t" \
    "v_mfma_f32_16x16x32_bf16 v[76:79], v[72:75], v[108:111], v[76:79]\n\t" \
    "s_nop 15\n\t" \
    "s_branch 99f\n\t" \
    "9:\n" \
    "s_mov_b32 s47, 0x40000\n\t" \
    "10:\n" \
    "s_sub_u32 s47, s47, 1\n\t" \
    "s_cbranch_scc1 12b\n\t" \
    "global_load_dwordx2 v[120:121], v170, %[ws] sc1\n\t" \
    "s_waitcnt vmcnt(0)\n\t" \
    "v_cmp_eq_u64_e64 s[42:43], v[120:121], v[140:141]\n\t" \
    "s_nop 4\n\t" \
    "s_bitcmp1_b64 s[42:43], s46\n\t" \
    "s_cbranch_scc1 12b\n\t" \
    "s_sleep 4\n\t" \
    "s_branch 10b\n\t" \
    "40:\n" \
    "s_add_u32 m0, %[wdst], 0\n\t" \
    "s_nop 0\n\t" \
    "global_load_lds_dwordx4 v116, %[wo01]\n\t" \
    "global_load_lds_dwordx4 v117, %[wo01] offset:1024\n\t" \
    "v_add_u32_e32 v116, 0x20000, v116\n\t" \
    "v_add_u32_e32 v117, 0x20000, v117\n\t" \
    "s_branch 24b\n\t" \
    "41:\n" \
    "s_add_u32 m0, %[wdst], 16384\n\t" \
    "s_nop 0\n\t" \
    "global_load_lds_dwordx4 v116, %[wo01]\n\t" \
    "global_load_lds_dwordx4 v117, %[wo01] offset:1024\n\t" \
    "v_subrev_u32_e32 v116, 0x20000, v116\n\t" \
    "v_subrev_u32_e32 v117, 0x20000, v117\n\t" \
    "s_branch 25b\n\t" \
    "42:\n" \
    "s_add_u32 m0, %[wdst], 32768\n\t" \
    "s_nop 0\n\t" \
    "global_load_lds_dwordx4 v116, %[wo23]\n\t" \
    "global_load_lds_dwordx4 v117, %[wo23] offset:1024\n\t" \
    "v_add_u32_e32 v116, 0x20000, v116\n\t" \
    "v_add_u32_e32 v117, 0x20000, v117\n\t" \
    "s_branch 26b\n\t" \
    "43:\n" \
    "s_add_u32 m0, %[wdst], 49152\n\t" \
    "s_nop 0\n\t" \
    "global_load_lds_dwordx4 v116, %[wo23]\n\t" \
    "global_load_lds_dwordx4 v117, %[wo23] offset:1024\n\t" \
    "global_load_dword v168, v119, %[wo]\n\t" \
    "s_branch 27b\n\t" \
    "30:\n" \
    "s_cmp_eq_u32 %[cnt], 1\n\t" \
    "s_cbranch_scc0 31f\n\t" \
    "s_mov_b64 vcc, 0\n\t" \
    "s_bitcmp1_b32 %[mode], 1\n\t" \
    "s_cbranch_scc1 7b\n\t" \
    "s_branch 2b\n\t" \
    "31:\n" \
    "33:\n" \
    "s_cmp_eq_u32 %[cnt], 5\n\t" \
    "s_cbranch_scc0 34f\n\t" \
    "global_store_dwordx2 v118, v[140:141], %[ws] sc1\n\t" \
    "s_branch 2b\n\t" \
    "34:\n" \
    "s_cmp_eq_u32 %[cnt], 3\n\t" \
    "s_cbranch_scc0 35f\n\t" \
    "global_load_dwordx2 v[168:169], v142, %[ws] sc1\n\t" \
    "s_branch 2b\n\t" \
    "35:\n" \
    "v_cmp_eq_u64_e64 s[42:43], v[168:169], v[140:141]\n\t" \
    "s_nop 4\n\t" \
    "s_cmp_eq_u64 s[42:43], -1\n\t" \
    "s_cbranch_scc0 2b\n\t" \
    "global_load_dwordx4 v[122:125], v143, %[ws] offset:-2048 sc1\n\t" \
    "global_load_dwordx4 v[126:129], v143, %[ws] offset:-1024 sc1\n\t" \
    "global_load_dwordx4 v[130:133], v143, %[ws] offset:0 sc1\n\t" \
    "global_load_dwordx4 v[134:137], v143, %[ws] offset:1024 sc1\n\t" \
    "global_load_dwordx2 v[138:139], v143, %[ws] offset:2048 sc1\n\t" \
    "s_bitset1_b32 %[mode], 2\n\t" \
    "s_branch 2b\n\t" \
    "99:\n"
#define ATT_TMP "v48", "v49", "v50", "v51", "v52", "v53", "v54", "v55", "v56", "v57", "v58", "v59", "v60", "v61", "v62", "v63", "v96", "v97", "v98", "v99", "v100", "v101", "v102", "v103", "v104", "v105", "v106", "v107"

constexpr int K1_X = 0, K1_W = 65536, K1_WB = 24576;
constexpr int K2_SLOT = 32768, K2_QX = 3 * K2_SLOT, K2_FL = 147456;
constexpr int LDS_BYTES = 163840;
constexpr int p3_wslot(int t) { return (t % 6) < 4 ? 98304 + (t % 6) * 16384 : 65536 + ((t % 6) - 4) * 16384; }
constexpr int p3_allow(int t) { return (((t < 2 || t - 2 >= 1) && t + 3 < 16) ? 4 : 0) + (t + 3 < 16 ? 4 : 0) + ((t >= 1 && t + 5 < 16) ? 4 : 0); }

__global__ __launch_bounds__(512, 2) void mk_fused(const u16* __restrict__ xA, const u16* __restrict__ wA, const u16* __restrict__ woA,
                                                   const u16* __restrict__ xB, const u16* __restrict__ wB, const u16* __restrict__ woB, char* ws0, u16* __restrict__ out) {
    char* ws = ws0;
    __shared__ __attribute__((aligned(1024))) char smem[LDS_BYTES];
    const unsigned lds0 = (unsigned)(uintptr_t)(LAS char*)smem;
    const int wid = __builtin_amdgcn_readfirstlane(threadIdx.x >> 6);
    int lane = threadIdx.x & 63;
#define BRX 1
    const int b = blockIdx.x, xcd = b & 7, j = b >> 3, br = (xcd >> 2) ^ BRX, qd = xcd & 3;
    const int mt = (qd & 1) * 4 + (j & 3), hp = (qd >> 1) * 8 + (j >> 2);
    const int mt3 = qd * 4 + (j & 3), nt3 = j >> 2;
    u64 tag;
    u32x4 fq[2][2], fk[2][2], fv[2][2];
    f32x16 band;
    STAMP_AT(0, 0);
    {
        const int g = wid >> 2, wi = wid & 3, mw = wi >> 1, hd = wi & 1;
        const char* x = (const char*)(br ? xB : xA) + (size_t)mt * 256 * 2048;
        const char* w = (const char*)(br ? wB : wA) + hp * 128;
        unsigned vx[4], vw[3];
#pragma unroll
        for (int ii = 0; ii < 4; ++ii) {
            const int row = br ? ((wi >> 1) * 64 + (wi & 1) + ii * 16 + 2 * (lane >> 3)) : (wi * 32 + ii * 8 + (lane >> 3));
            vx[ii] = row * 2048 + (((lane & 7) ^ (((lane >> 4) & 1) | ((ii >> 1) << 1) | ((lane >> 5) << 2))) << 4);
        }
#pragma unroll
        for (int jj = 0; jj < 3; ++jj) {
            const int jw = 12 * g + 3 * wi + jj, n = jw >> 3, k0 = (jw & 7) * 8 + (lane >> 3);
            vw[jj] = k0 * 6144 + n * 2048 + (((lane & 7) ^ ((((lane >> 4) & 1) | ((jw & 1) << 1)) << 1)) << 4);
        }
        const unsigned xdst_o = lds0 + K1_X + (1 - g) * 32768 + wi * 4096;
        const unsigned wdst = lds0 + K1_W + (12 * g + 3 * wi) * 1024;
        const char* xo = x + (size_t)(1 - g) * 128 * 2048;
#define ROTM 4
        const int kt0 = ROTM * (j & 3);
#define STAGE_X(kt, dst) do { const char* s_ = xo + (((kt) + kt0) & 15) * 128; _Pragma("unroll") for (int ii = 0; ii < 4; ++ii) glds16(s_, vx[ii], (dst) + ii * 1024); } while (0)
#define STAGE_W(kt, buf) do { const char* s_ = w + (size_t)(((kt) + kt0) & 15) * 64 * 6144; _Pragma("unroll") for (int jj = 0; jj < 3; ++jj) glds16(s_, vw[jj], wdst + (buf) * K1_WB + jj * 1024); } while (0)
        if (wid == 7) {
            const char* pc; unsigned len;
            asm volatile("s_getpc_b64 %0\n.Lmk_code_begin:\n\ts_mov_b32 %1, .Lmk_code_end-.Lmk_code_begin" : "=s"(pc), "=s"(len));
            const unsigned off = (unsigned)(j * 64 + lane) * 64u;
            asm volatile("s_mov_b32 m0, %2\n\ts_nop 0\n\tglobal_load_lds_dword %0, %1" :: "v"(off < len ? off : 0u), "s"(pc), "s"(lds0 + 159744) : "memory");
        }
        STAGE_X(0, xdst_o); STAGE_W(0, 0); STAGE_W(1, 1);
        if (g == 1) STAGE_X(1, xdst_o + 16384);
        { u64 e; asm volatile("s_load_dwordx2 %0, %1, 0x0\n\ts_waitcnt lgkmcnt(0)" : "=s"(e) : "s"(ws + WS_EP) : "memory");
          e += 0x9E3779B97F4A7C15ull; e = (e ^ (e >> 30)) * 0xBF58476D1CE4E5B9ull; e = (e ^ (e >> 27)) * 0x94D049BB133111EBull; tag = e ^ (e >> 31); }
        const int c16 = lane & 15, g4 = lane >> 4;
        unsigned ax[2], aw[2];
        ax[0] = lds0 + K1_X + g * 32768 + (mw * 64 + 16 * (c16 >> 3) + (c16 & 7)) * 128 + ((g4 ^ (((c16 >> 1) & 1) | ((c16 >> 3) << 1) | (((c16 >> 2) & 1) << 2))) << 4);
        ax[1] = ax[0] ^ 64;
        { const int q4 = c16 >> 2, p = lane & 3;
          aw[0] = lds0 + K1_W + (8 * g4 + q4) * 128 + (((hd * 4 + (p >> 1)) ^ ((((q4 >> 1) & 1) | ((g4 & 1) << 1)) << 1)) << 4) + (p & 1) * 8; aw[1] = aw[0] ^ 32; }
        band = band_mask(lane); asm volatile("" : "+v"(band));
        if (g == 1) WAITBAR_(7); else WAITBAR_(3);
        SA_(1);
        if (g == 1) asm volatile("s_barrier" ::: "memory");
        f32x4 acc[4][6];
#pragma unroll
        for (int m = 0; m < 4; ++m)
#pragma unroll
            for (int n = 0; n < 6; ++n) acc[m][n] = f32x4{};
        SBAR();
        unsigned xp = 0, wr_ = 0, ws_ = 2 * K1_WB;
        int tx = (1 + g + kt0) & 15, tw = (2 + kt0) & 15;
        unsigned dxp = (1 - g) * 16384;
#define RDW(n, ks) wf[n][ks] = cat8(ldstr(awc[(n) & 1] + ((n) >> 1) * 8192 + (ks) * 4096), ldstr(awc[(n) & 1] + ((n) >> 1) * 8192 + (ks) * 4096 + 512))
#define P1_TILE(DOX, DOW, WAIT, LASTBAR) do { \
            bf16x8 xf[4][2], wf[6][2]; \
            unsigned awc[2] = {aw[0] + wr_, aw[1] + wr_}; asm volatile("" : "+v"(awc[0]), "+v"(awc[1])); \
            const char* sx = xo + tx * 128; const char* sw = w + (size_t)tw * 64 * 6144; \
            _Pragma("unroll") for (int s = 0; s < 4; ++s) { \
                xf[s][0] = ldsrd128(ax[0] + xp + (s >> 1) * 4096 + (s & 1) * 1024); xf[s][1] = ldsrd128(ax[1] + xp + (s >> 1) * 4096 + (s & 1) * 1024); \
                RDW((3 * s) >> 1, (3 * s) & 1); \
                if (DOX) glds16(sx, vx[s], xdst_o + dxp + s * 1024); \
                RDW((3 * s + 1) >> 1, (3 * s + 1) & 1); RDW((3 * s + 2) >> 1, (3 * s + 2) & 1); \
                if ((DOW) && s < 3) glds16(sw, vw[s], wdst + ws_ + s * 1024); \
            } \
            WAIT; \
            SBAR(); \
            __builtin_amdgcn_s_setprio(1); \
            _Pragma("unroll") for (int m = 0; m < 4; ++m) \
                _Pragma("unroll") for (int n = 0; n < 6; ++n) { \
                    if (n < 4) { acc[m][n] = MFMA16(wf[n][0], xf[m][0], acc[m][n]); acc[m][n] = MFMA16(wf[n][1], xf[m][1], acc[m][n]); } \
                    else       { acc[m][n] = MFMA16(xf[m][0], wf[n][0], acc[m][n]); acc[m][n] = MFMA16(xf[m][1], wf[n][1], acc[m][n]); } \
                    CHAIN(); \
                } \
            __builtin_amdgcn_s_setprio(0); \
            xp ^= 16384; dxp ^= 16384; ws_ = wr_; wr_ = (wr_ == 2 * K1_WB) ? 0 : wr_ + K1_WB; tx = (tx + 1) & 15; tw = (tw + 1) & 15; \
            SBAR(); \
            if (LASTBAR) asm volatile("s_barrier" ::: "memory"); \
            SBAR(); \
        } while (0)
#pragma unroll 1
        for (int t = 0; t < 14; ++t) P1_TILE(true, true, WAITBAR_(7), true);
        P1_TILE(g == 0, false, if (g == 0) WAITBAR_(4); else WAITBAR_(0), true);
        P1_TILE(false, false, WAITBAR_(0), g == 0);
#undef P1_TILE
#undef RDW
#undef STAGE_X
#undef STAGE_W
        SA_(2); SC_(1);
        FRESH();
#pragma unroll
        for (int mb = 0; mb < 2; ++mb)
#pragma unroll
            for (int u = 0; u < 2; ++u) {
                const f32x4 a = acc[2 * mb + u][0] * QSCALE, c = acc[2 * mb + u][1] * QSCALE;
                fq[mb][u] = u32x4{cvtpk(a[0], a[1]), cvtpk(a[2], a[3]), cvtpk(c[0], c[1]), cvtpk(c[2], c[3])};
                fk[mb][u] = pack44(acc[2 * mb + u][2], acc[2 * mb + u][3]);
                fv[mb][u] = pack44(acc[2 * mb][4 + u], acc[2 * mb + 1][4 + u]);
            }
    }
    const char* wo = (const char*)(br ? woB : woA) + nt3 * 256;
    const int k30 = 2 * nt3;
    {
        const int g = wid >> 2, wr = wid >> 1, hd = wid & 1;
        const int ph1 = 2 * hp + hd, pb1 = mt * 8 + wr * 2;
        const int head = 2 * hp + br, blk = mt * 8 + wid;
#define RBLK(e) ((((e) % 7) + (((e) % 7) >= mt)) * 8 + (e) / 7)
        const char* kv = ws + WS_KV + (size_t)head * 262144;
        unsigned l16 = lane * 16;
        const unsigned dbeg = lds0 + wid * 4096, lbase = lds0 + l16;
        const unsigned fk0 = (unsigned)WS_FK + head * 32 * FS;
        const unsigned fxo = (unsigned)WS_FX + ((hp * 8 + mt) * 8 + wid) * FS, xrec0 = (unsigned)WS_XP + ((hp * 8 + mt) * 8 + wid) * 5120 + 2048;
        const f32x16 z = {};
        f32x16 sa, negm, o;
        u32x16 qol, kvf; u32x4 ph;

#define FIRST_TILE(base, q0, q1) do { const unsigned one2 = (((lane >> 4) & 1) == ((lane >> 2) & 1)) ? 0x3F803F80u : 0u; const u32x4 ones = {one2, one2, one2, one2}; \
            const bf16x8 k0_ = ldsrd128(base), k1_ = ldsrd128((base) + 1024); kvf = cat16(asu(ldsrd128((base) + 4096)), asu(ldsrd128((base) + 5120)), asu(ldsrd128((base) + 2048)), ones); \
            sa = MFMA32(k0_, q0, z); sa = MFMA32(k1_, q1, sa); float m_ = sa[0]; \
            _Pragma("unroll") for (int r = 1; r < 16; ++r) m_ = __builtin_fmaxf(m_, sa[r]); \
            m_ = -swap_max(m_); _Pragma("unroll") for (int r = 0; r < 16; ++r) { negm[r] = m_; sa[r] += m_; } \
            qol = cat16(asu(q0), asu(q1), ones, ones); } while (0)
#define L6_OUT "={v[0:15]}"(o), "+{v[32:47]}"(sa), "+{v[64:79]}"(qol), "+{v[80:95]}"(kvf), "={v[108:111]}"(ph)
#define L6C(HL, BASE) asm volatile(ATT_L6C : L6_OUT, "={v[144:159]}"(h0), "={v[160:167]}"(h1) \
            : "{v[16:31]}"(negm), "{v112}"(BASE), "{v[140:143]}"(u32x4{0u, 0u, FLADDR, (unsigned)WS_HALO + (mt * 32 + ph1) * 6144 + 3072 + l16}), \
              [ws] "s"(ws), [hl] "s"(HL), [fld] "s"(fld) : ATT_TMP, "scc", "memory")
#define FLADDR (fk0 + ((((lane & 7) == 7) ? blk : RBLK(8 * (lane & 7) + wid)) >> 1) * FS)
        const unsigned own = (ph1 * 32 + mt * 4 + wr) * FS;
        const unsigned fld = lds0 + K2_FL + wid * 1024 + br * (2 * K2_SLOT + 4096 - K2_FL + wid * 11264);
        const unsigned pv = (lane >> 5) * 1024 + ((lane & 15) + 32 * ((lane >> 4) & 1)) * 16, pq = pv + (lane & 8) * 16;
        if (br == 0) {
            const char* kp = ws + WS_KV + (size_t)((ph1 * 64 + pb1) * 4) * 1024;
#define PUB_KV() do { st16_dev<0>(kp, pq, fk[0][0]); st16_dev<128>(kp, pq, fk[0][1]); st16_dev<2048>(kp, pv, fv[0][0]); st16_dev<2304>(kp, pv, fv[0][1]); \
            st16_dev<0>(kp + 4096, pq, fk[1][0]); st16_dev<128>(kp + 4096, pq, fk[1][1]); st16_dev<2048>(kp + 4096, pv, fv[1][0]); st16_dev<2304>(kp + 4096, pv, fv[1][1]); } while (0)
            if (hd == 1) {
                const char* qp = ws + WS_QF + (size_t)((ph1 * 64 + pb1) * 2) * 1024;
                st16_dev<0>(qp, pq, fq[0][0]); st16_dev<128>(qp, pq, fq[0][1]); st16_dev<2048>(qp, pq, fq[1][0]); st16_dev<2176>(qp, pq, fq[1][1]);
            }
            if (wr == 0) PUB_KV();
#pragma unroll
            for (int mb = 0; mb < 2; ++mb) {
                const unsigned a = lds0 + hd * 2 * K2_SLOT + (wr * 2 + mb) * 4096, aq = lds0 + K2_QX + hd * 16384 + (wr * 2 + mb) * 2048 + pq;
                ldswr128(a + pq, fk[mb][0]); ldswr128(a + pq + 128, fk[mb][1]); ldswr128(a + 2048 + pv, fv[mb][0]); ldswr128(a + 2304 + pv, fv[mb][1]);
                if (hd == 0 || g == 0) { ldswr128(aq, fq[mb][0]); ldswr128(aq + 128, fq[mb][1]); }
            }
            BARRIER_L();
            if (wr == 0 || hd == 1) {
                drain();
                if (lane == 0) { if (wr == 0) flag_set(ws + WS_FK, own, tag); if (hd == 1) flag_set(ws + WS_FQ, own, tag); }
            }
            if (wr == 1) PUB_KV();
            const unsigned qa = lbase + K2_QX + (1 - g) * 16384 + wid * 2048, cx = lbase + (1 - g) * 2 * K2_SLOT;
            SA_(3); SC_(2);
            { const bf16x8 q0 = ldsrd128(qa), q1 = ldsrd128(qa + 1024); FIRST_TILE(cx, q0, q1); }
            asm volatile(ATT_L4 : L6_OUT : "{v[16:31]}"(negm), "{v112}"(cx) : ATT_TMP, "memory");
            SC_(3);
            if (g == 1) {
                const int b1 = RBLK(wid), b0 = RBLK(wid - 4);
                PUB_KV();
                flag_wait(ws, fk0 + (((lane & 1) ? b1 : b0) >> 1) * FS, tag);
                if (lane == 0) flag_set(ws + WS_FK, own, tag);
                glds4k_dev(kv + b1 * 4096, l16, dbeg + K2_SLOT);
                glds4k_dev(kv + b0 * 4096, l16, dbeg + K2_SLOT - 16384);
                glds16_dev(ws, FLADDR, fld);
            } else if (wr == 1) { drain(); if (lane == 0) flag_set(ws + WS_FK, own, tag); }
            SC_(4);
            asm volatile(ATT_L2 : "+{v[0:15]}"(o), "+{v[32:47]}"(sa), "+{v[64:79]}"(qol), "+{v[80:95]}"(kvf), "+{v[108:111]}"(ph) : "{v[16:31]}"(negm), "{v112}"(cx) : ATT_TMP, "memory");
            if (g == 0) {
                asm volatile(ATT_XT : "+{v[0:15]}"(o), "+{v[32:47]}"(sa), "+{v[64:79]}"(qol), "+{v[80:95]}"(kvf), "+{v[108:111]}"(ph) : "{v[16:31]}"(negm), "{v112}"(cx) : ATT_TMP, "memory");
                st16_dev<-2048>(ws, xrec0 + l16, quad(o, 0)); st16_dev<-1024>(ws, xrec0 + l16, quad(o, 1)); st16_dev<0>(ws, xrec0 + l16, quad(o, 2)); st16_dev<1024>(ws, xrec0 + l16, quad(o, 3));
                st16_dev<2048>(ws, xrec0 + l16, u32x4{qol[12], __float_as_uint(negm[0]), 0u, 0u});
                SC_(5);
                const bf16x8 q0 = ldsrd128(lbase + K2_QX + wid * 2048), q1 = ldsrd128(lbase + K2_QX + wid * 2048 + 1024);
                FIRST_TILE(lbase, q0, q1);
                u32x16 h0; u32x8 h1;
                L6C(0, lbase);
            }
#undef PUB_KV
        } else {
            if (wr == 3 && mt < 7) {
                const char* p = ws + WS_HALO + (size_t)(((mt + 1) * 32 + ph1) * 6) * 1024;
                st16_dev<0>(p, pq, fk[0][0]); st16_dev<128>(p, pq, fk[0][1]); st16_dev<3072>(p, pq, fk[1][0]); st16_dev<3200>(p, pq, fk[1][1]);
                if (lane >= 32) { st16_dev<1024>(p, pv, fv[0][0]); st16_dev<1280>(p, pv, fv[0][1]); st16_dev<1024>(p + 3072, pv, fv[1][0]); st16_dev<1280>(p + 3072, pv, fv[1][1]); }
            }
            const unsigned ex = lds0 + 2 * K2_SLOT + wid * 12288;
#pragma unroll
            for (int mb = 0; mb < 2; ++mb) {
                const unsigned a = ex + mb * 6144;
                ldswr128(a + pq, fq[mb][0]); ldswr128(a + pq + 128, fq[mb][1]); ldswr128(a + 2048 + pq, fk[mb][0]); ldswr128(a + 2176 + pq, fk[mb][1]);
                ldswr128(a + 4096 + pv, fv[mb][0]); ldswr128(a + 4352 + pv, fv[mb][1]);
            }
            BARRIER_L();
            if (wr == 3 && mt < 7) { drain(); if (lane == 0) flag_set(ws + WS_FH, (ph1 * 8 + mt + 1) * FS, tag); }
            SC_(2);
            LocalPart lp[2];
            bf16x8 bq[2][2];
#pragma unroll
            for (int mb = 0; mb < 2; ++mb) { bq[mb][0] = ldsrd128(ex + mb * 6144 + l16); bq[mb][1] = ldsrd128(ex + mb * 6144 + 1024 + l16); }
#define LOCAL_BLOCK(mb) do { const unsigned a_ = ex + mb * 6144 + l16; \
                const bf16x8 k0_ = ldsrd128(a_ + 2048), k1_ = ldsrd128(a_ + 3072), v0_ = ldsrd128(a_ + 4096), v1_ = ldsrd128(a_ + 5120); \
                if (wr > 0) local_own<true>(bq[mb][0], bq[mb][1], k0_, k1_, v0_, v1_, ldsrd128(a_ - 24576 + 2048), ldsrd128(a_ - 24576 + 3072), ldsrd128(a_ - 24576 + 5120), band, lane, lp[mb]); \
                else local_own<false>(bq[mb][0], bq[mb][1], k0_, k1_, v0_, v1_, k0_, k0_, k0_, band, lane, lp[mb]); \
                if (wr > 0 || mt == 0) local_fin(lp[mb]); } while (0)
            LOCAL_BLOCK(0);
            SC_(3);
            const int hl = (wid < 2) ? mt : 0;
            const unsigned fh = (unsigned)WS_FH + (ph1 * 8 + mt) * FS;
            const int b0 = RBLK(wid), b1 = RBLK(8 + wid);
            const int q = lane & 3;
            unsigned fa0 = fk0 - 1024 * FS + (blk >> 1) * FS, fa1 = fk0 + (b0 >> 1) * FS, fa2 = fk0 + (b1 >> 1) * FS, fa3 = fh;
            asm volatile("" : "+s"(fa0), "+s"(fa1), "+s"(fa2), "+s"(fa3));
            const unsigned lo_ = (q & 1) ? fa1 : fa0, hi_ = (q & 1) ? fa3 : fa2;
            const u64 fm = flag_wait_some(ws, (q & 2) ? hi_ : lo_, tag, 3);
            SC_(4);
            glds4k_dev(kv + b0 * 4096, l16, dbeg);
            const char* qp = ws + WS_QF + (size_t)((head * 64 + blk) * 2) * 1024;
            glds16_dev(qp, l16, ex); glds16_dev(qp + 1024, l16, ex + 1024);
            if (fm & 4) glds4k_dev(kv + b1 * 4096, l16, dbeg + K2_SLOT);
            LOCAL_BLOCK(1);
#undef LOCAL_BLOCK
            if (fm & 4) WAITBAR_(4);
            else {
                WAITBAR_(0);
                flag_wait(ws, fk0 + (b1 >> 1) * FS, tag);
                glds4k_dev(kv + b1 * 4096, l16, dbeg + K2_SLOT);
            }
            const bf16x8 q0 = ldsrd128(ex + l16), q1 = ldsrd128(ex + 1024 + l16);
            SA_(3); SC_(5);
            if (hl && !(fm & 8)) flag_wait(ws, fh, tag);
            FIRST_TILE(lbase, q0, q1);
            u32x16 h0; u32x8 h1;
            L6C(hl, lbase);
            if (hl) {
                local_prev(bq[0][0], bq[0][1], asbf(u32x4{h0[0], h0[1], h0[2], h0[3]}), asbf(u32x4{h0[4], h0[5], h0[6], h0[7]}), asbf(u32x4{h0[8], h0[9], h0[10], h0[11]}), lane, lp[0]);
                local_prev(bq[1][0], bq[1][1], asbf(u32x4{h0[12], h0[13], h0[14], h0[15]}), asbf(u32x4{h1[0], h1[1], h1[2], h1[3]}), asbf(u32x4{h1[4], h1[5], h1[6], h1[7]}), lane, lp[1]);
                local_fin(lp[0]); local_fin(lp[1]);
            }
            const char* op = ws + WS_OF + (size_t)(((64 + pb1) * 32 + ph1) * 2) * 1024;
            st16_dev<0>(op, l16, quad(lp[0].o, 0)); st16_dev<1024>(op, l16, quad(lp[0].o, 1)); st16_dev<0>(op + 65536, l16, quad(lp[1].o, 0)); st16_dev<1024>(op + 65536, l16, quad(lp[1].o, 1));
        }
#undef FIRST_TILE
#undef L6_OUT
#undef L6C
        FRESH();
        l16 = lane * 16;
        SA_(4); SC_(6);
        const int isx = br & (1 - g);
        unsigned cnt = 7, dst = dbeg + 2 * K2_SLOT, nx = K2_SLOT;
        unsigned mode = isx << 1;
        const unsigned act = 2u | (isx * 12u) | ((br | (1 - g)) << 5);
        u64 tab = (u64)blk << 42;
#pragma unroll
        for (int k = 1; k < 7; ++k) tab |= (u64)RBLK(8 * k + wid) << (6 * k);
        tab >>= 6 + 6 * br;
        u32x8 adr;
        { const int k = 8 * wid + (lane >> 4), c = lane & 15;
          adr = u32x8{lds0 + l16, lds0 + l16 + K2_SLOT, l16, lds0 + l16, (unsigned)(k * 2048 + ((c ^ ((k & 7) << 1)) << 4)), (unsigned)((k + 4) * 2048 + ((c ^ (((k + 4) & 7) << 1)) << 4) - 1024),
                      br ? (unsigned)WS_FO + ((64 + pb1 + (lane & 1)) * 32 + ph1) * FS : fxo, 0u};
          const int i = (mt3 * 8 + wid) * 12 + ((lane * 3) >> 4); adr[7] = ((((4 + (i >> 7) + k30) & 15) * 64 + ((i & 127) >> 1)) * 2048 + (i & 1) * 128); }
        f32x16 xo; f32x2 xl;
#define DBGOUT
        asm volatile(ATT_MAIN
                     : DBGOUT"+{v[0:15]}"(o), "+{v[32:47]}"(sa), "+{v[64:79]}"(qol), "+{v[80:95]}"(kvf), "+{v[108:111]}"(ph), "+{v[112:119]}"(adr), "={v[122:137]}"(xo), "={v[138:139]}"(xl),
                       [cnt] "+s"(cnt), [dst] "+s"(dst), [nx] "+s"(nx), [mode] "+s"(mode)
                     : "{v[16:31]}"(negm), "{v[140:143]}"(u32x4{(unsigned)tag, (unsigned)(tag >> 32), fxo, xrec0 + l16}),
                       "{v[170:173]}"(u32x4{FLADDR, 0u, 0u, fld + l16}),
                       [srclo] "s"((unsigned)(uintptr_t)kv), [srchi] "s"((unsigned)((uintptr_t)kv >> 32)), [dbeg] "s"(dbeg), [dend] "s"(dbeg + 3 * K2_SLOT),
                       [wo01] "s"(wo + (size_t)k30 * 131072), [wo23] "s"(wo + (size_t)((k30 + 2) & 15) * 131072), [wo] "s"(wo), [wdst] "s"(lds0 + 98304 + wid * 2048), [act] "s"(act), [tab] "s"(tab), [k0] "s"(br), [ws] "s"(ws)
                     : ATT_TMP, "v120", "v121", "v168", "v169", "s40", "s41", "s42", "s43", "s44", "s45", "s46", "s47", "s48", "s49", "s50", "s51", "vcc", "scc", "memory");
        FRESH();
        l16 = lane * 16;
        const unsigned xrec = xrec0 + l16;
        const bf16x8 q0 = asbf(u32x4{qol[0], qol[1], qol[2], qol[3]}), q1 = asbf(u32x4{qol[4], qol[5], qol[6], qol[7]});
        const u32x4 ones = {qol[8], qol[9], qol[10], qol[11]};
        SA_(5); SB_(1);
        float lsum = __uint_as_float(qol[12]);
        bool ok = lsum < 1.0e18f;
        if (isx) {
            if (__builtin_expect(!(mode & 4), 0)) {
                flag_wait(ws, fxo, tag);
                f32x4 a0, a1, a2, a3;
                asm volatile("global_load_dwordx4 %0, %5, %6 offset:-2048 sc1\n\tglobal_load_dwordx4 %1, %5, %6 offset:-1024 sc1\n\tglobal_load_dwordx4 %2, %5, %6 sc1\n\t"
                             "global_load_dwordx4 %3, %5, %6 offset:1024 sc1\n\tglobal_load_dwordx2 %4, %5, %6 offset:2048 sc1\n\ts_waitcnt vmcnt(0)"
                             : "=&v"(a0), "=&v"(a1), "=&v"(a2), "=&v"(a3), "=&v"(xl) : "v"(xrec), "s"(ws) : "memory");
#pragma unroll
                for (int r = 0; r < 4; ++r) { xo[r] = a0[r]; xo[4 + r] = a1[r]; xo[8 + r] = a2[r]; xo[12 + r] = a3[r]; }
            }
            const float nm = __builtin_fminf(negm[0], xl[1]), fa = EXP2(nm - negm[0]), fx = EXP2(nm - xl[1]);
            ok = ok && (xl[0] < 1.0e18f);
            lsum = lsum * fa + xl[0] * fx;
#pragma unroll
            for (int r = 0; r < 16; ++r) o[r] = o[r] * fa + xo[r] * fx;
        }
#pragma unroll
        for (int r = 0; r < 16; ++r) ok = ok && (__builtin_fabsf(o[r]) < 3.0e38f);
        if (__builtin_expect(__any(!ok), 0)) {
            float m = -__builtin_inff();
            f32x4 l;
            for (int pass = 0; pass < 2; ++pass) {
                o = z; l = f32x4{};
                for (int t = 0; t < 64; ++t) {
                    bf16x8 f0, f1, f2, f3;
                    asm volatile("global_load_dwordx4 %0, %4, %5 sc1\n\tglobal_load_dwordx4 %1, %4, %5 offset:1024 sc1\n\tglobal_load_dwordx4 %2, %4, %5 offset:2048 sc1\n\t"
                                 "global_load_dwordx4 %3, %4, %5 offset:3072 sc1\n\ts_waitcnt vmcnt(0)" : "=&v"(f0), "=&v"(f1), "=&v"(f2), "=&v"(f3) : "v"(l16 + t * 4096), "s"(kv) : "memory");
                    f32x16 s = MFMA32(f0, q0, z); s = MFMA32(f1, q1, s);
                    if (pass == 0) {
#pragma unroll
                        for (int r = 0; r < 16; ++r) m = __builtin_fmaxf(m, s[r]);
                    } else {
#pragma unroll
                        for (int r = 0; r < 16; ++r) s[r] = EXP2(s[r] - m);
                        const bf16x8 p0 = asbf(pack8(s, 0)), p1 = asbf(pack8(s, 1));
                        o = MFMA32(f2, p0, o); o = MFMA32(f3, p1, o);
                        l = MFMA16(asbf(ones), p0, l); l = MFMA16(asbf(ones), p1, l);
                    }
                }
                if (pass == 0) m = swap_max(m);
            }
            lsum = l[0];
        }
        const float inv = __builtin_amdgcn_rcpf(lsum);
#pragma unroll
        for (int r = 0; r < 16; ++r) o[r] *= inv;
        const char* op = ws + WS_OF + (size_t)((blk * 32 + head) * 2) * 1024;
        st16_dev<0>(op, l16, pack8(o, 0)); st16_dev<1024>(op, l16, pack8(o, 1));
    }
    {
        FRESH();
        const int g = wid >> 2, wi = wid & 3, wm = wi >> 1, wn = wi & 1, g4 = lane >> 4;
        const char* of = ws + WS_OF + (size_t)(br * 64 + mt3 * 4 + wi) * 65536;
        unsigned vw4[2];
#pragma unroll
        for (int ii = 0; ii < 2; ++ii) { const int k = 16 * wi + 4 * ii + g4; vw4[ii] = k * 2048 + (((lane & 15) ^ ((k & 7) << 1)) << 4); }
        const unsigned l16 = lane * 16, sdst = lds0 + wi * 4096;
#define STAGE_O(t) glds4k_dev(of + (((t) + k30) & 15) * 4096, l16, sdst + ((t) & 3) * 16384)
#define STAGE_WO(t, slot) do { const char* s_ = wo + (size_t)(((t) + k30) & 15) * 131072; glds16(s_, vw4[0], sdst + (slot)); glds16(s_, vw4[1], sdst + (slot) + 1024); \
            glds16(s_ + 16384, vw4[0], sdst + (slot) + 2048); glds16(s_ + 16384, vw4[1], sdst + (slot) + 3072); } while (0)
        const unsigned fo = (unsigned)WS_FO + ((br * 64 + mt3 * 4 + wi) * 32 + 4 * ((lane & 15) >> 1) + 2 * (1 - g) + (lane & 1)) * FS;
        drain();
        if (lane == 0) flag_set(ws + WS_FO, ((mt * 8 + wid) * 32 + 2 * hp + br) * FS, tag);
        SB_(2);
        asm volatile("s_barrier" ::: "memory");
        SB_(3);
        flag_wait(ws, fo, tag);
        STAGE_O(1 - g);
        if (g == 1) STAGE_O(2);
        STAGE_WO(5 - g, 65536 + (1 - g) * 16384);
        SA_(6); SB_(4);
        const unsigned ao = lds0 + g * 16384 + wm * 8192 + (g4 >> 1) * 1024 + ((lane & 15) + 32 * (g4 & 1)) * 16;
        unsigned aw[4];
        { const int q4 = (lane & 15) >> 2, p = lane & 3, k = 16 * (g4 >> 1) + 4 * (g4 & 1) + q4;
          const unsigned a0 = lds0 + g * 16384 + k * 256 + (((8 * wn + (p >> 1)) ^ ((k & 7) << 1)) << 4) + (p & 1) * 8;
#pragma unroll
          for (int nb = 0; nb < 4; ++nb) aw[nb] = a0 ^ (nb << 5); }
        f32x4 acc[4][4];
#pragma unroll
        for (int mb = 0; mb < 4; ++mb)
#pragma unroll
            for (int nb = 0; nb < 4; ++nb) acc[mb][nb] = f32x4{};
        if (g == 1) { WAITBAR_(8); asm volatile("s_barrier" ::: "memory"); } else asm volatile("s_barrier" ::: "memory");
        SB_(5);
        SBAR();
#pragma unroll
        for (int u = 0; u < 8; ++u) {
            bf16x8 of_[4][2], wf[4][2];
            const int to = 2 * u + 3 + g, tw = to + 2;
            const char* so = of + ((to + k30) & 15) * 4096; const char* sw = wo + (size_t)((tw + k30) & 15) * 131072;
            const unsigned dob = sdst + (to & 3) * 16384, dwb = sdst + (g ? p3_wslot(2 * u + 6) : p3_wslot(2 * u + 5));
            const bool doo = u < 6 || (u == 6 && g == 0), dow = (u >= 1 || g == 1) && (u < 5 || (u == 5 && g == 0));
#pragma unroll
            for (int i = 0; i < 8; ++i) {
                const int hh = i >> 2, q = i & 3;
                of_[q][hh] = ldsrd128(ao + ((2 * u) & 3) * 16384 + (q >> 1) * 4096 + (q & 1) * 256 + hh * 2048);
                wf[q][hh] = cat8(ldstr(aw[q] + p3_wslot(2 * u) + hh * 8192), ldstr(aw[q] + p3_wslot(2 * u) + hh * 8192 + 2048));
                if (i < 4) { if (u < 7 && doo) glds16_dev(so + i * 1024, l16, dob + i * 1024); }
                else if (u < 6 && dow) glds16(sw + ((i & 2) ? 16384 : 0), vw4[i & 1], dwb + (i - 4) * 1024);
            }
            if (p3_allow(2 * u) == p3_allow(2 * u + 1)) WAITBAR(p3_allow(2 * u)); else if (g == 0) WAITBAR(p3_allow(2 * u)); else WAITBAR(p3_allow(2 * u + 1));
            SBAR();
            __builtin_amdgcn_s_setprio(1);
#pragma unroll
            for (int mb = 0; mb < 4; ++mb)
#pragma unroll
                for (int nb = 0; nb < 4; ++nb) { acc[mb][nb] = MFMA16(wf[nb][0], of_[mb][0], acc[mb][nb]); acc[mb][nb] = MFMA16(wf[nb][1], of_[mb][1], acc[mb][nb]); CHAIN(); }
            __builtin_amdgcn_s_setprio(0);
            SBAR();
            if (u < 7 || g == 0) asm volatile("s_barrier" ::: "memory");
            SBAR();
        }
        SB_(6);
        FRESH();
        const unsigned xw = lds0 + wid * 8192 + l16, xr = lds0 + (wid ^ 4) * 8192 + l16;
#pragma unroll
        for (int i = 0; i < 8; ++i) { const f32x4 v = g ? acc[i >> 2][i & 3] : acc[2 + (i >> 2)][i & 3]; ldswr128(xw + i * 1024, __builtin_bit_cast(u32x4, v)); }
        BARRIER_L();
        const unsigned st = lds0 + 65536 + wid * 4096;
#pragma unroll
        for (int i = 0; i < 8; ++i) {
            const f32x4 v = (g ? acc[2 + (i >> 2)][i & 3] : acc[i >> 2][i & 3]) + __builtin_bit_cast(f32x4, ldsrd128(xr + i * 1024));
            const int row = 16 * (i >> 2) + (lane & 15), c = 2 * (i & 3) + (g4 >> 1);
            *(LAS u32x2*)(uintptr_t)(st + row * 128 + ((c ^ (row & 7)) << 4) + (g4 & 1) * 8) = u32x2{cvtpk(v[0], v[1]), cvtpk(v[2], v[3])};
        }
        const int blk = mt3 * 4 + 2 * wm + g;
        char* op = (char*)out + br * 2048 + nt3 * 256 + wn * 128 + (lane & 7) * 16;
#pragma unroll
        for (int i = 0; i < 4; ++i) {
            const int r = 8 * i + (lane >> 3), row = br ? ((blk >> 1) * 64 + 2 * r + (blk & 1)) : (blk * 32 + r);
            st16_dev<0>(out, (unsigned)(row * 4096 + br * 2048 + nt3 * 256 + wn * 128 + (lane & 7) * 16), __builtin_bit_cast(u32x4, ldsrd128(st + r * 128 + (((lane & 7) ^ (r & 7)) << 4))));
        }
#undef STAGE_O
#undef STAGE_WO
    }
    STAMP_AT(0, 7);
    if (b == 0 && wid == 0) {
#pragma unroll
        for (int i = 0; i < 2; ++i) { const int u = i * 64 + lane;
            flag_wait(ws0, (unsigned)WS_FK + ((2 * (u >> 3)) * 32 + (u & 7) * 4) * FS, tag);
            flag_wait(ws0, (unsigned)WS_FO + ((64 + (u & 7) * 8) * 32 + 2 * (u >> 3)) * FS, tag); }
        if (lane == 0) flag_set(ws0, (unsigned)WS_EP, tag);
    }
    asm volatile(".Lmk_code_end:");
}

extern "C" void kernel_launch(void* const* d_in, const int* in_sizes, int n_in, void* d_out, int out_size, void* d_ws, size_t ws_size, hipStream_t stream) {
    mk_fused<<<256, 512, 0, stream>>>((const u16*)d_in[0], (const u16*)d_in[1], (const u16*)d_in[2], (const u16*)d_in[3], (const u16*)d_in[4], (const u16*)d_in[5], (char*)d_ws, (u16*)d_out);
}
```

```cpp
#include <hip/hip_runtime.h>
#include <stdint.h>

typedef unsigned short u16;
typedef unsigned long long u64;
typedef __bf16 bf16x8 __attribute__((ext_vector_type(8)));
typedef __bf16 bf16x2 __attribute__((ext_vector_type(2)));
typedef short s16x4 __attribute__((ext_vector_type(4)));
typedef float f32x16 __attribute__((ext_vector_type(16)));
typedef float f32x4 __attribute__((ext_vector_type(4)));
typedef float f32x2 __attribute__((ext_vector_type(2)));
typedef unsigned u32x4 __attribute__((ext_vector_type(4)));
typedef unsigned u32x2 __attribute__((ext_vector_type(2)));
typedef unsigned u32x8 __attribute__((ext_vector_type(8)));
typedef unsigned u32x16 __attribute__((ext_vector_type(16)));
#define LAS __attribute__((address_space(3)))
#define DI __device__ __forceinline__
#define SBAR() __builtin_amdgcn_sched_barrier(0)
#define MFMA32(a, b, c) __builtin_amdgcn_mfma_f32_32x32x16_bf16((a), (b), (c), 0, 0, 0)
#define MFMA16(a, b, c) __builtin_amdgcn_mfma_f32_16x16x32_bf16((a), (b), (c), 0, 0, 0)
#define CHAIN() SBAR()
#define EXP2(x) __builtin_amdgcn_exp2f(x)

constexpr float QSCALE = 0.17677669529663687f * 1.4426950408889634f;

constexpr size_t WS_QF = 0;
constexpr size_t WS_KV = WS_QF + (4u << 20);
constexpr size_t WS_OF = WS_KV + (8u << 20);
constexpr size_t WS_HALO = WS_OF + (8u << 20);
constexpr size_t WS_XP = WS_HALO + (2u << 20);
constexpr size_t WS_FX = WS_XP + (5u << 20);
#define FS 64
constexpr size_t WS_FQ = WS_FX + 1024 * FS;
constexpr size_t WS_FK = WS_FQ + 1024 * FS;
constexpr size_t WS_FO = WS_FK + 1024 * FS;
constexpr size_t WS_FH = WS_FO + 4096 * FS;
constexpr size_t WS_EP = WS_FH + 256 * FS;

#define OUTST 1
#define SLP 1
#define STAMP_AT(K, I)
#define SA_(I)
#define SB_(I)

#define SC_(I)
#define JIT(p)
DI unsigned cvtpk(float lo, float hi) { f32x2 v = {lo, hi}; bf16x2 b = __builtin_convertvector(v, bf16x2); return __builtin_bit_cast(unsigned, b); }
DI u32x4 pack8(const f32x16& a, int s) { u32x4 r; r[0] = cvtpk(a[8 * s], a[8 * s + 1]); r[1] = cvtpk(a[8 * s + 2], a[8 * s + 3]); r[2] = cvtpk(a[8 * s + 4], a[8 * s + 5]); r[3] = cvtpk(a[8 * s + 6], a[8 * s + 7]); return r; }
DI u32x4 pack44(f32x4 a, f32x4 c) { return u32x4{cvtpk(a[0], a[1]), cvtpk(a[2], a[3]), cvtpk(c[0], c[1]), cvtpk(c[2], c[3])}; }
DI u32x4 asu(bf16x8 v) { return __builtin_bit_cast(u32x4, v); }
DI u32x16 cat16(u32x4 a, u32x4 b, u32x4 c, u32x4 d) { return u32x16{a[0], a[1], a[2], a[3], b[0], b[1], b[2], b[3], c[0], c[1], c[2], c[3], d[0], d[1], d[2], d[3]}; }
DI bf16x8 asbf(u32x4 v) { return __builtin_bit_cast(bf16x8, v); }
DI void glds16(const void* sbase, unsigned voff, unsigned lds) {
    asm volatile("s_mov_b32 m0, %2\n\ts_nop 0\n\tglobal_load_lds_dwordx4 %0, %1" :: "v"(voff), "s"(sbase), "s"(lds) : "memory");
}
DI void glds16_dev(const void* sbase, unsigned voff, unsigned lds) {
    asm volatile("s_mov_b32 m0, %2\n\ts_nop 0\n\tglobal_load_lds_dwordx4 %0, %1 sc1" :: "v"(voff), "s"(sbase), "s"(lds) : "memory");
}
DI void glds4k_dev(const void* sbase, unsigned voff, unsigned lds) {
    asm volatile("s_mov_b32 m0, %2\n\ts_nop 0\n\tglobal_load_lds_dwordx4 %0, %1 sc1\n\tglobal_load_lds_dwordx4 %0, %1 offset:1024 sc1\n\t"
                 "global_load_lds_dwordx4 %0, %1 offset:2048 sc1\n\tglobal_load_lds_dwordx4 %0, %1 offset:3072 sc1" :: "v"(voff), "s"(sbase), "s"(lds) : "memory");
}
template <int OFF> DI void st16_dev(const void* sbase, unsigned voff, u32x4 v) {
    asm volatile("global_store_dwordx4 %0, %1, %2 offset:%3 sc1\n\ts_nop 1" :: "v"(voff), "v"(v), "s"(sbase), "i"(OFF) : "memory");
}
DI void drain() { asm volatile("s_waitcnt vmcnt(0)" ::: "memory"); }
DI void flag_set(const void* sbase, unsigned voff, u64 tag) {
    asm volatile("global_store_dwordx2 %0, %1, %2 sc1" :: "v"(voff), "v"(tag), "s"(sbase) : "memory");
}
DI void flag_wait(const void* sbase, unsigned voff, u64 tag) {
    const u64 t0 = __builtin_amdgcn_s_memrealtime();
    for (;;) {
        u64 v;
        asm volatile("global_load_dwordx2 %0, %1, %2 sc1\n\ts_waitcnt vmcnt(0)" : "=v"(v) : "v"(voff), "s"(sbase) : "memory");
        if (__all(v == tag)) break;
        if (__builtin_amdgcn_s_memrealtime() - t0 > 2000000ull) break;
        __builtin_amdgcn_s_sleep(SLP);
    }
}
DI u64 flag_wait_some(const void* sbase, unsigned voff, u64 tag, u64 need) {
    const u64 t0 = __builtin_amdgcn_s_memrealtime();
    for (;;) {
        u64 v;
        asm volatile("global_load_dwordx2 %0, %1, %2 sc1\n\ts_waitcnt vmcnt(0)" : "=v"(v) : "v"(voff), "s"(sbase) : "memory");
        const u64 m = __ballot(v == tag);
        if ((m & need) == need || __builtin_amdgcn_s_memrealtime() - t0 > 2000000ull) return m;
        __builtin_amdgcn_s_sleep(SLP);
    }
}
DI bf16x8 ldsrd128(unsigned addr) { return *(const LAS bf16x8*)(uintptr_t)addr; }
DI void ldswr128(unsigned addr, u32x4 v) { *(LAS u32x4*)(uintptr_t)addr = v; }
DI s16x4 ldstr(unsigned addr) { return __builtin_amdgcn_ds_read_tr16_b64_v4i16((LAS s16x4*)(uintptr_t)addr); }
DI bf16x8 cat8(s16x4 lo, s16x4 hi) { typedef short s16x8 __attribute__((ext_vector_type(8))); s16x8 v = {lo[0], lo[1], lo[2], lo[3], hi[0], hi[1], hi[2], hi[3]}; return __builtin_bit_cast(bf16x8, v); }
DI float swap_max(float m) { auto r = __builtin_amdgcn_permlane32_swap(__float_as_uint(m), __float_as_uint(m), false, false); return __builtin_fmaxf(__uint_as_float(r[0]), __uint_as_float(r[1])); }
DI float swap_add(float m) { auto r = __builtin_amdgcn_permlane32_swap(__float_as_uint(m), __float_as_uint(m), false, false); return __uint_as_float(r[0]) + __uint_as_float(r[1]); }
#define FRESH() asm volatile("" : "+v"(lane), "+s"(ws))
#define BARRIER_L() asm volatile("s_waitcnt lgkmcnt(0)\n\ts_barrier" ::: "memory")
#define WAITBAR_(n) asm volatile("s_waitcnt vmcnt(%0) lgkmcnt(0)\n\ts_barrier" :: "i"(n) : "memory")
#define WAITBAR(n) do { switch (n) { case 0: WAITBAR_(0); break; case 2: WAITBAR_(2); break; case 3: WAITBAR_(3); break; case 4: WAITBAR_(4); break; case 6: WAITBAR_(6); break; \
    case 7: WAITBAR_(7); break; case 8: WAITBAR_(8); break; case 10: WAITBAR_(10); break; case 12: WAITBAR_(12); break; default: WAITBAR_(0); } } while (0)

struct LocalPart { f32x16 o; float m, l; };
DI f32x16 band_mask(int lane) {
    const int i = lane & 31, h = lane >> 5;
    f32x16 c;
#pragma unroll
    for (int r = 0; r < 16; ++r) { const int j = (r & 3) + 8 * (r >> 2) + 4 * h; c[r] = ((unsigned)(i - j) <= 7u) ? 0.f : -__builtin_inff(); }
    return c;
}
DI float max3(float a, float b, float c) { float r; asm("v_max3_f32 %0, %1, %2, %3" : "=v"(r) : "v"(a), "v"(b), "v"(c)); return r; }
template <bool PREV> DI void local_own(bf16x8 q0, bf16x8 q1, bf16x8 k0, bf16x8 k1, bf16x8 v0, bf16x8 v1, bf16x8 kp0, bf16x8 kp1, bf16x8 vp1, const f32x16& band, int lane, LocalPart& P) {
    const f32x16 z = {};
    f32x16 so = MFMA32(k0, q0, band), sp = z;
    if (PREV) sp = MFMA32(kp0, q0, z);
    so = MFMA32(k1, q1, so);
    if (PREV) sp = MFMA32(kp1, q1, sp);
    float m = __builtin_fmaxf(so[0], so[1]);
#pragma unroll
    for (int r = 2; r < 16; r += 2) m = __builtin_fmaxf(__builtin_fmaxf(m, so[r]), so[r + 1]);
    if (PREV) {
        const int d = 4 * (lane >> 5) - (lane & 31);
#pragma unroll
        for (int r = 12; r < 16; ++r) { sp[r] = (d + (r & 3) >= 1) ? sp[r] : -__builtin_inff(); m = __builtin_fmaxf(m, sp[r]); }
    }
    m = swap_max(m);
    float l = 0.f;
#pragma unroll
    for (int r = 0; r < 16; ++r) { so[r] = EXP2(so[r] - m); l += so[r]; }
    P.o = MFMA32(v0, asbf(pack8(so, 0)), z); P.o = MFMA32(v1, asbf(pack8(so, 1)), P.o);
    if (PREV) {
#pragma unroll
        for (int r = 12; r < 16; ++r) { sp[r] = EXP2(sp[r] - m); l += sp[r]; }
        P.o = MFMA32(vp1, asbf(u32x4{0u, 0u, cvtpk(sp[12], sp[13]), cvtpk(sp[14], sp[15])}), P.o);
    }
    P.m = m; P.l = l;
}
DI void local_prev(bf16x8 q0, bf16x8 q1, bf16x8 kp0, bf16x8 kp1, bf16x8 vp1, int lane, LocalPart& P) {
    const int i = lane & 31, h = lane >> 5;
    const f32x16 z = {};
    f32x16 sp = MFMA32(kp0, q0, z); sp = MFMA32(kp1, q1, sp);
    const float NEG = -__builtin_inff();
    float m = P.m;
#pragma unroll
    for (int r = 12; r < 16; ++r) { const int j = (r & 3) + 24 + 4 * h; sp[r] = (j >= i + 25) ? sp[r] : NEG; m = __builtin_fmaxf(m, sp[r]); }
    m = swap_max(m);
    const float f = EXP2(P.m - m);
    float l = P.l * f;
#pragma unroll
    for (int r = 12; r < 16; ++r) { sp[r] = EXP2(sp[r] - m); l += sp[r]; }
#pragma unroll
    for (int r = 0; r < 16; ++r) P.o[r] *= f;
    P.o = MFMA32(vp1, asbf(u32x4{0u, 0u, cvtpk(sp[12], sp[13]), cvtpk(sp[14], sp[15])}), P.o);
    P.m = m; P.l = l;
}
DI void local_fin(LocalPart& P) {
    const float inv = __builtin_amdgcn_rcpf(swap_add(P.l));
#pragma unroll
    for (int r = 0; r < 16; ++r) P.o[r] *= inv;
    const u32x4 o0 = pack8(P.o, 0), o1 = pack8(P.o, 1);
#pragma unroll
    for (int r = 0; r < 4; ++r) { P.o[r] = __uint_as_float(o0[r]); P.o[4 + r] = __uint_as_float(o1[r]); }
}
DI u32x4 quad(const f32x16& a, int i) { return __builtin_bit_cast(u32x4, f32x4{a[4 * i], a[4 * i + 1], a[4 * i + 2], a[4 * i + 3]}); }

#define ATT_L6C \
    "s_nop 1\n\t" \
    "s_cmp_eq_u32 %[hl], 0\n\t" \
    "s_cbranch_scc1 10f\n\t" \
    "global_load_dwordx4 v[144:147], v143, %[ws] offset:-3072 sc1\n\t" \
    "global_load_dwordx4 v[148:151], v143, %[ws] offset:-2048 sc1\n\t" \
    "global_load_dwordx4 v[152:155], v143, %[ws] offset:-1024 sc1\n\t" \
    "global_load_dwordx4 v[156:159], v143, %[ws] offset:0 sc1\n\t" \
    "global_load_dwordx4 v[160:163], v143, %[ws] offset:1024 sc1\n\t" \
    "global_load_dwordx4 v[164:167], v143, %[ws] offset:2048 sc1\n\t" \
    "10:\n" \
    "s_setprio 3\n\t" \
    "s_waitcnt lgkmcnt(0)\n\t" \
    "v_mfma_f32_32x32x16_bf16 v[48:63], v[80:83], v[64:67], v[16:31]\n\t" \
    "v_exp_f32_e32 v32, v32\n\t" \
    "v_exp_f32_e32 v33, v33\n\t" \
    "v_exp_f32_e32 v34, v34\n\t" \
    "v_exp_f32_e32 v35, v35\n\t" \
    "v_exp_f32_e32 v36, v36\n\t" \
    "v_mfma_f32_32x32x16_bf16 v[48:63], v[84:87], v[68:71], v[48:63]\n\t" \
    "ds_read_b128 v[80:83], v112 offset:8192\n\t" \
    "ds_read_b128 v[84:87], v112 offset:9216\n\t" \
    "v_cvt_pk_bf16_f32 v104, v32, v33\n\t" \
    "v_cvt_pk_bf16_f32 v105, v34, v35\n\t" \
    "v_exp_f32_e32 v37, v37\n\t" \
    "v_exp_f32_e32 v38, v38\n\t" \
    "v_exp_f32_e32 v39, v39\n\t" \
    "ds_read_b128 v[100:103], v112 offset:3072\n\t" \
    "ds_read_b128 v[96:99], v112 offset:6144\n\t" \
    "v_exp_f32_e32 v40, v40\n\t" \
    "v_cvt_pk_bf16_f32 v106, v36, v37\n\t" \
    "v_cvt_pk_bf16_f32 v107, v38, v39\n\t" \
    "v_exp_f32_e32 v41, v41\n\t" \
    "v_exp_f32_e32 v42, v42\n\t" \
    "v_exp_f32_e32 v43, v43\n\t" \
    "v_mfma_f32_32x32x16_bf16 v[0:15], v[88:91], v[104:107], 0\n\t" \
    "v_exp_f32_e32 v44, v44\n\t" \
    "v_exp_f32_e32 v45, v45\n\t" \
    "v_exp_f32_e32 v46, v46\n\t" \
    "v_exp_f32_e32 v47, v47\n\t" \
    "v_cvt_pk_bf16_f32 v108, v40, v41\n\t" \
    "v_cvt_pk_bf16_f32 v109, v42, v43\n\t" \
    "v_cvt_pk_bf16_f32 v110, v44, v45\n\t" \
    "v_cvt_pk_bf16_f32 v111, v46, v47\n\t" \
    "s_mov_b32 m0, %[fld]\n\t" \
    "s_nop 0\n\t" \
    "global_load_lds_dwordx4 v142, %[ws] sc1\n\t" \
    "s_waitcnt lgkmcnt(0)\n\t" \
    "v_mfma_f32_32x32x16_bf16 v[32:47], v[80:83], v[64:67], v[16:31]\n\t" \
    "v_exp_f32_e32 v48, v48\n\t" \
    "v_exp_f32_e32 v49, v49\n\t" \
    "v_exp_f32_e32 v50, v50\n\t" \
    "v_smfmac_f32_16x16x64_bf16 v[76:79], v[72:75], v[104:111], v174\n\t" \
    "v_exp_f32_e32 v51, v51\n\t" \
    "v_exp_f32_e32 v52, v52\n\t" \
    "v_mfma_f32_32x32x16_bf16 v[32:47], v[84:87], v[68:71], v[32:47]\n\t" \
    "ds_read_b128 v[80:83], v112 offset:12288\n\t" \
    "ds_read_b128 v[84:87], v112 offset:13312\n\t" \
    "v_cvt_pk_bf16_f32 v104, v48, v49\n\t" \
    "v_cvt_pk_bf16_f32 v105, v50, v51\n\t" \
    "v_exp_f32_e32 v53, v53\n\t" \
    "v_exp_f32_e32 v54, v54\n\t" \
    "v_exp_f32_e32 v55, v55\n\t" \
    "v_mfma_f32_32x32x16_bf16 v[0:15], v[100:103], v[108:111], v[0:15]\n\t" \
    "ds_read_b128 v[92:95], v112 offset:7168\n\t" \
    "ds_read_b128 v[88:91], v112 offset:10240\n\t" \
    "v_exp_f32_e32 v56, v56\n\t" \
    "v_cvt_pk_bf16_f32 v106, v52, v53\n\t" \
    "v_cvt_pk_bf16_f32 v107, v54, v55\n\t" \
    "v_exp_f32_e32 v57, v57\n\t" \
    "v_exp_f32_e32 v58, v58\n\t" \
    "v_exp_f32_e32 v59, v59\n\t" \
    "v_mfma_f32_32x32x16_bf16 v[0:15], v[96:99], v[104:107], v[0:15]\n\t" \
    "v_exp_f32_e32 v60, v60\n\t" \
    "v_exp_f32_e32 v61, v61\n\t" \
    "v_exp_f32_e32 v62, v62\n\t" \
    "v_exp_f32_e32 v63, v63\n\t" \
    "v_cvt_pk_bf16_f32 v108, v56, v57\n\t" \
    "v_cvt_pk_bf16_f32 v109, v58, v59\n\t" \
    "v_cvt_pk_bf16_f32 v110, v60, v61\n\t" \
    "v_cvt_pk_bf16_f32 v111, v62, v63\n\t" \
    "s_setprio 2\n\t" \
    "s_waitcnt lgkmcnt(0)\n\t" \
    "v_mfma_f32_32x32x16_bf16 v[48:63], v[80:83], v[64:67], v[16:31]\n\t" \
    "v_exp_f32_e32 v32, v32\n\t" \
    "v_exp_f32_e32 v33, v33\n\t" \
    "v_exp_f32_e32 v34, v34\n\t" \
    "v_smfmac_f32_16x16x64_bf16 v[76:79], v[72:75], v[104:111], v174\n\t" \
    "v_exp_f32_e32 v35, v35\n\t" \
    "v_exp_f32_e32 v36, v36\n\t" \
    "v_mfma_f32_32x32x16_bf16 v[48:63], v[84:87], v[68:71], v[48:63]\n\t" \
    "ds_read_b128 v[80:83], v112 offset:16384\n\t" \
    "ds_read_b128 v[84:87], v112 offset:17408\n\t" \
    "v_cvt_pk_bf16_f32 v104, v32, v33\n\t" \
    "v_cvt_pk_bf16_f32 v105, v34, v35\n\t" \
    "v_exp_f32_e32 v37, v37\n\t" \
    "v_exp_f32_e32 v38, v38\n\t" \
    "v_exp_f32_e32 v39, v39\n\t" \
    "v_mfma_f32_32x32x16_bf16 v[0:15], v[92:95], v[108:111], v[0:15]\n\t" \
    "ds_read_b128 v[100:103], v112 offset:11264\n\t" \
    "ds_read_b128 v[96:99], v112 offset:14336\n\t" \
    "v_exp_f32_e32 v40, v40\n\t" \
    "v_cvt_pk_bf16_f32 v106, v36, v37\n\t" \
    "v_cvt_pk_bf16_f32 v107, v38, v39\n\t" \
    "v_exp_f32_e32 v41, v41\n\t" \
    "v_exp_f32_e32 v42, v42\n\t" \
    "v_exp_f32_e32 v43, v43\n\t" \
    "v_mfma_f32_32x32x16_bf16 v[0:15], v[88:91], v[104:107], v[0:15]\n\t" \
    "v_exp_f32_e32 v44, v44\n\t" \
    "v_exp_f32_e32 v45, v45\n\t" \
    "v_exp_f32_e32 v46, v46\n\t" \
    "v_exp_f32_e32 v47, v47\n\t" \
    "v_cvt_pk_bf16_f32 v108, v40, v41\n\t" \
    "v_cvt_pk_bf16_f32 v109, v42, v43\n\t" \
    "v_cvt_pk_bf16_f32 v110, v44, v45\n\t" \
    "v_cvt_pk_bf16_f32 v111, v46, v47\n\t" \
    "s_waitcnt lgkmcnt(0)\n\t" \
    "v_mfma_f32_32x32x16_bf16 v[32:47], v[80:83], v[64:67], v[16:31]\n\t" \
    "v_exp_f32_e32 v48, v48\n\t" \
    "v_exp_f32_e32 v49, v49\n\t" \
    "v_exp_f32_e32 v50, v50\n\t" \
    "v_smfmac_f32_16x16x64_bf16 v[76:79], v[72:75], v[104:111], v174\n\t" \
    "v_exp_f32_e32 v51, v51\n\t" \
    "v_exp_f32_e32 v52, v52\n\t" \
    "v_mfma_f32_32x32x16_bf16 v[32:47], v[84:87], v[68:71], v[32:47]\n\t" \
    "ds_read_b128 v[80:83], v112 offset:20480\n\t" \
    "ds_read_b128 v[84:87], v112 offset:21504\n\t" \
    "v_cvt_pk_bf16_f32 v104, v48, v49\n\t" \
    "v_cvt_pk_bf16_f32 v105, v50, v51\n\t" \
    "v_exp_f32_e32 v53, v53\n\t" \
    "v_exp_f32_e32 v54, v54\n\t" \
    "v_exp_f32_e32 v55, v55\n\t" \
    "v_mfma_f32_32x32x16_bf16 v[0:15], v[100:103], v[108:111], v[0:15]\n\t" \
    "ds_read_b128 v[92:95], v112 offset:15360\n\t" \
    "ds_read_b128 v[88:91], v112 offset:18432\n\t" \
    "v_exp_f32_e32 v56, v56\n\t" \
    "v_cvt_pk_bf16_f32 v106, v52, v53\n\t" \
    "v_cvt_pk_bf16_f32 v107, v54, v55\n\t" \
    "v_exp_f32_e32 v57, v57\n\t" \
    "v_exp_f32_e32 v58, v58\n\t" \
    "v_exp_f32_e32 v59, v59\n\t" \
    "v_mfma_f32_32x32x16_bf16 v[0:15], v[96:99], v[104:107], v[0:15]\n\t" \
    "v_exp_f32_e32 v60, v60\n\t" \
    "v_exp_f32_e32 v61, v61\n\t" \
    "v_exp_f32_e32 v62, v62\n\t" \
    "v_exp_f32_e32 v63, v63\n\t" \
    "v_cvt_pk_bf16_f32 v108, v56, v57\n\t" \
    "v_cvt_pk_bf16_f32 v109, v58, v59\n\t" \
    "v_cvt_pk_bf16_f32 v110, v60, v61\n\t" \
    "v_cvt_pk_bf16_f32 v111, v62, v63\n\t" \
    "s_setprio 1\n\t" \
    "s_waitcnt lgkmcnt(0)\n\t" \
    "v_mfma_f32_32x32x16_bf16 v[48:63], v[80:83], v[64:67], v[16:31]\n\t" \
    "v_exp_f32_e32 v32, v32\n\t" \
    "v_exp_f32_e32 v33, v33\n\t" \
    "v_exp_f32_e32 v34, v34\n\t" \
    "v_smfmac_f32_16x16x64_bf16 v[76:79], v[72:75], v[104:111], v174\n\t" \
    "v_exp_f32_e32 v35, v35\n\t" \
    "v_exp_f32_e32 v36, v36\n\t" \
    "v_mfma_f32_32x32x16_bf16 v[48:63], v[84:87], v[68:71], v[48:63]\n\t" \
    "ds_read_b128 v[80:83], v112 offset:24576\n\t" \
    "ds_read_b128 v[84:87], v112 offset:25600\n\t" \
    "v_cvt_pk_bf16_f32 v104, v32, v33\n\t" \
    "v_cvt_pk_bf16_f32 v105, v34, v35\n\t" \
    "v_exp_f32_e32 v37, v37\n\t" \
    "v_exp_f32_e32 v38, v38\n\t" \
    "v_exp_f32_e32 v39, v39\n\t" \
    "v_mfma_f32_32x32x16_bf16 v[0:15], v[92:95], v[108:111], v[0:15]\n\t" \
    "ds_read_b128 v[100:103], v112 offset:19456\n\t" \
    "ds_read_b128 v[96:99], v112 offset:22528\n\t" \
    "v_exp_f32_e32 v40, v40\n\t" \
    "v_cvt_pk_bf16_f32 v106, v36, v37\n\t" \
    "v_cvt_pk_bf16_f32 v107, v38, v39\n\t" \
    "v_exp_f32_e32 v41, v41\n\t" \
    "v_exp_f32_e32 v42, v42\n\t" \
    "v_exp_f32_e32 v43, v43\n\t" \
    "v_mfma_f32_32x32x16_bf16 v[0:15], v[88:91], v[104:107], v[0:15]\n\t" \
    "v_exp_f32_e32 v44, v44\n\t" \
    "v_exp_f32_e32 v45, v45\n\t" \
    "v_exp_f32_e32 v46, v46\n\t" \
    "v_exp_f32_e32 v47, v47\n\t" \
    "v_cvt_pk_bf16_f32 v108, v40, v41\n\t" \
    "v_cvt_pk_bf16_f32 v109, v42, v43\n\t" \
    "v_cvt_pk_bf16_f32 v110, v44, v45\n\t" \
    "v_cvt_pk_bf16_f32 v111, v46, v47\n\t" \
    "s_waitcnt lgkmcnt(0)\n\t" \
    "v_mfma_f32_32x32x16_bf16 v[32:47], v[80:83], v[64:67], v[16:31]\n\t" \
    "v_exp_f32_e32 v48, v48\n\t" \
    "v_exp_f32_e32 v49, v49\n\t" \
    "v_exp_f32_e32 v50, v50\n\t" \
    "v_smfmac_f32_16x16x64_bf16 v[76:79], v[72:75], v[104:111], v174\n\t" \
    "v_exp_f32_e32 v51, v51\n\t" \
    "v_exp_f32_e32 v52, v52\n\t" \
    "v_mfma_f32_32x32x16_bf16 v[32:47], v[84:87], v[68:71], v[32:47]\n\t" \
    "ds_read_b128 v[80:83], v112 offset:28672\n\t" \
    "ds_read_b128 v[84:87], v112 offset:29696\n\t" \
    "v_cvt_pk_bf16_f32 v104, v48, v49\n\t" \
    "v_cvt_pk_bf16_f32 v105, v50, v51\n\t" \
    "v_exp_f32_e32 v53, v53\n\t" \
    "v_exp_f32_e32 v54, v54\n\t" \
    "v_exp_f32_e32 v55, v55\n\t" \
    "v_mfma_f32_32x32x16_bf16 v[0:15], v[100:103], v[108:111], v[0:15]\n\t" \
    "ds_read_b128 v[92:95], v112 offset:23552\n\t" \
    "ds_read_b128 v[88:91], v112 offset:26624\n\t" \
    "v_exp_f32_e32 v56, v56\n\t" \
    "v_cvt_pk_bf16_f32 v106, v52, v53\n\t" \
    "v_cvt_pk_bf16_f32 v107, v54, v55\n\t" \
    "v_exp_f32_e32 v57, v57\n\t" \
    "v_exp_f32_e32 v58, v58\n\t" \
    "v_exp_f32_e32 v59, v59\n\t" \
    "v_mfma_f32_32x32x16_bf16 v[0:15], v[96:99], v[104:107], v[0:15]\n\t" \
    "v_exp_f32_e32 v60, v60\n\t" \
    "v_exp_f32_e32 v61, v61\n\t" \
    "v_exp_f32_e32 v62, v62\n\t" \
    "v_exp_f32_e32 v63, v63\n\t" \
    "v_cvt_pk_bf16_f32 v108, v56, v57\n\t" \
    "v_cvt_pk_bf16_f32 v109, v58, v59\n\t" \
    "v_cvt_pk_bf16_f32 v110, v60, v61\n\t" \
    "v_cvt_pk_bf16_f32 v111, v62, v63\n\t" \
    "s_setprio 0\n\t" \
    "s_cmp_eq_u32 %[hl], 0\n\t" \
    "s_cbranch_scc1 11f\n\t" \
    "s_waitcnt vmcnt(1)\n\t" \
    "11:\n" \
    "s_waitcnt lgkmcnt(0)\n\t" \
    "s_nop 15\n\t"
#define ATT_L4 \
    "s_nop 1\n\t" \
    "s_setprio 3\n\t" \
    "s_waitcnt lgkmcnt(0)\n\t" \
    "v_mfma_f32_32x32x16_bf16 v[48:63], v[80:83], v[64:67], v[16:31]\n\t" \
    "v_exp_f32_e32 v32, v32\n\t" \
    "v_exp_f32_e32 v33, v33\n\t" \
    "v_exp_f32_e32 v34, v34\n\t" \
    "v_exp_f32_e32 v35, v35\n\t" \
    "v_exp_f32_e32 v36, v36\n\t" \
    "v_mfma_f32_32x32x16_bf16 v[48:63], v[84:87], v[68:71], v[48:63]\n\t" \
    "ds_read_b128 v[80:83], v112 offset:8192\n\t" \
    "ds_read_b128 v[84:87], v112 offset:9216\n\t" \
    "v_cvt_pk_bf16_f32 v104, v32, v33\n\t" \
    "v_cvt_pk_bf16_f32 v105, v34, v35\n\t" \
    "v_exp_f32_e32 v37, v37\n\t" \
    "v_exp_f32_e32 v38, v38\n\t" \
    "v_exp_f32_e32 v39, v39\n\t" \
    "ds_read_b128 v[100:103], v112 offset:3072\n\t" \
    "ds_read_b128 v[96:99], v112 offset:6144\n\t" \
    "v_exp_f32_e32 v40, v40\n\t" \
    "v_cvt_pk_bf16_f32 v106, v36, v37\n\t" \
    "v_cvt_pk_bf16_f32 v107, v38, v39\n\t" \
    "v_exp_f32_e32 v41, v41\n\t" \
    "v_exp_f32_e32 v42, v42\n\t" \
    "v_exp_f32_e32 v43, v43\n\t" \
    "v_mfma_f32_32x32x16_bf16 v[0:15], v[88:91], v[104:107], 0\n\t" \
    "v_exp_f32_e32 v44, v44\n\t" \
    "v_exp_f32_e32 v45, v45\n\t" \
    "v_exp_f32_e32 v46, v46\n\t" \
    "v_exp_f32_e32 v47, v47\n\t" \
    "v_cvt_pk_bf16_f32 v108, v40, v41\n\t" \
    "v_cvt_pk_bf16_f32 v109, v42, v43\n\t" \
    "v_cvt_pk_bf16_f32 v110, v44, v45\n\t" \
    "v_cvt_pk_bf16_f32 v111, v46, v47\n\t" \
    "s_waitcnt lgkmcnt(0)\n\t" \
    "v_mfma_f32_32x32x16_bf16 v[32:47], v[80:83], v[64:67], v[16:31]\n\t" \
    "v_exp_f32_e32 v48, v48\n\t" \
    "v_exp_f32_e32 v49, v49\n\t" \
    "v_exp_f32_e32 v50, v50\n\t" \
    "v_smfmac_f32_16x16x64_bf16 v[76:79], v[72:75], v[104:111], v174\n\t" \
    "v_exp_f32_e32 v51, v51\n\t" \
    "v_exp_f32_e32 v52, v52\n\t" \
    "v_mfma_f32_32x32x16_bf16 v[32:47], v[84:87], v[68:71], v[32:47]\n\t" \
    "ds_read_b128 v[80:83], v112 offset:12288\n\t" \
    "ds_read_b128 v[84:87], v112 offset:13312\n\t" \
    "v_cvt_pk_bf16_f32 v104, v48, v49\n\t" \
    "v_cvt_pk_bf16_f32 v105, v50, v51\n\t" \
    "v_exp_f32_e32 v53, v53\n\t" \
    "v_exp_f32_e32 v54, v54\n\t" \
    "v_exp_f32_e32 v55, v55\n\t" \
    "v_mfma_f32_32x32x16_bf16 v[0:15], v[100:103], v[108:111], v[0:15]\n\t" \
    "ds_read_b128 v[92:95], v112 offset:7168\n\t" \
    "ds_read_b128 v[88:91], v112 offset:10240\n\t" \
    "v_exp_f32_e32 v56, v56\n\t" \
    "v_cvt_pk_bf16_f32 v106, v52, v53\n\t" \
    "v_cvt_pk_bf16_f32 v107, v54, v55\n\t" \
    "v_exp_f32_e32 v57, v57\n\t" \
    "v_exp_f32_e32 v58, v58\n\t" \
    "v_exp_f32_e32 v59, v59\n\t" \
    "v_mfma_f32_32x32x16_bf16 v[0:15], v[96:99], v[104:107], v[0:15]\n\t" \
    "v_exp_f32_e32 v60, v60\n\t" \
    "v_exp_f32_e32 v61, v61\n\t" \
    "v_exp_f32_e32 v62, v62\n\t" \
    "v_exp_f32_e32 v63, v63\n\t" \
    "v_cvt_pk_bf16_f32 v108, v56, v57\n\t" \
    "v_cvt_pk_bf16_f32 v109, v58, v59\n\t" \
    "v_cvt_pk_bf16_f32 v110, v60, v61\n\t" \
    "v_cvt_pk_bf16_f32 v111, v62, v63\n\t" \
    "s_setprio 2\n\t" \
    "s_waitcnt lgkmcnt(0)\n\t" \
    "v_mfma_f32_32x32x16_bf16 v[48:63], v[80:83], v[64:67], v[16:31]\n\t" \
    "v_exp_f32_e32 v32, v32\n\t" \
    "v_exp_f32_e32 v33, v33\n\t" \
    "v_exp_f32_e32 v34, v34\n\t" \
    "v_smfmac_f32_16x16x64_bf16 v[76:79], v[72:75], v[104:111], v174\n\t" \
    "v_exp_f32_e32 v35, v35\n\t" \
    "v_exp_f32_e32 v36, v36\n\t" \
    "v_mfma_f32_32x32x16_bf16 v[48:63], v[84:87], v[68:71], v[48:63]\n\t" \
    "ds_read_b128 v[80:83], v112 offset:16384\n\t" \
    "ds_read_b128 v[84:87], v112 offset:17408\n\t" \
    "v_cvt_pk_bf16_f32 v104, v32, v33\n\t" \
    "v_cvt_pk_bf16_f32 v105, v34, v35\n\t" \
    "v_exp_f32_e32 v37, v37\n\t" \
    "v_exp_f32_e32 v38, v38\n\t" \
    "v_exp_f32_e32 v39, v39\n\t" \
    "v_mfma_f32_32x32x16_bf16 v[0:15], v[92:95], v[108:111], v[0:15]\n\t" \
    "ds_read_b128 v[100:103], v112 offset:11264\n\t" \
    "ds_read_b128 v[96:99], v112 offset:14336\n\t" \
    "v_exp_f32_e32 v40, v40\n\t" \
    "v_cvt_pk_bf16_f32 v106, v36, v37\n\t" \
    "v_cvt_pk_bf16_f32 v107, v38, v39\n\t" \
    "v_exp_f32_e32 v41, v41\n\t" \
    "v_exp_f32_e32 v42, v42\n\t" \
    "v_exp_f32_e32 v43, v43\n\t" \
    "v_mfma_f32_32x32x16_bf16 v[0:15], v[88:91], v[104:107], v[0:15]\n\t" \
    "v_exp_f32_e32 v44, v44\n\t" \
    "v_exp_f32_e32 v45, v45\n\t" \
    "v_exp_f32_e32 v46, v46\n\t" \
    "v_exp_f32_e32 v47, v47\n\t" \
    "v_cvt_pk_bf16_f32 v108, v40, v41\n\t" \
    "v_cvt_pk_bf16_f32 v109, v42, v43\n\t" \
    "v_cvt_pk_bf16_f32 v110, v44, v45\n\t" \
    "v_cvt_pk_bf16_f32 v111, v46, v47\n\t" \
    "s_waitcnt lgkmcnt(0)\n\t" \
    "v_mfma_f32_32x32x16_bf16 v[32:47], v[80:83], v[64:67], v[16:31]\n\t" \
    "v_exp_f32_e32 v48, v48\n\t" \
    "v_exp_f32_e32 v49, v49\n\t" \
    "v_exp_f32_e32 v50, v50\n\t" \
    "v_smfmac_f32_16x16x64_bf16 v[76:79], v[72:75], v[104:111], v174\n\t" \
    "v_exp_f32_e32 v51, v51\n\t" \
    "v_exp_f32_e32 v52, v52\n\t" \
    "v_mfma_f32_32x32x16_bf16 v[32:47], v[84:87], v[68:71], v[32:47]\n\t" \
    "ds_read_b128 v[80:83], v112 offset:20480\n\t" \
    "ds_read_b128 v[84:87], v112 offset:21504\n\t" \
    "v_cvt_pk_bf16_f32 v104, v48, v49\n\t" \
    "v_cvt_pk_bf16_f32 v105, v50, v51\n\t" \
    "v_exp_f32_e32 v53, v53\n\t" \
    "v_exp_f32_e32 v54, v54\n\t" \
    "v_exp_f32_e32 v55, v55\n\t" \
    "v_mfma_f32_32x32x16_bf16 v[0:15], v[100:103], v[108:111], v[0:15]\n\t" \
    "ds_read_b128 v[92:95], v112 offset:15360\n\t" \
    "ds_read_b128 v[88:91], v112 offset:18432\n\t" \
    "v_exp_f32_e32 v56, v56\n\t" \
    "v_cvt_pk_bf16_f32 v106, v52, v53\n\t" \
    "v_cvt_pk_bf16_f32 v107, v54, v55\n\t" \
    "v_exp_f32_e32 v57, v57\n\t" \
    "v_exp_f32_e32 v58, v58\n\t" \
    "v_exp_f32_e32 v59, v59\n\t" \
    "v_mfma_f32_32x32x16_bf16 v[0:15], v[96:99], v[104:107], v[0:15]\n\t" \
    "v_exp_f32_e32 v60, v60\n\t" \
    "v_exp_f32_e32 v61, v61\n\t" \
    "v_exp_f32_e32 v62, v62\n\t" \
    "v_exp_f32_e32 v63, v63\n\t" \
    "v_cvt_pk_bf16_f32 v108, v56, v57\n\t" \
    "v_cvt_pk_bf16_f32 v109, v58, v59\n\t" \
    "v_cvt_pk_bf16_f32 v110, v60, v61\n\t" \
    "v_cvt_pk_bf16_f32 v111, v62, v63\n\t" \
    "s_setprio 0\n\t" \
    "s_waitcnt lgkmcnt(0)\n\t" \
    "s_nop 15\n\t"
#define ATT_L2 \
    "s_nop 1\n\t" \
    "s_setprio 1\n\t" \
    "s_waitcnt lgkmcnt(0)\n\t" \
    "v_mfma_f32_32x32x16_bf16 v[48:63], v[80:83], v[64:67], v[16:31]\n\t" \
    "v_exp_f32_e32 v32, v32\n\t" \
    "v_exp_f32_e32 v33, v33\n\t" \
    "v_exp_f32_e32 v34, v34\n\t" \
    "v_smfmac_f32_16x16x64_bf16 v[76:79], v[72:75], v[104:111], v174\n\t" \
    "v_exp_f32_e32 v35, v35\n\t" \
    "v_exp_f32_e32 v36, v36\n\t" \
    "v_mfma_f32_32x32x16_bf16 v[48:63], v[84:87], v[68:71], v[48:63]\n\t" \
    "ds_read_b128 v[80:83], v112 offset:24576\n\t" \
    "ds_read_b128 v[84:87], v112 offset:25600\n\t" \
    "v_cvt_pk_bf16_f32 v104, v32, v33\n\t" \
    "v_cvt_pk_bf16_f32 v105, v34, v35\n\t" \
    "v_exp_f32_e32 v37, v37\n\t" \
    "v_exp_f32_e32 v38, v38\n\t" \
    "v_exp_f32_e32 v39, v39\n\t" \
    "v_mfma_f32_32x32x16_bf16 v[0:15], v[92:95], v[108:111], v[0:15]\n\t" \
    "ds_read_b128 v[100:103], v112 offset:19456\n\t" \
    "ds_read_b128 v[96:99], v112 offset:22528\n\t" \
    "v_exp_f32_e32 v40, v40\n\t" \
    "v_cvt_pk_bf16_f32 v106, v36, v37\n\t" \
    "v_cvt_pk_bf16_f32 v107, v38, v39\n\t" \
    "v_exp_f32_e32 v41, v41\n\t" \
    "v_exp_f32_e32 v42, v42\n\t" \
    "v_exp_f32_e32 v43, v43\n\t" \
    "v_mfma_f32_32x32x16_bf16 v[0:15], v[88:91], v[104:107], v[0:15]\n\t" \
    "v_exp_f32_e32 v44, v44\n\t" \
    "v_exp_f32_e32 v45, v45\n\t" \
    "v_exp_f32_e32 v46, v46\n\t" \
    "v_exp_f32_e32 v47, v47\n\t" \
    "v_cvt_pk_bf16_f32 v108, v40, v41\n\t" \
    "v_cvt_pk_bf16_f32 v109, v42, v43\n\t" \
    "v_cvt_pk_bf16_f32 v110, v44, v45\n\t" \
    "v_cvt_pk_bf16_f32 v111, v46, v47\n\t" \
    "s_waitcnt lgkmcnt(0)\n\t" \
    "v_mfma_f32_32x32x16_bf16 v[32:47], v[80:83], v[64:67], v[16:31]\n\t" \
    "v_exp_f32_e32 v48, v48\n\t" \
    "v_exp_f32_e32 v49, v49\n\t" \
    "v_exp_f32_e32 v50, v50\n\t" \
    "v_smfmac_f32_16x16x64_bf16 v[76:79], v[72:75], v[104:111], v174\n\t" \
    "v_exp_f32_e32 v51, v51\n\t" \
    "v_exp_f32_e32 v52, v52\n\t" \
    "v_mfma_f32_32x32x16_bf16 v[32:47], v[84:87], v[68:71], v[32:47]\n\t" \
    "ds_read_b128 v[80:83], v112 offset:28672\n\t" \
    "ds_read_b128 v[84:87], v112 offset:29696\n\t" \
    "v_cvt_pk_bf16_f32 v104, v48, v49\n\t" \
    "v_cvt_pk_bf16_f32 v105, v50, v51\n\t" \
    "v_exp_f32_e32 v53, v53\n\t" \
    "v_exp_f32_e32 v54, v54\n\t" \
    "v_exp_f32_e32 v55, v55\n\t" \
    "v_mfma_f32_32x32x16_bf16 v[0:15], v[100:103], v[108:111], v[0:15]\n\t" \
    "ds_read_b128 v[92:95], v112 offset:23552\n\t" \
    "ds_read_b128 v[88:91], v112 offset:26624\n\t" \
    "v_exp_f32_e32 v56, v56\n\t" \
    "v_cvt_pk_bf16_f32 v106, v52, v53\n\t" \
    "v_cvt_pk_bf16_f32 v107, v54, v55\n\t" \
    "v_exp_f32_e32 v57, v57\n\t" \
    "v_exp_f32_e32 v58, v58\n\t" \
    "v_exp_f32_e32 v59, v59\n\t" \
    "v_mfma_f32_32x32x16_bf16 v[0:15], v[96:99], v[104:107], v[0:15]\n\t" \
    "v_exp_f32_e32 v60, v60\n\t" \
    "v_exp_f32_e32 v61, v61\n\t" \
    "v_exp_f32_e32 v62, v62\n\t" \
    "v_exp_f32_e32 v63, v63\n\t" \
    "v_cvt_pk_bf16_f32 v108, v56, v57\n\t" \
    "v_cvt_pk_bf16_f32 v109, v58, v59\n\t" \
    "v_cvt_pk_bf16_f32 v110, v60, v61\n\t" \
    "v_cvt_pk_bf16_f32 v111, v62, v63\n\t" \
    "s_setprio 0\n\t" \
    "s_waitcnt lgkmcnt(0)\n\t" \
    "s_nop 15\n\t"
#define ATT_XT \
    "s_nop 1\n\t" \
    "s_waitcnt lgkmcnt(0)\n\t" \
    "v_mfma_f32_32x32x16_bf16 v[48:63], v[80:83], v[64:67], v[16:31]\n\t" \
    "v_exp_f32_e32 v32, v32\n\t" \
    "v_exp_f32_e32 v33, v33\n\t" \
    "v_exp_f32_e32 v34, v34\n\t" \
    "v_smfmac_f32_16x16x64_bf16 v[76:79], v[72:75], v[104:111], v174\n\t" \
    "v_exp_f32_e32 v35, v35\n\t" \
    "v_exp_f32_e32 v36, v36\n\t" \
    "v_mfma_f32_32x32x16_bf16 v[48:63], v[84:87], v[68:71], v[48:63]\n\t" \
    "v_cvt_pk_bf16_f32 v104, v32, v33\n\t" \
    "v_cvt_pk_bf16_f32 v105, v34, v35\n\t" \
    "v_exp_f32_e32 v37, v37\n\t" \
    "v_exp_f32_e32 v38, v38\n\t" \
    "v_exp_f32_e32 v39, v39\n\t" \
    "v_mfma_f32_32x32x16_bf16 v[0:15], v[92:95], v[108:111], v[0:15]\n\t" \
    "ds_read_b128 v[100:103], v112 offset:27648\n\t" \
    "ds_read_b128 v[96:99], v112 offset:30720\n\t" \
    "v_exp_f32_e32 v40, v40\n\t" \
    "v_cvt_pk_bf16_f32 v106, v36, v37\n\t" \
    "v_cvt_pk_bf16_f32 v107, v38, v39\n\t" \
    "v_exp_f32_e32 v41, v41\n\t" \
    "v_exp_f32_e32 v42, v42\n\t" \
    "v_exp_f32_e32 v43, v43\n\t" \
    "v_mfma_f32_32x32x16_bf16 v[0:15], v[88:91], v[104:107], v[0:15]\n\t" \
    "v_exp_f32_e32 v44, v44\n\t" \
    "v_exp_f32_e32 v45, v45\n\t" \
    "v_exp_f32_e32 v46, v46\n\t" \
    "v_exp_f32_e32 v47, v47\n\t" \
    "v_cvt_pk_bf16_f32 v108, v40, v41\n\t" \
    "v_cvt_pk_bf16_f32 v109, v42, v43\n\t" \
    "v_cvt_pk_bf16_f32 v110, v44, v45\n\t" \
    "v_cvt_pk_bf16_f32 v111, v46, v47\n\t" \
    "s_waitcnt lgkmcnt(0)\n\t" \
    "v_exp_f32_e32 v48, v48\n\t" \
    "v_exp_f32_e32 v49, v49\n\t" \
    "v_exp_f32_e32 v50, v50\n\t" \
    "v_smfmac_f32_16x16x64_bf16 v[76:79], v[72:75], v[104:111], v174\n\t" \
    "v_exp_f32_e32 v51, v51\n\t" \
    "v_exp_f32_e32 v52, v52\n\t" \
    "v_cvt_pk_bf16_f32 v104, v48, v49\n\t" \
    "v_cvt_pk_bf16_f32 v105, v50, v51\n\t" \
    "v_exp_f32_e32 v53, v53\n\t" \
    "v_exp_f32_e32 v54, v54\n\t" \
    "v_exp_f32_e32 v55, v55\n\t" \
    "v_mfma_f32_32x32x16_bf16 v[0:15], v[100:103], v[108:111], v[0:15]\n\t" \
    "ds_read_b128 v[92:95], v112 offset:31744\n\t" \
    "v_exp_f32_e32 v56, v56\n\t" \
    "v_cvt_pk_bf16_f32 v106, v52, v53\n\t" \
    "v_cvt_pk_bf16_f32 v107, v54, v55\n\t" \
    "v_exp_f32_e32 v57, v57\n\t" \
    "v_exp_f32_e32 v58, v58\n\t" \
    "v_exp_f32_e32 v59, v59\n\t" \
    "v_mfma_f32_32x32x16_bf16 v[0:15], v[96:99], v[104:107], v[0:15]\n\t" \
    "v_exp_f32_e32 v60, v60\n\t" \
    "v_exp_f32_e32 v61, v61\n\t" \
    "v_exp_f32_e32 v62, v62\n\t" \
    "v_exp_f32_e32 v63, v63\n\t" \
    "v_cvt_pk_bf16_f32 v108, v56, v57\n\t" \
    "v_cvt_pk_bf16_f32 v109, v58, v59\n\t" \
    "v_cvt_pk_bf16_f32 v110, v60, v61\n\t" \
    "v_cvt_pk_bf16_f32 v111, v62, v63\n\t" \
    "s_waitcnt lgkmcnt(0)\n\t" \
    "s_nop 0\n\t" \
    "v_mfma_f32_32x32x16_bf16 v[0:15], v[92:95], v[108:111], v[0:15]\n\t" \
    "v_smfmac_f32_16x16x64_bf16 v[76:79], v[72:75], v[104:111], v174\n\t" \
    "s_nop 15\n\t"
#define ATT_MAIN \
    "s_mov_b64 s[48:49], %[tab]\n\t" \
    "s_mov_b32 s46, %[k0]\n\t" \
    "s_and_b32 s40, s48, 63\n\t" \
    "s_lshl_b32 s40, s40, 12\n\t" \
    "s_add_u32 s44, %[srclo], s40\n\t" \
    "s_addc_u32 s45, %[srchi], 0\n\t" \
    "s_lshr_b64 s[48:49], s[48:49], 6\n\t" \
    "s_add_u32 s46, s46, 1\n\t" \
    "s_mov_b64 vcc, -1\n\t" \
    "s_waitcnt vmcnt(0)\n\t" \
    "ds_read_b64 v[120:121], v173\n\t" \
    "s_waitcnt lgkmcnt(0)\n\t" \
    "1:\n" \
    "s_cmp_eq_u32 %[cnt], 4\n\t" \
    "s_cbranch_scc1 5f\n\t" \
    "s_waitcnt vmcnt(0)\n\t" \
    "5:\n" \
    "s_waitcnt vmcnt(9)\n\t" \
    "s_barrier\n\t" \
    "s_bitcmp1_b32 %[act], %[cnt]\n\t" \
    "s_cbranch_scc1 30f\n\t" \
    "2:\n" \
    "s_setprio 3\n\t" \
    "s_waitcnt lgkmcnt(0)\n\t" \
    "v_mfma_f32_32x32x16_bf16 v[48:63], v[80:83], v[64:67], v[16:31]\n\t" \
    "v_exp_f32_e32 v32, v32\n\t" \
    "v_exp_f32_e32 v33, v33\n\t" \
    "v_exp_f32_e32 v34, v34\n\t" \
    "v_smfmac_f32_16x16x64_bf16 v[76:79], v[72:75], v[104:111], v174\n\t" \
    "v_exp_f32_e32 v35, v35\n\t" \
    "v_exp_f32_e32 v36, v36\n\t" \
    "v_mfma_f32_32x32x16_bf16 v[48:63], v[84:87], v[68:71], v[48:63]\n\t" \
    "ds_read_b128 v[80:83], v113 offset:0\n\t" \
    "ds_read_b128 v[84:87], v113 offset:1024\n\t" \
    "v_cvt_pk_bf16_f32 v104, v32, v33\n\t" \
    "v_cvt_pk_bf16_f32 v105, v34, v35\n\t" \
    "v_exp_f32_e32 v37, v37\n\t" \
    "v_exp_f32_e32 v38, v38\n\t" \
    "v_exp_f32_e32 v39, v39\n\t" \
    "v_mfma_f32_32x32x16_bf16 v[0:15], v[92:95], v[108:111], v[0:15]\n\t" \
    "ds_read_b128 v[100:103], v112 offset:27648\n\t" \
    "ds_read_b128 v[96:99], v112 offset:30720\n\t" \
    "v_exp_f32_e32 v40, v40\n\t" \
    "v_cvt_pk_bf16_f32 v106, v36, v37\n\t" \
    "v_cvt_pk_bf16_f32 v107, v38, v39\n\t" \
    "v_exp_f32_e32 v41, v41\n\t" \
    "v_exp_f32_e32 v42, v42\n\t" \
    "v_exp_f32_e32 v43, v43\n\t" \
    "v_mfma_f32_32x32x16_bf16 v[0:15], v[88:91], v[104:107], v[0:15]\n\t" \
    "s_cbranch_vccz 20f\n\t" \
    "v_cmp_eq_u64_e64 s[42:43], v[120:121], v[140:141]\n\t" \
    "s_mov_b32 m0, %[dst]\n\t" \
    "s_nop 3\n\t" \
    "s_bitcmp1_b64 s[42:43], s46\n\t" \
    "s_cbranch_scc0 9f\n\t" \
    "12:\n" \
    "global_load_lds_dwordx4 v114, s[44:45] offset:0 sc1\n\t" \
    "20:\n" \
    "v_exp_f32_e32 v44, v44\n\t" \
    "v_exp_f32_e32 v45, v45\n\t" \
    "v_exp_f32_e32 v46, v46\n\t" \
    "v_exp_f32_e32 v47, v47\n\t" \
    "v_cvt_pk_bf16_f32 v108, v40, v41\n\t" \
    "v_cvt_pk_bf16_f32 v109, v42, v43\n\t" \
    "v_cvt_pk_bf16_f32 v110, v44, v45\n\t" \
    "v_cvt_pk_bf16_f32 v111, v46, v47\n\t" \
    "s_waitcnt lgkmcnt(0)\n\t" \
    "v_mfma_f32_32x32x16_bf16 v[32:47], v[80:83], v[64:67], v[16:31]\n\t" \
    "v_exp_f32_e32 v48, v48\n\t" \
    "v_exp_f32_e32 v49, v49\n\t" \
    "v_exp_f32_e32 v50, v50\n\t" \
    "v_smfmac_f32_16x16x64_bf16 v[76:79], v[72:75], v[104:111], v174\n\t" \
    "v_exp_f32_e32 v51, v51\n\t" \
    "v_exp_f32_e32 v52, v52\n\t" \
    "v_mfma_f32_32x32x16_bf16 v[32:47], v[84:87], v[68:71], v[32:47]\n\t" \
    "ds_read_b128 v[80:83], v113 offset:4096\n\t" \
    "ds_read_b128 v[84:87], v113 offset:5120\n\t" \
    "v_cvt_pk_bf16_f32 v104, v48, v49\n\t" \
    "v_cvt_pk_bf16_f32 v105, v50, v51\n\t" \
    "v_exp_f32_e32 v53, v53\n\t" \
    "v_exp_f32_e32 v54, v54\n\t" \
    "v_exp_f32_e32 v55, v55\n\t" \
    "v_mfma_f32_32x32x16_bf16 v[0:15], v[100:103], v[108:111], v[0:15]\n\t" \
    "ds_read_b128 v[92:95], v112 offset:31744\n\t" \
    "ds_read_b128 v[88:91], v113 offset:2048\n\t" \
    "v_exp_f32_e32 v56, v56\n\t" \
    "v_cvt_pk_bf16_f32 v106, v52, v53\n\t" \
    "v_cvt_pk_bf16_f32 v107, v54, v55\n\t" \
    "v_exp_f32_e32 v57, v57\n\t" \
    "v_exp_f32_e32 v58, v58\n\t" \
    "v_exp_f32_e32 v59, v59\n\t" \
    "v_mfma_f32_32x32x16_bf16 v[0:15], v[96:99], v[104:107], v[0:15]\n\t" \
    "s_cbranch_vccz 21f\n\t" \
    "global_load_lds_dwordx4 v114, s[44:45] offset:1024 sc1\n\t" \
    "21:\n" \
    "v_exp_f32_e32 v60, v60\n\t" \
    "v_exp_f32_e32 v61, v61\n\t" \
    "v_exp_f32_e32 v62, v62\n\t" \
    "v_exp_f32_e32 v63, v63\n\t" \
    "v_cvt_pk_bf16_f32 v108, v56, v57\n\t" \
    "v_cvt_pk_bf16_f32 v109, v58, v59\n\t" \
    "v_cvt_pk_bf16_f32 v110, v60, v61\n\t" \
    "v_cvt_pk_bf16_f32 v111, v62, v63\n\t" \
    "s_setprio 2\n\t" \
    "s_waitcnt lgkmcnt(0)\n\t" \
    "v_mfma_f32_32x32x16_bf16 v[48:63], v[80:83], v[64:67], v[16:31]\n\t" \
    "v_exp_f32_e32 v32, v32\n\t" \
    "v_exp_f32_e32 v33, v33\n\t" \
    "v_exp_f32_e32 v34, v34\n\t" \
    "v_smfmac_f32_16x16x64_bf16 v[76:79], v[72:75], v[104:111], v174\n\t" \
    "v_exp_f32_e32 v35, v35\n\t" \
    "v_exp_f32_e32 v36, v36\n\t" \
    "v_mfma_f32_32x32x16_bf16 v[48:63], v[84:87], v[68:71], v[48:63]\n\t" \
    "ds_read_b128 v[80:83], v113 offset:8192\n\t" \
    "ds_read_b128 v[84:87], v113 offset:9216\n\t" \
    "v_cvt_pk_bf16_f32 v104, v32, v33\n\t" \
    "v_cvt_pk_bf16_f32 v105, v34, v35\n\t" \
    "v_exp_f32_e32 v37, v37\n\t" \
    "v_exp_f32_e32 v38, v38\n\t" \
    "v_exp_f32_e32 v39, v39\n\t" \
    "v_mfma_f32_32x32x16_bf16 v[0:15], v[92:95], v[108:111], v[0:15]\n\t" \
    "ds_read_b128 v[100:103], v113 offset:3072\n\t" \
    "ds_read_b128 v[96:99], v113 offset:6144\n\t" \
    "v_exp_f32_e32 v40, v40\n\t" \
    "v_cvt_pk_bf16_f32 v106, v36, v37\n\t" \
    "v_cvt_pk_bf16_f32 v107, v38, v39\n\t" \
    "v_exp_f32_e32 v41, v41\n\t" \
    "v_exp_f32_e32 v42, v42\n\t" \
    "v_exp_f32_e32 v43, v43\n\t" \
    "v_mfma_f32_32x32x16_bf16 v[0:15], v[88:91], v[104:107], v[0:15]\n\t" \
    "s_cbranch_vccz 22f\n\t" \
    "global_load_lds_dwordx4 v114, s[44:45] offset:2048 sc1\n\t" \
    "22:\n" \
    "v_exp_f32_e32 v44, v44\n\t" \
    "v_exp_f32_e32 v45, v45\n\t" \
    "v_exp_f32_e32 v46, v46\n\t" \
    "v_exp_f32_e32 v47, v47\n\t" \
    "v_cvt_pk_bf16_f32 v108, v40, v41\n\t" \
    "v_cvt_pk_bf16_f32 v109, v42, v43\n\t" \
    "v_cvt_pk_bf16_f32 v110, v44, v45\n\t" \
    "v_cvt_pk_bf16_f32 v111, v46, v47\n\t" \
    "s_waitcnt lgkmcnt(0)\n\t" \
    "v_mfma_f32_32x32x16_bf16 v[32:47], v[80:83], v[64:67], v[16:31]\n\t" \
    "v_exp_f32_e32 v48, v48\n\t" \
    "v_exp_f32_e32 v49, v49\n\t" \
    "v_exp_f32_e32 v50, v50\n\t" \
    "v_smfmac_f32_16x16x64_bf16 v[76:79], v[72:75], v[104:111], v174\n\t" \
    "v_exp_f32_e32 v51, v51\n\t" \
    "v_exp_f32_e32 v52, v52\n\t" \
    "v_mfma_f32_32x32x16_bf16 v[32:47], v[84:87], v[68:71], v[32:47]\n\t" \
    "ds_read_b128 v[80:83], v113 offset:12288\n\t" \
    "ds_read_b128 v[84:87], v113 offset:13312\n\t" \
    "v_cvt_pk_bf16_f32 v104, v48, v49\n\t" \
    "v_cvt_pk_bf16_f32 v105, v50, v51\n\t" \
    "v_exp_f32_e32 v53, v53\n\t" \
    "v_exp_f32_e32 v54, v54\n\t" \
    "v_exp_f32_e32 v55, v55\n\t" \
    "v_mfma_f32_32x32x16_bf16 v[0:15], v[100:103], v[108:111], v[0:15]\n\t" \
    "ds_read_b128 v[92:95], v113 offset:7168\n\t" \
    "ds_read_b128 v[88:91], v113 offset:10240\n\t" \
    "v_exp_f32_e32 v56, v56\n\t" \
    "v_cvt_pk_bf16_f32 v106, v52, v53\n\t" \
    "v_cvt_pk_bf16_f32 v107, v54, v55\n\t" \
    "v_exp_f32_e32 v57, v57\n\t" \
    "v_exp_f32_e32 v58, v58\n\t" \
    "v_exp_f32_e32 v59, v59\n\t" \
    "v_mfma_f32_32x32x16_bf16 v[0:15], v[96:99], v[104:107], v[0:15]\n\t" \
    "s_cbranch_vccz 23f\n\t" \
    "global_load_lds_dwordx4 v114, s[44:45] offset:3072 sc1\n\t" \
    "global_load_dwordx2 v[120:121], v170, %[ws] sc1\n\t" \
    "23:\n" \
    "v_exp_f32_e32 v60, v60\n\t" \
    "v_exp_f32_e32 v61, v61\n\t" \
    "v_exp_f32_e32 v62, v62\n\t" \
    "v_exp_f32_e32 v63, v63\n\t" \
    "v_cvt_pk_bf16_f32 v108, v56, v57\n\t" \
    "v_cvt_pk_bf16_f32 v109, v58, v59\n\t" \
    "v_cvt_pk_bf16_f32 v110, v60, v61\n\t" \
    "v_cvt_pk_bf16_f32 v111, v62, v63\n\t" \
    "s_setprio 1\n\t" \
    "s_waitcnt lgkmcnt(0)\n\t" \
    "v_mfma_f32_32x32x16_bf16 v[48:63], v[80:83], v[64:67], v[16:31]\n\t" \
    "v_exp_f32_e32 v32, v32\n\t" \
    "v_exp_f32_e32 v33, v33\n\t" \
    "v_exp_f32_e32 v34, v34\n\t" \
    "v_smfmac_f32_16x16x64_bf16 v[76:79], v[72:75], v[104:111], v174\n\t" \
    "v_exp_f32_e32 v35, v35\n\t" \
    "v_exp_f32_e32 v36, v36\n\t" \
    "v_mfma_f32_32x32x16_bf16 v[48:63], v[84:87], v[68:71], v[48:63]\n\t" \
    "ds_read_b128 v[80:83], v113 offset:16384\n\t" \
    "ds_read_b128 v[84:87], v113 offset:17408\n\t" \
    "v_cvt_pk_bf16_f32 v104, v32, v33\n\t" \
    "v_cvt_pk_bf16_f32 v105, v34, v35\n\t" \
    "v_exp_f32_e32 v37, v37\n\t" \
    "v_exp_f32_e32 v38, v38\n\t" \
    "v_exp_f32_e32 v39, v39\n\t" \
    "v_mfma_f32_32x32x16_bf16 v[0:15], v[92:95], v[108:111], v[0:15]\n\t" \
    "ds_read_b128 v[100:103], v113 offset:11264\n\t" \
    "ds_read_b128 v[96:99], v113 offset:14336\n\t" \
    "v_exp_f32_e32 v40, v40\n\t" \
    "v_cvt_pk_bf16_f32 v106, v36, v37\n\t" \
    "v_cvt_pk_bf16_f32 v107, v38, v39\n\t" \
    "v_exp_f32_e32 v41, v41\n\t" \
    "v_exp_f32_e32 v42, v42\n\t" \
    "v_exp_f32_e32 v43, v43\n\t" \
    "v_mfma_f32_32x32x16_bf16 v[0:15], v[88:91], v[104:107], v[0:15]\n\t" \
    "s_add_u32 %[dst], %[dst], 0x8000\n\t" \
    "s_cmp_eq_u32 %[dst], %[dend]\n\t" \
    "s_cselect_b32 %[dst], %[dbeg], %[dst]\n\t" \
    "s_add_u32 %[nx], %[nx], 0x8000\n\t" \
    "s_cmp_eq_u32 %[nx], 0x18000\n\t" \
    "s_cselect_b32 %[nx], 0, %[nx]\n\t" \
    "s_bitcmp1_b32 %[hk], %[cnt]\n\t" \
    "s_cbranch_scc1 40f\n\t" \
    "24:\n" \
    "v_exp_f32_e32 v44, v44\n\t" \
    "v_exp_f32_e32 v45, v45\n\t" \
    "v_exp_f32_e32 v46, v46\n\t" \
    "v_exp_f32_e32 v47, v47\n\t" \
    "v_cvt_pk_bf16_f32 v108, v40, v41\n\t" \
    "v_cvt_pk_bf16_f32 v109, v42, v43\n\t" \
    "v_cvt_pk_bf16_f32 v110, v44, v45\n\t" \
    "v_cvt_pk_bf16_f32 v111, v46, v47\n\t" \
    "s_waitcnt lgkmcnt(0)\n\t" \
    "v_mfma_f32_32x32x16_bf16 v[32:47], v[80:83], v[64:67], v[16:31]\n\t" \
    "v_exp_f32_e32 v48, v48\n\t" \
    "v_exp_f32_e32 v49, v49\n\t" \
    "v_exp_f32_e32 v50, v50\n\t" \
    "v_smfmac_f32_16x16x64_bf16 v[76:79], v[72:75], v[104:111], v174\n\t" \
    "v_exp_f32_e32 v51, v51\n\t" \
    "v_exp_f32_e32 v52, v52\n\t" \
    "v_mfma_f32_32x32x16_bf16 v[32:47], v[84:87], v[68:71], v[32:47]\n\t" \
    "ds_read_b128 v[80:83], v113 offset:20480\n\t" \
    "ds_read_b128 v[84:87], v113 offset:21504\n\t" \
    "v_cvt_pk_bf16_f32 v104, v48, v49\n\t" \
    "v_cvt_pk_bf16_f32 v105, v50, v51\n\t" \
    "v_exp_f32_e32 v53, v53\n\t" \
    "v_exp_f32_e32 v54, v54\n\t" \
    "v_exp_f32_e32 v55, v55\n\t" \
    "v_mfma_f32_32x32x16_bf16 v[0:15], v[100:103], v[108:111], v[0:15]\n\t" \
    "ds_read_b128 v[92:95], v113 offset:15360\n\t" \
    "ds_read_b128 v[88:91], v113 offset:18432\n\t" \
    "v_exp_f32_e32 v56, v56\n\t" \
    "v_cvt_pk_bf16_f32 v106, v52, v53\n\t" \
    "v_cvt_pk_bf16_f32 v107, v54, v55\n\t" \
    "v_exp_f32_e32 v57, v57\n\t" \
    "v_exp_f32_e32 v58, v58\n\t" \
    "v_exp_f32_e32 v59, v59\n\t" \
    "v_mfma_f32_32x32x16_bf16 v[0:15], v[96:99], v[104:107], v[0:15]\n\t" \
    "s_bitcmp1_b32 %[hk], %[cnt]\n\t" \
    "s_cbranch_scc1 41f\n\t" \
    "25:\n" \
    "v_exp_f32_e32 v60, v60\n\t" \
    "v_exp_f32_e32 v61, v61\n\t" \
    "v_exp_f32_e32 v62, v62\n\t" \
    "v_exp_f32_e32 v63, v63\n\t" \
    "v_cvt_pk_bf16_f32 v108, v56, v57\n\t" \
    "v_cvt_pk_bf16_f32 v109, v58, v59\n\t" \
    "v_cvt_pk_bf16_f32 v110, v60, v61\n\t" \
    "v_cvt_pk_bf16_f32 v111, v62, v63\n\t" \
    "s_setprio 0\n\t" \
    "s_waitcnt lgkmcnt(0)\n\t" \
    "v_mfma_f32_32x32x16_bf16 v[48:63], v[80:83], v[64:67], v[16:31]\n\t" \
    "v_exp_f32_e32 v32, v32\n\t" \
    "v_exp_f32_e32 v33, v33\n\t" \
    "v_exp_f32_e32 v34, v34\n\t" \
    "v_smfmac_f32_16x16x64_bf16 v[76:79], v[72:75], v[104:111], v174\n\t" \
    "v_exp_f32_e32 v35, v35\n\t" \
    "v_exp_f32_e32 v36, v36\n\t" \
    "v_mfma_f32_32x32x16_bf16 v[48:63], v[84:87], v[68:71], v[48:63]\n\t" \
    "ds_read_b128 v[80:83], v113 offset:24576\n\t" \
    "ds_read_b128 v[84:87], v113 offset:25600\n\t" \
    "v_cvt_pk_bf16_f32 v104, v32, v33\n\t" \
    "v_cvt_pk_bf16_f32 v105, v34, v35\n\t" \
    "v_exp_f32_e32 v37, v37\n\t" \
    "v_exp_f32_e32 v38, v38\n\t" \
    "v_exp_f32_e32 v39, v39\n\t" \
    "v_mfma_f32_32x32x16_bf16 v[0:15], v[92:95], v[108:111], v[0:15]\n\t" \
    "ds_read_b128 v[100:103], v113 offset:19456\n\t" \
    "ds_read_b128 v[96:99], v113 offset:22528\n\t" \
    "v_exp_f32_e32 v40, v40\n\t" \
    "v_cvt_pk_bf16_f32 v106, v36, v37\n\t" \
    "v_cvt_pk_bf16_f32 v107, v38, v39\n\t" \
    "v_exp_f32_e32 v41, v41\n\t" \
    "v_exp_f32_e32 v42, v42\n\t" \
    "v_exp_f32_e32 v43, v43\n\t" \
    "v_mfma_f32_32x32x16_bf16 v[0:15], v[88:91], v[104:107], v[0:15]\n\t" \
    "s_and_b32 s40, s48, 63\n\t" \
    "s_lshl_b32 s40, s40, 12\n\t" \
    "s_add_u32 s44, %[srclo], s40\n\t" \
    "s_addc_u32 s45, %[srchi], 0\n\t" \
    "s_lshr_b64 s[48:49], s[48:49], 6\n\t" \
    "s_add_u32 s46, s46, 1\n\t" \
    "s_bitcmp1_b32 %[hk], %[cnt]\n\t" \
    "s_cbranch_scc1 42f\n\t" \
    "26:\n" \
    "v_exp_f32_e32 v44, v44\n\t" \
    "v_exp_f32_e32 v45, v45\n\t" \
    "v_exp_f32_e32 v46, v46\n\t" \
    "v_exp_f32_e32 v47, v47\n\t" \
    "v_cvt_pk_bf16_f32 v108, v40, v41\n\t" \
    "v_cvt_pk_bf16_f32 v109, v42, v43\n\t" \
    "v_cvt_pk_bf16_f32 v110, v44, v45\n\t" \
    "v_cvt_pk_bf16_f32 v111, v46, v47\n\t" \
    "s_waitcnt lgkmcnt(0)\n\t" \
    "v_mfma_f32_32x32x16_bf16 v[32:47], v[80:83], v[64:67], v[16:31]\n\t" \
    "v_exp_f32_e32 v48, v48\n\t" \
    "v_exp_f32_e32 v49, v49\n\t" \
    "v_exp_f32_e32 v50, v50\n\t" \
    "v_smfmac_f32_16x16x64_bf16 v[76:79], v[72:75], v[104:111], v174\n\t" \
    "v_exp_f32_e32 v51, v51\n\t" \
    "v_exp_f32_e32 v52, v52\n\t" \
    "v_mfma_f32_32x32x16_bf16 v[32:47], v[84:87], v[68:71], v[32:47]\n\t" \
    "ds_read_b128 v[80:83], v113 offset:28672\n\t" \
    "ds_read_b128 v[84:87], v113 offset:29696\n\t" \
    "v_cvt_pk_bf16_f32 v104, v48, v49\n\t" \
    "v_cvt_pk_bf16_f32 v105, v50, v51\n\t" \
    "v_exp_f32_e32 v53, v53\n\t" \
    "v_exp_f32_e32 v54, v54\n\t" \
    "v_exp_f32_e32 v55, v55\n\t" \
    "v_mfma_f32_32x32x16_bf16 v[0:15], v[100:103], v[108:111], v[0:15]\n\t" \
    "ds_read_b128 v[92:95], v113 offset:23552\n\t" \
    "ds_read_b128 v[88:91], v113 offset:26624\n\t" \
    "v_exp_f32_e32 v56, v56\n\t" \
    "v_cvt_pk_bf16_f32 v106, v52, v53\n\t" \
    "v_cvt_pk_bf16_f32 v107, v54, v55\n\t" \
    "v_exp_f32_e32 v57, v57\n\t" \
    "v_exp_f32_e32 v58, v58\n\t" \
    "v_exp_f32_e32 v59, v59\n\t" \
    "v_mfma_f32_32x32x16_bf16 v[0:15], v[96:99], v[104:107], v[0:15]\n\t" \
    "s_bitcmp1_b32 %[hk], %[cnt]\n\t" \
    "s_cbranch_scc1 43f\n\t" \
    "27:\n" \
    "v_mov_b32_e32 v112, v113\n\t" \
    "v_add_u32_e32 v113, %[nx], v115\n\t" \
    "s_sub_u32 %[cnt], %[cnt], 1\n\t" \
    "s_cmp_lg_u32 %[cnt], 0\n\t" \
    "v_exp_f32_e32 v60, v60\n\t" \
    "v_exp_f32_e32 v61, v61\n\t" \
    "v_exp_f32_e32 v62, v62\n\t" \
    "v_exp_f32_e32 v63, v63\n\t" \
    "v_cvt_pk_bf16_f32 v108, v56, v57\n\t" \
    "v_cvt_pk_bf16_f32 v109, v58, v59\n\t" \
    "v_cvt_pk_bf16_f32 v110, v60, v61\n\t" \
    "v_cvt_pk_bf16_f32 v111, v62, v63\n\t" \
    "s_cbranch_scc1 1b\n\t" \
    "7:\n" \
    "s_setprio 0\n\t" \
    "s_waitcnt lgkmcnt(0)\n\t" \
    "v_mfma_f32_32x32x16_bf16 v[48:63], v[80:83], v[64:67], v[16:31]\n\t" \
    "v_exp_f32_e32 v32, v32\n\t" \
    "v_exp_f32_e32 v33, v33\n\t" \
    "v_exp_f32_e32 v34, v34\n\t" \
    "v_smfmac_f32_16x16x64_bf16 v[76:79], v[72:75], v[104:111], v174\n\t" \
    "v_exp_f32_e32 v35, v35\n\t" \
    "v_exp_f32_e32 v36, v36\n\t" \
    "v_mfma_f32_32x32x16_bf16 v[48:63], v[84:87], v[68:71], v[48:63]\n\t" \
    "v_cvt_pk_bf16_f32 v104, v32, v33\n\t" \
    "v_cvt_pk_bf16_f32 v105, v34, v35\n\t" \
    "v_exp_f32_e32 v37, v37\n\t" \
    "v_exp_f32_e32 v38, v38\n\t" \
    "v_exp_f32_e32 v39, v39\n\t" \
    "v_mfma_f32_32x32x16_bf16 v[0:15], v[92:95], v[108:111], v[0:15]\n\t" \
    "ds_read_b128 v[100:103], v112 offset:27648\n\t" \
    "ds_read_b128 v[96:99], v112 offset:30720\n\t" \
    "v_exp_f32_e32 v40, v40\n\t" \
    "v_cvt_pk_bf16_f32 v106, v36, v37\n\t" \
    "v_cvt_pk_bf16_f32 v107, v38, v39\n\t" \
    "v_exp_f32_e32 v41, v41\n\t" \
    "v_exp_f32_e32 v42, v42\n\t" \
    "v_exp_f32_e32 v43, v43\n\t" \
    "v_mfma_f32_32x32x16_bf16 v[0:15], v[88:91], v[104:107], v[0:15]\n\t" \
    "v_exp_f32_e32 v44, v44\n\t" \
    "v_exp_f32_e32 v45, v45\n\t" \
    "v_exp_f32_e32 v46, v46\n\t" \
    "v_exp_f32_e32 v47, v47\n\t" \
    "v_cvt_pk_bf16_f32 v108, v40, v41\n\t" \
    "v_cvt_pk_bf16_f32 v109, v42, v43\n\t" \
    "v_cvt_pk_bf16_f32 v110, v44, v45\n\t" \
    "v_cvt_pk_bf16_f32 v111, v46, v47\n\t" \
    "s_waitcnt lgkmcnt(0)\n\t" \
    "v_exp_f32_e32 v48, v48\n\t" \
    "v_exp_f32_e32 v49, v49\n\t" \
    "v_exp_f32_e32 v50, v50\n\t" \
    "v_smfmac_f32_16x16x64_bf16 v[76:79], v[72:75], v[104:111], v174\n\t" \
    "v_exp_f32_e32 v51, v51\n\t" \
    "v_exp_f32_e32 v52, v52\n\t" \
    "v_cvt_pk_bf16_f32 v104, v48, v49\n\t" \
    "v_cvt_pk_bf16_f32 v105, v50, v51\n\t" \
    "v_exp_f32_e32 v53, v53\n\t" \
    "v_exp_f32_e32 v54, v54\n\t" \
    "v_exp_f32_e32 v55, v55\n\t" \
    "v_mfma_f32_32x32x16_bf16 v[0:15], v[100:103], v[108:111], v[0:15]\n\t" \
    "ds_read_b128 v[92:95], v112 offset:31744\n\t" \
    "v_exp_f32_e32 v56, v56\n\t" \
    "v_cvt_pk_bf16_f32 v106, v52, v53\n\t" \
    "v_cvt_pk_bf16_f32 v107, v54, v55\n\t" \
    "v_exp_f32_e32 v57, v57\n\t" \
    "v_exp_f32_e32 v58, v58\n\t" \
    "v_exp_f32_e32 v59, v59\n\t" \
    "v_mfma_f32_32x32x16_bf16 v[0:15], v[96:99], v[104:107], v[0:15]\n\t" \
    "v_exp_f32_e32 v60, v60\n\t" \
    "v_exp_f32_e32 v61, v61\n\t" \
    "v_exp_f32_e32 v62, v62\n\t" \
    "v_exp_f32_e32 v63, v63\n\t" \
    "v_cvt_pk_bf16_f32 v108, v56, v57\n\t" \
    "v_cvt_pk_bf16_f32 v109, v58, v59\n\t" \
    "v_cvt_pk_bf16_f32 v110, v60, v61\n\t" \
    "v_cvt_pk_bf16_f32 v111, v62, v63\n\t" \
    "s_waitcnt lgkmcnt(0)\n\t" \
    "s_nop 0\n\t" \
    "v_mfma_f32_32x32x16_bf16 v[0:15], v[92:95], v[108:111], v[0:15]\n\t" \
    "v_smfmac_f32_16x16x64_bf16 v[76:79], v[72:75], v[104:111], v174\n\t" \
    "s_nop 15\n\t" \
    "s_branch 99f\n\t" \
    "9:\n" \
    "s_mov_b32 s47, 0x40000\n\t" \
    "10:\n" \
    "s_sub_u32 s47, s47, 1\n\t" \
    "s_cbranch_scc1 12b\n\t" \
    "global_load_dwordx2 v[120:121], v170, %[ws] sc1\n\t" \
    "s_waitcnt vmcnt(0)\n\t" \
    "v_cmp_eq_u64_e64 s[42:43], v[120:121], v[140:141]\n\t" \
    "s_nop 4\n\t" \
    "s_bitcmp1_b64 s[42:43], s46\n\t" \
    "s_cbranch_scc1 12b\n\t" \
    "s_sleep 4\n\t" \
    "s_branch 10b\n\t" \
    "50:\n" \
    "global_store_dwordx4 v171, v[176:179], %[ws] offset:-3072 sc1\n\t" \
    "global_store_dwordx4 v171, v[180:183], %[ws] offset:-2944 sc1\n\t" \
    "global_store_dwordx4 v172, v[184:187], %[ws] offset:-1024 sc1\n\t" \
    "global_store_dwordx4 v172, v[188:191], %[ws] offset:-768 sc1\n\t" \
    "s_branch 24b\n\t" \
    "51:\n" \
    "global_store_dwordx4 v171, v[192:195], %[ws] offset:1024 sc1\n\t" \
    "global_store_dwordx4 v171, v[196:199], %[ws] offset:1152 sc1\n\t" \
    "global_store_dwordx4 v172, v[200:203], %[ws] offset:3072 sc1\n\t" \
    "global_store_dwordx4 v172, v[204:207], %[ws] offset:3328 sc1\n\t" \
    "s_branch 25b\n\t" \
    "40:\n" \
    "s_cmp_eq_u32 %[cnt], 7\n\t" \
    "s_cbranch_scc1 50b\n\t" \
    "s_add_u32 m0, %[wdst], 0\n\t" \
    "s_nop 0\n\t" \
    "global_load_lds_dwordx4 v116, %[wo01]\n\t" \
    "global_load_lds_dwordx4 v117, %[wo01] offset:1024\n\t" \
    "v_add_u32_e32 v116, 0x20000, v116\n\t" \
    "v_add_u32_e32 v117, 0x20000, v117\n\t" \
    "s_branch 24b\n\t" \
    "41:\n" \
    "s_cmp_eq_u32 %[cnt], 7\n\t" \
    "s_cbranch_scc1 51b\n\t" \
    "s_add_u32 m0, %[wdst], 16384\n\t" \
    "s_nop 0\n\t" \
    "global_load_lds_dwordx4 v116, %[wo01]\n\t" \
    "global_load_lds_dwordx4 v117, %[wo01] offset:1024\n\t" \
    "v_subrev_u32_e32 v116, 0x20000, v116\n\t" \
    "v_subrev_u32_e32 v117, 0x20000, v117\n\t" \
    "s_branch 25b\n\t" \
    "42:\n" \
    "s_cmp_eq_u32 %[cnt], 7\n\t" \
    "s_cbranch_scc1 26b\n\t" \
    "s_add_u32 m0, %[wdst], 32768\n\t" \
    "s_nop 0\n\t" \
    "global_load_lds_dwordx4 v116, %[wo23]\n\t" \
    "global_load_lds_dwordx4 v117, %[wo23] offset:1024\n\t" \
    "v_add_u32_e32 v116, 0x20000, v116\n\t" \
    "v_add_u32_e32 v117, 0x20000, v117\n\t" \
    "s_branch 26b\n\t" \
    "43:\n" \
    "s_cmp_eq_u32 %[cnt], 7\n\t" \
    "s_cbranch_scc1 27b\n\t" \
    "s_add_u32 m0, %[wdst], 49152\n\t" \
    "s_nop 0\n\t" \
    "global_load_lds_dwordx4 v116, %[wo23]\n\t" \
    "global_load_lds_dwordx4 v117, %[wo23] offset:1024\n\t" \
    "global_load_dword v168, v119, %[wo]\n\t" \
    "s_branch 27b\n\t" \
    "30:\n" \
    "s_cmp_eq_u32 %[cnt], 1\n\t" \
    "s_cbranch_scc0 31f\n\t" \
    "s_mov_b64 vcc, 0\n\t" \
    "s_bitcmp1_b32 %[mode], 1\n\t" \
    "s_cbranch_scc1 7b\n\t" \
    "s_branch 2b\n\t" \
    "31:\n" \
    "s_cmp_eq_u32 %[cnt], 6\n\t" \
    "s_cbranch_scc0 33f\n\t" \
    "global_store_dwordx2 v175, v[140:141], %[ws] sc1\n\t" \
    "s_branch 2b\n\t" \
    "33:\n" \
    "s_cmp_eq_u32 %[cnt], 5\n\t" \
    "s_cbranch_scc0 34f\n\t" \
    "global_store_dwordx2 v118, v[140:141], %[ws] sc1\n\t" \
    "s_branch 2b\n\t" \
    "34:\n" \
    "s_cmp_eq_u32 %[cnt], 3\n\t" \
    "s_cbranch_scc0 35f\n\t" \
    "global_load_dwordx2 v[168:169], v142, %[ws] sc1\n\t" \
    "s_branch 2b\n\t" \
    "35:\n" \
    "v_cmp_eq_u64_e64 s[42:43], v[168:169], v[140:141]\n\t" \
    "s_nop 4\n\t" \
    "s_cmp_eq_u64 s[42:43], -1\n\t" \
    "s_cbranch_scc0 2b\n\t" \
    "global_load_dwordx4 v[122:125], v143, %[ws] offset:-2048 sc1\n\t" \
    "global_load_dwordx4 v[126:129], v143, %[ws] offset:-1024 sc1\n\t" \
    "global_load_dwordx4 v[130:133], v143, %[ws] offset:0 sc1\n\t" \
    "global_load_dwordx4 v[134:137], v143, %[ws] offset:1024 sc1\n\t" \
    "global_load_dwordx2 v[138:139], v143, %[ws] offset:2048 sc1\n\t" \
    "s_bitset1_b32 %[mode], 2\n\t" \
    "s_branch 2b\n\t" \
    "99:\n"
#define ATT_TMP "v48", "v49", "v50", "v51", "v52", "v53", "v54", "v55", "v56", "v57", "v58", "v59", "v60", "v61", "v62", "v63", "v96", "v97", "v98", "v99", "v100", "v101", "v102", "v103"

constexpr int K1_X = 0, K1_W = 65536, K1_WB = 24576;
constexpr int K2_SLOT = 32768, K2_QX = 3 * K2_SLOT, K2_FL = 147456;
constexpr int LDS_BYTES = 163840;
constexpr int p3_wslot(int t) { return (t % 6) < 4 ? 98304 + (t % 6) * 16384 : 65536 + ((t % 6) - 4) * 16384; }
constexpr int p3_allow(int t) { return (((t < 2 || t - 2 >= 1) && t + 3 < 16) ? 4 : 0) + (t + 3 < 16 ? 4 : 0) + ((t >= 1 && t + 5 < 16) ? 4 : 0); }

__global__ __launch_bounds__(512, 2) void mk_fused(const u16* __restrict__ xA, const u16* __restrict__ wA, const u16* __restrict__ woA,
                                                   const u16* __restrict__ xB, const u16* __restrict__ wB, const u16* __restrict__ woB, char* ws0, u16* __restrict__ out) {
    char* ws = ws0;
    __shared__ __attribute__((aligned(1024))) char smem[LDS_BYTES];
    const unsigned lds0 = (unsigned)(uintptr_t)(LAS char*)smem;
    const int wid = __builtin_amdgcn_readfirstlane(threadIdx.x >> 6);
    int lane = threadIdx.x & 63;
#define BRX 1
    const int b = blockIdx.x, xcd = b & 7, j = b >> 3, br = (xcd >> 2) ^ BRX, qd = xcd & 3;
    const int mt = (qd & 1) * 4 + (j & 3), hp = (qd >> 1) * 8 + (j >> 2);
    const int mt3 = qd * 4 + (j & 3), nt3 = j >> 2;
    u64 tag;
    u32x4 fq[2][2], fk[2][2], fv[2][2];
    f32x16 band;
    STAMP_AT(0, 0);
    {
        const int g = wid >> 2, wi = wid & 3, mw = wi >> 1, hd = wi & 1;
        const char* x = (const char*)(br ? xB : xA) + (size_t)mt * 256 * 2048;
        const char* w = (const char*)(br ? wB : wA) + hp * 128;
        unsigned vx[4], vw[3];
#pragma unroll
        for (int ii = 0; ii < 4; ++ii) {
            const int row = br ? ((wi >> 1) * 64 + (wi & 1) + ii * 16 + 2 * (lane >> 3)) : (wi * 32 + ii * 8 + (lane >> 3));
            vx[ii] = row * 2048 + (((lane & 7) ^ (((lane >> 4) & 1) | ((ii >> 1) << 1) | ((lane >> 5) << 2))) << 4);
        }
#pragma unroll
        for (int jj = 0; jj < 3; ++jj) {
            const int jw = 12 * g + 3 * wi + jj, n = jw >> 3, k0 = (jw & 7) * 8 + (lane >> 3);
            vw[jj] = k0 * 6144 + n * 2048 + (((lane & 7) ^ ((((lane >> 4) & 1) | ((jw & 1) << 1)) << 1)) << 4);
        }
        const unsigned xdst_o = lds0 + K1_X + (1 - g) * 32768 + wi * 4096;
        const unsigned wdst = lds0 + K1_W + (12 * g + 3 * wi) * 1024;
        const char* xo = x + (size_t)(1 - g) * 128 * 2048;
#define ROTM 4
        const int kt0 = ROTM * (j & 3);
#define STAGE_X(kt, dst) do { const char* s_ = xo + (((kt) + kt0) & 15) * 128; _Pragma("unroll") for (int ii = 0; ii < 4; ++ii) glds16(s_, vx[ii], (dst) + ii * 1024); } while (0)
#define STAGE_W(kt, buf) do { const char* s_ = w + (size_t)(((kt) + kt0) & 15) * 64 * 6144; _Pragma("unroll") for (int jj = 0; jj < 3; ++jj) glds16(s_, vw[jj], wdst + (buf) * K1_WB + jj * 1024); } while (0)
        if (wid == 7) {
            const char* pc; unsigned len;
            asm volatile("s_getpc_b64 %0\n.Lmk_code_begin:\n\ts_mov_b32 %1, .Lmk_code_end-.Lmk_code_begin" : "=s"(pc), "=s"(len));
            const unsigned off = (unsigned)(j * 64 + lane) * 64u;
            asm volatile("s_mov_b32 m0, %2\n\ts_nop 0\n\tglobal_load_lds_dword %0, %1" :: "v"(off < len ? off : 0u), "s"(pc), "s"(lds0 + 159744) : "memory");
        }
        JIT(14);
        STAGE_X(0, xdst_o); STAGE_W(0, 0); STAGE_W(1, 1);
        if (g == 1) STAGE_X(1, xdst_o + 16384);
        { u64 e; asm volatile("s_load_dwordx2 %0, %1, 0x0\n\ts_waitcnt lgkmcnt(0)" : "=s"(e) : "s"(ws + WS_EP) : "memory");
          e += 0x9E3779B97F4A7C15ull; e = (e ^ (e >> 30)) * 0xBF58476D1CE4E5B9ull; e = (e ^ (e >> 27)) * 0x94D049BB133111EBull; tag = e ^ (e >> 31); }
        const int c16 = lane & 15, g4 = lane >> 4;
        unsigned ax[2], aw[2];
        ax[0] = lds0 + K1_X + g * 32768 + (mw * 64 + 16 * (c16 >> 3) + (c16 & 7)) * 128 + ((g4 ^ (((c16 >> 1) & 1) | ((c16 >> 3) << 1) | (((c16 >> 2) & 1) << 2))) << 4);
        ax[1] = ax[0] ^ 64;
        { const int q4 = c16 >> 2, p = lane & 3;
          aw[0] = lds0 + K1_W + (8 * g4 + q4) * 128 + (((hd * 4 + (p >> 1)) ^ ((((q4 >> 1) & 1) | ((g4 & 1) << 1)) << 1)) << 4) + (p & 1) * 8; aw[1] = aw[0] ^ 32; }
        band = band_mask(lane); asm volatile("" : "+v"(band));
        if (g == 1) WAITBAR_(7); else WAITBAR_(3);
        SA_(1);
        if (g == 1) asm volatile("s_barrier" ::: "memory");
        f32x4 acc[4][6];
#pragma unroll
        for (int m = 0; m < 4; ++m)
#pragma unroll
            for (int n = 0; n < 6; ++n) acc[m][n] = f32x4{};
        SBAR();
        unsigned xp = 0, wr_ = 0, ws_ = 2 * K1_WB;
        int tx = (1 + g + kt0) & 15, tw = (2 + kt0) & 15;
        unsigned dxp = (1 - g) * 16384;
#define RDW(n, ks) wf[n][ks] = cat8(ldstr(awc[(n) & 1] + ((n) >> 1) * 8192 + (ks) * 4096), ldstr(awc[(n) & 1] + ((n) >> 1) * 8192 + (ks) * 4096 + 512))
#define P1_TILE(DOX, DOW, WAIT, LASTBAR) do { \
            bf16x8 xf[4][2], wf[6][2]; \
            unsigned awc[2] = {aw[0] + wr_, aw[1] + wr_}; asm volatile("" : "+v"(awc[0]), "+v"(awc[1])); \
            const char* sx = xo + tx * 128; const char* sw = w + (size_t)tw * 64 * 6144; \
            _Pragma("unroll") for (int s = 0; s < 4; ++s) { \
                xf[s][0] = ldsrd128(ax[0] + xp + (s >> 1) * 4096 + (s & 1) * 1024); xf[s][1] = ldsrd128(ax[1] + xp + (s >> 1) * 4096 + (s & 1) * 1024); \
                RDW((3 * s) >> 1, (3 * s) & 1); \
                if (DOX) glds16(sx, vx[s], xdst_o + dxp + s * 1024); \
                RDW((3 * s + 1) >> 1, (3 * s + 1) & 1); RDW((3 * s + 2) >> 1, (3 * s + 2) & 1); \
                if ((DOW) && s < 3) glds16(sw, vw[s], wdst + ws_ + s * 1024); \
            } \
            WAIT; \
            SBAR(); \
            __builtin_amdgcn_s_setprio(1); \
            _Pragma("unroll") for (int m = 0; m < 4; ++m) \
                _Pragma("unroll") for (int n = 0; n < 6; ++n) { \
                    if (n < 4) { acc[m][n] = MFMA16(wf[n][0], xf[m][0], acc[m][n]); acc[m][n] = MFMA16(wf[n][1], xf[m][1], acc[m][n]); } \
                    else       { acc[m][n] = MFMA16(xf[m][0], wf[n][0], acc[m][n]); acc[m][n] = MFMA16(xf[m][1], wf[n][1], acc[m][n]); } \
                    CHAIN(); \
                } \
            __builtin_amdgcn_s_setprio(0); \
            xp ^= 16384; dxp ^= 16384; ws_ = wr_; wr_ = (wr_ == 2 * K1_WB) ? 0 : wr_ + K1_WB; tx = (tx + 1) & 15; tw = (tw + 1) & 15; \
            SBAR(); \
            if (LASTBAR) asm volatile("s_barrier" ::: "memory"); \
            SBAR(); \
        } while (0)
#pragma unroll 1
        for (int t = 0; t < 14; ++t) P1_TILE(true, true, WAITBAR_(7), true);
        P1_TILE(g == 0, false, if (g == 0) WAITBAR_(4); else WAITBAR_(0), true);
        P1_TILE(false, false, WAITBAR_(0), g == 0);
#undef P1_TILE
#undef RDW
#undef STAGE_X
#undef STAGE_W
        SA_(2); SC_(1);
        JIT(1);
        FRESH();
#pragma unroll
        for (int mb = 0; mb < 2; ++mb)
#pragma unroll
            for (int u = 0; u < 2; ++u) {
                const f32x4 a = acc[2 * mb + u][0] * QSCALE, c = acc[2 * mb + u][1] * QSCALE;
                fq[mb][u] = u32x4{cvtpk(a[0], a[1]), cvtpk(a[2], a[3]), cvtpk(c[0], c[1]), cvtpk(c[2], c[3])};
                fk[mb][u] = pack44(acc[2 * mb + u][2], acc[2 * mb + u][3]);
                fv[mb][u] = pack44(acc[2 * mb][4 + u], acc[2 * mb + 1][4 + u]);
            }
    }
    const char* wo = (const char*)(br ? woB : woA) + nt3 * 256;
    const int k30 = 2 * nt3;
    {
        const int g = wid >> 2, wr = wid >> 1, hd = wid & 1;
        const int ph1 = 2 * hp + hd, pb1 = mt * 8 + wr * 2;
        const int head = 2 * hp + br, blk = mt * 8 + wid;
#define RBLK(e) ((((e) % 7) + (((e) % 7) >= mt)) * 8 + (e) / 7)
        const char* kv = ws + WS_KV + (size_t)head * 262144;
        unsigned l16 = lane * 16;
        const unsigned dbeg = lds0 + wid * 4096, lbase = lds0 + l16;
        const unsigned fk0 = (unsigned)WS_FK + head * 32 * FS;
        const unsigned fxo = (unsigned)WS_FX + ((hp * 8 + mt) * 8 + wid) * FS, xrec0 = (unsigned)WS_XP + ((hp * 8 + mt) * 8 + wid) * 5120 + 2048;
        const f32x16 z = {};
        f32x16 sa, negm, o;
        u32x16 qol, kvf, pub0, pub1; u32x8 ph;
        const unsigned sidx = (lane & 1) ? 0xEEEEEEEEu : 0x44444444u;

#define FIRST_TILE(base, q0, q1) do { const unsigned on_ = (lane & 2) ? 0u : 0x3F803F80u, e_ = (lane & 4) ? 0u : on_, f_ = (lane & 4) ? on_ : 0u; const u32x4 ones = {e_, e_, f_, f_}; \
            const bf16x8 k0_ = ldsrd128(base), k1_ = ldsrd128((base) + 1024); kvf = cat16(asu(ldsrd128((base) + 4096)), asu(ldsrd128((base) + 5120)), asu(ldsrd128((base) + 2048)), ones); \
            sa = MFMA32(k0_, q0, z); sa = MFMA32(k1_, q1, sa); float m_ = sa[0]; \
            _Pragma("unroll") for (int r = 1; r < 16; ++r) m_ = __builtin_fmaxf(m_, sa[r]); \
            m_ = -swap_max(m_); _Pragma("unroll") for (int r = 0; r < 16; ++r) { negm[r] = m_; sa[r] += m_; } \
            qol = cat16(asu(q0), asu(q1), ones, u32x4{0u, 0u, 0u, 0u}); } while (0)
#define L6_OUT "={v[0:15]}"(o), "+{v[32:47]}"(sa), "+{v[64:79]}"(qol), "+{v[80:95]}"(kvf), "={v[104:111]}"(ph)
#define L6C(HL, BASE) asm volatile(ATT_L6C : L6_OUT, "={v[144:159]}"(h0), "={v[160:167]}"(h1) \
            : "{v[16:31]}"(negm), "{v174}"(sidx), "{v112}"(BASE), "{v[140:143]}"(u32x4{0u, 0u, FLADDR, (unsigned)WS_HALO + (mt * 32 + ph1) * 6144 + 3072 + l16}), \
              [ws] "s"(ws), [hl] "s"(HL), [fld] "s"(fld) : ATT_TMP, "scc", "memory")
#define FLADDR (fk0 + ((((lane & 7) == 7) ? blk : RBLK(8 * (lane & 7) + wid)) >> 1) * FS)
        const unsigned own = (ph1 * 32 + mt * 4 + wr) * FS;
        const unsigned fld = lds0 + K2_FL + wid * 1024 + br * (2 * K2_SLOT + 4096 - K2_FL + wid * 11264);
        const unsigned pv = (lane >> 5) * 1024 + ((lane & 15) + 32 * ((lane >> 4) & 1)) * 16, pq = pv + (lane & 8) * 16;
        if (br == 0) {
            const char* kp = ws + WS_KV + (size_t)((ph1 * 64 + pb1) * 4) * 1024;
#define PUB_KV() do { st16_dev<0>(kp, pq, fk[0][0]); st16_dev<128>(kp, pq, fk[0][1]); st16_dev<2048>(kp, pv, fv[0][0]); st16_dev<2304>(kp, pv, fv[0][1]); \
            st16_dev<0>(kp + 4096, pq, fk[1][0]); st16_dev<128>(kp + 4096, pq, fk[1][1]); st16_dev<2048>(kp + 4096, pv, fv[1][0]); st16_dev<2304>(kp + 4096, pv, fv[1][1]); } while (0)
            if (hd == 1) {
                const char* qp = ws + WS_QF + (size_t)((ph1 * 64 + pb1) * 2) * 1024;
                st16_dev<0>(qp, pq, fq[0][0]); st16_dev<128>(qp, pq, fq[0][1]); st16_dev<2048>(qp, pq, fq[1][0]); st16_dev<2176>(qp, pq, fq[1][1]);
            }
            JIT(2);
            if (wr == 0) PUB_KV();
#pragma unroll
            for (int mb = 0; mb < 2; ++mb) {
                const unsigned a = lds0 + hd * 2 * K2_SLOT + (wr * 2 + mb) * 4096, aq = lds0 + K2_QX + hd * 16384 + (wr * 2 + mb) * 2048 + pq;
                ldswr128(a + pq, fk[mb][0]); ldswr128(a + pq + 128, fk[mb][1]); ldswr128(a + 2048 + pv, fv[mb][0]); ldswr128(a + 2304 + pv, fv[mb][1]);
                if (hd == 0 || g == 0) { ldswr128(aq, fq[mb][0]); ldswr128(aq + 128, fq[mb][1]); }
            }
            BARRIER_L();
            if (wr == 0 || hd == 1) {
                drain();
                if (lane == 0) { if (wr == 0) flag_set(ws + WS_FK, own, tag); if (hd == 1) flag_set(ws + WS_FQ, own, tag); }
            }
            JIT(3);
            if (wr == 1) PUB_KV();
            const unsigned qa = lbase + K2_QX + (1 - g) * 16384 + wid * 2048, cx = lbase + (1 - g) * 2 * K2_SLOT;
            SA_(3); SC_(2);
            pub0 = cat16(fk[0][0], fk[0][1], fv[0][0], fv[0][1]); pub1 = cat16(fk[1][0], fk[1][1], fv[1][0], fv[1][1]);
            { const bf16x8 q0 = ldsrd128(qa), q1 = ldsrd128(qa + 1024); FIRST_TILE(cx, q0, q1); }
            JIT(4);
            asm volatile(ATT_L4 : L6_OUT : "{v[16:31]}"(negm), "{v174}"(sidx), "{v112}"(cx) : ATT_TMP, "memory");
            SC_(3);
            if (g == 1) {
                const int b1 = RBLK(wid), b0 = RBLK(wid - 4);
                flag_wait(ws, fk0 + (((lane & 1) ? b1 : b0) >> 1) * FS, tag);
                glds4k_dev(kv + b1 * 4096, l16, dbeg + K2_SLOT);
                glds4k_dev(kv + b0 * 4096, l16, dbeg + K2_SLOT - 16384);
                glds16_dev(ws, FLADDR, fld);
            } else if (wr == 1) { drain(); if (lane == 0) flag_set(ws + WS_FK, own, tag); }
            SC_(4);
            JIT(5);
            asm volatile(ATT_L2 : "+{v[0:15]}"(o), "+{v[32:47]}"(sa), "+{v[64:79]}"(qol), "+{v[80:95]}"(kvf), "+{v[104:111]}"(ph) : "{v[16:31]}"(negm), "{v174}"(sidx), "{v112}"(cx) : ATT_TMP, "memory");
            if (g == 0) {
                asm volatile(ATT_XT : "+{v[0:15]}"(o), "+{v[32:47]}"(sa), "+{v[64:79]}"(qol), "+{v[80:95]}"(kvf), "+{v[104:111]}"(ph) : "{v[16:31]}"(negm), "{v174}"(sidx), "{v112}"(cx) : ATT_TMP, "memory");
                st16_dev<-2048>(ws, xrec0 + l16, quad(o, 0)); st16_dev<-1024>(ws, xrec0 + l16, quad(o, 1)); st16_dev<0>(ws, xrec0 + l16, quad(o, 2)); st16_dev<1024>(ws, xrec0 + l16, quad(o, 3));
                st16_dev<2048>(ws, xrec0 + l16, u32x4{__float_as_uint(__uint_as_float(qol[12]) + __uint_as_float(qol[13])), __float_as_uint(negm[0]), 0u, 0u});
                SC_(5);
                JIT(16);
                const bf16x8 q0 = ldsrd128(lbase + K2_QX + wid * 2048), q1 = ldsrd128(lbase + K2_QX + wid * 2048 + 1024);
                FIRST_TILE(lbase, q0, q1);
                u32x16 h0; u32x8 h1;
                L6C(0, lbase);
            }
#undef PUB_KV
        } else {
            if (wr == 3 && mt < 7) {
                const char* p = ws + WS_HALO + (size_t)(((mt + 1) * 32 + ph1) * 6) * 1024;
                st16_dev<0>(p, pq, fk[0][0]); st16_dev<128>(p, pq, fk[0][1]); st16_dev<3072>(p, pq, fk[1][0]); st16_dev<3200>(p, pq, fk[1][1]);
                if (lane >= 32) { st16_dev<1024>(p, pv, fv[0][0]); st16_dev<1280>(p, pv, fv[0][1]); st16_dev<1024>(p + 3072, pv, fv[1][0]); st16_dev<1280>(p + 3072, pv, fv[1][1]); }
            }
            JIT(15);
            const unsigned ex = lds0 + 2 * K2_SLOT + wid * 12288;
#pragma unroll
            for (int mb = 0; mb < 2; ++mb) {
                const unsigned a = ex + mb * 6144;
                ldswr128(a + pq, fq[mb][0]); ldswr128(a + pq + 128, fq[mb][1]); ldswr128(a + 2048 + pq, fk[mb][0]); ldswr128(a + 2176 + pq, fk[mb][1]);
                ldswr128(a + 4096 + pv, fv[mb][0]); ldswr128(a + 4352 + pv, fv[mb][1]);
            }
            BARRIER_L();
            if (wr == 3 && mt < 7) { drain(); if (lane == 0) flag_set(ws + WS_FH, (ph1 * 8 + mt + 1) * FS, tag); }
            SC_(2);
            LocalPart lp[2];
            bf16x8 bq[2][2];
#pragma unroll
            for (int mb = 0; mb < 2; ++mb) { bq[mb][0] = ldsrd128(ex + mb * 6144 + l16); bq[mb][1] = ldsrd128(ex + mb * 6144 + 1024 + l16); }
#define LOCAL_BLOCK(mb) do { const unsigned a_ = ex + mb * 6144 + l16; \
                const bf16x8 k0_ = ldsrd128(a_ + 2048), k1_ = ldsrd128(a_ + 3072), v0_ = ldsrd128(a_ + 4096), v1_ = ldsrd128(a_ + 5120); \
                if (wr > 0) local_own<true>(bq[mb][0], bq[mb][1], k0_, k1_, v0_, v1_, ldsrd128(a_ - 24576 + 2048), ldsrd128(a_ - 24576 + 3072), ldsrd128(a_ - 24576 + 5120), band, lane, lp[mb]); \
                else local_own<false>(bq[mb][0], bq[mb][1], k0_, k1_, v0_, v1_, k0_, k0_, k0_, band, lane, lp[mb]); \
                if (wr > 0 || mt == 0) local_fin(lp[mb]); } while (0)
            JIT(6);
            LOCAL_BLOCK(0);
            SC_(3);
            const int hl = (wid < 2) ? mt : 0;
            const unsigned fh = (unsigned)WS_FH + (ph1 * 8 + mt) * FS;
            const int b0 = RBLK(wid), b1 = RBLK(8 + wid);
            const int q = lane & 3;
            unsigned fa0 = fk0 - 1024 * FS + (blk >> 1) * FS, fa1 = fk0 + (b0 >> 1) * FS, fa2 = fk0 + (b1 >> 1) * FS, fa3 = fh;
            asm volatile("" : "+s"(fa0), "+s"(fa1), "+s"(fa2), "+s"(fa3));
            const unsigned lo_ = (q & 1) ? fa1 : fa0, hi_ = (q & 1) ? fa3 : fa2;
            const u64 fm = flag_wait_some(ws, (q & 2) ? hi_ : lo_, tag, 3);
            SC_(4);
            glds4k_dev(kv + b0 * 4096, l16, dbeg);
            const char* qp = ws + WS_QF + (size_t)((head * 64 + blk) * 2) * 1024;
            glds16_dev(qp, l16, ex); glds16_dev(qp + 1024, l16, ex + 1024);
            if (fm & 4) glds4k_dev(kv + b1 * 4096, l16, dbeg + K2_SLOT);
            JIT(7);
            LOCAL_BLOCK(1);
#undef LOCAL_BLOCK
            if (fm & 4) WAITBAR_(4);
            else {
                WAITBAR_(0);
                flag_wait(ws, fk0 + (b1 >> 1) * FS, tag);
                glds4k_dev(kv + b1 * 4096, l16, dbeg + K2_SLOT);
            }
            JIT(8);
            const bf16x8 q0 = ldsrd128(ex + l16), q1 = ldsrd128(ex + 1024 + l16);
            SA_(3); SC_(5);
            if (hl && !(fm & 8)) flag_wait(ws, fh, tag);
            FIRST_TILE(lbase, q0, q1);
            u32x16 h0; u32x8 h1;
            JIT(9);
            L6C(hl, lbase);
            if (hl) {
                local_prev(bq[0][0], bq[0][1], asbf(u32x4{h0[0], h0[1], h0[2], h0[3]}), asbf(u32x4{h0[4], h0[5], h0[6], h0[7]}), asbf(u32x4{h0[8], h0[9], h0[10], h0[11]}), lane, lp[0]);
                local_prev(bq[1][0], bq[1][1], asbf(u32x4{h0[12], h0[13], h0[14], h0[15]}), asbf(u32x4{h1[0], h1[1], h1[2], h1[3]}), asbf(u32x4{h1[4], h1[5], h1[6], h1[7]}), lane, lp[1]);
                local_fin(lp[0]); local_fin(lp[1]);
            }
            const char* op = ws + WS_OF + (size_t)(((64 + pb1) * 32 + ph1) * 2) * 1024;
            st16_dev<0>(op, l16, quad(lp[0].o, 0)); st16_dev<1024>(op, l16, quad(lp[0].o, 1)); st16_dev<0>(op + 65536, l16, quad(lp[1].o, 0)); st16_dev<1024>(op + 65536, l16, quad(lp[1].o, 1));
        }
#undef FIRST_TILE
#undef L6_OUT
#undef L6C
        FRESH();
        l16 = lane * 16;
        SA_(4); SC_(6);
        const int isx = br & (1 - g);
        unsigned cnt = 7, dst = dbeg + 2 * K2_SLOT, nx = K2_SLOT;
        unsigned mode = isx << 1;
        const unsigned late = (1 - br) & g;
        const unsigned act = 2u | (isx * 12u) | ((br | (1 - g)) << 5) | (late << 6), hk = 32u | (late << 7);
        u64 tab = (u64)blk << 42;
#pragma unroll
        for (int k = 1; k < 7; ++k) tab |= (u64)RBLK(8 * k + wid) << (6 * k);
        tab >>= 6 + 6 * br;
        u32x8 adr;
        { const int k = 8 * wid + (lane >> 4), c = lane & 15;
          adr = u32x8{lds0 + l16, lds0 + l16 + K2_SLOT, l16, lds0 + l16, (unsigned)(k * 2048 + ((c ^ ((k & 7) << 1)) << 4)), (unsigned)((k + 4) * 2048 + ((c ^ (((k + 4) & 7) << 1)) << 4) - 1024),
                      br ? (unsigned)WS_FO + ((64 + pb1 + (lane & 1)) * 32 + ph1) * FS : fxo, 0u};
          const int i = (mt3 * 8 + wid) * 12 + ((lane * 3) >> 4); adr[7] = ((((4 + (i >> 7) + k30) & 15) * 64 + ((i & 127) >> 1)) * 2048 + (i & 1) * 128); }
        const unsigned kvrec = (unsigned)WS_KV + (ph1 * 64 + pb1) * 4096 + 3072, pv2 = (lane >> 5) * 1024 + ((lane & 15) + 32 * ((lane >> 4) & 1)) * 16;
        f32x16 xo; f32x2 xl;
#define DBGOUT
        JIT(10);
        asm volatile(ATT_MAIN
                     : DBGOUT"+{v[0:15]}"(o), "+{v[32:47]}"(sa), "+{v[64:79]}"(qol), "+{v[80:95]}"(kvf), "+{v[104:111]}"(ph), "+{v[112:119]}"(adr), "={v[122:137]}"(xo), "={v[138:139]}"(xl),
                       [cnt] "+s"(cnt), [dst] "+s"(dst), [nx] "+s"(nx), [mode] "+s"(mode)
                     : "{v[16:31]}"(negm), "{v[174:175]}"(u32x2{sidx, (unsigned)WS_FK + own}), "{v[140:143]}"(u32x4{(unsigned)tag, (unsigned)(tag >> 32), fxo, xrec0 + l16}),
                       "{v[170:173]}"(u32x4{FLADDR, kvrec + pv2 + (lane & 8) * 16, kvrec + pv2, fld + l16}), "{v[176:191]}"(pub0), "{v[192:207]}"(pub1),
                       [srclo] "s"((unsigned)(uintptr_t)kv), [srchi] "s"((unsigned)((uintptr_t)kv >> 32)), [dbeg] "s"(dbeg), [dend] "s"(dbeg + 3 * K2_SLOT),
                       [wo01] "s"(wo + (size_t)k30 * 131072), [wo23] "s"(wo + (size_t)((k30 + 2) & 15) * 131072), [wo] "s"(wo), [wdst] "s"(lds0 + 98304 + wid * 2048), [act] "s"(act), [hk] "s"(hk), [tab] "s"(tab), [k0] "s"(br), [ws] "s"(ws)
                     : ATT_TMP, "v120", "v121", "v168", "v169", "s40", "s41", "s42", "s43", "s44", "s45", "s46", "s47", "s48", "s49", "s50", "s51", "vcc", "scc", "memory");
        FRESH();
        l16 = lane * 16;
        const unsigned xrec = xrec0 + l16;
        const bf16x8 q0 = asbf(u32x4{qol[0], qol[1], qol[2], qol[3]}), q1 = asbf(u32x4{qol[4], qol[5], qol[6], qol[7]});
        const unsigned one2 = (((lane >> 4) & 1) == ((lane >> 2) & 1)) ? 0x3F803F80u : 0u;
        const u32x4 ones = {one2, one2, one2, one2};
        SA_(5); SB_(1);
        JIT(11);
        float lsum = __uint_as_float(qol[12]) + __uint_as_float(qol[13]);
        bool ok = lsum < 1.0e18f;
        if (isx) {
            if (__builtin_expect(!(mode & 4), 0)) {
                flag_wait(ws, fxo, tag);
                f32x4 a0, a1, a2, a3;
                asm volatile("global_load_dwordx4 %0, %5, %6 offset:-2048 sc1\n\tglobal_load_dwordx4 %1, %5, %6 offset:-1024 sc1\n\tglobal_load_dwordx4 %2, %5, %6 sc1\n\t"
                             "global_load_dwordx4 %3, %5, %6 offset:1024 sc1\n\tglobal_load_dwordx2 %4, %5, %6 offset:2048 sc1\n\ts_waitcnt vmcnt(0)"
                             : "=&v"(a0), "=&v"(a1), "=&v"(a2), "=&v"(a3), "=&v"(xl) : "v"(xrec), "s"(ws) : "memory");
#pragma unroll
                for (int r = 0; r < 4; ++r) { xo[r] = a0[r]; xo[4 + r] = a1[r]; xo[8 + r] = a2[r]; xo[12 + r] = a3[r]; }
            }
            const float nm = __builtin_fminf(negm[0], xl[1]), fa = EXP2(nm - negm[0]), fx = EXP2(nm - xl[1]);
            ok = ok && (xl[0] < 1.0e18f);
            lsum = lsum * fa + xl[0] * fx;
#pragma unroll
            for (int r = 0; r < 16; ++r) o[r] = o[r] * fa + xo[r] * fx;
        }
#pragma unroll
        for (int r = 0; r < 16; ++r) ok = ok && (__builtin_fabsf(o[r]) < 3.0e38f);
        if (__builtin_expect(__any(!ok), 0)) {
            float m = -__builtin_inff();
            f32x4 l;
            for (int pass = 0; pass < 2; ++pass) {
                o = z; l = f32x4{};
                for (int t = 0; t < 64; ++t) {
                    bf16x8 f0, f1, f2, f3;
                    asm volatile("global_load_dwordx4 %0, %4, %5 sc1\n\tglobal_load_dwordx4 %1, %4, %5 offset:1024 sc1\n\tglobal_load_dwordx4 %2, %4, %5 offset:2048 sc1\n\t"
                                 "global_load_dwordx4 %3, %4, %5 offset:3072 sc1\n\ts_waitcnt vmcnt(0)" : "=&v"(f0), "=&v"(f1), "=&v"(f2), "=&v"(f3) : "v"(l16 + t * 4096), "s"(kv) : "memory");
                    f32x16 s = MFMA32(f0, q0, z); s = MFMA32(f1, q1, s);
                    if (pass == 0) {
#pragma unroll
                        for (int r = 0; r < 16; ++r) m = __builtin_fmaxf(m, s[r]);
                    } else {
#pragma unroll
                        for (int r = 0; r < 16; ++r) s[r] = EXP2(s[r] - m);
                        const bf16x8 p0 = asbf(pack8(s, 0)), p1 = asbf(pack8(s, 1));
                        o = MFMA32(f2, p0, o); o = MFMA32(f3, p1, o);
                        l = MFMA16(asbf(ones), p0, l); l = MFMA16(asbf(ones), p1, l);
                    }
                }
                if (pass == 0) m = swap_max(m);
            }
            lsum = l[0];
        }
        const float inv = __builtin_amdgcn_rcpf(lsum);
#pragma unroll
        for (int r = 0; r < 16; ++r) o[r] *= inv;
        const char* op = ws + WS_OF + (size_t)((blk * 32 + head) * 2) * 1024;
        st16_dev<0>(op, l16, pack8(o, 0)); st16_dev<1024>(op, l16, pack8(o, 1));
    }
    {
        FRESH();
        const int g = wid >> 2, wi = wid & 3, wm = wi >> 1, wn = wi & 1, g4 = lane >> 4;
        const char* of = ws + WS_OF + (size_t)(br * 64 + mt3 * 4 + wi) * 65536;
        unsigned vw4[2];
#pragma unroll
        for (int ii = 0; ii < 2; ++ii) { const int k = 16 * wi + 4 * ii + g4; vw4[ii] = k * 2048 + (((lane & 15) ^ ((k & 7) << 1)) << 4); }
        const unsigned l16 = lane * 16, sdst = lds0 + wi * 4096;
#define STAGE_O(t) glds4k_dev(of + (((t) + k30) & 15) * 4096, l16, sdst + ((t) & 3) * 16384)
#define STAGE_WO(t, slot) do { const char* s_ = wo + (size_t)(((t) + k30) & 15) * 131072; glds16(s_, vw4[0], sdst + (slot)); glds16(s_, vw4[1], sdst + (slot) + 1024); \
            glds16(s_ + 16384, vw4[0], sdst + (slot) + 2048); glds16(s_ + 16384, vw4[1], sdst + (slot) + 3072); } while (0)
        const unsigned fo = (unsigned)WS_FO + ((br * 64 + mt3 * 4 + wi) * 32 + 4 * ((lane & 15) >> 1) + 2 * (1 - g) + (lane & 1)) * FS;
        drain();
        if (lane == 0) flag_set(ws + WS_FO, ((mt * 8 + wid) * 32 + 2 * hp + br) * FS, tag);
        SB_(2);
        asm volatile("s_barrier" ::: "memory");
        SB_(3);
        JIT(12);
        flag_wait(ws, fo, tag);
        STAGE_O(1 - g);
        if (g == 1) STAGE_O(2);
        STAGE_WO(5 - g, 65536 + (1 - g) * 16384);
        SB_(4);
        SA_(6);
        const unsigned ao = lds0 + g * 16384 + wm * 8192 + (g4 >> 1) * 1024 + ((lane & 15) + 32 * (g4 & 1)) * 16;
        unsigned aw[4];
        { const int q4 = (lane & 15) >> 2, p = lane & 3, k = 16 * (g4 >> 1) + 4 * (g4 & 1) + q4;
          const unsigned a0 = lds0 + g * 16384 + k * 256 + (((8 * wn + (p >> 1)) ^ ((k & 7) << 1)) << 4) + (p & 1) * 8;
#pragma unroll
          for (int nb = 0; nb < 4; ++nb) aw[nb] = a0 ^ (nb << 5); }
        f32x4 acc[4][4];
#pragma unroll
        for (int mb = 0; mb < 4; ++mb)
#pragma unroll
            for (int nb = 0; nb < 4; ++nb) acc[mb][nb] = f32x4{};
        if (g == 1) { WAITBAR_(8); asm volatile("s_barrier" ::: "memory"); } else asm volatile("s_barrier" ::: "memory");
        SB_(5);
        SBAR();
#pragma unroll
        for (int u = 0; u < 8; ++u) {
            bf16x8 of_[4][2], wf[4][2];
            const int to = 2 * u + 3 + g, tw = to + 2;
            const char* so = of + ((to + k30) & 15) * 4096; const char* sw = wo + (size_t)((tw + k30) & 15) * 131072;
            const unsigned dob = sdst + (to & 3) * 16384, dwb = sdst + (g ? p3_wslot(2 * u + 6) : p3_wslot(2 * u + 5));
            const bool doo = u < 6 || (u == 6 && g == 0), dow = (u >= 1 || g == 1) && (u < 5 || (u == 5 && g == 0));
#pragma unroll
            for (int i = 0; i < 8; ++i) {
                const int hh = i >> 2, q = i & 3;
                of_[q][hh] = ldsrd128(ao + ((2 * u) & 3) * 16384 + (q >> 1) * 4096 + (q & 1) * 256 + hh * 2048);
                wf[q][hh] = cat8(ldstr(aw[q] + p3_wslot(2 * u) + hh * 8192), ldstr(aw[q] + p3_wslot(2 * u) + hh * 8192 + 2048));
                if (i < 4) { if (u < 7 && doo) glds16_dev(so + i * 1024, l16, dob + i * 1024); }
                else if (u < 6 && dow) glds16(sw + ((i & 2) ? 16384 : 0), vw4[i & 1], dwb + (i - 4) * 1024);
            }
            if (p3_allow(2 * u) == p3_allow(2 * u + 1)) WAITBAR(p3_allow(2 * u)); else if (g == 0) WAITBAR(p3_allow(2 * u)); else WAITBAR(p3_allow(2 * u + 1));
            SBAR();
            __builtin_amdgcn_s_setprio(1);
#pragma unroll
            for (int mb = 0; mb < 4; ++mb)
#pragma unroll
                for (int nb = 0; nb < 4; ++nb) { acc[mb][nb] = MFMA16(wf[nb][0], of_[mb][0], acc[mb][nb]); acc[mb][nb] = MFMA16(wf[nb][1], of_[mb][1], acc[mb][nb]); CHAIN(); }
            __builtin_amdgcn_s_setprio(0);
            SBAR();
            if (u < 7 || g == 0) asm volatile("s_barrier" ::: "memory");
            SBAR();
        }
        SB_(6);
        FRESH();
        JIT(13);
        const unsigned xw = lds0 + wid * 8192 + l16, xr = lds0 + (wid ^ 4) * 8192 + l16;
#pragma unroll
        for (int i = 0; i < 8; ++i) { const f32x4 v = g ? acc[i >> 2][i & 3] : acc[2 + (i >> 2)][i & 3]; ldswr128(xw + i * 1024, __builtin_bit_cast(u32x4, v)); }
        BARRIER_L();
        const unsigned st = lds0 + 65536 + wid * 4096;
#pragma unroll
        for (int i = 0; i < 8; ++i) {
            const f32x4 v = (g ? acc[2 + (i >> 2)][i & 3] : acc[i >> 2][i & 3]) + __builtin_bit_cast(f32x4, ldsrd128(xr + i * 1024));
            const int row = 16 * (i >> 2) + (lane & 15), c = 2 * (i & 3) + (g4 >> 1);
            *(LAS u32x2*)(uintptr_t)(st + row * 128 + ((c ^ (row & 7)) << 4) + (g4 & 1) * 8) = u32x2{cvtpk(v[0], v[1]), cvtpk(v[2], v[3])};
        }
        const int blk = mt3 * 4 + 2 * wm + g;
        char* op = (char*)out + br * 2048 + nt3 * 256 + wn * 128 + (lane & 7) * 16;
#pragma unroll
        for (int i = 0; i < 4; ++i) {
            const int r = 8 * i + (lane >> 3), row = br ? ((blk >> 1) * 64 + 2 * r + (blk & 1)) : (blk * 32 + r);
            st16_dev<0>(out, (unsigned)(row * 4096 + br * 2048 + nt3 * 256 + wn * 128 + (lane & 7) * 16), __builtin_bit_cast(u32x4, ldsrd128(st + r * 128 + (((lane & 7) ^ (r & 7)) << 4))));
        }
#undef STAGE_O
#undef STAGE_WO
    }
    STAMP_AT(0, 7);
    if (b == 0 && wid == 0) {
#pragma unroll
        for (int i = 0; i < 2; ++i) { const int u = i * 64 + lane;
            flag_wait(ws0, (unsigned)WS_FK + ((2 * (u >> 3)) * 32 + (u & 7) * 4) * FS, tag);
            flag_wait(ws0, (unsigned)WS_FO + ((64 + (u & 7) * 8) * 32 + 2 * (u >> 3)) * FS, tag); }
        if (lane == 0) flag_set(ws0, (unsigned)WS_EP, tag);
    }
    asm volatile(".Lmk_code_end:");
}

extern "C" void kernel_launch(void* const* d_in, const int* in_sizes, int n_in, void* d_out, int out_size, void* d_ws, size_t ws_size, hipStream_t stream) {
    mk_fused<<<256, 512, 0, stream>>>((const u16*)d_in[0], (const u16*)d_in[1], (const u16*)d_in[2], (const u16*)d_in[3], (const u16*)d_in[4], (const u16*)d_in[5], (char*)d_ws, (u16*)d_out);
}
```

```cpp
#include <hip/hip_runtime.h>
#include <stdint.h>

typedef unsigned short u16;
typedef unsigned long long u64;
typedef __bf16 bf16x8 __attribute__((ext_vector_type(8)));
typedef __bf16 bf16x2 __attribute__((ext_vector_type(2)));
typedef short s16x4 __attribute__((ext_vector_type(4)));
typedef float f32x16 __attribute__((ext_vector_type(16)));
typedef float f32x4 __attribute__((ext_vector_type(4)));
typedef float f32x2 __attribute__((ext_vector_type(2)));
typedef unsigned u32x4 __attribute__((ext_vector_type(4)));
typedef unsigned u32x2 __attribute__((ext_vector_type(2)));
typedef unsigned u32x8 __attribute__((ext_vector_type(8)));
typedef unsigned u32x16 __attribute__((ext_vector_type(16)));
#define LAS __attribute__((address_space(3)))
#define DI __device__ __forceinline__
#define SBAR() __builtin_amdgcn_sched_barrier(0)
#define MFMA32(a, b, c) __builtin_amdgcn_mfma_f32_32x32x16_bf16((a), (b), (c), 0, 0, 0)
#define MFMA16(a, b, c) __builtin_amdgcn_mfma_f32_16x16x32_bf16((a), (b), (c), 0, 0, 0)
#define CHAIN() SBAR()
#define EXP2(x) __builtin_amdgcn_exp2f(x)

constexpr float QSCALE = 0.17677669529663687f * 1.4426950408889634f;

constexpr size_t WS_QF = 0;
constexpr size_t WS_KV = WS_QF + (4u << 20);
constexpr size_t WS_OF = WS_KV + (8u << 20);
constexpr size_t WS_HALO = WS_OF + (8u << 20);
constexpr size_t WS_XP = WS_HALO + (2u << 20);
constexpr size_t WS_FX = WS_XP + (5u << 20);
#define FS 64
constexpr size_t WS_FQ = WS_FX + 1024 * FS;
constexpr size_t WS_FK = WS_FQ + 1024 * FS;
constexpr size_t WS_FO = WS_FK + 1024 * FS;
constexpr size_t WS_FH = WS_FO + 4096 * FS;
constexpr size_t WS_EP = WS_FH + 256 * FS;

#define OUTST 1
#define SLP 1
#define STAMP_AT(K, I)
#define SA_(I)
#define SB_(I)

#define SC_(I)
#define JIT(p)
DI unsigned cvtpk(float lo, float hi) { f32x2 v = {lo, hi}; bf16x2 b = __builtin_convertvector(v, bf16x2); return __builtin_bit_cast(unsigned, b); }
DI u32x4 pack8(const f32x16& a, int s) { u32x4 r; r[0] = cvtpk(a[8 * s], a[8 * s + 1]); r[1] = cvtpk(a[8 * s + 2], a[8 * s + 3]); r[2] = cvtpk(a[8 * s + 4], a[8 * s + 5]); r[3] = cvtpk(a[8 * s + 6], a[8 * s + 7]); return r; }
DI u32x4 pack44(f32x4 a, f32x4 c) { return u32x4{cvtpk(a[0], a[1]), cvtpk(a[2], a[3]), cvtpk(c[0], c[1]), cvtpk(c[2], c[3])}; }
DI u32x4 asu(bf16x8 v) { return __builtin_bit_cast(u32x4, v); }
DI u32x16 cat16(u32x4 a, u32x4 b, u32x4 c, u32x4 d) { return u32x16{a[0], a[1], a[2], a[3], b[0], b[1], b[2], b[3], c[0], c[1], c[2], c[3], d[0], d[1], d[2], d[3]}; }
DI bf16x8 asbf(u32x4 v) { return __builtin_bit_cast(bf16x8, v); }
DI void glds16(const void* sbase, unsigned voff, unsigned lds) {
    asm volatile("s_mov_b32 m0, %2\n\ts_nop 0\n\tglobal_load_lds_dwordx4 %0, %1" :: "v"(voff), "s"(sbase), "s"(lds) : "memory");
}
template <int I> DI void glds_run(const void* sbase, unsigned voff, unsigned lds) {
    if (I == 0) asm volatile("s_mov_b32 m0, %2\n\ts_nop 0\n\tglobal_load_lds_dwordx4 %0, %1" :: "v"(voff), "s"(sbase), "s"(lds) : "memory");
    else asm volatile("global_load_lds_dwordx4 %0, %1 offset:%2" :: "v"(voff), "s"(sbase), "i"(I * 1024) : "memory");
}
template <int I> DI void glds_run_dev(const void* sbase, unsigned voff, unsigned lds) {
    if (I == 0) asm volatile("s_mov_b32 m0, %2\n\ts_nop 0\n\tglobal_load_lds_dwordx4 %0, %1 sc1" :: "v"(voff), "s"(sbase), "s"(lds) : "memory");
    else asm volatile("global_load_lds_dwordx4 %0, %1 offset:%2 sc1" :: "v"(voff), "s"(sbase), "i"(I * 1024) : "memory");
}
DI void glds16_dev(const void* sbase, unsigned voff, unsigned lds) {
    asm volatile("s_mov_b32 m0, %2\n\ts_nop 0\n\tglobal_load_lds_dwordx4 %0, %1 sc1" :: "v"(voff), "s"(sbase), "s"(lds) : "memory");
}
DI void glds4k_dev(const void* sbase, unsigned voff, unsigned lds) {
    asm volatile("s_mov_b32 m0, %2\n\ts_nop 0\n\tglobal_load_lds_dwordx4 %0, %1 sc1\n\tglobal_load_lds_dwordx4 %0, %1 offset:1024 sc1\n\t"
                 "global_load_lds_dwordx4 %0, %1 offset:2048 sc1\n\tglobal_load_lds_dwordx4 %0, %1 offset:3072 sc1" :: "v"(voff), "s"(sbase), "s"(lds) : "memory");
}
template <int OFF> DI void st16_dev(const void* sbase, unsigned voff, u32x4 v) {
    asm volatile("global_store_dwordx4 %0, %1, %2 offset:%3 sc1\n\ts_nop 1" :: "v"(voff), "v"(v), "s"(sbase), "i"(OFF) : "memory");
}
DI void drain() { asm volatile("s_waitcnt vmcnt(0)" ::: "memory"); }
DI void flag_set(const void* sbase, unsigned voff, u64 tag) {
    asm volatile("global_store_dwordx2 %0, %1, %2 sc1" :: "v"(voff), "v"(tag), "s"(sbase) : "memory");
}
DI void flag_wait(const void* sbase, unsigned voff, u64 tag) {
    const u64 t0 = __builtin_amdgcn_s_memrealtime();
    for (;;) {
        u64 v;
        asm volatile("global_load_dwordx2 %0, %1, %2 sc1\n\ts_waitcnt vmcnt(0)" : "=v"(v) : "v"(voff), "s"(sbase) : "memory");
        if (__all(v == tag)) break;
        if (__builtin_amdgcn_s_memrealtime() - t0 > 2000000ull) break;
        __builtin_amdgcn_s_sleep(SLP);
    }
}
DI u64 flag_wait_some(const void* sbase, unsigned voff, u64 tag, u64 need) {
    const u64 t0 = __builtin_amdgcn_s_memrealtime();
    for (;;) {
        u64 v;
        asm volatile("global_load_dwordx2 %0, %1, %2 sc1\n\ts_waitcnt vmcnt(0)" : "=v"(v) : "v"(voff), "s"(sbase) : "memory");
        const u64 m = __ballot(v == tag);
        if ((m & need) == need || __builtin_amdgcn_s_memrealtime() - t0 > 2000000ull) return m;
        __builtin_amdgcn_s_sleep(SLP);
    }
}
DI bf16x8 ldsrd128(unsigned addr) { return *(const LAS bf16x8*)(uintptr_t)addr; }
DI void ldswr128(unsigned addr, u32x4 v) { *(LAS u32x4*)(uintptr_t)addr = v; }
DI s16x4 ldstr(unsigned addr) { return __builtin_amdgcn_ds_read_tr16_b64_v4i16((LAS s16x4*)(uintptr_t)addr); }
DI bf16x8 cat8(s16x4 lo, s16x4 hi) { typedef short s16x8 __attribute__((ext_vector_type(8))); s16x8 v = {lo[0], lo[1], lo[2], lo[3], hi[0], hi[1], hi[2], hi[3]}; return __builtin_bit_cast(bf16x8, v); }
DI float swap_max(float m) { auto r = __builtin_amdgcn_permlane32_swap(__float_as_uint(m), __float_as_uint(m), false, false); return __builtin_fmaxf(__uint_as_float(r[0]), __uint_as_float(r[1])); }
DI float swap_add(float m) { auto r = __builtin_amdgcn_permlane32_swap(__float_as_uint(m), __float_as_uint(m), false, false); return __uint_as_float(r[0]) + __uint_as_float(r[1]); }
#define FRESH() asm volatile("" : "+v"(lane), "+s"(ws))
#define BARRIER_L() asm volatile("s_waitcnt lgkmcnt(0)\n\ts_barrier" ::: "memory")
#define WAITBAR_(n) asm volatile("s_waitcnt vmcnt(%0) lgkmcnt(0)\n\ts_barrier" :: "i"(n) : "memory")
#define WAITBAR(n) do { switch (n) { case 0: WAITBAR_(0); break; case 2: WAITBAR_(2); break; case 3: WAITBAR_(3); break; case 4: WAITBAR_(4); break; case 6: WAITBAR_(6); break; \
    case 7: WAITBAR_(7); break; case 8: WAITBAR_(8); break; case 10: WAITBAR_(10); break; case 12: WAITBAR_(12); break; default: WAITBAR_(0); } } while (0)

struct LocalPart { f32x16 o; float m, l; };
DI f32x16 band_mask(int lane) {
    const int i = lane & 31, h = lane >> 5;
    f32x16 c;
#pragma unroll
    for (int r = 0; r < 16; ++r) { const int j = (r & 3) + 8 * (r >> 2) + 4 * h; c[r] = ((unsigned)(i - j) <= 7u) ? 0.f : -__builtin_inff(); }
    return c;
}
DI float max3(float a, float b, float c) { float r; asm("v_max3_f32 %0, %1, %2, %3" : "=v"(r) : "v"(a), "v"(b), "v"(c)); return r; }
template <bool PREV> DI void local_own(bf16x8 q0, bf16x8 q1, bf16x8 k0, bf16x8 k1, bf16x8 v0, bf16x8 v1, bf16x8 kp0, bf16x8 kp1, bf16x8 vp1, const f32x16& band, int lane, LocalPart& P) {
    const f32x16 z = {};
    f32x16 so = MFMA32(k0, q0, band), sp = z;
    if (PREV) sp = MFMA32(kp0, q0, z);
    so = MFMA32(k1, q1, so);
    if (PREV) sp = MFMA32(kp1, q1, sp);
    float m = __builtin_fmaxf(so[0], so[1]);
#pragma unroll
    for (int r = 2; r < 16; r += 2) m = __builtin_fmaxf(__builtin_fmaxf(m, so[r]), so[r + 1]);
    if (PREV) {
        const int d = 4 * (lane >> 5) - (lane & 31);
#pragma unroll
        for (int r = 12; r < 16; ++r) { sp[r] = (d + (r & 3) >= 1) ? sp[r] : -__builtin_inff(); m = __builtin_fmaxf(m, sp[r]); }
    }
    m = swap_max(m);
    float l = 0.f;
#pragma unroll
    for (int r = 0; r < 16; ++r) { so[r] = EXP2(so[r] - m); l += so[r]; }
    P.o = MFMA32(v0, asbf(pack8(so, 0)), z); P.o = MFMA32(v1, asbf(pack8(so, 1)), P.o);
    if (PREV) {
#pragma unroll
        for (int r = 12; r < 16; ++r) { sp[r] = EXP2(sp[r] - m); l += sp[r]; }
        P.o = MFMA32(vp1, asbf(u32x4{0u, 0u, cvtpk(sp[12], sp[13]), cvtpk(sp[14], sp[15])}), P.o);
    }
    P.m = m; P.l = l;
}
DI void local_prev(bf16x8 q0, bf16x8 q1, bf16x8 kp0, bf16x8 kp1, bf16x8 vp1, int lane, LocalPart& P) {
    const int i = lane & 31, h = lane >> 5;
    const f32x16 z = {};
    f32x16 sp = MFMA32(kp0, q0, z); sp = MFMA32(kp1, q1, sp);
    const float NEG = -__builtin_inff();
    float m = P.m;
#pragma unroll
    for (int r = 12; r < 16; ++r) { const int j = (r & 3) + 24 + 4 * h; sp[r] = (j >= i + 25) ? sp[r] : NEG; m = __builtin_fmaxf(m, sp[r]); }
    m = swap_max(m);
    const float f = EXP2(P.m - m);
    float l = P.l * f;
#pragma unroll
    for (int r = 12; r < 16; ++r) { sp[r] = EXP2(sp[r] - m); l += sp[r]; }
#pragma unroll
    for (int r = 0; r < 16; ++r) P.o[r] *= f;
    P.o = MFMA32(vp1, asbf(u32x4{0u, 0u, cvtpk(sp[12], sp[13]), cvtpk(sp[14], sp[15])}), P.o);
    P.m = m; P.l = l;
}
DI void local_fin(LocalPart& P) {
    const float inv = __builtin_amdgcn_rcpf(swap_add(P.l));
#pragma unroll
    for (int r = 0; r < 16; ++r) P.o[r] *= inv;
    const u32x4 o0 = pack8(P.o, 0), o1 = pack8(P.o, 1);
#pragma unroll
    for (int r = 0; r < 4; ++r) { P.o[r] = __uint_as_float(o0[r]); P.o[4 + r] = __uint_as_float(o1[r]); }
}
DI u32x4 quad(const f32x16& a, int i) { return __builtin_bit_cast(u32x4, f32x4{a[4 * i], a[4 * i + 1], a[4 * i + 2], a[4 * i + 3]}); }

#define ATT_L6C \
    "s_nop 1\n\t" \
    "s_cmp_eq_u32 %[hl], 0\n\t" \
    "s_cbranch_scc1 10f\n\t" \
    "global_load_dwordx4 v[144:147], v143, %[ws] offset:-3072 sc1\n\t" \
    "global_load_dwordx4 v[148:151], v143, %[ws] offset:-2048 sc1\n\t" \
    "global_load_dwordx4 v[152:155], v143, %[ws] offset:-1024 sc1\n\t" \
    "global_load_dwordx4 v[156:159], v143, %[ws] offset:0 sc1\n\t" \
    "global_load_dwordx4 v[160:163], v143, %[ws] offset:1024 sc1\n\t" \
    "global_load_dwordx4 v[164:167], v143, %[ws] offset:2048 sc1\n\t" \
    "10:\n" \
    "s_setprio 3\n\t" \
    "s_waitcnt lgkmcnt(0)\n\t" \
    "v_mfma_f32_32x32x16_bf16 v[48:63], v[80:83], v[64:67], v[16:31]\n\t" \
    "v_exp_f32_e32 v32, v32\n\t" \
    "v_exp_f32_e32 v33, v33\n\t" \
    "v_exp_f32_e32 v34, v34\n\t" \
    "v_exp_f32_e32 v35, v35\n\t" \
    "v_exp_f32_e32 v36, v36\n\t" \
    "v_mfma_f32_32x32x16_bf16 v[48:63], v[84:87], v[68:71], v[48:63]\n\t" \
    "ds_read_b128 v[80:83], v112 offset:8192\n\t" \
    "ds_read_b128 v[84:87], v112 offset:9216\n\t" \
    "v_cvt_pk_bf16_f32 v104, v32, v33\n\t" \
    "v_cvt_pk_bf16_f32 v105, v34, v35\n\t" \
    "v_exp_f32_e32 v37, v37\n\t" \
    "v_exp_f32_e32 v38, v38\n\t" \
    "v_exp_f32_e32 v39, v39\n\t" \
    "ds_read_b128 v[100:103], v112 offset:3072\n\t" \
    "ds_read_b128 v[96:99], v112 offset:6144\n\t" \
    "v_exp_f32_e32 v40, v40\n\t" \
    "v_cvt_pk_bf16_f32 v106, v36, v37\n\t" \
    "v_cvt_pk_bf16_f32 v107, v38, v39\n\t" \
    "v_exp_f32_e32 v41, v41\n\t" \
    "v_exp_f32_e32 v42, v42\n\t" \
    "v_exp_f32_e32 v43, v43\n\t" \
    "v_mfma_f32_32x32x16_bf16 v[0:15], v[88:91], v[104:107], 0\n\t" \
    "v_exp_f32_e32 v44, v44\n\t" \
    "v_exp_f32_e32 v45, v45\n\t" \
    "v_exp_f32_e32 v46, v46\n\t" \
    "v_exp_f32_e32 v47, v47\n\t" \
    "v_cvt_pk_bf16_f32 v108, v40, v41\n\t" \
    "v_cvt_pk_bf16_f32 v109, v42, v43\n\t" \
    "v_cvt_pk_bf16_f32 v110, v44, v45\n\t" \
    "v_cvt_pk_bf16_f32 v111, v46, v47\n\t" \
    "s_mov_b32 m0, %[fld]\n\t" \
    "s_nop 0\n\t" \
    "global_load_lds_dwordx4 v142, %[ws] sc1\n\t" \
    "s_waitcnt lgkmcnt(0)\n\t" \
    "v_mfma_f32_32x32x16_bf16 v[32:47], v[80:83], v[64:67], v[16:31]\n\t" \
    "v_exp_f32_e32 v48, v48\n\t" \
    "v_exp_f32_e32 v49, v49\n\t" \
    "v_exp_f32_e32 v50, v50\n\t" \
    "v_smfmac_f32_16x16x64_bf16 v[76:79], v[72:75], v[104:111], v174\n\t" \
    "v_exp_f32_e32 v51, v51\n\t" \
    "v_exp_f32_e32 v52, v52\n\t" \
    "v_mfma_f32_32x32x16_bf16 v[32:47], v[84:87], v[68:71], v[32:47]\n\t" \
    "ds_read_b128 v[80:83], v112 offset:12288\n\t" \
    "ds_read_b128 v[84:87], v112 offset:13312\n\t" \
    "v_cvt_pk_bf16_f32 v104, v48, v49\n\t" \
    "v_cvt_pk_bf16_f32 v105, v50, v51\n\t" \
    "v_exp_f32_e32 v53, v53\n\t" \
    "v_exp_f32_e32 v54, v54\n\t" \
    "v_exp_f32_e32 v55, v55\n\t" \
    "v_mfma_f32_32x32x16_bf16 v[0:15], v[100:103], v[108:111], v[0:15]\n\t" \
    "ds_read_b128 v[92:95], v112 offset:7168\n\t" \
    "ds_read_b128 v[88:91], v112 offset:10240\n\t" \
    "v_exp_f32_e32 v56, v56\n\t" \
    "v_cvt_pk_bf16_f32 v106, v52, v53\n\t" \
    "v_cvt_pk_bf16_f32 v107, v54, v55\n\t" \
    "v_exp_f32_e32 v57, v57\n\t" \
    "v_exp_f32_e32 v58, v58\n\t" \
    "v_exp_f32_e32 v59, v59\n\t" \
    "v_mfma_f32_32x32x16_bf16 v[0:15], v[96:99], v[104:107], v[0:15]\n\t" \
    "v_exp_f32_e32 v60, v60\n\t" \
    "v_exp_f32_e32 v61, v61\n\t" \
    "v_exp_f32_e32 v62, v62\n\t" \
    "v_exp_f32_e32 v63, v63\n\t" \
    "v_cvt_pk_bf16_f32 v108, v56, v57\n\t" \
    "v_cvt_pk_bf16_f32 v109, v58, v59\n\t" \
    "v_cvt_pk_bf16_f32 v110, v60, v61\n\t" \
    "v_cvt_pk_bf16_f32 v111, v62, v63\n\t" \
    "s_setprio 2\n\t" \
    "s_waitcnt lgkmcnt(0)\n\t" \
    "v_mfma_f32_32x32x16_bf16 v[48:63], v[80:83], v[64:67], v[16:31]\n\t" \
    "v_exp_f32_e32 v32, v32\n\t" \
    "v_exp_f32_e32 v33, v33\n\t" \
    "v_exp_f32_e32 v34, v34\n\t" \
    "v_smfmac_f32_16x16x64_bf16 v[76:79], v[72:75], v[104:111], v174\n\t" \
    "v_exp_f32_e32 v35, v35\n\t" \
    "v_exp_f32_e32 v36, v36\n\t" \
    "v_mfma_f32_32x32x16_bf16 v[48:63], v[84:87], v[68:71], v[48:63]\n\t" \
    "ds_read_b128 v[80:83], v112 offset:16384\n\t" \
    "ds_read_b128 v[84:87], v112 offset:17408\n\t" \
    "v_cvt_pk_bf16_f32 v104, v32, v33\n\t" \
    "v_cvt_pk_bf16_f32 v105, v34, v35\n\t" \
    "v_exp_f32_e32 v37, v37\n\t" \
    "v_exp_f32_e32 v38, v38\n\t" \
    "v_exp_f32_e32 v39, v39\n\t" \
    "v_mfma_f32_32x32x16_bf16 v[0:15], v[92:95], v[108:111], v[0:15]\n\t" \
    "ds_read_b128 v[100:103], v112 offset:11264\n\t" \
    "ds_read_b128 v[96:99], v112 offset:14336\n\t" \
    "v_exp_f32_e32 v40, v40\n\t" \
    "v_cvt_pk_bf16_f32 v106, v36, v37\n\t" \
    "v_cvt_pk_bf16_f32 v107, v38, v39\n\t" \
    "v_exp_f32_e32 v41, v41\n\t" \
    "v_exp_f32_e32 v42, v42\n\t" \
    "v_exp_f32_e32 v43, v43\n\t" \
    "v_mfma_f32_32x32x16_bf16 v[0:15], v[88:91], v[104:107], v[0:15]\n\t" \
    "v_exp_f32_e32 v44, v44\n\t" \
    "v_exp_f32_e32 v45, v45\n\t" \
    "v_exp_f32_e32 v46, v46\n\t" \
    "v_exp_f32_e32 v47, v47\n\t" \
    "v_cvt_pk_bf16_f32 v108, v40, v41\n\t" \
    "v_cvt_pk_bf16_f32 v109, v42, v43\n\t" \
    "v_cvt_pk_bf16_f32 v110, v44, v45\n\t" \
    "v_cvt_pk_bf16_f32 v111, v46, v47\n\t" \
    "s_waitcnt lgkmcnt(0)\n\t" \
    "v_mfma_f32_32x32x16_bf16 v[32:47], v[80:83], v[64:67], v[16:31]\n\t" \
    "v_exp_f32_e32 v48, v48\n\t" \
    "v_exp_f32_e32 v49, v49\n\t" \
    "v_exp_f32_e32 v50, v50\n\t" \
    "v_smfmac_f32_16x16x64_bf16 v[76:79], v[72:75], v[104:111], v174\n\t" \
    "v_exp_f32_e32 v51, v51\n\t" \
    "v_exp_f32_e32 v52, v52\n\t" \
    "v_mfma_f32_32x32x16_bf16 v[32:47], v[84:87], v[68:71], v[32:47]\n\t" \
    "ds_read_b128 v[80:83], v112 offset:20480\n\t" \
    "ds_read_b128 v[84:87], v112 offset:21504\n\t" \
    "v_cvt_pk_bf16_f32 v104, v48, v49\n\t" \
    "v_cvt_pk_bf16_f32 v105, v50, v51\n\t" \
    "v_exp_f32_e32 v53, v53\n\t" \
    "v_exp_f32_e32 v54, v54\n\t" \
    "v_exp_f32_e32 v55, v55\n\t" \
    "v_mfma_f32_32x32x16_bf16 v[0:15], v[100:103], v[108:111], v[0:15]\n\t" \
    "ds_read_b128 v[92:95], v112 offset:15360\n\t" \
    "ds_read_b128 v[88:91], v112 offset:18432\n\t" \
    "v_exp_f32_e32 v56, v56\n\t" \
    "v_cvt_pk_bf16_f32 v106, v52, v53\n\t" \
    "v_cvt_pk_bf16_f32 v107, v54, v55\n\t" \
    "v_exp_f32_e32 v57, v57\n\t" \
    "v_exp_f32_e32 v58, v58\n\t" \
    "v_exp_f32_e32 v59, v59\n\t" \
    "v_mfma_f32_32x32x16_bf16 v[0:15], v[96:99], v[104:107], v[0:15]\n\t" \
    "v_exp_f32_e32 v60, v60\n\t" \
    "v_exp_f32_e32 v61, v61\n\t" \
    "v_exp_f32_e32 v62, v62\n\t" \
    "v_exp_f32_e32 v63, v63\n\t" \
    "v_cvt_pk_bf16_f32 v108, v56, v57\n\t" \
    "v_cvt_pk_bf16_f32 v109, v58, v59\n\t" \
    "v_cvt_pk_bf16_f32 v110, v60, v61\n\t" \
    "v_cvt_pk_bf16_f32 v111, v62, v63\n\t" \
    "s_setprio 1\n\t" \
    "s_waitcnt lgkmcnt(0)\n\t" \
    "v_mfma_f32_32x32x16_bf16 v[48:63], v[80:83], v[64:67], v[16:31]\n\t" \
    "v_exp_f32_e32 v32, v32\n\t" \
    "v_exp_f32_e32 v33, v33\n\t" \
    "v_exp_f32_e32 v34, v34\n\t" \
    "v_smfmac_f32_16x16x64_bf16 v[76:79], v[72:75], v[104:111], v174\n\t" \
    "v_exp_f32_e32 v35, v35\n\t" \
    "v_exp_f32_e32 v36, v36\n\t" \
    "v_mfma_f32_32x32x16_bf16 v[48:63], v[84:87], v[68:71], v[48:63]\n\t" \
    "ds_read_b128 v[80:83], v112 offset:24576\n\t" \
    "ds_read_b128 v[84:87], v112 offset:25600\n\t" \
    "v_cvt_pk_bf16_f32 v104, v32, v33\n\t" \
    "v_cvt_pk_bf16_f32 v105, v34, v35\n\t" \
    "v_exp_f32_e32 v37, v37\n\t" \
    "v_exp_f32_e32 v38, v38\n\t" \
    "v_exp_f32_e32 v39, v39\n\t" \
    "v_mfma_f32_32x32x16_bf16 v[0:15], v[92:95], v[108:111], v[0:15]\n\t" \
    "ds_read_b128 v[100:103], v112 offset:19456\n\t" \
    "ds_read_b128 v[96:99], v112 offset:22528\n\t" \
    "v_exp_f32_e32 v40, v40\n\t" \
    "v_cvt_pk_bf16_f32 v106, v36, v37\n\t" \
    "v_cvt_pk_bf16_f32 v107, v38, v39\n\t" \
    "v_exp_f32_e32 v41, v41\n\t" \
    "v_exp_f32_e32 v42, v42\n\t" \
    "v_exp_f32_e32 v43, v43\n\t" \
    "v_mfma_f32_32x32x16_bf16 v[0:15], v[88:91], v[104:107], v[0:15]\n\t" \
    "v_exp_f32_e32 v44, v44\n\t" \
    "v_exp_f32_e32 v45, v45\n\t" \
    "v_exp_f32_e32 v46, v46\n\t" \
    "v_exp_f32_e32 v47, v47\n\t" \
    "v_cvt_pk_bf16_f32 v108, v40, v41\n\t" \
    "v_cvt_pk_bf16_f32 v109, v42, v43\n\t" \
    "v_cvt_pk_bf16_f32 v110, v44, v45\n\t" \
    "v_cvt_pk_bf16_f32 v111, v46, v47\n\t" \
    "s_waitcnt lgkmcnt(0)\n\t" \
    "v_mfma_f32_32x32x16_bf16 v[32:47], v[80:83], v[64:67], v[16:31]\n\t" \
    "v_exp_f32_e32 v48, v48\n\t" \
    "v_exp_f32_e32 v49, v49\n\t" \
    "v_exp_f32_e32 v50, v50\n\t" \
    "v_smfmac_f32_16x16x64_bf16 v[76:79], v[72:75], v[104:111], v174\n\t" \
    "v_exp_f32_e32 v51, v51\n\t" \
    "v_exp_f32_e32 v52, v52\n\t" \
    "v_mfma_f32_32x32x16_bf16 v[32:47], v[84:87], v[68:71], v[32:47]\n\t" \
    "ds_read_b128 v[80:83], v112 offset:28672\n\t" \
    "ds_read_b128 v[84:87], v112 offset:29696\n\t" \
    "v_cvt_pk_bf16_f32 v104, v48, v49\n\t" \
    "v_cvt_pk_bf16_f32 v105, v50, v51\n\t" \
    "v_exp_f32_e32 v53, v53\n\t" \
    "v_exp_f32_e32 v54, v54\n\t" \
    "v_exp_f32_e32 v55, v55\n\t" \
    "v_mfma_f32_32x32x16_bf16 v[0:15], v[100:103], v[108:111], v[0:15]\n\t" \
    "ds_read_b128 v[92:95], v112 offset:23552\n\t" \
    "ds_read_b128 v[88:91], v112 offset:26624\n\t" \
    "v_exp_f32_e32 v56, v56\n\t" \
    "v_cvt_pk_bf16_f32 v106, v52, v53\n\t" \
    "v_cvt_pk_bf16_f32 v107, v54, v55\n\t" \
    "v_exp_f32_e32 v57, v57\n\t" \
    "v_exp_f32_e32 v58, v58\n\t" \
    "v_exp_f32_e32 v59, v59\n\t" \
    "v_mfma_f32_32x32x16_bf16 v[0:15], v[96:99], v[104:107], v[0:15]\n\t" \
    "v_exp_f32_e32 v60, v60\n\t" \
    "v_exp_f32_e32 v61, v61\n\t" \
    "v_exp_f32_e32 v62, v62\n\t" \
    "v_exp_f32_e32 v63, v63\n\t" \
    "v_cvt_pk_bf16_f32 v108, v56, v57\n\t" \
    "v_cvt_pk_bf16_f32 v109, v58, v59\n\t" \
    "v_cvt_pk_bf16_f32 v110, v60, v61\n\t" \
    "v_cvt_pk_bf16_f32 v111, v62, v63\n\t" \
    "s_setprio 0\n\t" \
    "s_cmp_eq_u32 %[hl], 0\n\t" \
    "s_cbranch_scc1 11f\n\t" \
    "s_waitcnt vmcnt(1)\n\t" \
    "11:\n" \
    "s_waitcnt lgkmcnt(0)\n\t" \
    "s_nop 15\n\t"
#define ATT_L4 \
    "s_nop 1\n\t" \
    "s_setprio 3\n\t" \
    "s_waitcnt lgkmcnt(0)\n\t" \
    "v_mfma_f32_32x32x16_bf16 v[48:63], v[80:83], v[64:67], v[16:31]\n\t" \
    "v_exp_f32_e32 v32, v32\n\t" \
    "v_exp_f32_e32 v33, v33\n\t" \
    "v_exp_f32_e32 v34, v34\n\t" \
    "v_exp_f32_e32 v35, v35\n\t" \
    "v_exp_f32_e32 v36, v36\n\t" \
    "v_mfma_f32_32x32x16_bf16 v[48:63], v[84:87], v[68:71], v[48:63]\n\t" \
    "ds_read_b128 v[80:83], v112 offset:8192\n\t" \
    "ds_read_b128 v[84:87], v112 offset:9216\n\t" \
    "v_cvt_pk_bf16_f32 v104, v32, v33\n\t" \
    "v_cvt_pk_bf16_f32 v105, v34, v35\n\t" \
    "v_exp_f32_e32 v37, v37\n\t" \
    "v_exp_f32_e32 v38, v38\n\t" \
    "v_exp_f32_e32 v39, v39\n\t" \
    "ds_read_b128 v[100:103], v112 offset:3072\n\t" \
    "ds_read_b128 v[96:99], v112 offset:6144\n\t" \
    "v_exp_f32_e32 v40, v40\n\t" \
    "v_cvt_pk_bf16_f32 v106, v36, v37\n\t" \
    "v_cvt_pk_bf16_f32 v107, v38, v39\n\t" \
    "v_exp_f32_e32 v41, v41\n\t" \
    "v_exp_f32_e32 v42, v42\n\t" \
    "v_exp_f32_e32 v43, v43\n\t" \
    "v_mfma_f32_32x32x16_bf16 v[0:15], v[88:91], v[104:107], 0\n\t" \
    "v_exp_f32_e32 v44, v44\n\t" \
    "v_exp_f32_e32 v45, v45\n\t" \
    "v_exp_f32_e32 v46, v46\n\t" \
    "v_exp_f32_e32 v47, v47\n\t" \
    "v_cvt_pk_bf16_f32 v108, v40, v41\n\t" \
    "v_cvt_pk_bf16_f32 v109, v42, v43\n\t" \
    "v_cvt_pk_bf16_f32 v110, v44, v45\n\t" \
    "v_cvt_pk_bf16_f32 v111, v46, v47\n\t" \
    "s_waitcnt lgkmcnt(0)\n\t" \
    "v_mfma_f32_32x32x16_bf16 v[32:47], v[80:83], v[64:67], v[16:31]\n\t" \
    "v_exp_f32_e32 v48, v48\n\t" \
    "v_exp_f32_e32 v49, v49\n\t" \
    "v_exp_f32_e32 v50, v50\n\t" \
    "v_smfmac_f32_16x16x64_bf16 v[76:79], v[72:75], v[104:111], v174\n\t" \
    "v_exp_f32_e32 v51, v51\n\t" \
    "v_exp_f32_e32 v52, v52\n\t" \
    "v_mfma_f32_32x32x16_bf16 v[32:47], v[84:87], v[68:71], v[32:47]\n\t" \
    "ds_read_b128 v[80:83], v112 offset:12288\n\t" \
    "ds_read_b128 v[84:87], v112 offset:13312\n\t" \
    "v_cvt_pk_bf16_f32 v104, v48, v49\n\t" \
    "v_cvt_pk_bf16_f32 v105, v50, v51\n\t" \
    "v_exp_f32_e32 v53, v53\n\t" \
    "v_exp_f32_e32 v54, v54\n\t" \
    "v_exp_f32_e32 v55, v55\n\t" \
    "v_mfma_f32_32x32x16_bf16 v[0:15], v[100:103], v[108:111], v[0:15]\n\t" \
    "ds_read_b128 v[92:95], v112 offset:7168\n\t" \
    "ds_read_b128 v[88:91], v112 offset:10240\n\t" \
    "v_exp_f32_e32 v56, v56\n\t" \
    "v_cvt_pk_bf16_f32 v106, v52, v53\n\t" \
    "v_cvt_pk_bf16_f32 v107, v54, v55\n\t" \
    "v_exp_f32_e32 v57, v57\n\t" \
    "v_exp_f32_e32 v58, v58\n\t" \
    "v_exp_f32_e32 v59, v59\n\t" \
    "v_mfma_f32_32x32x16_bf16 v[0:15], v[96:99], v[104:107], v[0:15]\n\t" \
    "v_exp_f32_e32 v60, v60\n\t" \
    "v_exp_f32_e32 v61, v61\n\t" \
    "v_exp_f32_e32 v62, v62\n\t" \
    "v_exp_f32_e32 v63, v63\n\t" \
    "v_cvt_pk_bf16_f32 v108, v56, v57\n\t" \
    "v_cvt_pk_bf16_f32 v109, v58, v59\n\t" \
    "v_cvt_pk_bf16_f32 v110, v60, v61\n\t" \
    "v_cvt_pk_bf16_f32 v111, v62, v63\n\t" \
    "s_setprio 2\n\t" \
    "s_waitcnt lgkmcnt(0)\n\t" \
    "v_mfma_f32_32x32x16_bf16 v[48:63], v[80:83], v[64:67], v[16:31]\n\t" \
    "v_exp_f32_e32 v32, v32\n\t" \
    "v_exp_f32_e32 v33, v33\n\t" \
    "v_exp_f32_e32 v34, v34\n\t" \
    "v_smfmac_f32_16x16x64_bf16 v[76:79], v[72:75], v[104:111], v174\n\t" \
    "v_exp_f32_e32 v35, v35\n\t" \
    "v_exp_f32_e32 v36, v36\n\t" \
    "v_mfma_f32_32x32x16_bf16 v[48:63], v[84:87], v[68:71], v[48:63]\n\t" \
    "ds_read_b128 v[80:83], v112 offset:16384\n\t" \
    "ds_read_b128 v[84:87], v112 offset:17408\n\t" \
    "v_cvt_pk_bf16_f32 v104, v32, v33\n\t" \
    "v_cvt_pk_bf16_f32 v105, v34, v35\n\t" \
    "v_exp_f32_e32 v37, v37\n\t" \
    "v_exp_f32_e32 v38, v38\n\t" \
    "v_exp_f32_e32 v39, v39\n\t" \
    "v_mfma_f32_32x32x16_bf16 v[0:15], v[92:95], v[108:111], v[0:15]\n\t" \
    "ds_read_b128 v[100:103], v112 offset:11264\n\t" \
    "ds_read_b128 v[96:99], v112 offset:14336\n\t" \
    "v_exp_f32_e32 v40, v40\n\t" \
    "v_cvt_pk_bf16_f32 v106, v36, v37\n\t" \
    "v_cvt_pk_bf16_f32 v107, v38, v39\n\t" \
    "v_exp_f32_e32 v41, v41\n\t" \
    "v_exp_f32_e32 v42, v42\n\t" \
    "v_exp_f32_e32 v43, v43\n\t" \
    "v_mfma_f32_32x32x16_bf16 v[0:15], v[88:91], v[104:107], v[0:15]\n\t" \
    "v_exp_f32_e32 v44, v44\n\t" \
    "v_exp_f32_e32 v45, v45\n\t" \
    "v_exp_f32_e32 v46, v46\n\t" \
    "v_exp_f32_e32 v47, v47\n\t" \
    "v_cvt_pk_bf16_f32 v108, v40, v41\n\t" \
    "v_cvt_pk_bf16_f32 v109, v42, v43\n\t" \
    "v_cvt_pk_bf16_f32 v110, v44, v45\n\t" \
    "v_cvt_pk_bf16_f32 v111, v46, v47\n\t" \
    "s_waitcnt lgkmcnt(0)\n\t" \
    "v_mfma_f32_32x32x16_bf16 v[32:47], v[80:83], v[64:67], v[16:31]\n\t" \
    "v_exp_f32_e32 v48, v48\n\t" \
    "v_exp_f32_e32 v49, v49\n\t" \
    "v_exp_f32_e32 v50, v50\n\t" \
    "v_smfmac_f32_16x16x64_bf16 v[76:79], v[72:75], v[104:111], v174\n\t" \
    "v_exp_f32_e32 v51, v51\n\t" \
    "v_exp_f32_e32 v52, v52\n\t" \
    "v_mfma_f32_32x32x16_bf16 v[32:47], v[84:87], v[68:71], v[32:47]\n\t" \
    "ds_read_b128 v[80:83], v112 offset:20480\n\t" \
    "ds_read_b128 v[84:87], v112 offset:21504\n\t" \
    "v_cvt_pk_bf16_f32 v104, v48, v49\n\t" \
    "v_cvt_pk_bf16_f32 v105, v50, v51\n\t" \
    "v_exp_f32_e32 v53, v53\n\t" \
    "v_exp_f32_e32 v54, v54\n\t" \
    "v_exp_f32_e32 v55, v55\n\t" \
    "v_mfma_f32_32x32x16_bf16 v[0:15], v[100:103], v[108:111], v[0:15]\n\t" \
    "ds_read_b128 v[92:95], v112 offset:15360\n\t" \
    "ds_read_b128 v[88:91], v112 offset:18432\n\t" \
    "v_exp_f32_e32 v56, v56\n\t" \
    "v_cvt_pk_bf16_f32 v106, v52, v53\n\t" \
    "v_cvt_pk_bf16_f32 v107, v54, v55\n\t" \
    "v_exp_f32_e32 v57, v57\n\t" \
    "v_exp_f32_e32 v58, v58\n\t" \
    "v_exp_f32_e32 v59, v59\n\t" \
    "v_mfma_f32_32x32x16_bf16 v[0:15], v[96:99], v[104:107], v[0:15]\n\t" \
    "v_exp_f32_e32 v60, v60\n\t" \
    "v_exp_f32_e32 v61, v61\n\t" \
    "v_exp_f32_e32 v62, v62\n\t" \
    "v_exp_f32_e32 v63, v63\n\t" \
    "v_cvt_pk_bf16_f32 v108, v56, v57\n\t" \
    "v_cvt_pk_bf16_f32 v109, v58, v59\n\t" \
    "v_cvt_pk_bf16_f32 v110, v60, v61\n\t" \
    "v_cvt_pk_bf16_f32 v111, v62, v63\n\t" \
    "s_setprio 0\n\t" \
    "s_waitcnt lgkmcnt(0)\n\t" \
    "s_nop 15\n\t"
#define ATT_L2 \
    "s_nop 1\n\t" \
    "s_setprio 1\n\t" \
    "s_waitcnt lgkmcnt(0)\n\t" \
    "v_mfma_f32_32x32x16_bf16 v[48:63], v[80:83], v[64:67], v[16:31]\n\t" \
    "v_exp_f32_e32 v32, v32\n\t" \
    "v_exp_f32_e32 v33, v33\n\t" \
    "v_exp_f32_e32 v34, v34\n\t" \
    "v_smfmac_f32_16x16x64_bf16 v[76:79], v[72:75], v[104:111], v174\n\t" \
    "v_exp_f32_e32 v35, v35\n\t" \
    "v_exp_f32_e32 v36, v36\n\t" \
    "v_mfma_f32_32x32x16_bf16 v[48:63], v[84:87], v[68:71], v[48:63]\n\t" \
    "ds_read_b128 v[80:83], v112 offset:24576\n\t" \
    "ds_read_b128 v[84:87], v112 offset:25600\n\t" \
    "v_cvt_pk_bf16_f32 v104, v32, v33\n\t" \
    "v_cvt_pk_bf16_f32 v105, v34, v35\n\t" \
    "v_exp_f32_e32 v37, v37\n\t" \
    "v_exp_f32_e32 v38, v38\n\t" \
    "v_exp_f32_e32 v39, v39\n\t" \
    "v_mfma_f32_32x32x16_bf16 v[0:15], v[92:95], v[108:111], v[0:15]\n\t" \
    "ds_read_b128 v[100:103], v112 offset:19456\n\t" \
    "ds_read_b128 v[96:99], v112 offset:22528\n\t" \
    "v_exp_f32_e32 v40, v40\n\t" \
    "v_cvt_pk_bf16_f32 v106, v36, v37\n\t" \
    "v_cvt_pk_bf16_f32 v107, v38, v39\n\t" \
    "v_exp_f32_e32 v41, v41\n\t" \
    "v_exp_f32_e32 v42, v42\n\t" \
    "v_exp_f32_e32 v43, v43\n\t" \
    "v_mfma_f32_32x32x16_bf16 v[0:15], v[88:91], v[104:107], v[0:15]\n\t" \
    "v_exp_f32_e32 v44, v44\n\t" \
    "v_exp_f32_e32 v45, v45\n\t" \
    "v_exp_f32_e32 v46, v46\n\t" \
    "v_exp_f32_e32 v47, v47\n\t" \
    "v_cvt_pk_bf16_f32 v108, v40, v41\n\t" \
    "v_cvt_pk_bf16_f32 v109, v42, v43\n\t" \
    "v_cvt_pk_bf16_f32 v110, v44, v45\n\t" \
    "v_cvt_pk_bf16_f32 v111, v46, v47\n\t" \
    "s_waitcnt lgkmcnt(0)\n\t" \
    "v_mfma_f32_32x32x16_bf16 v[32:47], v[80:83], v[64:67], v[16:31]\n\t" \
    "v_exp_f32_e32 v48, v48\n\t" \
    "v_exp_f32_e32 v49, v49\n\t" \
    "v_exp_f32_e32 v50, v50\n\t" \
    "v_smfmac_f32_16x16x64_bf16 v[76:79], v[72:75], v[104:111], v174\n\t" \
    "v_exp_f32_e32 v51, v51\n\t" \
    "v_exp_f32_e32 v52, v52\n\t" \
    "v_mfma_f32_32x32x16_bf16 v[32:47], v[84:87], v[68:71], v[32:47]\n\t" \
    "ds_read_b128 v[80:83], v112 offset:28672\n\t" \
    "ds_read_b128 v[84:87], v112 offset:29696\n\t" \
    "v_cvt_pk_bf16_f32 v104, v48, v49\n\t" \
    "v_cvt_pk_bf16_f32 v105, v50, v51\n\t" \
    "v_exp_f32_e32 v53, v53\n\t" \
    "v_exp_f32_e32 v54, v54\n\t" \
    "v_exp_f32_e32 v55, v55\n\t" \
    "v_mfma_f32_32x32x16_bf16 v[0:15], v[100:103], v[108:111], v[0:15]\n\t" \
    "ds_read_b128 v[92:95], v112 offset:23552\n\t" \
    "ds_read_b128 v[88:91], v112 offset:26624\n\t" \
    "v_exp_f32_e32 v56, v56\n\t" \
    "v_cvt_pk_bf16_f32 v106, v52, v53\n\t" \
    "v_cvt_pk_bf16_f32 v107, v54, v55\n\t" \
    "v_exp_f32_e32 v57, v57\n\t" \
    "v_exp_f32_e32 v58, v58\n\t" \
    "v_exp_f32_e32 v59, v59\n\t" \
    "v_mfma_f32_32x32x16_bf16 v[0:15], v[96:99], v[104:107], v[0:15]\n\t" \
    "v_exp_f32_e32 v60, v60\n\t" \
    "v_exp_f32_e32 v61, v61\n\t" \
    "v_exp_f32_e32 v62, v62\n\t" \
    "v_exp_f32_e32 v63, v63\n\t" \
    "v_cvt_pk_bf16_f32 v108, v56, v57\n\t" \
    "v_cvt_pk_bf16_f32 v109, v58, v59\n\t" \
    "v_cvt_pk_bf16_f32 v110, v60, v61\n\t" \
    "v_cvt_pk_bf16_f32 v111, v62, v63\n\t" \
    "s_setprio 0\n\t" \
    "s_waitcnt lgkmcnt(0)\n\t" \
    "s_nop 15\n\t"
#define ATT_XT \
    "s_nop 1\n\t" \
    "s_waitcnt lgkmcnt(0)\n\t" \
    "v_mfma_f32_32x32x16_bf16 v[48:63], v[80:83], v[64:67], v[16:31]\n\t" \
    "v_exp_f32_e32 v32, v32\n\t" \
    "v_exp_f32_e32 v33, v33\n\t" \
    "v_exp_f32_e32 v34, v34\n\t" \
    "v_smfmac_f32_16x16x64_bf16 v[76:79], v[72:75], v[104:111], v174\n\t" \
    "v_exp_f32_e32 v35, v35\n\t" \
    "v_exp_f32_e32 v36, v36\n\t" \
    "v_mfma_f32_32x32x16_bf16 v[48:63], v[84:87], v[68:71], v[48:63]\n\t" \
    "v_cvt_pk_bf16_f32 v104, v32, v33\n\t" \
    "v_cvt_pk_bf16_f32 v105, v34, v35\n\t" \
    "v_exp_f32_e32 v37, v37\n\t" \
    "v_exp_f32_e32 v38, v38\n\t" \
    "v_exp_f32_e32 v39, v39\n\t" \
    "v_mfma_f32_32x32x16_bf16 v[0:15], v[92:95], v[108:111], v[0:15]\n\t" \
    "ds_read_b128 v[100:103], v112 offset:27648\n\t" \
    "ds_read_b128 v[96:99], v112 offset:30720\n\t" \
    "v_exp_f32_e32 v40, v40\n\t" \
    "v_cvt_pk_bf16_f32 v106, v36, v37\n\t" \
    "v_cvt_pk_bf16_f32 v107, v38, v39\n\t" \
    "v_exp_f32_e32 v41, v41\n\t" \
    "v_exp_f32_e32 v42, v42\n\t" \
    "v_exp_f32_e32 v43, v43\n\t" \
    "v_mfma_f32_32x32x16_bf16 v[0:15], v[88:91], v[104:107], v[0:15]\n\t" \
    "v_exp_f32_e32 v44, v44\n\t" \
    "v_exp_f32_e32 v45, v45\n\t" \
    "v_exp_f32_e32 v46, v46\n\t" \
    "v_exp_f32_e32 v47, v47\n\t" \
    "v_cvt_pk_bf16_f32 v108, v40, v41\n\t" \
    "v_cvt_pk_bf16_f32 v109, v42, v43\n\t" \
    "v_cvt_pk_bf16_f32 v110, v44, v45\n\t" \
    "v_cvt_pk_bf16_f32 v111, v46, v47\n\t" \
    "s_waitcnt lgkmcnt(0)\n\t" \
    "v_exp_f32_e32 v48, v48\n\t" \
    "v_exp_f32_e32 v49, v49\n\t" \
    "v_exp_f32_e32 v50, v50\n\t" \
    "v_smfmac_f32_16x16x64_bf16 v[76:79], v[72:75], v[104:111], v174\n\t" \
    "v_exp_f32_e32 v51, v51\n\t" \
    "v_exp_f32_e32 v52, v52\n\t" \
    "v_cvt_pk_bf16_f32 v104, v48, v49\n\t" \
    "v_cvt_pk_bf16_f32 v105, v50, v51\n\t" \
    "v_exp_f32_e32 v53, v53\n\t" \
    "v_exp_f32_e32 v54, v54\n\t" \
    "v_exp_f32_e32 v55, v55\n\t" \
    "v_mfma_f32_32x32x16_bf16 v[0:15], v[100:103], v[108:111], v[0:15]\n\t" \
    "ds_read_b128 v[92:95], v112 offset:31744\n\t" \
    "v_exp_f32_e32 v56, v56\n\t" \
    "v_cvt_pk_bf16_f32 v106, v52, v53\n\t" \
    "v_cvt_pk_bf16_f32 v107, v54, v55\n\t" \
    "v_exp_f32_e32 v57, v57\n\t" \
    "v_exp_f32_e32 v58, v58\n\t" \
    "v_exp_f32_e32 v59, v59\n\t" \
    "v_mfma_f32_32x32x16_bf16 v[0:15], v[96:99], v[104:107], v[0:15]\n\t" \
    "v_exp_f32_e32 v60, v60\n\t" \
    "v_exp_f32_e32 v61, v61\n\t" \
    "v_exp_f32_e32 v62, v62\n\t" \
    "v_exp_f32_e32 v63, v63\n\t" \
    "v_cvt_pk_bf16_f32 v108, v56, v57\n\t" \
    "v_cvt_pk_bf16_f32 v109, v58, v59\n\t" \
    "v_cvt_pk_bf16_f32 v110, v60, v61\n\t" \
    "v_cvt_pk_bf16_f32 v111, v62, v63\n\t" \
    "s_waitcnt lgkmcnt(0)\n\t" \
    "s_nop 0\n\t" \
    "v_mfma_f32_32x32x16_bf16 v[0:15], v[92:95], v[108:111], v[0:15]\n\t" \
    "v_smfmac_f32_16x16x64_bf16 v[76:79], v[72:75], v[104:111], v174\n\t" \
    "s_nop 15\n\t"
#define ATT_MAIN \
    "s_mov_b64 s[48:49], %[tab]\n\t" \
    "s_mov_b32 s46, %[k0]\n\t" \
    "s_and_b32 s40, s48, 63\n\t" \
    "s_lshl_b32 s40, s40, 12\n\t" \
    "s_add_u32 s44, %[srclo], s40\n\t" \
    "s_addc_u32 s45, %[srchi], 0\n\t" \
    "s_lshr_b64 s[48:49], s[48:49], 6\n\t" \
    "s_add_u32 s46, s46, 1\n\t" \
    "s_mov_b64 vcc, -1\n\t" \
    "s_waitcnt vmcnt(0)\n\t" \
    "ds_read_b64 v[120:121], v173\n\t" \
    "s_waitcnt lgkmcnt(0)\n\t" \
    "1:\n" \
    "s_cmp_eq_u32 %[cnt], 4\n\t" \
    "s_cbranch_scc1 5f\n\t" \
    "s_waitcnt vmcnt(0)\n\t" \
    "5:\n" \
    "s_waitcnt vmcnt(9)\n\t" \
    "s_barrier\n\t" \
    "s_bitcmp1_b32 %[act], %[cnt]\n\t" \
    "s_cbranch_scc1 30f\n\t" \
    "2:\n" \
    "s_setprio 3\n\t" \
    "s_waitcnt lgkmcnt(0)\n\t" \
    "v_mfma_f32_32x32x16_bf16 v[48:63], v[80:83], v[64:67], v[16:31]\n\t" \
    "v_exp_f32_e32 v32, v32\n\t" \
    "v_exp_f32_e32 v33, v33\n\t" \
    "v_exp_f32_e32 v34, v34\n\t" \
    "v_smfmac_f32_16x16x64_bf16 v[76:79], v[72:75], v[104:111], v174\n\t" \
    "v_exp_f32_e32 v35, v35\n\t" \
    "v_exp_f32_e32 v36, v36\n\t" \
    "v_mfma_f32_32x32x16_bf16 v[48:63], v[84:87], v[68:71], v[48:63]\n\t" \
    "ds_read_b128 v[80:83], v113 offset:0\n\t" \
    "ds_read_b128 v[84:87], v113 offset:1024\n\t" \
    "v_cvt_pk_bf16_f32 v104, v32, v33\n\t" \
    "v_cvt_pk_bf16_f32 v105, v34, v35\n\t" \
    "v_exp_f32_e32 v37, v37\n\t" \
    "v_exp_f32_e32 v38, v38\n\t" \
    "v_exp_f32_e32 v39, v39\n\t" \
    "v_mfma_f32_32x32x16_bf16 v[0:15], v[92:95], v[108:111], v[0:15]\n\t" \
    "ds_read_b128 v[100:103], v112 offset:27648\n\t" \
    "ds_read_b128 v[96:99], v112 offset:30720\n\t" \
    "v_exp_f32_e32 v40, v40\n\t" \
    "v_cvt_pk_bf16_f32 v106, v36, v37\n\t" \
    "v_cvt_pk_bf16_f32 v107, v38, v39\n\t" \
    "v_exp_f32_e32 v41, v41\n\t" \
    "v_exp_f32_e32 v42, v42\n\t" \
    "v_exp_f32_e32 v43, v43\n\t" \
    "v_mfma_f32_32x32x16_bf16 v[0:15], v[88:91], v[104:107], v[0:15]\n\t" \
    "s_cbranch_vccz 20f\n\t" \
    "v_cmp_eq_u64_e64 s[42:43], v[120:121], v[140:141]\n\t" \
    "s_mov_b32 m0, %[dst]\n\t" \
    "s_nop 3\n\t" \
    "s_bitcmp1_b64 s[42:43], s46\n\t" \
    "s_cbranch_scc0 9f\n\t" \
    "12:\n" \
    "global_load_lds_dwordx4 v114, s[44:45] offset:0 sc1\n\t" \
    "20:\n" \
    "v_exp_f32_e32 v44, v44\n\t" \
    "v_exp_f32_e32 v45, v45\n\t" \
    "v_exp_f32_e32 v46, v46\n\t" \
    "v_exp_f32_e32 v47, v47\n\t" \
    "v_cvt_pk_bf16_f32 v108, v40, v41\n\t" \
    "v_cvt_pk_bf16_f32 v109, v42, v43\n\t" \
    "v_cvt_pk_bf16_f32 v110, v44, v45\n\t" \
    "v_cvt_pk_bf16_f32 v111, v46, v47\n\t" \
    "s_waitcnt lgkmcnt(0)\n\t" \
    "v_mfma_f32_32x32x16_bf16 v[32:47], v[80:83], v[64:67], v[16:31]\n\t" \
    "v_exp_f32_e32 v48, v48\n\t" \
    "v_exp_f32_e32 v49, v49\n\t" \
    "v_exp_f32_e32 v50, v50\n\t" \
    "v_smfmac_f32_16x16x64_bf16 v[76:79], v[72:75], v[104:111], v174\n\t" \
    "v_exp_f32_e32 v51, v51\n\t" \
    "v_exp_f32_e32 v52, v52\n\t" \
    "v_mfma_f32_32x32x16_bf16 v[32:47], v[84:87], v[68:71], v[32:47]\n\t" \
    "ds_read_b128 v[80:83], v113 offset:4096\n\t" \
    "ds_read_b128 v[84:87], v113 offset:5120\n\t" \
    "v_cvt_pk_bf16_f32 v104, v48, v49\n\t" \
    "v_cvt_pk_bf16_f32 v105, v50, v51\n\t" \
    "v_exp_f32_e32 v53, v53\n\t" \
    "v_exp_f32_e32 v54, v54\n\t" \
    "v_exp_f32_e32 v55, v55\n\t" \
    "v_mfma_f32_32x32x16_bf16 v[0:15], v[100:103], v[108:111], v[0:15]\n\t" \
    "ds_read_b128 v[92:95], v112 offset:31744\n\t" \
    "ds_read_b128 v[88:91], v113 offset:2048\n\t" \
    "v_exp_f32_e32 v56, v56\n\t" \
    "v_cvt_pk_bf16_f32 v106, v52, v53\n\t" \
    "v_cvt_pk_bf16_f32 v107, v54, v55\n\t" \
    "v_exp_f32_e32 v57, v57\n\t" \
    "v_exp_f32_e32 v58, v58\n\t" \
    "v_exp_f32_e32 v59, v59\n\t" \
    "v_mfma_f32_32x32x16_bf16 v[0:15], v[96:99], v[104:107], v[0:15]\n\t" \
    "s_cbranch_vccz 21f\n\t" \
    "global_load_lds_dwordx4 v114, s[44:45] offset:1024 sc1\n\t" \
    "21:\n" \
    "v_exp_f32_e32 v60, v60\n\t" \
    "v_exp_f32_e32 v61, v61\n\t" \
    "v_exp_f32_e32 v62, v62\n\t" \
    "v_exp_f32_e32 v63, v63\n\t" \
    "v_cvt_pk_bf16_f32 v108, v56, v57\n\t" \
    "v_cvt_pk_bf16_f32 v109, v58, v59\n\t" \
    "v_cvt_pk_bf16_f32 v110, v60, v61\n\t" \
    "v_cvt_pk_bf16_f32 v111, v62, v63\n\t" \
    "s_setprio 2\n\t" \
    "s_waitcnt lgkmcnt(0)\n\t" \
    "v_mfma_f32_32x32x16_bf16 v[48:63], v[80:83], v[64:67], v[16:31]\n\t" \
    "v_exp_f32_e32 v32, v32\n\t" \
    "v_exp_f32_e32 v33, v33\n\t" \
    "v_exp_f32_e32 v34, v34\n\t" \
    "v_smfmac_f32_16x16x64_bf16 v[76:79], v[72:75], v[104:111], v174\n\t" \
    "v_exp_f32_e32 v35, v35\n\t" \
    "v_exp_f32_e32 v36, v36\n\t" \
    "v_mfma_f32_32x32x16_bf16 v[48:63], v[84:87], v[68:71], v[48:63]\n\t" \
    "ds_read_b128 v[80:83], v113 offset:8192\n\t" \
    "ds_read_b128 v[84:87], v113 offset:9216\n\t" \
    "v_cvt_pk_bf16_f32 v104, v32, v33\n\t" \
    "v_cvt_pk_bf16_f32 v105, v34, v35\n\t" \
    "v_exp_f32_e32 v37, v37\n\t" \
    "v_exp_f32_e32 v38, v38\n\t" \
    "v_exp_f32_e32 v39, v39\n\t" \
    "v_mfma_f32_32x32x16_bf16 v[0:15], v[92:95], v[108:111], v[0:15]\n\t" \
    "ds_read_b128 v[100:103], v113 offset:3072\n\t" \
    "ds_read_b128 v[96:99], v113 offset:6144\n\t" \
    "v_exp_f32_e32 v40, v40\n\t" \
    "v_cvt_pk_bf16_f32 v106, v36, v37\n\t" \
    "v_cvt_pk_bf16_f32 v107, v38, v39\n\t" \
    "v_exp_f32_e32 v41, v41\n\t" \
    "v_exp_f32_e32 v42, v42\n\t" \
    "v_exp_f32_e32 v43, v43\n\t" \
    "v_mfma_f32_32x32x16_bf16 v[0:15], v[88:91], v[104:107], v[0:15]\n\t" \
    "s_cbranch_vccz 22f\n\t" \
    "global_load_lds_dwordx4 v114, s[44:45] offset:2048 sc1\n\t" \
    "22:\n" \
    "v_exp_f32_e32 v44, v44\n\t" \
    "v_exp_f32_e32 v45, v45\n\t" \
    "v_exp_f32_e32 v46, v46\n\t" \
    "v_exp_f32_e32 v47, v47\n\t" \
    "v_cvt_pk_bf16_f32 v108, v40, v41\n\t" \
    "v_cvt_pk_bf16_f32 v109, v42, v43\n\t" \
    "v_cvt_pk_bf16_f32 v110, v44, v45\n\t" \
    "v_cvt_pk_bf16_f32 v111, v46, v47\n\t" \
    "s_waitcnt lgkmcnt(0)\n\t" \
    "v_mfma_f32_32x32x16_bf16 v[32:47], v[80:83], v[64:67], v[16:31]\n\t" \
    "v_exp_f32_e32 v48, v48\n\t" \
    "v_exp_f32_e32 v49, v49\n\t" \
    "v_exp_f32_e32 v50, v50\n\t" \
    "v_smfmac_f32_16x16x64_bf16 v[76:79], v[72:75], v[104:111], v174\n\t" \
    "v_exp_f32_e32 v51, v51\n\t" \
    "v_exp_f32_e32 v52, v52\n\t" \
    "v_mfma_f32_32x32x16_bf16 v[32:47], v[84:87], v[68:71], v[32:47]\n\t" \
    "ds_read_b128 v[80:83], v113 offset:12288\n\t" \
    "ds_read_b128 v[84:87], v113 offset:13312\n\t" \
    "v_cvt_pk_bf16_f32 v104, v48, v49\n\t" \
    "v_cvt_pk_bf16_f32 v105, v50, v51\n\t" \
    "v_exp_f32_e32 v53, v53\n\t" \
    "v_exp_f32_e32 v54, v54\n\t" \
    "v_exp_f32_e32 v55, v55\n\t" \
    "v_mfma_f32_32x32x16_bf16 v[0:15], v[100:103], v[108:111], v[0:15]\n\t" \
    "ds_read_b128 v[92:95], v113 offset:7168\n\t" \
    "ds_read_b128 v[88:91], v113 offset:10240\n\t" \
    "v_exp_f32_e32 v56, v56\n\t" \
    "v_cvt_pk_bf16_f32 v106, v52, v53\n\t" \
    "v_cvt_pk_bf16_f32 v107, v54, v55\n\t" \
    "v_exp_f32_e32 v57, v57\n\t" \
    "v_exp_f32_e32 v58, v58\n\t" \
    "v_exp_f32_e32 v59, v59\n\t" \
    "v_mfma_f32_32x32x16_bf16 v[0:15], v[96:99], v[104:107], v[0:15]\n\t" \
    "s_cbranch_vccz 23f\n\t" \
    "global_load_lds_dwordx4 v114, s[44:45] offset:3072 sc1\n\t" \
    "global_load_dwordx2 v[120:121], v170, %[ws] sc1\n\t" \
    "23:\n" \
    "v_exp_f32_e32 v60, v60\n\t" \
    "v_exp_f32_e32 v61, v61\n\t" \
    "v_exp_f32_e32 v62, v62\n\t" \
    "v_exp_f32_e32 v63, v63\n\t" \
    "v_cvt_pk_bf16_f32 v108, v56, v57\n\t" \
    "v_cvt_pk_bf16_f32 v109, v58, v59\n\t" \
    "v_cvt_pk_bf16_f32 v110, v60, v61\n\t" \
    "v_cvt_pk_bf16_f32 v111, v62, v63\n\t" \
    "s_setprio 1\n\t" \
    "s_waitcnt lgkmcnt(0)\n\t" \
    "v_mfma_f32_32x32x16_bf16 v[48:63], v[80:83], v[64:67], v[16:31]\n\t" \
    "v_exp_f32_e32 v32, v32\n\t" \
    "v_exp_f32_e32 v33, v33\n\t" \
    "v_exp_f32_e32 v34, v34\n\t" \
    "v_smfmac_f32_16x16x64_bf16 v[76:79], v[72:75], v[104:111], v174\n\t" \
    "v_exp_f32_e32 v35, v35\n\t" \
    "v_exp_f32_e32 v36, v36\n\t" \
    "v_mfma_f32_32x32x16_bf16 v[48:63], v[84:87], v[68:71], v[48:63]\n\t" \
    "ds_read_b128 v[80:83], v113 offset:16384\n\t" \
    "ds_read_b128 v[84:87], v113 offset:17408\n\t" \
    "v_cvt_pk_bf16_f32 v104, v32, v33\n\t" \
    "v_cvt_pk_bf16_f32 v105, v34, v35\n\t" \
    "v_exp_f32_e32 v37, v37\n\t" \
    "v_exp_f32_e32 v38, v38\n\t" \
    "v_exp_f32_e32 v39, v39\n\t" \
    "v_mfma_f32_32x32x16_bf16 v[0:15], v[92:95], v[108:111], v[0:15]\n\t" \
    "ds_read_b128 v[100:103], v113 offset:11264\n\t" \
    "ds_read_b128 v[96:99], v113 offset:14336\n\t" \
    "v_exp_f32_e32 v40, v40\n\t" \
    "v_cvt_pk_bf16_f32 v106, v36, v37\n\t" \
    "v_cvt_pk_bf16_f32 v107, v38, v39\n\t" \
    "v_exp_f32_e32 v41, v41\n\t" \
    "v_exp_f32_e32 v42, v42\n\t" \
    "v_exp_f32_e32 v43, v43\n\t" \
    "v_mfma_f32_32x32x16_bf16 v[0:15], v[88:91], v[104:107], v[0:15]\n\t" \
    "s_add_u32 %[dst], %[dst], 0x8000\n\t" \
    "s_cmp_eq_u32 %[dst], %[dend]\n\t" \
    "s_cselect_b32 %[dst], %[dbeg], %[dst]\n\t" \
    "s_add_u32 %[nx], %[nx], 0x8000\n\t" \
    "s_cmp_eq_u32 %[nx], 0x18000\n\t" \
    "s_cselect_b32 %[nx], 0, %[nx]\n\t" \
    "s_bitcmp1_b32 %[hk], %[cnt]\n\t" \
    "s_cbranch_scc1 40f\n\t" \
    "24:\n" \
    "v_exp_f32_e32 v44, v44\n\t" \
    "v_exp_f32_e32 v45, v45\n\t" \
    "v_exp_f32_e32 v46, v46\n\t" \
    "v_exp_f32_e32 v47, v47\n\t" \
    "v_cvt_pk_bf16_f32 v108, v40, v41\n\t" \
    "v_cvt_pk_bf16_f32 v109, v42, v43\n\t" \
    "v_cvt_pk_bf16_f32 v110, v44, v45\n\t" \
    "v_cvt_pk_bf16_f32 v111, v46, v47\n\t" \
    "s_waitcnt lgkmcnt(0)\n\t" \
    "v_mfma_f32_32x32x16_bf16 v[32:47], v[80:83], v[64:67], v[16:31]\n\t" \
    "v_exp_f32_e32 v48, v48\n\t" \
    "v_exp_f32_e32 v49, v49\n\t" \
    "v_exp_f32_e32 v50, v50\n\t" \
    "v_smfmac_f32_16x16x64_bf16 v[76:79], v[72:75], v[104:111], v174\n\t" \
    "v_exp_f32_e32 v51, v51\n\t" \
    "v_exp_f32_e32 v52, v52\n\t" \
    "v_mfma_f32_32x32x16_bf16 v[32:47], v[84:87], v[68:71], v[32:47]\n\t" \
    "ds_read_b128 v[80:83], v113 offset:20480\n\t" \
    "ds_read_b128 v[84:87], v113 offset:21504\n\t" \
    "v_cvt_pk_bf16_f32 v104, v48, v49\n\t" \
    "v_cvt_pk_bf16_f32 v105, v50, v51\n\t" \
    "v_exp_f32_e32 v53, v53\n\t" \
    "v_exp_f32_e32 v54, v54\n\t" \
    "v_exp_f32_e32 v55, v55\n\t" \
    "v_mfma_f32_32x32x16_bf16 v[0:15], v[100:103], v[108:111], v[0:15]\n\t" \
    "ds_read_b128 v[92:95], v113 offset:15360\n\t" \
    "ds_read_b128 v[88:91], v113 offset:18432\n\t" \
    "v_exp_f32_e32 v56, v56\n\t" \
    "v_cvt_pk_bf16_f32 v106, v52, v53\n\t" \
    "v_cvt_pk_bf16_f32 v107, v54, v55\n\t" \
    "v_exp_f32_e32 v57, v57\n\t" \
    "v_exp_f32_e32 v58, v58\n\t" \
    "v_exp_f32_e32 v59, v59\n\t" \
    "v_mfma_f32_32x32x16_bf16 v[0:15], v[96:99], v[104:107], v[0:15]\n\t" \
    "s_bitcmp1_b32 %[hk], %[cnt]\n\t" \
    "s_cbranch_scc1 41f\n\t" \
    "25:\n" \
    "v_exp_f32_e32 v60, v60\n\t" \
    "v_exp_f32_e32 v61, v61\n\t" \
    "v_exp_f32_e32 v62, v62\n\t" \
    "v_exp_f32_e32 v63, v63\n\t" \
    "v_cvt_pk_bf16_f32 v108, v56, v57\n\t" \
    "v_cvt_pk_bf16_f32 v109, v58, v59\n\t" \
    "v_cvt_pk_bf16_f32 v110, v60, v61\n\t" \
    "v_cvt_pk_bf16_f32 v111, v62, v63\n\t" \
    "s_setprio 0\n\t" \
    "s_waitcnt lgkmcnt(0)\n\t" \
    "v_mfma_f32_32x32x16_bf16 v[48:63], v[80:83], v[64:67], v[16:31]\n\t" \
    "v_exp_f32_e32 v32, v32\n\t" \
    "v_exp_f32_e32 v33, v33\n\t" \
    "v_exp_f32_e32 v34, v34\n\t" \
    "v_smfmac_f32_16x16x64_bf16 v[76:79], v[72:75], v[104:111], v174\n\t" \
    "v_exp_f32_e32 v35, v35\n\t" \
    "v_exp_f32_e32 v36, v36\n\t" \
    "v_mfma_f32_32x32x16_bf16 v[48:63], v[84:87], v[68:71], v[48:63]\n\t" \
    "ds_read_b128 v[80:83], v113 offset:24576\n\t" \
    "ds_read_b128 v[84:87], v113 offset:25600\n\t" \
    "v_cvt_pk_bf16_f32 v104, v32, v33\n\t" \
    "v_cvt_pk_bf16_f32 v105, v34, v35\n\t" \
    "v_exp_f32_e32 v37, v37\n\t" \
    "v_exp_f32_e32 v38, v38\n\t" \
    "v_exp_f32_e32 v39, v39\n\t" \
    "v_mfma_f32_32x32x16_bf16 v[0:15], v[92:95], v[108:111], v[0:15]\n\t" \
    "ds_read_b128 v[100:103], v113 offset:19456\n\t" \
    "ds_read_b128 v[96:99], v113 offset:22528\n\t" \
    "v_exp_f32_e32 v40, v40\n\t" \
    "v_cvt_pk_bf16_f32 v106, v36, v37\n\t" \
    "v_cvt_pk_bf16_f32 v107, v38, v39\n\t" \
    "v_exp_f32_e32 v41, v41\n\t" \
    "v_exp_f32_e32 v42, v42\n\t" \
    "v_exp_f32_e32 v43, v43\n\t" \
    "v_mfma_f32_32x32x16_bf16 v[0:15], v[88:91], v[104:107], v[0:15]\n\t" \
    "s_and_b32 s40, s48, 63\n\t" \
    "s_lshl_b32 s40, s40, 12\n\t" \
    "s_add_u32 s44, %[srclo], s40\n\t" \
    "s_addc_u32 s45, %[srchi], 0\n\t" \
    "s_lshr_b64 s[48:49], s[48:49], 6\n\t" \
    "s_add_u32 s46, s46, 1\n\t" \
    "s_bitcmp1_b32 %[hk], %[cnt]\n\t" \
    "s_cbranch_scc1 42f\n\t" \
    "26:\n" \
    "v_exp_f32_e32 v44, v44\n\t" \
    "v_exp_f32_e32 v45, v45\n\t" \
    "v_exp_f32_e32 v46, v46\n\t" \
    "v_exp_f32_e32 v47, v47\n\t" \
    "v_cvt_pk_bf16_f32 v108, v40, v41\n\t" \
    "v_cvt_pk_bf16_f32 v109, v42, v43\n\t" \
    "v_cvt_pk_bf16_f32 v110, v44, v45\n\t" \
    "v_cvt_pk_bf16_f32 v111, v46, v47\n\t" \
    "s_waitcnt lgkmcnt(0)\n\t" \
    "v_mfma_f32_32x32x16_bf16 v[32:47], v[80:83], v[64:67], v[16:31]\n\t" \
    "v_exp_f32_e32 v48, v48\n\t" \
    "v_exp_f32_e32 v49, v49\n\t" \
    "v_exp_f32_e32 v50, v50\n\t" \
    "v_smfmac_f32_16x16x64_bf16 v[76:79], v[72:75], v[104:111], v174\n\t" \
    "v_exp_f32_e32 v51, v51\n\t" \
    "v_exp_f32_e32 v52, v52\n\t" \
    "v_mfma_f32_32x32x16_bf16 v[32:47], v[84:87], v[68:71], v[32:47]\n\t" \
    "ds_read_b128 v[80:83], v113 offset:28672\n\t" \
    "ds_read_b128 v[84:87], v113 offset:29696\n\t" \
    "v_cvt_pk_bf16_f32 v104, v48, v49\n\t" \
    "v_cvt_pk_bf16_f32 v105, v50, v51\n\t" \
    "v_exp_f32_e32 v53, v53\n\t" \
    "v_exp_f32_e32 v54, v54\n\t" \
    "v_exp_f32_e32 v55, v55\n\t" \
    "v_mfma_f32_32x32x16_bf16 v[0:15], v[100:103], v[108:111], v[0:15]\n\t" \
    "ds_read_b128 v[92:95], v113 offset:23552\n\t" \
    "ds_read_b128 v[88:91], v113 offset:26624\n\t" \
    "v_exp_f32_e32 v56, v56\n\t" \
    "v_cvt_pk_bf16_f32 v106, v52, v53\n\t" \
    "v_cvt_pk_bf16_f32 v107, v54, v55\n\t" \
    "v_exp_f32_e32 v57, v57\n\t" \
    "v_exp_f32_e32 v58, v58\n\t" \
    "v_exp_f32_e32 v59, v59\n\t" \
    "v_mfma_f32_32x32x16_bf16 v[0:15], v[96:99], v[104:107], v[0:15]\n\t" \
    "s_bitcmp1_b32 %[hk], %[cnt]\n\t" \
    "s_cbranch_scc1 43f\n\t" \
    "27:\n" \
    "v_mov_b32_e32 v112, v113\n\t" \
    "v_add_u32_e32 v113, %[nx], v115\n\t" \
    "s_sub_u32 %[cnt], %[cnt], 1\n\t" \
    "s_cmp_lg_u32 %[cnt], 0\n\t" \
    "v_exp_f32_e32 v60, v60\n\t" \
    "v_exp_f32_e32 v61, v61\n\t" \
    "v_exp_f32_e32 v62, v62\n\t" \
    "v_exp_f32_e32 v63, v63\n\t" \
    "v_cvt_pk_bf16_f32 v108, v56, v57\n\t" \
    "v_cvt_pk_bf16_f32 v109, v58, v59\n\t" \
    "v_cvt_pk_bf16_f32 v110, v60, v61\n\t" \
    "v_cvt_pk_bf16_f32 v111, v62, v63\n\t" \
    "s_cbranch_scc1 1b\n\t" \
    "7:\n" \
    "s_setprio 0\n\t" \
    "s_waitcnt lgkmcnt(0)\n\t" \
    "v_mfma_f32_32x32x16_bf16 v[48:63], v[80:83], v[64:67], v[16:31]\n\t" \
    "v_exp_f32_e32 v32, v32\n\t" \
    "v_exp_f32_e32 v33, v33\n\t" \
    "v_exp_f32_e32 v34, v34\n\t" \
    "v_smfmac_f32_16x16x64_bf16 v[76:79], v[72:75], v[104:111], v174\n\t" \
    "v_exp_f32_e32 v35, v35\n\t" \
    "v_exp_f32_e32 v36, v36\n\t" \
    "v_mfma_f32_32x32x16_bf16 v[48:63], v[84:87], v[68:71], v[48:63]\n\t" \
    "v_cvt_pk_bf16_f32 v104, v32, v33\n\t" \
    "v_cvt_pk_bf16_f32 v105, v34, v35\n\t" \
    "v_exp_f32_e32 v37, v37\n\t" \
    "v_exp_f32_e32 v38, v38\n\t" \
    "v_exp_f32_e32 v39, v39\n\t" \
    "v_mfma_f32_32x32x16_bf16 v[0:15], v[92:95], v[108:111], v[0:15]\n\t" \
    "ds_read_b128 v[100:103], v112 offset:27648\n\t" \
    "ds_read_b128 v[96:99], v112 offset:30720\n\t" \
    "v_exp_f32_e32 v40, v40\n\t" \
    "v_cvt_pk_bf16_f32 v106, v36, v37\n\t" \
    "v_cvt_pk_bf16_f32 v107, v38, v39\n\t" \
    "v_exp_f32_e32 v41, v41\n\t" \
    "v_exp_f32_e32 v42, v42\n\t" \
    "v_exp_f32_e32 v43, v43\n\t" \
    "v_mfma_f32_32x32x16_bf16 v[0:15], v[88:91], v[104:107], v[0:15]\n\t" \
    "v_exp_f32_e32 v44, v44\n\t" \
    "v_exp_f32_e32 v45, v45\n\t" \
    "v_exp_f32_e32 v46, v46\n\t" \
    "v_exp_f32_e32 v47, v47\n\t" \
    "v_cvt_pk_bf16_f32 v108, v40, v41\n\t" \
    "v_cvt_pk_bf16_f32 v109, v42, v43\n\t" \
    "v_cvt_pk_bf16_f32 v110, v44, v45\n\t" \
    "v_cvt_pk_bf16_f32 v111, v46, v47\n\t" \
    "s_waitcnt lgkmcnt(0)\n\t" \
    "v_exp_f32_e32 v48, v48\n\t" \
    "v_exp_f32_e32 v49, v49\n\t" \
    "v_exp_f32_e32 v50, v50\n\t" \
    "v_smfmac_f32_16x16x64_bf16 v[76:79], v[72:75], v[104:111], v174\n\t" \
    "v_exp_f32_e32 v51, v51\n\t" \
    "v_exp_f32_e32 v52, v52\n\t" \
    "v_cvt_pk_bf16_f32 v104, v48, v49\n\t" \
    "v_cvt_pk_bf16_f32 v105, v50, v51\n\t" \
    "v_exp_f32_e32 v53, v53\n\t" \
    "v_exp_f32_e32 v54, v54\n\t" \
    "v_exp_f32_e32 v55, v55\n\t" \
    "v_mfma_f32_32x32x16_bf16 v[0:15], v[100:103], v[108:111], v[0:15]\n\t" \
    "ds_read_b128 v[92:95], v112 offset:31744\n\t" \
    "v_exp_f32_e32 v56, v56\n\t" \
    "v_cvt_pk_bf16_f32 v106, v52, v53\n\t" \
    "v_cvt_pk_bf16_f32 v107, v54, v55\n\t" \
    "v_exp_f32_e32 v57, v57\n\t" \
    "v_exp_f32_e32 v58, v58\n\t" \
    "v_exp_f32_e32 v59, v59\n\t" \
    "v_mfma_f32_32x32x16_bf16 v[0:15], v[96:99], v[104:107], v[0:15]\n\t" \
    "v_exp_f32_e32 v60, v60\n\t" \
    "v_exp_f32_e32 v61, v61\n\t" \
    "v_exp_f32_e32 v62, v62\n\t" \
    "v_exp_f32_e32 v63, v63\n\t" \
    "v_cvt_pk_bf16_f32 v108, v56, v57\n\t" \
    "v_cvt_pk_bf16_f32 v109, v58, v59\n\t" \
    "v_cvt_pk_bf16_f32 v110, v60, v61\n\t" \
    "v_cvt_pk_bf16_f32 v111, v62, v63\n\t" \
    "s_waitcnt lgkmcnt(0)\n\t" \
    "s_nop 0\n\t" \
    "v_mfma_f32_32x32x16_bf16 v[0:15], v[92:95], v[108:111], v[0:15]\n\t" \
    "v_smfmac_f32_16x16x64_bf16 v[76:79], v[72:75], v[104:111], v174\n\t" \
    "s_nop 15\n\t" \
    "s_branch 99f\n\t" \
    "9:\n" \
    "s_mov_b32 s47, 0x40000\n\t" \
    "10:\n" \
    "s_sub_u32 s47, s47, 1\n\t" \
    "s_cbranch_scc1 12b\n\t" \
    "global_load_dwordx2 v[120:121], v170, %[ws] sc1\n\t" \
    "s_waitcnt vmcnt(0)\n\t" \
    "v_cmp_eq_u64_e64 s[42:43], v[120:121], v[140:141]\n\t" \
    "s_nop 4\n\t" \
    "s_bitcmp1_b64 s[42:43], s46\n\t" \
    "s_cbranch_scc1 12b\n\t" \
    "s_sleep 4\n\t" \
    "s_branch 10b\n\t" \
    "50:\n" \
    "global_store_dwordx4 v171, v[176:179], %[ws] offset:-3072 sc1\n\t" \
    "global_store_dwordx4 v171, v[180:183], %[ws] offset:-2944 sc1\n\t" \
    "global_store_dwordx4 v172, v[184:187], %[ws] offset:-1024 sc1\n\t" \
    "global_store_dwordx4 v172, v[188:191], %[ws] offset:-768 sc1\n\t" \
    "s_branch 24b\n\t" \
    "51:\n" \
    "global_store_dwordx4 v171, v[192:195], %[ws] offset:1024 sc1\n\t" \
    "global_store_dwordx4 v171, v[196:199], %[ws] offset:1152 sc1\n\t" \
    "global_store_dwordx4 v172, v[200:203], %[ws] offset:3072 sc1\n\t" \
    "global_store_dwordx4 v172, v[204:207], %[ws] offset:3328 sc1\n\t" \
    "s_branch 25b\n\t" \
    "40:\n" \
    "s_cmp_eq_u32 %[cnt], 7\n\t" \
    "s_cbranch_scc1 50b\n\t" \
    "s_add_u32 m0, %[wdst], 0\n\t" \
    "s_nop 0\n\t" \
    "global_load_lds_dwordx4 v116, %[wo01]\n\t" \
    "global_load_lds_dwordx4 v117, %[wo01] offset:1024\n\t" \
    "v_add_u32_e32 v116, 0x20000, v116\n\t" \
    "v_add_u32_e32 v117, 0x20000, v117\n\t" \
    "s_branch 24b\n\t" \
    "41:\n" \
    "s_cmp_eq_u32 %[cnt], 7\n\t" \
    "s_cbranch_scc1 51b\n\t" \
    "s_add_u32 m0, %[wdst], 16384\n\t" \
    "s_nop 0\n\t" \
    "global_load_lds_dwordx4 v116, %[wo01]\n\t" \
    "global_load_lds_dwordx4 v117, %[wo01] offset:1024\n\t" \
    "v_subrev_u32_e32 v116, 0x20000, v116\n\t" \
    "v_subrev_u32_e32 v117, 0x20000, v117\n\t" \
    "s_branch 25b\n\t" \
    "42:\n" \
    "s_cmp_eq_u32 %[cnt], 7\n\t" \
    "s_cbranch_scc1 26b\n\t" \
    "s_add_u32 m0, %[wdst], 32768\n\t" \
    "s_nop 0\n\t" \
    "global_load_lds_dwordx4 v116, %[wo23]\n\t" \
    "global_load_lds_dwordx4 v117, %[wo23] offset:1024\n\t" \
    "v_add_u32_e32 v116, 0x20000, v116\n\t" \
    "v_add_u32_e32 v117, 0x20000, v117\n\t" \
    "s_branch 26b\n\t" \
    "43:\n" \
    "s_cmp_eq_u32 %[cnt], 7\n\t" \
    "s_cbranch_scc1 27b\n\t" \
    "s_add_u32 m0, %[wdst], 49152\n\t" \
    "s_nop 0\n\t" \
    "global_load_lds_dwordx4 v116, %[wo23]\n\t" \
    "global_load_lds_dwordx4 v117, %[wo23] offset:1024\n\t" \
    "global_load_dword v168, v119, %[wo]\n\t" \
    "s_branch 27b\n\t" \
    "30:\n" \
    "s_cmp_eq_u32 %[cnt], 1\n\t" \
    "s_cbranch_scc0 31f\n\t" \
    "s_mov_b64 vcc, 0\n\t" \
    "s_bitcmp1_b32 %[mode], 1\n\t" \
    "s_cbranch_scc1 7b\n\t" \
    "s_branch 2b\n\t" \
    "31:\n" \
    "s_cmp_eq_u32 %[cnt], 6\n\t" \
    "s_cbranch_scc0 33f\n\t" \
    "global_store_dwordx2 v175, v[140:141], %[ws] sc1\n\t" \
    "s_branch 2b\n\t" \
    "33:\n" \
    "s_cmp_eq_u32 %[cnt], 5\n\t" \
    "s_cbranch_scc0 34f\n\t" \
    "global_store_dwordx2 v118, v[140:141], %[ws] sc1\n\t" \
    "s_branch 2b\n\t" \
    "34:\n" \
    "s_cmp_eq_u32 %[cnt], 3\n\t" \
    "s_cbranch_scc0 35f\n\t" \
    "global_load_dwordx2 v[168:169], v142, %[ws] sc1\n\t" \
    "s_branch 2b\n\t" \
    "35:\n" \
    "v_cmp_eq_u64_e64 s[42:43], v[168:169], v[140:141]\n\t" \
    "s_nop 4\n\t" \
    "s_cmp_eq_u64 s[42:43], -1\n\t" \
    "s_cbranch_scc0 2b\n\t" \
    "global_load_dwordx4 v[122:125], v143, %[ws] offset:-2048 sc1\n\t" \
    "global_load_dwordx4 v[126:129], v143, %[ws] offset:-1024 sc1\n\t" \
    "global_load_dwordx4 v[130:133], v143, %[ws] offset:0 sc1\n\t" \
    "global_load_dwordx4 v[134:137], v143, %[ws] offset:1024 sc1\n\t" \
    "global_load_dwordx2 v[138:139], v143, %[ws] offset:2048 sc1\n\t" \
    "s_bitset1_b32 %[mode], 2\n\t" \
    "s_branch 2b\n\t" \
    "99:\n"
#define ATT_TMP "v48", "v49", "v50", "v51", "v52", "v53", "v54", "v55", "v56", "v57", "v58", "v59", "v60", "v61", "v62", "v63", "v96", "v97", "v98", "v99", "v100", "v101", "v102", "v103"

constexpr int K1_X = 0, K1_W = 65536, K1_WB = 24576;
constexpr int K2_SLOT = 32768, K2_QX = 3 * K2_SLOT, K2_FL = 147456;
constexpr int LDS_BYTES = 163840;
constexpr int p3_wslot(int t) { return (t % 6) < 4 ? 98304 + (t % 6) * 16384 : 65536 + ((t % 6) - 4) * 16384; }
constexpr int p3_allow(int t) { return (((t < 2 || t - 2 >= 1) && t + 3 < 16) ? 4 : 0) + (t + 3 < 16 ? 4 : 0) + ((t >= 1 && t + 5 < 16) ? 4 : 0); }

__global__ __launch_bounds__(512, 2) void mk_fused(const u16* __restrict__ xA, const u16* __restrict__ wA, const u16* __restrict__ woA,
                                                   const u16* __restrict__ xB, const u16* __restrict__ wB, const u16* __restrict__ woB, char* ws0, u16* __restrict__ out) {
    char* ws = ws0;
    __shared__ __attribute__((aligned(1024))) char smem[LDS_BYTES];
    const unsigned lds0 = (unsigned)(uintptr_t)(LAS char*)smem;
    const int wid = __builtin_amdgcn_readfirstlane(threadIdx.x >> 6);
    int lane = threadIdx.x & 63;
#define BRX 1
    const int b = blockIdx.x, xcd = b & 7, j = b >> 3, br = (xcd >> 2) ^ BRX, qd = xcd & 3;
    const int mt = (qd & 1) * 4 + (j & 3), hp = (qd >> 1) * 8 + (j >> 2);
    const int mt3 = qd * 4 + (j & 3), nt3 = j >> 2;
    u64 tag;
    u32x4 fq[2][2], fk[2][2], fv[2][2];
    f32x16 band;
    STAMP_AT(0, 0);
    {
        const int g = wid >> 2, wi = wid & 3, mw = wi >> 1, hd = wi & 1;
        const char* x = (const char*)(br ? xB : xA) + (size_t)mt * 256 * 2048;
        const char* w = (const char*)(br ? wB : wA) + hp * 128;
        unsigned vx[4], vw[3];
#pragma unroll
        for (int ii = 0; ii < 4; ++ii) {
            const int row = br ? ((wi >> 1) * 64 + (wi & 1) + ii * 16 + 2 * (lane >> 3)) : (wi * 32 + ii * 8 + (lane >> 3));
            vx[ii] = row * 2048 + (((lane & 7) ^ (((lane >> 4) & 1) | ((ii >> 1) << 1) | ((lane >> 5) << 2))) << 4) - ii * 1024;
        }
#pragma unroll
        for (int jj = 0; jj < 3; ++jj) {
            const int jw = 12 * g + 3 * wi + jj, n = jw >> 3, k0 = (jw & 7) * 8 + (lane >> 3);
            vw[jj] = k0 * 6144 + n * 2048 + (((lane & 7) ^ ((((lane >> 4) & 1) | ((jw & 1) << 1)) << 1)) << 4) - jj * 1024;
        }
        const unsigned xdst_o = lds0 + K1_X + (1 - g) * 32768 + wi * 4096;
        const unsigned wdst = lds0 + K1_W + (12 * g + 3 * wi) * 1024;
        const char* xo = x + (size_t)(1 - g) * 128 * 2048;
#define ROTM 4
        const int kt0 = ROTM * (j & 3);
#define STAGE_X(kt, dst) do { const char* s_ = xo + (((kt) + kt0) & 15) * 128; glds_run<0>(s_, vx[0], dst); glds_run<1>(s_, vx[1], 0); glds_run<2>(s_, vx[2], 0); glds_run<3>(s_, vx[3], 0); } while (0)
#define STAGE_W(kt, buf) do { const char* s_ = w + (size_t)(((kt) + kt0) & 15) * 64 * 6144; glds_run<0>(s_, vw[0], wdst + (buf) * K1_WB); glds_run<1>(s_, vw[1], 0); glds_run<2>(s_, vw[2], 0); } while (0)
        if (wid == 7) {
            const char* pc; unsigned len;
            asm volatile("s_getpc_b64 %0\n.Lmk_code_begin:\n\ts_mov_b32 %1, .Lmk_code_end-.Lmk_code_begin" : "=s"(pc), "=s"(len));
            const unsigned off = (unsigned)(j * 64 + lane) * 64u;
            asm volatile("s_mov_b32 m0, %2\n\ts_nop 0\n\tglobal_load_lds_dword %0, %1" :: "v"(off < len ? off : 0u), "s"(pc), "s"(lds0 + 159744) : "memory");
        }
        JIT(14);
        STAGE_X(0, xdst_o); STAGE_W(0, 0); STAGE_W(1, 1);
        if (g == 1) STAGE_X(1, xdst_o + 16384);
        { u64 e; asm volatile("s_load_dwordx2 %0, %1, 0x0\n\ts_waitcnt lgkmcnt(0)" : "=s"(e) : "s"(ws + WS_EP) : "memory");
          e += 0x9E3779B97F4A7C15ull; e = (e ^ (e >> 30)) * 0xBF58476D1CE4E5B9ull; e = (e ^ (e >> 27)) * 0x94D049BB133111EBull; tag = e ^ (e >> 31); }
        const int c16 = lane & 15, g4 = lane >> 4;
        unsigned ax[2], aw[2];
        ax[0] = lds0 + K1_X + g * 32768 + (mw * 64 + 16 * (c16 >> 3) + (c16 & 7)) * 128 + ((g4 ^ (((c16 >> 1) & 1) | ((c16 >> 3) << 1) | (((c16 >> 2) & 1) << 2))) << 4);
        ax[1] = ax[0] ^ 64;
        { const int q4 = c16 >> 2, p = lane & 3;
          aw[0] = lds0 + K1_W + (8 * g4 + q4) * 128 + (((hd * 4 + (p >> 1)) ^ ((((q4 >> 1) & 1) | ((g4 & 1) << 1)) << 1)) << 4) + (p & 1) * 8; aw[1] = aw[0] ^ 32; }
        band = band_mask(lane); asm volatile("" : "+v"(band));
        if (g == 1) WAITBAR_(7); else WAITBAR_(3);
        SA_(1);
        if (g == 1) asm volatile("s_barrier" ::: "memory");
        f32x4 acc[4][6];
#pragma unroll
        for (int m = 0; m < 4; ++m)
#pragma unroll
            for (int n = 0; n < 6; ++n) acc[m][n] = f32x4{};
        SBAR();
        unsigned wr_ = 0, dw = wdst + 2 * K1_WB, dx = xdst_o + (1 - g) * 16384;
        int tx = (1 + g + kt0) & 15, tw = (2 + kt0) & 15;
        const char* sx = xo + tx * 128; const char* sw = w + (size_t)tw * 64 * 6144;
#define RDX(m) do { xf[m][0] = ldsrd128(ax[0] + ((m) >> 1) * 4096 + ((m) & 1) * 1024); xf[m][1] = ldsrd128(ax[1] + ((m) >> 1) * 4096 + ((m) & 1) * 1024); } while (0)
#define RDW(n, ks) wf[n][ks] = cat8(ldstr(aw[(n) & 1] + ((n) >> 1) * 8192 + (ks) * 4096), ldstr(aw[(n) & 1] + ((n) >> 1) * 8192 + (ks) * 4096 + 512))
#define P1_TILE(DOX, DOW, WAIT, LASTBAR) do { \
            bf16x8 xf[4][2], wf[6][2]; \
            RDX(0); RDW(0, 0); if (DOX) glds_run<0>(sx, vx[0], dx); \
            RDW(0, 1); RDW(1, 0); if (DOX) glds_run<1>(sx, vx[1], 0); \
            RDX(1); RDW(1, 1); if (DOX) glds_run<2>(sx, vx[2], 0); \
            RDW(2, 0); RDW(2, 1); if (DOX) glds_run<3>(sx, vx[3], 0); \
            RDX(2); RDW(3, 0); if (DOW) glds_run<0>(sw, vw[0], dw); \
            RDW(3, 1); RDW(4, 0); if (DOW) glds_run<1>(sw, vw[1], 0); \
            RDX(3); RDW(4, 1); if (DOW) glds_run<2>(sw, vw[2], 0); \
            RDW(5, 0); RDW(5, 1); \
            SBAR(); \
            {   \
              const unsigned d_ = (wr_ == 2 * K1_WB) ? (unsigned)(-2 * K1_WB) : (unsigned)K1_WB; dw = wdst + wr_; wr_ += d_; aw[0] += d_; aw[1] += d_; ax[0] ^= 16384; ax[1] ^= 16384; dx ^= 16384; \
              tx = (tx + 1) & 15; tw = (tw + 1) & 15; sx = xo + tx * 128; sw = w + (size_t)tw * 64 * 6144; \
              asm volatile("" : "+v"(aw[0]), "+v"(aw[1]), "+v"(ax[0]), "+v"(ax[1]), "+s"(sx), "+s"(sw)); } \
            WAIT; \
            SBAR(); \
            __builtin_amdgcn_s_setprio(1); \
            _Pragma("unroll") for (int m = 0; m < 4; ++m) \
                _Pragma("unroll") for (int n = 0; n < 6; ++n) { \
                    if (n < 4) { acc[m][n] = MFMA16(wf[n][0], xf[m][0], acc[m][n]); acc[m][n] = MFMA16(wf[n][1], xf[m][1], acc[m][n]); } \
                    else       { acc[m][n] = MFMA16(xf[m][0], wf[n][0], acc[m][n]); acc[m][n] = MFMA16(xf[m][1], wf[n][1], acc[m][n]); } \
                    CHAIN(); \
                } \
            __builtin_amdgcn_s_setprio(0); \
            SBAR(); \
            if (LASTBAR) asm volatile("s_barrier" ::: "memory"); \
            SBAR(); \
        } while (0)
#pragma unroll 1
        for (int t = 0; t < 14; ++t) P1_TILE(true, true, WAITBAR_(7), true);
        P1_TILE(g == 0, false, if (g == 0) WAITBAR_(4); else WAITBAR_(0), true);
        P1_TILE(false, false, WAITBAR_(0), g == 0);
#undef P1_TILE
#undef RDW
#undef RDX
#undef STAGE_X
#undef STAGE_W
        SA_(2); SC_(1);
        JIT(1);
        FRESH();
#pragma unroll
        for (int mb = 0; mb < 2; ++mb)
#pragma unroll
            for (int u = 0; u < 2; ++u) {
                const f32x4 a = acc[2 * mb + u][0] * QSCALE, c = acc[2 * mb + u][1] * QSCALE;
                fq[mb][u] = u32x4{cvtpk(a[0], a[1]), cvtpk(a[2], a[3]), cvtpk(c[0], c[1]), cvtpk(c[2], c[3])};
                fk[mb][u] = pack44(acc[2 * mb + u][2], acc[2 * mb + u][3]);
                fv[mb][u] = pack44(acc[2 * mb][4 + u], acc[2 * mb + 1][4 + u]);
            }
    }
    const char* wo = (const char*)(br ? woB : woA) + nt3 * 256;
    const int k30 = 2 * nt3;
    {
        const int g = wid >> 2, wr = wid >> 1, hd = wid & 1;
        const int ph1 = 2 * hp + hd, pb1 = mt * 8 + wr * 2;
        const int head = 2 * hp + br, blk = mt * 8 + wid;
#define RBLK(e) ((((e) % 7) + (((e) % 7) >= mt)) * 8 + (e) / 7)
        const char* kv = ws + WS_KV + (size_t)head * 262144;
        unsigned l16 = lane * 16;
        const unsigned dbeg = lds0 + wid * 4096, lbase = lds0 + l16;
        const unsigned fk0 = (unsigned)WS_FK + head * 32 * FS;
        const unsigned fxo = (unsigned)WS_FX + ((hp * 8 + mt) * 8 + wid) * FS, xrec0 = (unsigned)WS_XP + ((hp * 8 + mt) * 8 + wid) * 5120 + 2048;
        const f32x16 z = {};
        f32x16 sa, negm, o;
        u32x16 qol, kvf, pub0, pub1; u32x8 ph;
        const unsigned sidx = (lane & 1) ? 0xEEEEEEEEu : 0x44444444u;

#define FIRST_TILE(base, q0, q1) do { const unsigned on_ = (lane & 2) ? 0u : 0x3F803F80u, e_ = (lane & 4) ? 0u : on_, f_ = (lane & 4) ? on_ : 0u; const u32x4 ones = {e_, e_, f_, f_}; \
            const bf16x8 k0_ = ldsrd128(base), k1_ = ldsrd128((base) + 1024); kvf = cat16(asu(ldsrd128((base) + 4096)), asu(ldsrd128((base) + 5120)), asu(ldsrd128((base) + 2048)), ones); \
            sa = MFMA32(k0_, q0, z); sa = MFMA32(k1_, q1, sa); float m_ = sa[0]; \
            _Pragma("unroll") for (int r = 1; r < 16; ++r) m_ = __builtin_fmaxf(m_, sa[r]); \
            m_ = -swap_max(m_); _Pragma("unroll") for (int r = 0; r < 16; ++r) { negm[r] = m_; sa[r] += m_; } \
            qol = cat16(asu(q0), asu(q1), ones, u32x4{0u, 0u, 0u, 0u}); } while (0)
#define L6_OUT "={v[0:15]}"(o), "+{v[32:47]}"(sa), "+{v[64:79]}"(qol), "+{v[80:95]}"(kvf), "={v[104:111]}"(ph)
#define L6C(HL, BASE) asm volatile(ATT_L6C : L6_OUT, "={v[144:159]}"(h0), "={v[160:167]}"(h1) \
            : "{v[16:31]}"(negm), "{v174}"(sidx), "{v112}"(BASE), "{v[140:143]}"(u32x4{0u, 0u, FLADDR, (unsigned)WS_HALO + (mt * 32 + ph1) * 6144 + 3072 + l16}), \
              [ws] "s"(ws), [hl] "s"(HL), [fld] "s"(fld) : ATT_TMP, "scc", "memory")
#define FLADDR (fk0 + ((((lane & 7) == 7) ? blk : RBLK(8 * (lane & 7) + wid)) >> 1) * FS)
        const unsigned own = (ph1 * 32 + mt * 4 + wr) * FS;
        const unsigned fld = lds0 + K2_FL + wid * 1024 + br * (2 * K2_SLOT + 4096 - K2_FL + wid * 11264);
        const unsigned pv = (lane >> 5) * 1024 + ((lane & 15) + 32 * ((lane >> 4) & 1)) * 16, pq = pv + (lane & 8) * 16;
        if (br == 0) {
            const char* kp = ws + WS_KV + (size_t)((ph1 * 64 + pb1) * 4) * 1024;
#define PUB_KV() do { st16_dev<0>(kp, pq, fk[0][0]); st16_dev<128>(kp, pq, fk[0][1]); st16_dev<2048>(kp, pv, fv[0][0]); st16_dev<2304>(kp, pv, fv[0][1]); \
            st16_dev<0>(kp + 4096, pq, fk[1][0]); st16_dev<128>(kp + 4096, pq, fk[1][1]); st16_dev<2048>(kp + 4096, pv, fv[1][0]); st16_dev<2304>(kp + 4096, pv, fv[1][1]); } while (0)
            if (hd == 1) {
                const char* qp = ws + WS_QF + (size_t)((ph1 * 64 + pb1) * 2) * 1024;
                st16_dev<0>(qp, pq, fq[0][0]); st16_dev<128>(qp, pq, fq[0][1]); st16_dev<2048>(qp, pq, fq[1][0]); st16_dev<2176>(qp, pq, fq[1][1]);
            }
            JIT(2);
            if (wr == 0) PUB_KV();
#pragma unroll
            for (int mb = 0; mb < 2; ++mb) {
                const unsigned a = lds0 + hd * 2 * K2_SLOT + (wr * 2 + mb) * 4096, aq = lds0 + K2_QX + hd * 16384 + (wr * 2 + mb) * 2048 + pq;
                ldswr128(a + pq, fk[mb][0]); ldswr128(a + pq + 128, fk[mb][1]); ldswr128(a + 2048 + pv, fv[mb][0]); ldswr128(a + 2304 + pv, fv[mb][1]);
                if (hd == 0 || g == 0) { ldswr128(aq, fq[mb][0]); ldswr128(aq + 128, fq[mb][1]); }
            }
            BARRIER_L();
            if (wr == 0 || hd == 1) {
                drain();
                if (lane == 0) { if (wr == 0) flag_set(ws + WS_FK, own, tag); if (hd == 1) flag_set(ws + WS_FQ, own, tag); }
            }
            JIT(3);
            if (wr == 1) PUB_KV();
            const unsigned qa = lbase + K2_QX + (1 - g) * 16384 + wid * 2048, cx = lbase + (1 - g) * 2 * K2_SLOT;
            SA_(3); SC_(2);
            pub0 = cat16(fk[0][0], fk[0][1], fv[0][0], fv[0][1]); pub1 = cat16(fk[1][0], fk[1][1], fv[1][0], fv[1][1]);
            { const bf16x8 q0 = ldsrd128(qa), q1 = ldsrd128(qa + 1024); FIRST_TILE(cx, q0, q1); }
            JIT(4);
            asm volatile(ATT_L4 : L6_OUT : "{v[16:31]}"(negm), "{v174}"(sidx), "{v112}"(cx) : ATT_TMP, "memory");
            SC_(3);
            if (g == 1) {
                const int b1 = RBLK(wid), b0 = RBLK(wid - 4);
                flag_wait(ws, fk0 + (((lane & 1) ? b1 : b0) >> 1) * FS, tag);
                glds4k_dev(kv + b1 * 4096, l16, dbeg + K2_SLOT);
                glds4k_dev(kv + b0 * 4096, l16, dbeg + K2_SLOT - 16384);
                glds16_dev(ws, FLADDR, fld);
            } else if (wr == 1) { drain(); if (lane == 0) flag_set(ws + WS_FK, own, tag); }
            SC_(4);
            JIT(5);
            asm volatile(ATT_L2 : "+{v[0:15]}"(o), "+{v[32:47]}"(sa), "+{v[64:79]}"(qol), "+{v[80:95]}"(kvf), "+{v[104:111]}"(ph) : "{v[16:31]}"(negm), "{v174}"(sidx), "{v112}"(cx) : ATT_TMP, "memory");
            if (g == 0) {
                asm volatile(ATT_XT : "+{v[0:15]}"(o), "+{v[32:47]}"(sa), "+{v[64:79]}"(qol), "+{v[80:95]}"(kvf), "+{v[104:111]}"(ph) : "{v[16:31]}"(negm), "{v174}"(sidx), "{v112}"(cx) : ATT_TMP, "memory");
                st16_dev<-2048>(ws, xrec0 + l16, quad(o, 0)); st16_dev<-1024>(ws, xrec0 + l16, quad(o, 1)); st16_dev<0>(ws, xrec0 + l16, quad(o, 2)); st16_dev<1024>(ws, xrec0 + l16, quad(o, 3));
                st16_dev<2048>(ws, xrec0 + l16, u32x4{__float_as_uint(__uint_as_float(qol[12]) + __uint_as_float(qol[13])), __float_as_uint(negm[0]), 0u, 0u});
                SC_(5);
                JIT(16);
                const bf16x8 q0 = ldsrd128(lbase + K2_QX + wid * 2048), q1 = ldsrd128(lbase + K2_QX + wid * 2048 + 1024);
                FIRST_TILE(lbase, q0, q1);
                u32x16 h0; u32x8 h1;
                L6C(0, lbase);
            }
#undef PUB_KV
        } else {
            if (wr == 3 && mt < 7) {
                const char* p = ws + WS_HALO + (size_t)(((mt + 1) * 32 + ph1) * 6) * 1024;
                st16_dev<0>(p, pq, fk[0][0]); st16_dev<128>(p, pq, fk[0][1]); st16_dev<3072>(p, pq, fk[1][0]); st16_dev<3200>(p, pq, fk[1][1]);
                if (lane >= 32) { st16_dev<1024>(p, pv, fv[0][0]); st16_dev<1280>(p, pv, fv[0][1]); st16_dev<1024>(p + 3072, pv, fv[1][0]); st16_dev<1280>(p + 3072, pv, fv[1][1]); }
            }
            JIT(15);
            const unsigned ex = lds0 + 2 * K2_SLOT + wid * 12288;
#pragma unroll
            for (int mb = 0; mb < 2; ++mb) {
                const unsigned a = ex + mb * 6144;
                ldswr128(a + pq, fq[mb][0]); ldswr128(a + pq + 128, fq[mb][1]); ldswr128(a + 2048 + pq, fk[mb][0]); ldswr128(a + 2176 + pq, fk[mb][1]);
                ldswr128(a + 4096 + pv, fv[mb][0]); ldswr128(a + 4352 + pv, fv[mb][1]);
            }
            BARRIER_L();
            if (wr == 3 && mt < 7) { drain(); if (lane == 0) flag_set(ws + WS_FH, (ph1 * 8 + mt + 1) * FS, tag); }
            SC_(2);
            LocalPart lp[2];
            bf16x8 bq[2][2];
#pragma unroll
            for (int mb = 0; mb < 2; ++mb) { bq[mb][0] = ldsrd128(ex + mb * 6144 + l16); bq[mb][1] = ldsrd128(ex + mb * 6144 + 1024 + l16); }
#define LOCAL_BLOCK(mb) do { const unsigned a_ = ex + mb * 6144 + l16; \
                const bf16x8 k0_ = ldsrd128(a_ + 2048), k1_ = ldsrd128(a_ + 3072), v0_ = ldsrd128(a_ + 4096), v1_ = ldsrd128(a_ + 5120); \
                if (wr > 0) local_own<true>(bq[mb][0], bq[mb][1], k0_, k1_, v0_, v1_, ldsrd128(a_ - 24576 + 2048), ldsrd128(a_ - 24576 + 3072), ldsrd128(a_ - 24576 + 5120), band, lane, lp[mb]); \
                else local_own<false>(bq[mb][0], bq[mb][1], k0_, k1_, v0_, v1_, k0_, k0_, k0_, band, lane, lp[mb]); \
                if (wr > 0 || mt == 0) local_fin(lp[mb]); } while (0)
            JIT(6);
            LOCAL_BLOCK(0);
            SC_(3);
            const int hl = (wid < 2) ? mt : 0;
            const unsigned fh = (unsigned)WS_FH + (ph1 * 8 + mt) * FS;
            const int b0 = RBLK(wid), b1 = RBLK(8 + wid);
            const int q = lane & 3;
            unsigned fa0 = fk0 - 1024 * FS + (blk >> 1) * FS, fa1 = fk0 + (b0 >> 1) * FS, fa2 = fk0 + (b1 >> 1) * FS, fa3 = fh;
            asm volatile("" : "+s"(fa0), "+s"(fa1), "+s"(fa2), "+s"(fa3));
            const unsigned lo_ = (q & 1) ? fa1 : fa0, hi_ = (q & 1) ? fa3 : fa2;
            const u64 fm = flag_wait_some(ws, (q & 2) ? hi_ : lo_, tag, 3);
            SC_(4);
            glds4k_dev(kv + b0 * 4096, l16, dbeg);
            const char* qp = ws + WS_QF + (size_t)((head * 64 + blk) * 2) * 1024;
            glds16_dev(qp, l16, ex); glds16_dev(qp + 1024, l16, ex + 1024);
            if (fm & 4) glds4k_dev(kv + b1 * 4096, l16, dbeg + K2_SLOT);
            JIT(7);
            LOCAL_BLOCK(1);
#undef LOCAL_BLOCK
            if (fm & 4) WAITBAR_(4);
            else {
                WAITBAR_(0);
                flag_wait(ws, fk0 + (b1 >> 1) * FS, tag);
                glds4k_dev(kv + b1 * 4096, l16, dbeg + K2_SLOT);
            }
            JIT(8);
            const bf16x8 q0 = ldsrd128(ex + l16), q1 = ldsrd128(ex + 1024 + l16);
            SA_(3); SC_(5);
            if (hl && !(fm & 8)) flag_wait(ws, fh, tag);
            FIRST_TILE(lbase, q0, q1);
            u32x16 h0; u32x8 h1;
            JIT(9);
            L6C(hl, lbase);
            if (hl) {
                local_prev(bq[0][0], bq[0][1], asbf(u32x4{h0[0], h0[1], h0[2], h0[3]}), asbf(u32x4{h0[4], h0[5], h0[6], h0[7]}), asbf(u32x4{h0[8], h0[9], h0[10], h0[11]}), lane, lp[0]);
                local_prev(bq[1][0], bq[1][1], asbf(u32x4{h0[12], h0[13], h0[14], h0[15]}), asbf(u32x4{h1[0], h1[1], h1[2], h1[3]}), asbf(u32x4{h1[4], h1[5], h1[6], h1[7]}), lane, lp[1]);
                local_fin(lp[0]); local_fin(lp[1]);
            }
            const char* op = ws + WS_OF + (size_t)(((64 + pb1) * 32 + ph1) * 2) * 1024;
            st16_dev<0>(op, l16, quad(lp[0].o, 0)); st16_dev<1024>(op, l16, quad(lp[0].o, 1)); st16_dev<0>(op + 65536, l16, quad(lp[1].o, 0)); st16_dev<1024>(op + 65536, l16, quad(lp[1].o, 1));
        }
#undef FIRST_TILE
#undef L6_OUT
#undef L6C
        FRESH();
        l16 = lane * 16;
        SA_(4); SC_(6);
        const int isx = br & (1 - g);
        unsigned cnt = 7, dst = dbeg + 2 * K2_SLOT, nx = K2_SLOT;
        unsigned mode = isx << 1;
        const unsigned late = (1 - br) & g;
        const unsigned act = 2u | (isx * 12u) | ((br | (1 - g)) << 5) | (late << 6), hk = 32u | (late << 7);
        u64 tab = (u64)blk << 42;
#pragma unroll
        for (int k = 1; k < 7; ++k) tab |= (u64)RBLK(8 * k + wid) << (6 * k);
        tab >>= 6 + 6 * br;
        u32x8 adr;
        { const int k = 8 * wid + (lane >> 4), c = lane & 15;
          adr = u32x8{lds0 + l16, lds0 + l16 + K2_SLOT, l16, lds0 + l16, (unsigned)(k * 2048 + ((c ^ ((k & 7) << 1)) << 4)), (unsigned)((k + 4) * 2048 + ((c ^ (((k + 4) & 7) << 1)) << 4) - 1024),
                      br ? (unsigned)WS_FO + ((64 + pb1 + (lane & 1)) * 32 + ph1) * FS : fxo, 0u};
          const int i = (mt3 * 8 + wid) * 12 + ((lane * 3) >> 4); adr[7] = ((((4 + (i >> 7) + k30) & 15) * 64 + ((i & 127) >> 1)) * 2048 + (i & 1) * 128); }
        const unsigned kvrec = (unsigned)WS_KV + (ph1 * 64 + pb1) * 4096 + 3072, pv2 = (lane >> 5) * 1024 + ((lane & 15) + 32 * ((lane >> 4) & 1)) * 16;
        f32x16 xo; f32x2 xl;
#define DBGOUT
        JIT(10);
        asm volatile(ATT_MAIN
                     : DBGOUT"+{v[0:15]}"(o), "+{v[32:47]}"(sa), "+{v[64:79]}"(qol), "+{v[80:95]}"(kvf), "+{v[104:111]}"(ph), "+{v[112:119]}"(adr), "={v[122:137]}"(xo), "={v[138:139]}"(xl),
                       [cnt] "+s"(cnt), [dst] "+s"(dst), [nx] "+s"(nx), [mode] "+s"(mode)
                     : "{v[16:31]}"(negm), "{v[174:175]}"(u32x2{sidx, (unsigned)WS_FK + own}), "{v[140:143]}"(u32x4{(unsigned)tag, (unsigned)(tag >> 32), fxo, xrec0 + l16}),
                       "{v[170:173]}"(u32x4{FLADDR, kvrec + pv2 + (lane & 8) * 16, kvrec + pv2, fld + l16}), "{v[176:191]}"(pub0), "{v[192:207]}"(pub1),
                       [srclo] "s"((unsigned)(uintptr_t)kv), [srchi] "s"((unsigned)((uintptr_t)kv >> 32)), [dbeg] "s"(dbeg), [dend] "s"(dbeg + 3 * K2_SLOT),
                       [wo01] "s"(wo + (size_t)k30 * 131072), [wo23] "s"(wo + (size_t)((k30 + 2) & 15) * 131072), [wo] "s"(wo), [wdst] "s"(lds0 + 98304 + wid * 2048), [act] "s"(act), [hk] "s"(hk), [tab] "s"(tab), [k0] "s"(br), [ws] "s"(ws)
                     : ATT_TMP, "v120", "v121", "v168", "v169", "s40", "s41", "s42", "s43", "s44", "s45", "s46", "s47", "s48", "s49", "s50", "s51", "vcc", "scc", "memory");
        FRESH();
        l16 = lane * 16;
        const unsigned xrec = xrec0 + l16;
        const bf16x8 q0 = asbf(u32x4{qol[0], qol[1], qol[2], qol[3]}), q1 = asbf(u32x4{qol[4], qol[5], qol[6], qol[7]});
        const unsigned one2 = (((lane >> 4) & 1) == ((lane >> 2) & 1)) ? 0x3F803F80u : 0u;
        const u32x4 ones = {one2, one2, one2, one2};
        SA_(5); SB_(1);
        JIT(11);
        float lsum = __uint_as_float(qol[12]) + __uint_as_float(qol[13]);
        bool ok = lsum < 1.0e18f;
        if (isx) {
            if (__builtin_expect(!(mode & 4), 0)) {
                flag_wait(ws, fxo, tag);
                f32x4 a0, a1, a2, a3;
                asm volatile("global_load_dwordx4 %0, %5, %6 offset:-2048 sc1\n\tglobal_load_dwordx4 %1, %5, %6 offset:-1024 sc1\n\tglobal_load_dwordx4 %2, %5, %6 sc1\n\t"
                             "global_load_dwordx4 %3, %5, %6 offset:1024 sc1\n\tglobal_load_dwordx2 %4, %5, %6 offset:2048 sc1\n\ts_waitcnt vmcnt(0)"
                             : "=&v"(a0), "=&v"(a1), "=&v"(a2), "=&v"(a3), "=&v"(xl) : "v"(xrec), "s"(ws) : "memory");
#pragma unroll
                for (int r = 0; r < 4; ++r) { xo[r] = a0[r]; xo[4 + r] = a1[r]; xo[8 + r] = a2[r]; xo[12 + r] = a3[r]; }
            }
            const float nm = __builtin_fminf(negm[0], xl[1]), fa = EXP2(nm - negm[0]), fx = EXP2(nm - xl[1]);
            ok = ok && (xl[0] < 1.0e18f);
            lsum = lsum * fa + xl[0] * fx;
#pragma unroll
            for (int r = 0; r < 16; ++r) o[r] = o[r] * fa + xo[r] * fx;
        }
#pragma unroll
        for (int r = 0; r < 16; ++r) ok = ok && (__builtin_fabsf(o[r]) < 3.0e38f);
        if (__builtin_expect(__any(!ok), 0)) {
            float m = -__builtin_inff();
            f32x4 l;
            for (int pass = 0; pass < 2; ++pass) {
                o = z; l = f32x4{};
                for (int t = 0; t < 64; ++t) {
                    bf16x8 f0, f1, f2, f3;
                    asm volatile("global_load_dwordx4 %0, %4, %5 sc1\n\tglobal_load_dwordx4 %1, %4, %5 offset:1024 sc1\n\tglobal_load_dwordx4 %2, %4, %5 offset:2048 sc1\n\t"
                                 "global_load_dwordx4 %3, %4, %5 offset:3072 sc1\n\ts_waitcnt vmcnt(0)" : "=&v"(f0), "=&v"(f1), "=&v"(f2), "=&v"(f3) : "v"(l16 + t * 4096), "s"(kv) : "memory");
                    f32x16 s = MFMA32(f0, q0, z); s = MFMA32(f1, q1, s);
                    if (pass == 0) {
#pragma unroll
                        for (int r = 0; r < 16; ++r) m = __builtin_fmaxf(m, s[r]);
                    } else {
#pragma unroll
                        for (int r = 0; r < 16; ++r) s[r] = EXP2(s[r] - m);
                        const bf16x8 p0 = asbf(pack8(s, 0)), p1 = asbf(pack8(s, 1));
                        o = MFMA32(f2, p0, o); o = MFMA32(f3, p1, o);
                        l = MFMA16(asbf(ones), p0, l); l = MFMA16(asbf(ones), p1, l);
                    }
                }
                if (pass == 0) m = swap_max(m);
            }
            lsum = l[0];
        }
        const float inv = __builtin_amdgcn_rcpf(lsum);
#pragma unroll
        for (int r = 0; r < 16; ++r) o[r] *= inv;
        const char* op = ws + WS_OF + (size_t)((blk * 32 + head) * 2) * 1024;
        st16_dev<0>(op, l16, pack8(o, 0)); st16_dev<1024>(op, l16, pack8(o, 1));
    }
    {
        FRESH();
        const int g = wid >> 2, wi = wid & 3, wm = wi >> 1, wn = wi & 1, g4 = lane >> 4;
        const char* of = ws + WS_OF + (size_t)(br * 64 + mt3 * 4 + wi) * 65536;
        unsigned vw4[4];
#pragma unroll
        for (int ii = 0; ii < 4; ++ii) { const int k = 16 * wi + 4 * ii + g4; vw4[ii] = k * 2048 + (((lane & 15) ^ ((k & 7) << 1)) << 4) - ii * 1024; }
        const unsigned l16 = lane * 16, sdst = lds0 + wi * 4096;
#define STAGE_O(t) glds4k_dev(of + (((t) + k30) & 15) * 4096, l16, sdst + ((t) & 3) * 16384)
#define STAGE_WO(t, slot) do { const char* s_ = wo + (size_t)(((t) + k30) & 15) * 131072; glds_run<0>(s_, vw4[0], sdst + (slot)); glds_run<1>(s_, vw4[1], 0); glds_run<2>(s_, vw4[2], 0); glds_run<3>(s_, vw4[3], 0); } while (0)
        const unsigned fo = (unsigned)WS_FO + ((br * 64 + mt3 * 4 + wi) * 32 + 4 * ((lane & 15) >> 1) + 2 * (1 - g) + (lane & 1)) * FS;
        drain();
        if (lane == 0) flag_set(ws + WS_FO, ((mt * 8 + wid) * 32 + 2 * hp + br) * FS, tag);
        SB_(2);
        asm volatile("s_barrier" ::: "memory");
        SB_(3);
        JIT(12);
        flag_wait(ws, fo, tag);
        STAGE_O(1 - g);
        if (g == 1) STAGE_O(2);
        STAGE_WO(5 - g, 65536 + (1 - g) * 16384);
        SB_(4);
        SA_(6);
        unsigned ao = lds0 + g * 16384 + wm * 8192 + (g4 >> 1) * 1024 + ((lane & 15) + 32 * (g4 & 1)) * 16;
        unsigned aw[4];
        { const int q4 = (lane & 15) >> 2, p = lane & 3, k = 16 * (g4 >> 1) + 4 * (g4 & 1) + q4;
          const unsigned a0 = lds0 + p3_wslot(0) + g * 16384 + k * 256 + (((8 * wn + (p >> 1)) ^ ((k & 7) << 1)) << 4) + (p & 1) * 8;
#pragma unroll
          for (int nb = 0; nb < 4; ++nb) aw[nb] = a0 ^ (nb << 5); }
        const char* so = of + ((3 + g + k30) & 15) * 4096; const char* sw = wo + (size_t)((5 + g + k30) & 15) * 131072;
        unsigned dob = sdst + ((3 + g) & 3) * 16384, dwb = sdst + p3_wslot(6);
        f32x4 acc[4][4];
#pragma unroll
        for (int mb = 0; mb < 4; ++mb)
#pragma unroll
            for (int nb = 0; nb < 4; ++nb) acc[mb][nb] = f32x4{};
        if (g == 1) { WAITBAR_(8); asm volatile("s_barrier" ::: "memory"); } else asm volatile("s_barrier" ::: "memory");
        SB_(5);
        SBAR();
#pragma unroll
        for (int u = 0; u < 8; ++u) {
            bf16x8 of_[4][2], wf[4][2];
            const bool doo = u < 6 || (u == 6 && g == 0), dow = (u >= 1 || g == 1) && (u < 5 || (u == 5 && g == 0));
#define P3RD(i) do { of_[(i) & 3][(i) >> 2] = ldsrd128(ao + (((i) & 3) >> 1) * 4096 + ((i) & 1) * 256 + ((i) >> 2) * 2048); \
                wf[(i) & 3][(i) >> 2] = cat8(ldstr(aw[(i) & 3] + ((i) >> 2) * 8192), ldstr(aw[(i) & 3] + ((i) >> 2) * 8192 + 2048)); } while (0)
            P3RD(0); if (u < 7 && doo) glds_run_dev<0>(so, l16, dob);
            P3RD(1); if (u < 7 && doo) glds_run_dev<1>(so, l16, 0);
            P3RD(2); if (u < 7 && doo) glds_run_dev<2>(so, l16, 0);
            P3RD(3); if (u < 7 && doo) glds_run_dev<3>(so, l16, 0);
            P3RD(4); if (u < 6 && dow) glds_run<0>(sw, vw4[0], dwb);
            P3RD(5); if (u < 6 && dow) glds_run<1>(sw, vw4[1], 0);
            P3RD(6); if (u < 6 && dow) glds_run<2>(sw, vw4[2], 0);
            P3RD(7); if (u < 6 && dow) glds_run<3>(sw, vw4[3], 0);
#undef P3RD
            SBAR();
            if (u < 7) {
                { const int to = 2 * u + 5 + g; so = of + ((to + k30) & 15) * 4096; sw = wo + (size_t)((to + 2 + k30) & 15) * 131072;
                  dob = sdst + (to & 3) * 16384; dwb = sdst + (g ? p3_wslot(2 * u + 8) : p3_wslot(2 * u + 7)); }
                ao ^= 32768;
#pragma unroll
                for (int q = 0; q < 4; ++q) aw[q] += p3_wslot(2 * u + 2) - p3_wslot(2 * u);
                asm volatile("" : "+v"(ao), "+v"(aw[0]), "+v"(aw[1]), "+v"(aw[2]), "+v"(aw[3]), "+s"(so), "+s"(sw), "+s"(dob), "+s"(dwb));
            }
            if (p3_allow(2 * u) == p3_allow(2 * u + 1)) WAITBAR(p3_allow(2 * u)); else if (g == 0) WAITBAR(p3_allow(2 * u)); else WAITBAR(p3_allow(2 * u + 1));
            SBAR();
            __builtin_amdgcn_s_setprio(1);
#pragma unroll
            for (int mb = 0; mb < 4; ++mb)
#pragma unroll
                for (int nb = 0; nb < 4; ++nb) { acc[mb][nb] = MFMA16(wf[nb][0], of_[mb][0], acc[mb][nb]); acc[mb][nb] = MFMA16(wf[nb][1], of_[mb][1], acc[mb][nb]); CHAIN();
                }
            __builtin_amdgcn_s_setprio(0);
            SBAR();
            if (u < 7 || g == 0) asm volatile("s_barrier" ::: "memory");
            SBAR();
        }
        SB_(6);
        FRESH();
        JIT(13);
        const unsigned xw = lds0 + wid * 8192 + l16, xr = lds0 + (wid ^ 4) * 8192 + l16;
#pragma unroll
        for (int i = 0; i < 8; ++i) { const f32x4 v = g ? acc[i >> 2][i & 3] : acc[2 + (i >> 2)][i & 3]; ldswr128(xw + i * 1024, __builtin_bit_cast(u32x4, v)); }
        BARRIER_L();
        const unsigned st = lds0 + 65536 + wid * 4096;
#pragma unroll
        for (int i = 0; i < 8; ++i) {
            const f32x4 v = (g ? acc[2 + (i >> 2)][i & 3] : acc[i >> 2][i & 3]) + __builtin_bit_cast(f32x4, ldsrd128(xr + i * 1024));
            const int row = 16 * (i >> 2) + (lane & 15), c = 2 * (i & 3) + (g4 >> 1);
            *(LAS u32x2*)(uintptr_t)(st + row * 128 + ((c ^ (row & 7)) << 4) + (g4 & 1) * 8) = u32x2{cvtpk(v[0], v[1]), cvtpk(v[2], v[3])};
        }
        const int blk = mt3 * 4 + 2 * wm + g;
        char* op = (char*)out + br * 2048 + nt3 * 256 + wn * 128 + (lane & 7) * 16;
#pragma unroll
        for (int i = 0; i < 4; ++i) {
            const int r = 8 * i + (lane >> 3), row = br ? ((blk >> 1) * 64 + 2 * r + (blk & 1)) : (blk * 32 + r);
            st16_dev<0>(out, (unsigned)(row * 4096 + br * 2048 + nt3 * 256 + wn * 128 + (lane & 7) * 16), __builtin_bit_cast(u32x4, ldsrd128(st + r * 128 + (((lane & 7) ^ (r & 7)) << 4))));
        }
#undef STAGE_O
#undef STAGE_WO
    }
    STAMP_AT(0, 7);
    if (b == 0 && wid == 0) {
#pragma unroll
        for (int i = 0; i < 2; ++i) { const int u = i * 64 + lane;
            flag_wait(ws0, (unsigned)WS_FK + ((2 * (u >> 3)) * 32 + (u & 7) * 4) * FS, tag);
            flag_wait(ws0, (unsigned)WS_FO + ((64 + (u & 7) * 8) * 32 + 2 * (u >> 3)) * FS, tag); }
        if (lane == 0) flag_set(ws0, (unsigned)WS_EP, tag);
    }
    asm volatile(".Lmk_code_end:");
}

extern "C" void kernel_launch(void* const* d_in, const int* in_sizes, int n_in, void* d_out, int out_size, void* d_ws, size_t ws_size, hipStream_t stream) {
    mk_fused<<<256, 512, 0, stream>>>((const u16*)d_in[0], (const u16*)d_in[1], (const u16*)d_in[2], (const u16*)d_in[3], (const u16*)d_in[4], (const u16*)d_in[5], (char*)d_ws, (u16*)d_out);
}
```

```cpp
#define PROBE 8
#define ENTRY
#include <hip/hip_runtime.h>
#include <stdint.h>

typedef unsigned short u16;
typedef unsigned long long u64;
typedef __bf16 bf16x8 __attribute__((ext_vector_type(8)));
typedef __bf16 bf16x2 __attribute__((ext_vector_type(2)));
typedef short s16x4 __attribute__((ext_vector_type(4)));
typedef float f32x16 __attribute__((ext_vector_type(16)));
typedef float f32x4 __attribute__((ext_vector_type(4)));
typedef float f32x2 __attribute__((ext_vector_type(2)));
typedef unsigned u32x4 __attribute__((ext_vector_type(4)));
typedef unsigned u32x2 __attribute__((ext_vector_type(2)));
typedef unsigned u32x8 __attribute__((ext_vector_type(8)));
typedef unsigned u32x16 __attribute__((ext_vector_type(16)));
#define LAS __attribute__((address_space(3)))
#define DI __device__ __forceinline__
#define SBAR() __builtin_amdgcn_sched_barrier(0)
#define MFMA32(a, b, c) __builtin_amdgcn_mfma_f32_32x32x16_bf16((a), (b), (c), 0, 0, 0)
#define MFMA16(a, b, c) __builtin_amdgcn_mfma_f32_16x16x32_bf16((a), (b), (c), 0, 0, 0)
#define CHAIN() SBAR()
#define EXP2(x) __builtin_amdgcn_exp2f(x)

constexpr float QSCALE = 0.17677669529663687f * 1.4426950408889634f;

constexpr size_t WS_QF = 0;
constexpr size_t WS_KV = WS_QF + (4u << 20);
constexpr size_t WS_OF = WS_KV + (8u << 20);
constexpr size_t WS_HALO = WS_OF + (8u << 20);
constexpr size_t WS_XP = WS_HALO + (2u << 20);
constexpr size_t WS_FX = WS_XP + (5u << 20);
#define FS 64
constexpr size_t WS_FQ = WS_FX + 1024 * FS;
constexpr size_t WS_FK = WS_FQ + 1024 * FS;
constexpr size_t WS_FO = WS_FK + 1024 * FS;
constexpr size_t WS_FH = WS_FO + 4096 * FS;
constexpr size_t WS_EP = WS_FH + 256 * FS;

#define OUTST 1
#define SLP 1
#define STAMP_AT(K, I)
#define SA_(I)
#define SB_(I)

#define SC_(I)
#undef STAMP_AT
#undef SA_
#define STAMP_AT(K, I) do { pst[I] = (unsigned)__builtin_amdgcn_s_memrealtime(); } while (0)
#define SA_(I) STAMP_AT(0, I)
#define JIT(p)
DI unsigned cvtpk(float lo, float hi) { f32x2 v = {lo, hi}; bf16x2 b = __builtin_convertvector(v, bf16x2); return __builtin_bit_cast(unsigned, b); }
DI u32x4 pack8(const f32x16& a, int s) { u32x4 r; r[0] = cvtpk(a[8 * s], a[8 * s + 1]); r[1] = cvtpk(a[8 * s + 2], a[8 * s + 3]); r[2] = cvtpk(a[8 * s + 4], a[8 * s + 5]); r[3] = cvtpk(a[8 * s + 6], a[8 * s + 7]); return r; }
DI u32x4 pack44(f32x4 a, f32x4 c) { return u32x4{cvtpk(a[0], a[1]), cvtpk(a[2], a[3]), cvtpk(c[0], c[1]), cvtpk(c[2], c[3])}; }
DI u32x4 asu(bf16x8 v) { return __builtin_bit_cast(u32x4, v); }
DI u32x16 cat16(u32x4 a, u32x4 b, u32x4 c, u32x4 d) { return u32x16{a[0], a[1], a[2], a[3], b[0], b[1], b[2], b[3], c[0], c[1], c[2], c[3], d[0], d[1], d[2], d[3]}; }
DI bf16x8 asbf(u32x4 v) { return __builtin_bit_cast(bf16x8, v); }
DI void glds16(const void* sbase, unsigned voff, unsigned lds) {
    asm volatile("s_mov_b32 m0, %2\n\ts_nop 0\n\tglobal_load_lds_dwordx4 %0, %1" :: "v"(voff), "s"(sbase), "s"(lds) : "memory");
}
template <int I> DI void glds_run(const void* sbase, unsigned voff, unsigned lds) {
    if (I == 0) asm volatile("s_mov_b32 m0, %2\n\ts_nop 0\n\tglobal_load_lds_dwordx4 %0, %1" :: "v"(voff), "s"(sbase), "s"(lds) : "memory");
    else asm volatile("global_load_lds_dwordx4 %0, %1 offset:%2" :: "v"(voff), "s"(sbase), "i"(I * 1024) : "memory");
}
template <int I> DI void glds_run_dev(const void* sbase, unsigned voff, unsigned lds) {
    if (I == 0) asm volatile("s_mov_b32 m0, %2\n\ts_nop 0\n\tglobal_load_lds_dwordx4 %0, %1 sc1" :: "v"(voff), "s"(sbase), "s"(lds) : "memory");
    else asm volatile("global_load_lds_dwordx4 %0, %1 offset:%2 sc1" :: "v"(voff), "s"(sbase), "i"(I * 1024) : "memory");
}
DI void glds16_dev(const void* sbase, unsigned voff, unsigned lds) {
    asm volatile("s_mov_b32 m0, %2\n\ts_nop 0\n\tglobal_load_lds_dwordx4 %0, %1 sc1" :: "v"(voff), "s"(sbase), "s"(lds) : "memory");
}
DI void glds4k_dev(const void* sbase, unsigned voff, unsigned lds) {
    asm volatile("s_mov_b32 m0, %2\n\ts_nop 0\n\tglobal_load_lds_dwordx4 %0, %1 sc1\n\tglobal_load_lds_dwordx4 %0, %1 offset:1024 sc1\n\t"
                 "global_load_lds_dwordx4 %0, %1 offset:2048 sc1\n\tglobal_load_lds_dwordx4 %0, %1 offset:3072 sc1" :: "v"(voff), "s"(sbase), "s"(lds) : "memory");
}
template <int OFF> DI void st16_dev(const void* sbase, unsigned voff, u32x4 v) {
    asm volatile("global_store_dwordx4 %0, %1, %2 offset:%3 sc1\n\ts_nop 1" :: "v"(voff), "v"(v), "s"(sbase), "i"(OFF) : "memory");
}
DI void drain() { asm volatile("s_waitcnt vmcnt(0)" ::: "memory"); }
DI void flag_set(const void* sbase, unsigned voff, u64 tag) {
    asm volatile("global_store_dwordx2 %0, %1, %2 sc1" :: "v"(voff), "v"(tag), "s"(sbase) : "memory");
}
DI void flag_wait(const void* sbase, unsigned voff, u64 tag) {
    const u64 t0 = __builtin_amdgcn_s_memrealtime();
    for (;;) {
        u64 v;
        asm volatile("global_load_dwordx2 %0, %1, %2 sc1\n\ts_waitcnt vmcnt(0)" : "=v"(v) : "v"(voff), "s"(sbase) : "memory");
        if (__all(v == tag)) break;
        if (__builtin_amdgcn_s_memrealtime() - t0 > 2000000ull) break;
        __builtin_amdgcn_s_sleep(SLP);
    }
}
DI u64 flag_wait_some(const void* sbase, unsigned voff, u64 tag, u64 need) {
    const u64 t0 = __builtin_amdgcn_s_memrealtime();
    for (;;) {
        u64 v;
        asm volatile("global_load_dwordx2 %0, %1, %2 sc1\n\ts_waitcnt vmcnt(0)" : "=v"(v) : "v"(voff), "s"(sbase) : "memory");
        const u64 m = __ballot(v == tag);
        if ((m & need) == need || __builtin_amdgcn_s_memrealtime() - t0 > 2000000ull) return m;
        __builtin_amdgcn_s_sleep(SLP);
    }
}
DI bf16x8 ldsrd128(unsigned addr) { return *(const LAS bf16x8*)(uintptr_t)addr; }
DI void ldswr128(unsigned addr, u32x4 v) { *(LAS u32x4*)(uintptr_t)addr = v; }
DI s16x4 ldstr(unsigned addr) { return __builtin_amdgcn_ds_read_tr16_b64_v4i16((LAS s16x4*)(uintptr_t)addr); }
DI bf16x8 cat8(s16x4 lo, s16x4 hi) { typedef short s16x8 __attribute__((ext_vector_type(8))); s16x8 v = {lo[0], lo[1], lo[2], lo[3], hi[0], hi[1], hi[2], hi[3]}; return __builtin_bit_cast(bf16x8, v); }
DI float swap_max(float m) { auto r = __builtin_amdgcn_permlane32_swap(__float_as_uint(m), __float_as_uint(m), false, false); return __builtin_fmaxf(__uint_as_float(r[0]), __uint_as_float(r[1])); }
DI float swap_add(float m) { auto r = __builtin_amdgcn_permlane32_swap(__float_as_uint(m), __float_as_uint(m), false, false); return __uint_as_float(r[0]) + __uint_as_float(r[1]); }
#define FRESH() asm volatile("" : "+v"(lane), "+s"(ws))
#define BARRIER_L() asm volatile("s_waitcnt lgkmcnt(0)\n\ts_barrier" ::: "memory")
#define WAITBAR_(n) asm volatile("s_waitcnt vmcnt(%0) lgkmcnt(0)\n\ts_barrier" :: "i"(n) : "memory")
#define WAITBAR(n) do { switch (n) { case 0: WAITBAR_(0); break; case 2: WAITBAR_(2); break; case 3: WAITBAR_(3); break; case 4: WAITBAR_(4); break; case 6: WAITBAR_(6); break; \
    case 7: WAITBAR_(7); break; case 8: WAITBAR_(8); break; case 10: WAITBAR_(10); break; case 12: WAITBAR_(12); break; default: WAITBAR_(0); } } while (0)

struct LocalPart { f32x16 o; float m, l; };
DI f32x16 band_mask(int lane) {
    const int i = lane & 31, h = lane >> 5;
    f32x16 c;
#pragma unroll
    for (int r = 0; r < 16; ++r) { const int j = (r & 3) + 8 * (r >> 2) + 4 * h; c[r] = ((unsigned)(i - j) <= 7u) ? 0.f : -__builtin_inff(); }
    return c;
}
DI float max3(float a, float b, float c) { float r; asm("v_max3_f32 %0, %1, %2, %3" : "=v"(r) : "v"(a), "v"(b), "v"(c)); return r; }
template <bool PREV> DI void local_own(bf16x8 q0, bf16x8 q1, bf16x8 k0, bf16x8 k1, bf16x8 v0, bf16x8 v1, bf16x8 kp0, bf16x8 kp1, bf16x8 vp1, const f32x16& band, int lane, LocalPart& P) {
    const f32x16 z = {};
    f32x16 so = MFMA32(k0, q0, band), sp = z;
    if (PREV) sp = MFMA32(kp0, q0, z);
    so = MFMA32(k1, q1, so);
    if (PREV) sp = MFMA32(kp1, q1, sp);
    float m = __builtin_fmaxf(so[0], so[1]);
#pragma unroll
    for (int r = 2; r < 16; r += 2) m = __builtin_fmaxf(__builtin_fmaxf(m, so[r]), so[r + 1]);
    if (PREV) {
        const int d = 4 * (lane >> 5) - (lane & 31);
#pragma unroll
        for (int r = 12; r < 16; ++r) { sp[r] = (d + (r & 3) >= 1) ? sp[r] : -__builtin_inff(); m = __builtin_fmaxf(m, sp[r]); }
    }
    m = swap_max(m);
    float l = 0.f;
#pragma unroll
    for (int r = 0; r < 16; ++r) { so[r] = EXP2(so[r] - m); l += so[r]; }
    P.o = MFMA32(v0, asbf(pack8(so, 0)), z); P.o = MFMA32(v1, asbf(pack8(so, 1)), P.o);
    if (PREV) {
#pragma unroll
        for (int r = 12; r < 16; ++r) { sp[r] = EXP2(sp[r] - m); l += sp[r]; }
        P.o = MFMA32(vp1, asbf(u32x4{0u, 0u, cvtpk(sp[12], sp[13]), cvtpk(sp[14], sp[15])}), P.o);
    }
    P.m = m; P.l = l;
}
DI void local_prev(bf16x8 q0, bf16x8 q1, bf16x8 kp0, bf16x8 kp1, bf16x8 vp1, int lane, LocalPart& P) {
    const int i = lane & 31, h = lane >> 5;
    const f32x16 z = {};
    f32x16 sp = MFMA32(kp0, q0, z); sp = MFMA32(kp1, q1, sp);
    const float NEG = -__builtin_inff();
    float m = P.m;
#pragma unroll
    for (int r = 12; r < 16; ++r) { const int j = (r & 3) + 24 + 4 * h; sp[r] = (j >= i + 25) ? sp[r] : NEG; m = __builtin_fmaxf(m, sp[r]); }
    m = swap_max(m);
    const float f = EXP2(P.m - m);
    float l = P.l * f;
#pragma unroll
    for (int r = 12; r < 16; ++r) { sp[r] = EXP2(sp[r] - m); l += sp[r]; }
#pragma unroll
    for (int r = 0; r < 16; ++r) P.o[r] *= f;
    P.o = MFMA32(vp1, asbf(u32x4{0u, 0u, cvtpk(sp[12], sp[13]), cvtpk(sp[14], sp[15])}), P.o);
    P.m = m; P.l = l;
}
DI void local_fin(LocalPart& P) {
    const float inv = __builtin_amdgcn_rcpf(swap_add(P.l));
#pragma unroll
    for (int r = 0; r < 16; ++r) P.o[r] *= inv;
    const u32x4 o0 = pack8(P.o, 0), o1 = pack8(P.o, 1);
#pragma unroll
    for (int r = 0; r < 4; ++r) { P.o[r] = __uint_as_float(o0[r]); P.o[4 + r] = __uint_as_float(o1[r]); }
}
DI u32x4 quad(const f32x16& a, int i) { return __builtin_bit_cast(u32x4, f32x4{a[4 * i], a[4 * i + 1], a[4 * i + 2], a[4 * i + 3]}); }

#define ATT_L6C \
    "s_nop 1\n\t" \
    "s_cmp_eq_u32 %[hl], 0\n\t" \
    "s_cbranch_scc1 10f\n\t" \
    "global_load_dwordx4 v[144:147], v143, %[ws] offset:-3072 sc1\n\t" \
    "global_load_dwordx4 v[148:151], v143, %[ws] offset:-2048 sc1\n\t" \
    "global_load_dwordx4 v[152:155], v143, %[ws] offset:-1024 sc1\n\t" \
    "global_load_dwordx4 v[156:159], v143, %[ws] offset:0 sc1\n\t" \
    "global_load_dwordx4 v[160:163], v143, %[ws] offset:1024 sc1\n\t" \
    "global_load_dwordx4 v[164:167], v143, %[ws] offset:2048 sc1\n\t" \
    "10:\n" \
    "s_setprio 3\n\t" \
    "s_waitcnt lgkmcnt(0)\n\t" \
    "v_mfma_f32_32x32x16_bf16 v[48:63], v[80:83], v[64:67], v[16:31]\n\t" \
    "v_exp_f32_e32 v32, v32\n\t" \
    "v_exp_f32_e32 v33, v33\n\t" \
    "v_exp_f32_e32 v34, v34\n\t" \
    "v_exp_f32_e32 v35, v35\n\t" \
    "v_exp_f32_e32 v36, v36\n\t" \
    "v_mfma_f32_32x32x16_bf16 v[48:63], v[84:87], v[68:71], v[48:63]\n\t" \
    "ds_read_b128 v[80:83], v112 offset:8192\n\t" \
    "ds_read_b128 v[84:87], v112 offset:9216\n\t" \
    "v_cvt_pk_bf16_f32 v104, v32, v33\n\t" \
    "v_cvt_pk_bf16_f32 v105, v34, v35\n\t" \
    "v_exp_f32_e32 v37, v37\n\t" \
    "v_exp_f32_e32 v38, v38\n\t" \
    "v_exp_f32_e32 v39, v39\n\t" \
    "ds_read_b128 v[100:103], v112 offset:3072\n\t" \
    "ds_read_b128 v[96:99], v112 offset:6144\n\t" \
    "v_exp_f32_e32 v40, v40\n\t" \
    "v_cvt_pk_bf16_f32 v106, v36, v37\n\t" \
    "v_cvt_pk_bf16_f32 v107, v38, v39\n\t" \
    "v_exp_f32_e32 v41, v41\n\t" \
    "v_exp_f32_e32 v42, v42\n\t" \
    "v_exp_f32_e32 v43, v43\n\t" \
    "v_mfma_f32_32x32x16_bf16 v[0:15], v[88:91], v[104:107], 0\n\t" \
    "v_exp_f32_e32 v44, v44\n\t" \
    "v_exp_f32_e32 v45, v45\n\t" \
    "v_exp_f32_e32 v46, v46\n\t" \
    "v_exp_f32_e32 v47, v47\n\t" \
    "v_cvt_pk_bf16_f32 v108, v40, v41\n\t" \
    "v_cvt_pk_bf16_f32 v109, v42, v43\n\t" \
    "v_cvt_pk_bf16_f32 v110, v44, v45\n\t" \
    "v_cvt_pk_bf16_f32 v111, v46, v47\n\t" \
    "s_mov_b32 m0, %[fld]\n\t" \
    "s_nop 0\n\t" \
    "global_load_lds_dwordx4 v142, %[ws] sc1\n\t" \
    "s_waitcnt lgkmcnt(0)\n\t" \
    "v_mfma_f32_32x32x16_bf16 v[32:47], v[80:83], v[64:67], v[16:31]\n\t" \
    "v_exp_f32_e32 v48, v48\n\t" \
    "v_exp_f32_e32 v49, v49\n\t" \
    "v_exp_f32_e32 v50, v50\n\t" \
    "v_smfmac_f32_16x16x64_bf16 v[76:79], v[72:75], v[104:111], v174\n\t" \
    "v_exp_f32_e32 v51, v51\n\t" \
    "v_exp_f32_e32 v52, v52\n\t" \
    "v_mfma_f32_32x32x16_bf16 v[32:47], v[84:87], v[68:71], v[32:47]\n\t" \
    "ds_read_b128 v[80:83], v112 offset:12288\n\t" \
    "ds_read_b128 v[84:87], v112 offset:13312\n\t" \
    "v_cvt_pk_bf16_f32 v104, v48, v49\n\t" \
    "v_cvt_pk_bf16_f32 v105, v50, v51\n\t" \
    "v_exp_f32_e32 v53, v53\n\t" \
    "v_exp_f32_e32 v54, v54\n\t" \
    "v_exp_f32_e32 v55, v55\n\t" \
    "v_mfma_f32_32x32x16_bf16 v[0:15], v[100:103], v[108:111], v[0:15]\n\t" \
    "ds_read_b128 v[92:95], v112 offset:7168\n\t" \
    "ds_read_b128 v[88:91], v112 offset:10240\n\t" \
    "v_exp_f32_e32 v56, v56\n\t" \
    "v_cvt_pk_bf16_f32 v106, v52, v53\n\t" \
    "v_cvt_pk_bf16_f32 v107, v54, v55\n\t" \
    "v_exp_f32_e32 v57, v57\n\t" \
    "v_exp_f32_e32 v58, v58\n\t" \
    "v_exp_f32_e32 v59, v59\n\t" \
    "v_mfma_f32_32x32x16_bf16 v[0:15], v[96:99], v[104:107], v[0:15]\n\t" \
    "v_exp_f32_e32 v60, v60\n\t" \
    "v_exp_f32_e32 v61, v61\n\t" \
    "v_exp_f32_e32 v62, v62\n\t" \
    "v_exp_f32_e32 v63, v63\n\t" \
    "v_cvt_pk_bf16_f32 v108, v56, v57\n\t" \
    "v_cvt_pk_bf16_f32 v109, v58, v59\n\t" \
    "v_cvt_pk_bf16_f32 v110, v60, v61\n\t" \
    "v_cvt_pk_bf16_f32 v111, v62, v63\n\t" \
    "s_setprio 2\n\t" \
    "s_waitcnt lgkmcnt(0)\n\t" \
    "v_mfma_f32_32x32x16_bf16 v[48:63], v[80:83], v[64:67], v[16:31]\n\t" \
    "v_exp_f32_e32 v32, v32\n\t" \
    "v_exp_f32_e32 v33, v33\n\t" \
    "v_exp_f32_e32 v34, v34\n\t" \
    "v_smfmac_f32_16x16x64_bf16 v[76:79], v[72:75], v[104:111], v174\n\t" \
    "v_exp_f32_e32 v35, v35\n\t" \
    "v_exp_f32_e32 v36, v36\n\t" \
    "v_mfma_f32_32x32x16_bf16 v[48:63], v[84:87], v[68:71], v[48:63]\n\t" \
    "ds_read_b128 v[80:83], v112 offset:16384\n\t" \
    "ds_read_b128 v[84:87], v112 offset:17408\n\t" \
    "v_cvt_pk_bf16_f32 v104, v32, v33\n\t" \
    "v_cvt_pk_bf16_f32 v105, v34, v35\n\t" \
    "v_exp_f32_e32 v37, v37\n\t" \
    "v_exp_f32_e32 v38, v38\n\t" \
    "v_exp_f32_e32 v39, v39\n\t" \
    "v_mfma_f32_32x32x16_bf16 v[0:15], v[92:95], v[108:111], v[0:15]\n\t" \
    "ds_read_b128 v[100:103], v112 offset:11264\n\t" \
    "ds_read_b128 v[96:99], v112 offset:14336\n\t" \
    "v_exp_f32_e32 v40, v40\n\t" \
    "v_cvt_pk_bf16_f32 v106, v36, v37\n\t" \
    "v_cvt_pk_bf16_f32 v107, v38, v39\n\t" \
    "v_exp_f32_e32 v41, v41\n\t" \
    "v_exp_f32_e32 v42, v42\n\t" \
    "v_exp_f32_e32 v43, v43\n\t" \
    "v_mfma_f32_32x32x16_bf16 v[0:15], v[88:91], v[104:107], v[0:15]\n\t" \
    "v_exp_f32_e32 v44, v44\n\t" \
    "v_exp_f32_e32 v45, v45\n\t" \
    "v_exp_f32_e32 v46, v46\n\t" \
    "v_exp_f32_e32 v47, v47\n\t" \
    "v_cvt_pk_bf16_f32 v108, v40, v41\n\t" \
    "v_cvt_pk_bf16_f32 v109, v42, v43\n\t" \
    "v_cvt_pk_bf16_f32 v110, v44, v45\n\t" \
    "v_cvt_pk_bf16_f32 v111, v46, v47\n\t" \
    "s_waitcnt lgkmcnt(0)\n\t" \
    "v_mfma_f32_32x32x16_bf16 v[32:47], v[80:83], v[64:67], v[16:31]\n\t" \
    "v_exp_f32_e32 v48, v48\n\t" \
    "v_exp_f32_e32 v49, v49\n\t" \
    "v_exp_f32_e32 v50, v50\n\t" \
    "v_smfmac_f32_16x16x64_bf16 v[76:79], v[72:75], v[104:111], v174\n\t" \
    "v_exp_f32_e32 v51, v51\n\t" \
    "v_exp_f32_e32 v52, v52\n\t" \
    "v_mfma_f32_32x32x16_bf16 v[32:47], v[84:87], v[68:71], v[32:47]\n\t" \
    "ds_read_b128 v[80:83], v112 offset:20480\n\t" \
    "ds_read_b128 v[84:87], v112 offset:21504\n\t" \
    "v_cvt_pk_bf16_f32 v104, v48, v49\n\t" \
    "v_cvt_pk_bf16_f32 v105, v50, v51\n\t" \
    "v_exp_f32_e32 v53, v53\n\t" \
    "v_exp_f32_e32 v54, v54\n\t" \
    "v_exp_f32_e32 v55, v55\n\t" \
    "v_mfma_f32_32x32x16_bf16 v[0:15], v[100:103], v[108:111], v[0:15]\n\t" \
    "ds_read_b128 v[92:95], v112 offset:15360\n\t" \
    "ds_read_b128 v[88:91], v112 offset:18432\n\t" \
    "v_exp_f32_e32 v56, v56\n\t" \
    "v_cvt_pk_bf16_f32 v106, v52, v53\n\t" \
    "v_cvt_pk_bf16_f32 v107, v54, v55\n\t" \
    "v_exp_f32_e32 v57, v57\n\t" \
    "v_exp_f32_e32 v58, v58\n\t" \
    "v_exp_f32_e32 v59, v59\n\t" \
    "v_mfma_f32_32x32x16_bf16 v[0:15], v[96:99], v[104:107], v[0:15]\n\t" \
    "v_exp_f32_e32 v60, v60\n\t" \
    "v_exp_f32_e32 v61, v61\n\t" \
    "v_exp_f32_e32 v62, v62\n\t" \
    "v_exp_f32_e32 v63, v63\n\t" \
    "v_cvt_pk_bf16_f32 v108, v56, v57\n\t" \
    "v_cvt_pk_bf16_f32 v109, v58, v59\n\t" \
    "v_cvt_pk_bf16_f32 v110, v60, v61\n\t" \
    "v_cvt_pk_bf16_f32 v111, v62, v63\n\t" \
    "s_setprio 1\n\t" \
    "s_waitcnt lgkmcnt(0)\n\t" \
    "v_mfma_f32_32x32x16_bf16 v[48:63], v[80:83], v[64:67], v[16:31]\n\t" \
    "v_exp_f32_e32 v32, v32\n\t" \
    "v_exp_f32_e32 v33, v33\n\t" \
    "v_exp_f32_e32 v34, v34\n\t" \
    "v_smfmac_f32_16x16x64_bf16 v[76:79], v[72:75], v[104:111], v174\n\t" \
    "v_exp_f32_e32 v35, v35\n\t" \
    "v_exp_f32_e32 v36, v36\n\t" \
    "v_mfma_f32_32x32x16_bf16 v[48:63], v[84:87], v[68:71], v[48:63]\n\t" \
    "ds_read_b128 v[80:83], v112 offset:24576\n\t" \
    "ds_read_b128 v[84:87], v112 offset:25600\n\t" \
    "v_cvt_pk_bf16_f32 v104, v32, v33\n\t" \
    "v_cvt_pk_bf16_f32 v105, v34, v35\n\t" \
    "v_exp_f32_e32 v37, v37\n\t" \
    "v_exp_f32_e32 v38, v38\n\t" \
    "v_exp_f32_e32 v39, v39\n\t" \
    "v_mfma_f32_32x32x16_bf16 v[0:15], v[92:95], v[108:111], v[0:15]\n\t" \
    "ds_read_b128 v[100:103], v112 offset:19456\n\t" \
    "ds_read_b128 v[96:99], v112 offset:22528\n\t" \
    "v_exp_f32_e32 v40, v40\n\t" \
    "v_cvt_pk_bf16_f32 v106, v36, v37\n\t" \
    "v_cvt_pk_bf16_f32 v107, v38, v39\n\t" \
    "v_exp_f32_e32 v41, v41\n\t" \
    "v_exp_f32_e32 v42, v42\n\t" \
    "v_exp_f32_e32 v43, v43\n\t" \
    "v_mfma_f32_32x32x16_bf16 v[0:15], v[88:91], v[104:107], v[0:15]\n\t" \
    "v_exp_f32_e32 v44, v44\n\t" \
    "v_exp_f32_e32 v45, v45\n\t" \
    "v_exp_f32_e32 v46, v46\n\t" \
    "v_exp_f32_e32 v47, v47\n\t" \
    "v_cvt_pk_bf16_f32 v108, v40, v41\n\t" \
    "v_cvt_pk_bf16_f32 v109, v42, v43\n\t" \
    "v_cvt_pk_bf16_f32 v110, v44, v45\n\t" \
    "v_cvt_pk_bf16_f32 v111, v46, v47\n\t" \
    "s_waitcnt lgkmcnt(0)\n\t" \
    "v_mfma_f32_32x32x16_bf16 v[32:47], v[80:83], v[64:67], v[16:31]\n\t" \
    "v_exp_f32_e32 v48, v48\n\t" \
    "v_exp_f32_e32 v49, v49\n\t" \
    "v_exp_f32_e32 v50, v50\n\t" \
    "v_smfmac_f32_16x16x64_bf16 v[76:79], v[72:75], v[104:111], v174\n\t" \
    "v_exp_f32_e32 v51, v51\n\t" \
    "v_exp_f32_e32 v52, v52\n\t" \
    "v_mfma_f32_32x32x16_bf16 v[32:47], v[84:87], v[68:71], v[32:47]\n\t" \
    "ds_read_b128 v[80:83], v112 offset:28672\n\t" \
    "ds_read_b128 v[84:87], v112 offset:29696\n\t" \
    "v_cvt_pk_bf16_f32 v104, v48, v49\n\t" \
    "v_cvt_pk_bf16_f32 v105, v50, v51\n\t" \
    "v_exp_f32_e32 v53, v53\n\t" \
    "v_exp_f32_e32 v54, v54\n\t" \
    "v_exp_f32_e32 v55, v55\n\t" \
    "v_mfma_f32_32x32x16_bf16 v[0:15], v[100:103], v[108:111], v[0:15]\n\t" \
    "ds_read_b128 v[92:95], v112 offset:23552\n\t" \
    "ds_read_b128 v[88:91], v112 offset:26624\n\t" \
    "v_exp_f32_e32 v56, v56\n\t" \
    "v_cvt_pk_bf16_f32 v106, v52, v53\n\t" \
    "v_cvt_pk_bf16_f32 v107, v54, v55\n\t" \
    "v_exp_f32_e32 v57, v57\n\t" \
    "v_exp_f32_e32 v58, v58\n\t" \
    "v_exp_f32_e32 v59, v59\n\t" \
    "v_mfma_f32_32x32x16_bf16 v[0:15], v[96:99], v[104:107], v[0:15]\n\t" \
    "v_exp_f32_e32 v60, v60\n\t" \
    "v_exp_f32_e32 v61, v61\n\t" \
    "v_exp_f32_e32 v62, v62\n\t" \
    "v_exp_f32_e32 v63, v63\n\t" \
    "v_cvt_pk_bf16_f32 v108, v56, v57\n\t" \
    "v_cvt_pk_bf16_f32 v109, v58, v59\n\t" \
    "v_cvt_pk_bf16_f32 v110, v60, v61\n\t" \
    "v_cvt_pk_bf16_f32 v111, v62, v63\n\t" \
    "s_setprio 0\n\t" \
    "s_cmp_eq_u32 %[hl], 0\n\t" \
    "s_cbranch_scc1 11f\n\t" \
    "s_waitcnt vmcnt(1)\n\t" \
    "11:\n" \
    "s_waitcnt lgkmcnt(0)\n\t" \
    "s_nop 15\n\t"
#define ATT_L4 \
    "s_nop 1\n\t" \
    "s_setprio 3\n\t" \
    "s_waitcnt lgkmcnt(0)\n\t" \
    "v_mfma_f32_32x32x16_bf16 v[48:63], v[80:83], v[64:67], v[16:31]\n\t" \
    "v_exp_f32_e32 v32, v32\n\t" \
    "v_exp_f32_e32 v33, v33\n\t" \
    "v_exp_f32_e32 v34, v34\n\t" \
    "v_exp_f32_e32 v35, v35\n\t" \
    "v_exp_f32_e32 v36, v36\n\t" \
    "v_mfma_f32_32x32x16_bf16 v[48:63], v[84:87], v[68:71], v[48:63]\n\t" \
    "ds_read_b128 v[80:83], v112 offset:8192\n\t" \
    "ds_read_b128 v[84:87], v112 offset:9216\n\t" \
    "v_cvt_pk_bf16_f32 v104, v32, v33\n\t" \
    "v_cvt_pk_bf16_f32 v105, v34, v35\n\t" \
    "v_exp_f32_e32 v37, v37\n\t" \
    "v_exp_f32_e32 v38, v38\n\t" \
    "v_exp_f32_e32 v39, v39\n\t" \
    "ds_read_b128 v[100:103], v112 offset:3072\n\t" \
    "ds_read_b128 v[96:99], v112 offset:6144\n\t" \
    "v_exp_f32_e32 v40, v40\n\t" \
    "v_cvt_pk_bf16_f32 v106, v36, v37\n\t" \
    "v_cvt_pk_bf16_f32 v107, v38, v39\n\t" \
    "v_exp_f32_e32 v41, v41\n\t" \
    "v_exp_f32_e32 v42, v42\n\t" \
    "v_exp_f32_e32 v43, v43\n\t" \
    "v_mfma_f32_32x32x16_bf16 v[0:15], v[88:91], v[104:107], 0\n\t" \
    "v_exp_f32_e32 v44, v44\n\t" \
    "v_exp_f32_e32 v45, v45\n\t" \
    "v_exp_f32_e32 v46, v46\n\t" \
    "v_exp_f32_e32 v47, v47\n\t" \
    "v_cvt_pk_bf16_f32 v108, v40, v41\n\t" \
    "v_cvt_pk_bf16_f32 v109, v42, v43\n\t" \
    "v_cvt_pk_bf16_f32 v110, v44, v45\n\t" \
    "v_cvt_pk_bf16_f32 v111, v46, v47\n\t" \
    "s_waitcnt lgkmcnt(0)\n\t" \
    "v_mfma_f32_32x32x16_bf16 v[32:47], v[80:83], v[64:67], v[16:31]\n\t" \
    "v_exp_f32_e32 v48, v48\n\t" \
    "v_exp_f32_e32 v49, v49\n\t" \
    "v_exp_f32_e32 v50, v50\n\t" \
    "v_smfmac_f32_16x16x64_bf16 v[76:79], v[72:75], v[104:111], v174\n\t" \
    "v_exp_f32_e32 v51, v51\n\t" \
    "v_exp_f32_e32 v52, v52\n\t" \
    "v_mfma_f32_32x32x16_bf16 v[32:47], v[84:87], v[68:71], v[32:47]\n\t" \
    "ds_read_b128 v[80:83], v112 offset:12288\n\t" \
    "ds_read_b128 v[84:87], v112 offset:13312\n\t" \
    "v_cvt_pk_bf16_f32 v104, v48, v49\n\t" \
    "v_cvt_pk_bf16_f32 v105, v50, v51\n\t" \
    "v_exp_f32_e32 v53, v53\n\t" \
    "v_exp_f32_e32 v54, v54\n\t" \
    "v_exp_f32_e32 v55, v55\n\t" \
    "v_mfma_f32_32x32x16_bf16 v[0:15], v[100:103], v[108:111], v[0:15]\n\t" \
    "ds_read_b128 v[92:95], v112 offset:7168\n\t" \
    "ds_read_b128 v[88:91], v112 offset:10240\n\t" \
    "v_exp_f32_e32 v56, v56\n\t" \
    "v_cvt_pk_bf16_f32 v106, v52, v53\n\t" \
    "v_cvt_pk_bf16_f32 v107, v54, v55\n\t" \
    "v_exp_f32_e32 v57, v57\n\t" \
    "v_exp_f32_e32 v58, v58\n\t" \
    "v_exp_f32_e32 v59, v59\n\t" \
    "v_mfma_f32_32x32x16_bf16 v[0:15], v[96:99], v[104:107], v[0:15]\n\t" \
    "v_exp_f32_e32 v60, v60\n\t" \
    "v_exp_f32_e32 v61, v61\n\t" \
    "v_exp_f32_e32 v62, v62\n\t" \
    "v_exp_f32_e32 v63, v63\n\t" \
    "v_cvt_pk_bf16_f32 v108, v56, v57\n\t" \
    "v_cvt_pk_bf16_f32 v109, v58, v59\n\t" \
    "v_cvt_pk_bf16_f32 v110, v60, v61\n\t" \
    "v_cvt_pk_bf16_f32 v111, v62, v63\n\t" \
    "s_setprio 2\n\t" \
    "s_waitcnt lgkmcnt(0)\n\t" \
    "v_mfma_f32_32x32x16_bf16 v[48:63], v[80:83], v[64:67], v[16:31]\n\t" \
    "v_exp_f32_e32 v32, v32\n\t" \
    "v_exp_f32_e32 v33, v33\n\t" \
    "v_exp_f32_e32 v34, v34\n\t" \
    "v_smfmac_f32_16x16x64_bf16 v[76:79], v[72:75], v[104:111], v174\n\t" \
    "v_exp_f32_e32 v35, v35\n\t" \
    "v_exp_f32_e32 v36, v36\n\t" \
    "v_mfma_f32_32x32x16_bf16 v[48:63], v[84:87], v[68:71], v[48:63]\n\t" \
    "ds_read_b128 v[80:83], v112 offset:16384\n\t" \
    "ds_read_b128 v[84:87], v112 offset:17408\n\t" \
    "v_cvt_pk_bf16_f32 v104, v32, v33\n\t" \
    "v_cvt_pk_bf16_f32 v105, v34, v35\n\t" \
    "v_exp_f32_e32 v37, v37\n\t" \
    "v_exp_f32_e32 v38, v38\n\t" \
    "v_exp_f32_e32 v39, v39\n\t" \
    "v_mfma_f32_32x32x16_bf16 v[0:15], v[92:95], v[108:111], v[0:15]\n\t" \
    "ds_read_b128 v[100:103], v112 offset:11264\n\t" \
    "ds_read_b128 v[96:99], v112 offset:14336\n\t" \
    "v_exp_f32_e32 v40, v40\n\t" \
    "v_cvt_pk_bf16_f32 v106, v36, v37\n\t" \
    "v_cvt_pk_bf16_f32 v107, v38, v39\n\t" \
    "v_exp_f32_e32 v41, v41\n\t" \
    "v_exp_f32_e32 v42, v42\n\t" \
    "v_exp_f32_e32 v43, v43\n\t" \
    "v_mfma_f32_32x32x16_bf16 v[0:15], v[88:91], v[104:107], v[0:15]\n\t" \
    "v_exp_f32_e32 v44, v44\n\t" \
    "v_exp_f32_e32 v45, v45\n\t" \
    "v_exp_f32_e32 v46, v46\n\t" \
    "v_exp_f32_e32 v47, v47\n\t" \
    "v_cvt_pk_bf16_f32 v108, v40, v41\n\t" \
    "v_cvt_pk_bf16_f32 v109, v42, v43\n\t" \
    "v_cvt_pk_bf16_f32 v110, v44, v45\n\t" \
    "v_cvt_pk_bf16_f32 v111, v46, v47\n\t" \
    "s_waitcnt lgkmcnt(0)\n\t" \
    "v_mfma_f32_32x32x16_bf16 v[32:47], v[80:83], v[64:67], v[16:31]\n\t" \
    "v_exp_f32_e32 v48, v48\n\t" \
    "v_exp_f32_e32 v49, v49\n\t" \
    "v_exp_f32_e32 v50, v50\n\t" \
    "v_smfmac_f32_16x16x64_bf16 v[76:79], v[72:75], v[104:111], v174\n\t" \
    "v_exp_f32_e32 v51, v51\n\t" \
    "v_exp_f32_e32 v52, v52\n\t" \
    "v_mfma_f32_32x32x16_bf16 v[32:47], v[84:87], v[68:71], v[32:47]\n\t" \
    "ds_read_b128 v[80:83], v112 offset:20480\n\t" \
    "ds_read_b128 v[84:87], v112 offset:21504\n\t" \
    "v_cvt_pk_bf16_f32 v104, v48, v49\n\t" \
    "v_cvt_pk_bf16_f32 v105, v50, v51\n\t" \
    "v_exp_f32_e32 v53, v53\n\t" \
    "v_exp_f32_e32 v54, v54\n\t" \
    "v_exp_f32_e32 v55, v55\n\t" \
    "v_mfma_f32_32x32x16_bf16 v[0:15], v[100:103], v[108:111], v[0:15]\n\t" \
    "ds_read_b128 v[92:95], v112 offset:15360\n\t" \
    "ds_read_b128 v[88:91], v112 offset:18432\n\t" \
    "v_exp_f32_e32 v56, v56\n\t" \
    "v_cvt_pk_bf16_f32 v106, v52, v53\n\t" \
    "v_cvt_pk_bf16_f32 v107, v54, v55\n\t" \
    "v_exp_f32_e32 v57, v57\n\t" \
    "v_exp_f32_e32 v58, v58\n\t" \
    "v_exp_f32_e32 v59, v59\n\t" \
    "v_mfma_f32_32x32x16_bf16 v[0:15], v[96:99], v[104:107], v[0:15]\n\t" \
    "v_exp_f32_e32 v60, v60\n\t" \
    "v_exp_f32_e32 v61, v61\n\t" \
    "v_exp_f32_e32 v62, v62\n\t" \
    "v_exp_f32_e32 v63, v63\n\t" \
    "v_cvt_pk_bf16_f32 v108, v56, v57\n\t" \
    "v_cvt_pk_bf16_f32 v109, v58, v59\n\t" \
    "v_cvt_pk_bf16_f32 v110, v60, v61\n\t" \
    "v_cvt_pk_bf16_f32 v111, v62, v63\n\t" \
    "s_setprio 0\n\t" \
    "s_waitcnt lgkmcnt(0)\n\t" \
    "s_nop 15\n\t"
#define ATT_L2 \
    "s_nop 1\n\t" \
    "s_setprio 1\n\t" \
    "s_waitcnt lgkmcnt(0)\n\t" \
    "v_mfma_f32_32x32x16_bf16 v[48:63], v[80:83], v[64:67], v[16:31]\n\t" \
    "v_exp_f32_e32 v32, v32\n\t" \
    "v_exp_f32_e32 v33, v33\n\t" \
    "v_exp_f32_e32 v34, v34\n\t" \
    "v_smfmac_f32_16x16x64_bf16 v[76:79], v[72:75], v[104:111], v174\n\t" \
    "v_exp_f32_e32 v35, v35\n\t" \
    "v_exp_f32_e32 v36, v36\n\t" \
    "v_mfma_f32_32x32x16_bf16 v[48:63], v[84:87], v[68:71], v[48:63]\n\t" \
    "ds_read_b128 v[80:83], v112 offset:24576\n\t" \
    "ds_read_b128 v[84:87], v112 offset:25600\n\t" \
    "v_cvt_pk_bf16_f32 v104, v32, v33\n\t" \
    "v_cvt_pk_bf16_f32 v105, v34, v35\n\t" \
    "v_exp_f32_e32 v37, v37\n\t" \
    "v_exp_f32_e32 v38, v38\n\t" \
    "v_exp_f32_e32 v39, v39\n\t" \
    "v_mfma_f32_32x32x16_bf16 v[0:15], v[92:95], v[108:111], v[0:15]\n\t" \
    "ds_read_b128 v[100:103], v112 offset:19456\n\t" \
    "ds_read_b128 v[96:99], v112 offset:22528\n\t" \
    "v_exp_f32_e32 v40, v40\n\t" \
    "v_cvt_pk_bf16_f32 v106, v36, v37\n\t" \
    "v_cvt_pk_bf16_f32 v107, v38, v39\n\t" \
    "v_exp_f32_e32 v41, v41\n\t" \
    "v_exp_f32_e32 v42, v42\n\t" \
    "v_exp_f32_e32 v43, v43\n\t" \
    "v_mfma_f32_32x32x16_bf16 v[0:15], v[88:91], v[104:107], v[0:15]\n\t" \
    "v_exp_f32_e32 v44, v44\n\t" \
    "v_exp_f32_e32 v45, v45\n\t" \
    "v_exp_f32_e32 v46, v46\n\t" \
    "v_exp_f32_e32 v47, v47\n\t" \
    "v_cvt_pk_bf16_f32 v108, v40, v41\n\t" \
    "v_cvt_pk_bf16_f32 v109, v42, v43\n\t" \
    "v_cvt_pk_bf16_f32 v110, v44, v45\n\t" \
    "v_cvt_pk_bf16_f32 v111, v46, v47\n\t" \
    "s_waitcnt lgkmcnt(0)\n\t" \
    "v_mfma_f32_32x32x16_bf16 v[32:47], v[80:83], v[64:67], v[16:31]\n\t" \
    "v_exp_f32_e32 v48, v48\n\t" \
    "v_exp_f32_e32 v49, v49\n\t" \
    "v_exp_f32_e32 v50, v50\n\t" \
    "v_smfmac_f32_16x16x64_bf16 v[76:79], v[72:75], v[104:111], v174\n\t" \
    "v_exp_f32_e32 v51, v51\n\t" \
    "v_exp_f32_e32 v52, v52\n\t" \
    "v_mfma_f32_32x32x16_bf16 v[32:47], v[84:87], v[68:71], v[32:47]\n\t" \
    "ds_read_b128 v[80:83], v112 offset:28672\n\t" \
    "ds_read_b128 v[84:87], v112 offset:29696\n\t" \
    "v_cvt_pk_bf16_f32 v104, v48, v49\n\t" \
    "v_cvt_pk_bf16_f32 v105, v50, v51\n\t" \
    "v_exp_f32_e32 v53, v53\n\t" \
    "v_exp_f32_e32 v54, v54\n\t" \
    "v_exp_f32_e32 v55, v55\n\t" \
    "v_mfma_f32_32x32x16_bf16 v[0:15], v[100:103], v[108:111], v[0:15]\n\t" \
    "ds_read_b128 v[92:95], v112 offset:23552\n\t" \
    "ds_read_b128 v[88:91], v112 offset:26624\n\t" \
    "v_exp_f32_e32 v56, v56\n\t" \
    "v_cvt_pk_bf16_f32 v106, v52, v53\n\t" \
    "v_cvt_pk_bf16_f32 v107, v54, v55\n\t" \
    "v_exp_f32_e32 v57, v57\n\t" \
    "v_exp_f32_e32 v58, v58\n\t" \
    "v_exp_f32_e32 v59, v59\n\t" \
    "v_mfma_f32_32x32x16_bf16 v[0:15], v[96:99], v[104:107], v[0:15]\n\t" \
    "v_exp_f32_e32 v60, v60\n\t" \
    "v_exp_f32_e32 v61, v61\n\t" \
    "v_exp_f32_e32 v62, v62\n\t" \
    "v_exp_f32_e32 v63, v63\n\t" \
    "v_cvt_pk_bf16_f32 v108, v56, v57\n\t" \
    "v_cvt_pk_bf16_f32 v109, v58, v59\n\t" \
    "v_cvt_pk_bf16_f32 v110, v60, v61\n\t" \
    "v_cvt_pk_bf16_f32 v111, v62, v63\n\t" \
    "s_setprio 0\n\t" \
    "s_waitcnt lgkmcnt(0)\n\t" \
    "s_nop 15\n\t"
#define ATT_XT \
    "s_nop 1\n\t" \
    "s_waitcnt lgkmcnt(0)\n\t" \
    "v_mfma_f32_32x32x16_bf16 v[48:63], v[80:83], v[64:67], v[16:31]\n\t" \
    "v_exp_f32_e32 v32, v32\n\t" \
    "v_exp_f32_e32 v33, v33\n\t" \
    "v_exp_f32_e32 v34, v34\n\t" \
    "v_smfmac_f32_16x16x64_bf16 v[76:79], v[72:75], v[104:111], v174\n\t" \
    "v_exp_f32_e32 v35, v35\n\t" \
    "v_exp_f32_e32 v36, v36\n\t" \
    "v_mfma_f32_32x32x16_bf16 v[48:63], v[84:87], v[68:71], v[48:63]\n\t" \
    "v_cvt_pk_bf16_f32 v104, v32, v33\n\t" \
    "v_cvt_pk_bf16_f32 v105, v34, v35\n\t" \
    "v_exp_f32_e32 v37, v37\n\t" \
    "v_exp_f32_e32 v38, v38\n\t" \
    "v_exp_f32_e32 v39, v39\n\t" \
    "v_mfma_f32_32x32x16_bf16 v[0:15], v[92:95], v[108:111], v[0:15]\n\t" \
    "ds_read_b128 v[100:103], v112 offset:27648\n\t" \
    "ds_read_b128 v[96:99], v112 offset:30720\n\t" \
    "v_exp_f32_e32 v40, v40\n\t" \
    "v_cvt_pk_bf16_f32 v106, v36, v37\n\t" \
    "v_cvt_pk_bf16_f32 v107, v38, v39\n\t" \
    "v_exp_f32_e32 v41, v41\n\t" \
    "v_exp_f32_e32 v42, v42\n\t" \
    "v_exp_f32_e32 v43, v43\n\t" \
    "v_mfma_f32_32x32x16_bf16 v[0:15], v[88:91], v[104:107], v[0:15]\n\t" \
    "v_exp_f32_e32 v44, v44\n\t" \
    "v_exp_f32_e32 v45, v45\n\t" \
    "v_exp_f32_e32 v46, v46\n\t" \
    "v_exp_f32_e32 v47, v47\n\t" \
    "v_cvt_pk_bf16_f32 v108, v40, v41\n\t" \
    "v_cvt_pk_bf16_f32 v109, v42, v43\n\t" \
    "v_cvt_pk_bf16_f32 v110, v44, v45\n\t" \
    "v_cvt_pk_bf16_f32 v111, v46, v47\n\t" \
    "s_waitcnt lgkmcnt(0)\n\t" \
    "v_exp_f32_e32 v48, v48\n\t" \
    "v_exp_f32_e32 v49, v49\n\t" \
    "v_exp_f32_e32 v50, v50\n\t" \
    "v_smfmac_f32_16x16x64_bf16 v[76:79], v[72:75], v[104:111], v174\n\t" \
    "v_exp_f32_e32 v51, v51\n\t" \
    "v_exp_f32_e32 v52, v52\n\t" \
    "v_cvt_pk_bf16_f32 v104, v48, v49\n\t" \
    "v_cvt_pk_bf16_f32 v105, v50, v51\n\t" \
    "v_exp_f32_e32 v53, v53\n\t" \
    "v_exp_f32_e32 v54, v54\n\t" \
    "v_exp_f32_e32 v55, v55\n\t" \
    "v_mfma_f32_32x32x16_bf16 v[0:15], v[100:103], v[108:111], v[0:15]\n\t" \
    "ds_read_b128 v[92:95], v112 offset:31744\n\t" \
    "v_exp_f32_e32 v56, v56\n\t" \
    "v_cvt_pk_bf16_f32 v106, v52, v53\n\t" \
    "v_cvt_pk_bf16_f32 v107, v54, v55\n\t" \
    "v_exp_f32_e32 v57, v57\n\t" \
    "v_exp_f32_e32 v58, v58\n\t" \
    "v_exp_f32_e32 v59, v59\n\t" \
    "v_mfma_f32_32x32x16_bf16 v[0:15], v[96:99], v[104:107], v[0:15]\n\t" \
    "v_exp_f32_e32 v60, v60\n\t" \
    "v_exp_f32_e32 v61, v61\n\t" \
    "v_exp_f32_e32 v62, v62\n\t" \
    "v_exp_f32_e32 v63, v63\n\t" \
    "v_cvt_pk_bf16_f32 v108, v56, v57\n\t" \
    "v_cvt_pk_bf16_f32 v109, v58, v59\n\t" \
    "v_cvt_pk_bf16_f32 v110, v60, v61\n\t" \
    "v_cvt_pk_bf16_f32 v111, v62, v63\n\t" \
    "s_waitcnt lgkmcnt(0)\n\t" \
    "s_nop 0\n\t" \
    "v_mfma_f32_32x32x16_bf16 v[0:15], v[92:95], v[108:111], v[0:15]\n\t" \
    "v_smfmac_f32_16x16x64_bf16 v[76:79], v[72:75], v[104:111], v174\n\t" \
    "s_nop 15\n\t"
#define ATT_MAIN \
    "s_mov_b64 s[48:49], %[tab]\n\t" \
    "s_mov_b32 s46, %[k0]\n\t" \
    "s_and_b32 s40, s48, 63\n\t" \
    "s_lshl_b32 s40, s40, 12\n\t" \
    "s_add_u32 s44, %[srclo], s40\n\t" \
    "s_addc_u32 s45, %[srchi], 0\n\t" \
    "s_lshr_b64 s[48:49], s[48:49], 6\n\t" \
    "s_add_u32 s46, s46, 1\n\t" \
    "s_mov_b64 vcc, -1\n\t" \
    "s_waitcnt vmcnt(0)\n\t" \
    "ds_read_b64 v[120:121], v173\n\t" \
    "s_waitcnt lgkmcnt(0)\n\t" \
    "1:\n" \
    "s_cmp_eq_u32 %[cnt], 4\n\t" \
    "s_cbranch_scc1 5f\n\t" \
    "s_waitcnt vmcnt(0)\n\t" \
    "5:\n" \
    "s_waitcnt vmcnt(9)\n\t" \
    "s_barrier\n\t" \
    "s_bitcmp1_b32 %[act], %[cnt]\n\t" \
    "s_cbranch_scc1 30f\n\t" \
    "2:\n" \
    "s_setprio 3\n\t" \
    "s_waitcnt lgkmcnt(0)\n\t" \
    "v_mfma_f32_32x32x16_bf16 v[48:63], v[80:83], v[64:67], v[16:31]\n\t" \
    "v_exp_f32_e32 v32, v32\n\t" \
    "v_exp_f32_e32 v33, v33\n\t" \
    "v_exp_f32_e32 v34, v34\n\t" \
    "v_smfmac_f32_16x16x64_bf16 v[76:79], v[72:75], v[104:111], v174\n\t" \
    "v_exp_f32_e32 v35, v35\n\t" \
    "v_exp_f32_e32 v36, v36\n\t" \
    "v_mfma_f32_32x32x16_bf16 v[48:63], v[84:87], v[68:71], v[48:63]\n\t" \
    "ds_read_b128 v[80:83], v113 offset:0\n\t" \
    "ds_read_b128 v[84:87], v113 offset:1024\n\t" \
    "v_cvt_pk_bf16_f32 v104, v32, v33\n\t" \
    "v_cvt_pk_bf16_f32 v105, v34, v35\n\t" \
    "v_exp_f32_e32 v37, v37\n\t" \
    "v_exp_f32_e32 v38, v38\n\t" \
    "v_exp_f32_e32 v39, v39\n\t" \
    "v_mfma_f32_32x32x16_bf16 v[0:15], v[92:95], v[108:111], v[0:15]\n\t" \
    "ds_read_b128 v[100:103], v112 offset:27648\n\t" \
    "ds_read_b128 v[96:99], v112 offset:30720\n\t" \
    "v_exp_f32_e32 v40, v40\n\t" \
    "v_cvt_pk_bf16_f32 v106, v36, v37\n\t" \
    "v_cvt_pk_bf16_f32 v107, v38, v39\n\t" \
    "v_exp_f32_e32 v41, v41\n\t" \
    "v_exp_f32_e32 v42, v42\n\t" \
    "v_exp_f32_e32 v43, v43\n\t" \
    "v_mfma_f32_32x32x16_bf16 v[0:15], v[88:91], v[104:107], v[0:15]\n\t" \
    "s_cbranch_vccz 20f\n\t" \
    "v_cmp_eq_u64_e64 s[42:43], v[120:121], v[140:141]\n\t" \
    "s_mov_b32 m0, %[dst]\n\t" \
    "s_nop 3\n\t" \
    "s_bitcmp1_b64 s[42:43], s46\n\t" \
    "s_cbranch_scc0 9f\n\t" \
    "12:\n" \
    "global_load_lds_dwordx4 v114, s[44:45] offset:0 sc1\n\t" \
    "20:\n" \
    "v_exp_f32_e32 v44, v44\n\t" \
    "v_exp_f32_e32 v45, v45\n\t" \
    "v_exp_f32_e32 v46, v46\n\t" \
    "v_exp_f32_e32 v47, v47\n\t" \
    "v_cvt_pk_bf16_f32 v108, v40, v41\n\t" \
    "v_cvt_pk_bf16_f32 v109, v42, v43\n\t" \
    "v_cvt_pk_bf16_f32 v110, v44, v45\n\t" \
    "v_cvt_pk_bf16_f32 v111, v46, v47\n\t" \
    "s_waitcnt lgkmcnt(0)\n\t" \
    "v_mfma_f32_32x32x16_bf16 v[32:47], v[80:83], v[64:67], v[16:31]\n\t" \
    "v_exp_f32_e32 v48, v48\n\t" \
    "v_exp_f32_e32 v49, v49\n\t" \
    "v_exp_f32_e32 v50, v50\n\t" \
    "v_smfmac_f32_16x16x64_bf16 v[76:79], v[72:75], v[104:111], v174\n\t" \
    "v_exp_f32_e32 v51, v51\n\t" \
    "v_exp_f32_e32 v52, v52\n\t" \
    "v_mfma_f32_32x32x16_bf16 v[32:47], v[84:87], v[68:71], v[32:47]\n\t" \
    "ds_read_b128 v[80:83], v113 offset:4096\n\t" \
    "ds_read_b128 v[84:87], v113 offset:5120\n\t" \
    "v_cvt_pk_bf16_f32 v104, v48, v49\n\t" \
    "v_cvt_pk_bf16_f32 v105, v50, v51\n\t" \
    "v_exp_f32_e32 v53, v53\n\t" \
    "v_exp_f32_e32 v54, v54\n\t" \
    "v_exp_f32_e32 v55, v55\n\t" \
    "v_mfma_f32_32x32x16_bf16 v[0:15], v[100:103], v[108:111], v[0:15]\n\t" \
    "ds_read_b128 v[92:95], v112 offset:31744\n\t" \
    "ds_read_b128 v[88:91], v113 offset:2048\n\t" \
    "v_exp_f32_e32 v56, v56\n\t" \
    "v_cvt_pk_bf16_f32 v106, v52, v53\n\t" \
    "v_cvt_pk_bf16_f32 v107, v54, v55\n\t" \
    "v_exp_f32_e32 v57, v57\n\t" \
    "v_exp_f32_e32 v58, v58\n\t" \
    "v_exp_f32_e32 v59, v59\n\t" \
    "v_mfma_f32_32x32x16_bf16 v[0:15], v[96:99], v[104:107], v[0:15]\n\t" \
    "s_cbranch_vccz 21f\n\t" \
    "global_load_lds_dwordx4 v114, s[44:45] offset:1024 sc1\n\t" \
    "21:\n" \
    "v_exp_f32_e32 v60, v60\n\t" \
    "v_exp_f32_e32 v61, v61\n\t" \
    "v_exp_f32_e32 v62, v62\n\t" \
    "v_exp_f32_e32 v63, v63\n\t" \
    "v_cvt_pk_bf16_f32 v108, v56, v57\n\t" \
    "v_cvt_pk_bf16_f32 v109, v58, v59\n\t" \
    "v_cvt_pk_bf16_f32 v110, v60, v61\n\t" \
    "v_cvt_pk_bf16_f32 v111, v62, v63\n\t" \
    "s_setprio 2\n\t" \
    "s_waitcnt lgkmcnt(0)\n\t" \
    "v_mfma_f32_32x32x16_bf16 v[48:63], v[80:83], v[64:67], v[16:31]\n\t" \
    "v_exp_f32_e32 v32, v32\n\t" \
    "v_exp_f32_e32 v33, v33\n\t" \
    "v_exp_f32_e32 v34, v34\n\t" \
    "v_smfmac_f32_16x16x64_bf16 v[76:79], v[72:75], v[104:111], v174\n\t" \
    "v_exp_f32_e32 v35, v35\n\t" \
    "v_exp_f32_e32 v36, v36\n\t" \
    "v_mfma_f32_32x32x16_bf16 v[48:63], v[84:87], v[68:71], v[48:63]\n\t" \
    "ds_read_b128 v[80:83], v113 offset:8192\n\t" \
    "ds_read_b128 v[84:87], v113 offset:9216\n\t" \
    "v_cvt_pk_bf16_f32 v104, v32, v33\n\t" \
    "v_cvt_pk_bf16_f32 v105, v34, v35\n\t" \
    "v_exp_f32_e32 v37, v37\n\t" \
    "v_exp_f32_e32 v38, v38\n\t" \
    "v_exp_f32_e32 v39, v39\n\t" \
    "v_mfma_f32_32x32x16_bf16 v[0:15], v[92:95], v[108:111], v[0:15]\n\t" \
    "ds_read_b128 v[100:103], v113 offset:3072\n\t" \
    "ds_read_b128 v[96:99], v113 offset:6144\n\t" \
    "v_exp_f32_e32 v40, v40\n\t" \
    "v_cvt_pk_bf16_f32 v106, v36, v37\n\t" \
    "v_cvt_pk_bf16_f32 v107, v38, v39\n\t" \
    "v_exp_f32_e32 v41, v41\n\t" \
    "v_exp_f32_e32 v42, v42\n\t" \
    "v_exp_f32_e32 v43, v43\n\t" \
    "v_mfma_f32_32x32x16_bf16 v[0:15], v[88:91], v[104:107], v[0:15]\n\t" \
    "s_cbranch_vccz 22f\n\t" \
    "global_load_lds_dwordx4 v114, s[44:45] offset:2048 sc1\n\t" \
    "22:\n" \
    "v_exp_f32_e32 v44, v44\n\t" \
    "v_exp_f32_e32 v45, v45\n\t" \
    "v_exp_f32_e32 v46, v46\n\t" \
    "v_exp_f32_e32 v47, v47\n\t" \
    "v_cvt_pk_bf16_f32 v108, v40, v41\n\t" \
    "v_cvt_pk_bf16_f32 v109, v42, v43\n\t" \
    "v_cvt_pk_bf16_f32 v110, v44, v45\n\t" \
    "v_cvt_pk_bf16_f32 v111, v46, v47\n\t" \
    "s_waitcnt lgkmcnt(0)\n\t" \
    "v_mfma_f32_32x32x16_bf16 v[32:47], v[80:83], v[64:67], v[16:31]\n\t" \
    "v_exp_f32_e32 v48, v48\n\t" \
    "v_exp_f32_e32 v49, v49\n\t" \
    "v_exp_f32_e32 v50, v50\n\t" \
    "v_smfmac_f32_16x16x64_bf16 v[76:79], v[72:75], v[104:111], v174\n\t" \
    "v_exp_f32_e32 v51, v51\n\t" \
    "v_exp_f32_e32 v52, v52\n\t" \
    "v_mfma_f32_32x32x16_bf16 v[32:47], v[84:87], v[68:71], v[32:47]\n\t" \
    "ds_read_b128 v[80:83], v113 offset:12288\n\t" \
    "ds_read_b128 v[84:87], v113 offset:13312\n\t" \
    "v_cvt_pk_bf16_f32 v104, v48, v49\n\t" \
    "v_cvt_pk_bf16_f32 v105, v50, v51\n\t" \
    "v_exp_f32_e32 v53, v53\n\t" \
    "v_exp_f32_e32 v54, v54\n\t" \
    "v_exp_f32_e32 v55, v55\n\t" \
    "v_mfma_f32_32x32x16_bf16 v[0:15], v[100:103], v[108:111], v[0:15]\n\t" \
    "ds_read_b128 v[92:95], v113 offset:7168\n\t" \
    "ds_read_b128 v[88:91], v113 offset:10240\n\t" \
    "v_exp_f32_e32 v56, v56\n\t" \
    "v_cvt_pk_bf16_f32 v106, v52, v53\n\t" \
    "v_cvt_pk_bf16_f32 v107, v54, v55\n\t" \
    "v_exp_f32_e32 v57, v57\n\t" \
    "v_exp_f32_e32 v58, v58\n\t" \
    "v_exp_f32_e32 v59, v59\n\t" \
    "v_mfma_f32_32x32x16_bf16 v[0:15], v[96:99], v[104:107], v[0:15]\n\t" \
    "s_cbranch_vccz 23f\n\t" \
    "global_load_lds_dwordx4 v114, s[44:45] offset:3072 sc1\n\t" \
    "global_load_dwordx2 v[120:121], v170, %[ws] sc1\n\t" \
    "23:\n" \
    "v_exp_f32_e32 v60, v60\n\t" \
    "v_exp_f32_e32 v61, v61\n\t" \
    "v_exp_f32_e32 v62, v62\n\t" \
    "v_exp_f32_e32 v63, v63\n\t" \
    "v_cvt_pk_bf16_f32 v108, v56, v57\n\t" \
    "v_cvt_pk_bf16_f32 v109, v58, v59\n\t" \
    "v_cvt_pk_bf16_f32 v110, v60, v61\n\t" \
    "v_cvt_pk_bf16_f32 v111, v62, v63\n\t" \
    "s_setprio 1\n\t" \
    "s_waitcnt lgkmcnt(0)\n\t" \
    "v_mfma_f32_32x32x16_bf16 v[48:63], v[80:83], v[64:67], v[16:31]\n\t" \
    "v_exp_f32_e32 v32, v32\n\t" \
    "v_exp_f32_e32 v33, v33\n\t" \
    "v_exp_f32_e32 v34, v34\n\t" \
    "v_smfmac_f32_16x16x64_bf16 v[76:79], v[72:75], v[104:111], v174\n\t" \
    "v_exp_f32_e32 v35, v35\n\t" \
    "v_exp_f32_e32 v36, v36\n\t" \
    "v_mfma_f32_32x32x16_bf16 v[48:63], v[84:87], v[68:71], v[48:63]\n\t" \
    "ds_read_b128 v[80:83], v113 offset:16384\n\t" \
    "ds_read_b128 v[84:87], v113 offset:17408\n\t" \
    "v_cvt_pk_bf16_f32 v104, v32, v33\n\t" \
    "v_cvt_pk_bf16_f32 v105, v34, v35\n\t" \
    "v_exp_f32_e32 v37, v37\n\t" \
    "v_exp_f32_e32 v38, v38\n\t" \
    "v_exp_f32_e32 v39, v39\n\t" \
    "v_mfma_f32_32x32x16_bf16 v[0:15], v[92:95], v[108:111], v[0:15]\n\t" \
    "ds_read_b128 v[100:103], v113 offset:11264\n\t" \
    "ds_read_b128 v[96:99], v113 offset:14336\n\t" \
    "v_exp_f32_e32 v40, v40\n\t" \
    "v_cvt_pk_bf16_f32 v106, v36, v37\n\t" \
    "v_cvt_pk_bf16_f32 v107, v38, v39\n\t" \
    "v_exp_f32_e32 v41, v41\n\t" \
    "v_exp_f32_e32 v42, v42\n\t" \
    "v_exp_f32_e32 v43, v43\n\t" \
    "v_mfma_f32_32x32x16_bf16 v[0:15], v[88:91], v[104:107], v[0:15]\n\t" \
    "s_add_u32 %[dst], %[dst], 0x8000\n\t" \
    "s_cmp_eq_u32 %[dst], %[dend]\n\t" \
    "s_cselect_b32 %[dst], %[dbeg], %[dst]\n\t" \
    "s_add_u32 %[nx], %[nx], 0x8000\n\t" \
    "s_cmp_eq_u32 %[nx], 0x18000\n\t" \
    "s_cselect_b32 %[nx], 0, %[nx]\n\t" \
    "s_bitcmp1_b32 %[hk], %[cnt]\n\t" \
    "s_cbranch_scc1 40f\n\t" \
    "24:\n" \
    "v_exp_f32_e32 v44, v44\n\t" \
    "v_exp_f32_e32 v45, v45\n\t" \
    "v_exp_f32_e32 v46, v46\n\t" \
    "v_exp_f32_e32 v47, v47\n\t" \
    "v_cvt_pk_bf16_f32 v108, v40, v41\n\t" \
    "v_cvt_pk_bf16_f32 v109, v42, v43\n\t" \
    "v_cvt_pk_bf16_f32 v110, v44, v45\n\t" \
    "v_cvt_pk_bf16_f32 v111, v46, v47\n\t" \
    "s_waitcnt lgkmcnt(0)\n\t" \
    "v_mfma_f32_32x32x16_bf16 v[32:47], v[80:83], v[64:67], v[16:31]\n\t" \
    "v_exp_f32_e32 v48, v48\n\t" \
    "v_exp_f32_e32 v49, v49\n\t" \
    "v_exp_f32_e32 v50, v50\n\t" \
    "v_smfmac_f32_16x16x64_bf16 v[76:79], v[72:75], v[104:111], v174\n\t" \
    "v_exp_f32_e32 v51, v51\n\t" \
    "v_exp_f32_e32 v52, v52\n\t" \
    "v_mfma_f32_32x32x16_bf16 v[32:47], v[84:87], v[68:71], v[32:47]\n\t" \
    "ds_read_b128 v[80:83], v113 offset:20480\n\t" \
    "ds_read_b128 v[84:87], v113 offset:21504\n\t" \
    "v_cvt_pk_bf16_f32 v104, v48, v49\n\t" \
    "v_cvt_pk_bf16_f32 v105, v50, v51\n\t" \
    "v_exp_f32_e32 v53, v53\n\t" \
    "v_exp_f32_e32 v54, v54\n\t" \
    "v_exp_f32_e32 v55, v55\n\t" \
    "v_mfma_f32_32x32x16_bf16 v[0:15], v[100:103], v[108:111], v[0:15]\n\t" \
    "ds_read_b128 v[92:95], v113 offset:15360\n\t" \
    "ds_read_b128 v[88:91], v113 offset:18432\n\t" \
    "v_exp_f32_e32 v56, v56\n\t" \
    "v_cvt_pk_bf16_f32 v106, v52, v53\n\t" \
    "v_cvt_pk_bf16_f32 v107, v54, v55\n\t" \
    "v_exp_f32_e32 v57, v57\n\t" \
    "v_exp_f32_e32 v58, v58\n\t" \
    "v_exp_f32_e32 v59, v59\n\t" \
    "v_mfma_f32_32x32x16_bf16 v[0:15], v[96:99], v[104:107], v[0:15]\n\t" \
    "s_bitcmp1_b32 %[hk], %[cnt]\n\t" \
    "s_cbranch_scc1 41f\n\t" \
    "25:\n" \
    "v_exp_f32_e32 v60, v60\n\t" \
    "v_exp_f32_e32 v61, v61\n\t" \
    "v_exp_f32_e32 v62, v62\n\t" \
    "v_exp_f32_e32 v63, v63\n\t" \
    "v_cvt_pk_bf16_f32 v108, v56, v57\n\t" \
    "v_cvt_pk_bf16_f32 v109, v58, v59\n\t" \
    "v_cvt_pk_bf16_f32 v110, v60, v61\n\t" \
    "v_cvt_pk_bf16_f32 v111, v62, v63\n\t" \
    "s_setprio 0\n\t" \
    "s_waitcnt lgkmcnt(0)\n\t" \
    "v_mfma_f32_32x32x16_bf16 v[48:63], v[80:83], v[64:67], v[16:31]\n\t" \
    "v_exp_f32_e32 v32, v32\n\t" \
    "v_exp_f32_e32 v33, v33\n\t" \
    "v_exp_f32_e32 v34, v34\n\t" \
    "v_smfmac_f32_16x16x64_bf16 v[76:79], v[72:75], v[104:111], v174\n\t" \
    "v_exp_f32_e32 v35, v35\n\t" \
    "v_exp_f32_e32 v36, v36\n\t" \
    "v_mfma_f32_32x32x16_bf16 v[48:63], v[84:87], v[68:71], v[48:63]\n\t" \
    "ds_read_b128 v[80:83], v113 offset:24576\n\t" \
    "ds_read_b128 v[84:87], v113 offset:25600\n\t" \
    "v_cvt_pk_bf16_f32 v104, v32, v33\n\t" \
    "v_cvt_pk_bf16_f32 v105, v34, v35\n\t" \
    "v_exp_f32_e32 v37, v37\n\t" \
    "v_exp_f32_e32 v38, v38\n\t" \
    "v_exp_f32_e32 v39, v39\n\t" \
    "v_mfma_f32_32x32x16_bf16 v[0:15], v[92:95], v[108:111], v[0:15]\n\t" \
    "ds_read_b128 v[100:103], v113 offset:19456\n\t" \
    "ds_read_b128 v[96:99], v113 offset:22528\n\t" \
    "v_exp_f32_e32 v40, v40\n\t" \
    "v_cvt_pk_bf16_f32 v106, v36, v37\n\t" \
    "v_cvt_pk_bf16_f32 v107, v38, v39\n\t" \
    "v_exp_f32_e32 v41, v41\n\t" \
    "v_exp_f32_e32 v42, v42\n\t" \
    "v_exp_f32_e32 v43, v43\n\t" \
    "v_mfma_f32_32x32x16_bf16 v[0:15], v[88:91], v[104:107], v[0:15]\n\t" \
    "s_and_b32 s40, s48, 63\n\t" \
    "s_lshl_b32 s40, s40, 12\n\t" \
    "s_add_u32 s44, %[srclo], s40\n\t" \
    "s_addc_u32 s45, %[srchi], 0\n\t" \
    "s_lshr_b64 s[48:49], s[48:49], 6\n\t" \
    "s_add_u32 s46, s46, 1\n\t" \
    "s_bitcmp1_b32 %[hk], %[cnt]\n\t" \
    "s_cbranch_scc1 42f\n\t" \
    "26:\n" \
    "v_exp_f32_e32 v44, v44\n\t" \
    "v_exp_f32_e32 v45, v45\n\t" \
    "v_exp_f32_e32 v46, v46\n\t" \
    "v_exp_f32_e32 v47, v47\n\t" \
    "v_cvt_pk_bf16_f32 v108, v40, v41\n\t" \
    "v_cvt_pk_bf16_f32 v109, v42, v43\n\t" \
    "v_cvt_pk_bf16_f32 v110, v44, v45\n\t" \
    "v_cvt_pk_bf16_f32 v111, v46, v47\n\t" \
    "s_waitcnt lgkmcnt(0)\n\t" \
    "v_mfma_f32_32x32x16_bf16 v[32:47], v[80:83], v[64:67], v[16:31]\n\t" \
    "v_exp_f32_e32 v48, v48\n\t" \
    "v_exp_f32_e32 v49, v49\n\t" \
    "v_exp_f32_e32 v50, v50\n\t" \
    "v_smfmac_f32_16x16x64_bf16 v[76:79], v[72:75], v[104:111], v174\n\t" \
    "v_exp_f32_e32 v51, v51\n\t" \
    "v_exp_f32_e32 v52, v52\n\t" \
    "v_mfma_f32_32x32x16_bf16 v[32:47], v[84:87], v[68:71], v[32:47]\n\t" \
    "ds_read_b128 v[80:83], v113 offset:28672\n\t" \
    "ds_read_b128 v[84:87], v113 offset:29696\n\t" \
    "v_cvt_pk_bf16_f32 v104, v48, v49\n\t" \
    "v_cvt_pk_bf16_f32 v105, v50, v51\n\t" \
    "v_exp_f32_e32 v53, v53\n\t" \
    "v_exp_f32_e32 v54, v54\n\t" \
    "v_exp_f32_e32 v55, v55\n\t" \
    "v_mfma_f32_32x32x16_bf16 v[0:15], v[100:103], v[108:111], v[0:15]\n\t" \
    "ds_read_b128 v[92:95], v113 offset:23552\n\t" \
    "ds_read_b128 v[88:91], v113 offset:26624\n\t" \
    "v_exp_f32_e32 v56, v56\n\t" \
    "v_cvt_pk_bf16_f32 v106, v52, v53\n\t" \
    "v_cvt_pk_bf16_f32 v107, v54, v55\n\t" \
    "v_exp_f32_e32 v57, v57\n\t" \
    "v_exp_f32_e32 v58, v58\n\t" \
    "v_exp_f32_e32 v59, v59\n\t" \
    "v_mfma_f32_32x32x16_bf16 v[0:15], v[96:99], v[104:107], v[0:15]\n\t" \
    "s_bitcmp1_b32 %[hk], %[cnt]\n\t" \
    "s_cbranch_scc1 43f\n\t" \
    "27:\n" \
    "v_mov_b32_e32 v112, v113\n\t" \
    "v_add_u32_e32 v113, %[nx], v115\n\t" \
    "s_sub_u32 %[cnt], %[cnt], 1\n\t" \
    "s_cmp_lg_u32 %[cnt], 0\n\t" \
    "v_exp_f32_e32 v60, v60\n\t" \
    "v_exp_f32_e32 v61, v61\n\t" \
    "v_exp_f32_e32 v62, v62\n\t" \
    "v_exp_f32_e32 v63, v63\n\t" \
    "v_cvt_pk_bf16_f32 v108, v56, v57\n\t" \
    "v_cvt_pk_bf16_f32 v109, v58, v59\n\t" \
    "v_cvt_pk_bf16_f32 v110, v60, v61\n\t" \
    "v_cvt_pk_bf16_f32 v111, v62, v63\n\t" \
    "s_cbranch_scc1 1b\n\t" \
    "7:\n" \
    "s_setprio 0\n\t" \
    "s_waitcnt lgkmcnt(0)\n\t" \
    "v_mfma_f32_32x32x16_bf16 v[48:63], v[80:83], v[64:67], v[16:31]\n\t" \
    "v_exp_f32_e32 v32, v32\n\t" \
    "v_exp_f32_e32 v33, v33\n\t" \
    "v_exp_f32_e32 v34, v34\n\t" \
    "v_smfmac_f32_16x16x64_bf16 v[76:79], v[72:75], v[104:111], v174\n\t" \
    "v_exp_f32_e32 v35, v35\n\t" \
    "v_exp_f32_e32 v36, v36\n\t" \
    "v_mfma_f32_32x32x16_bf16 v[48:63], v[84:87], v[68:71], v[48:63]\n\t" \
    "v_cvt_pk_bf16_f32 v104, v32, v33\n\t" \
    "v_cvt_pk_bf16_f32 v105, v34, v35\n\t" \
    "v_exp_f32_e32 v37, v37\n\t" \
    "v_exp_f32_e32 v38, v38\n\t" \
    "v_exp_f32_e32 v39, v39\n\t" \
    "v_mfma_f32_32x32x16_bf16 v[0:15], v[92:95], v[108:111], v[0:15]\n\t" \
    "ds_read_b128 v[100:103], v112 offset:27648\n\t" \
    "ds_read_b128 v[96:99], v112 offset:30720\n\t" \
    "v_exp_f32_e32 v40, v40\n\t" \
    "v_cvt_pk_bf16_f32 v106, v36, v37\n\t" \
    "v_cvt_pk_bf16_f32 v107, v38, v39\n\t" \
    "v_exp_f32_e32 v41, v41\n\t" \
    "v_exp_f32_e32 v42, v42\n\t" \
    "v_exp_f32_e32 v43, v43\n\t" \
    "v_mfma_f32_32x32x16_bf16 v[0:15], v[88:91], v[104:107], v[0:15]\n\t" \
    "v_exp_f32_e32 v44, v44\n\t" \
    "v_exp_f32_e32 v45, v45\n\t" \
    "v_exp_f32_e32 v46, v46\n\t" \
    "v_exp_f32_e32 v47, v47\n\t" \
    "v_cvt_pk_bf16_f32 v108, v40, v41\n\t" \
    "v_cvt_pk_bf16_f32 v109, v42, v43\n\t" \
    "v_cvt_pk_bf16_f32 v110, v44, v45\n\t" \
    "v_cvt_pk_bf16_f32 v111, v46, v47\n\t" \
    "s_waitcnt lgkmcnt(0)\n\t" \
    "v_exp_f32_e32 v48, v48\n\t" \
    "v_exp_f32_e32 v49, v49\n\t" \
    "v_exp_f32_e32 v50, v50\n\t" \
    "v_smfmac_f32_16x16x64_bf16 v[76:79], v[72:75], v[104:111], v174\n\t" \
    "v_exp_f32_e32 v51, v51\n\t" \
    "v_exp_f32_e32 v52, v52\n\t" \
    "v_cvt_pk_bf16_f32 v104, v48, v49\n\t" \
    "v_cvt_pk_bf16_f32 v105, v50, v51\n\t" \
    "v_exp_f32_e32 v53, v53\n\t" \
    "v_exp_f32_e32 v54, v54\n\t" \
    "v_exp_f32_e32 v55, v55\n\t" \
    "v_mfma_f32_32x32x16_bf16 v[0:15], v[100:103], v[108:111], v[0:15]\n\t" \
    "ds_read_b128 v[92:95], v112 offset:31744\n\t" \
    "v_exp_f32_e32 v56, v56\n\t" \
    "v_cvt_pk_bf16_f32 v106, v52, v53\n\t" \
    "v_cvt_pk_bf16_f32 v107, v54, v55\n\t" \
    "v_exp_f32_e32 v57, v57\n\t" \
    "v_exp_f32_e32 v58, v58\n\t" \
    "v_exp_f32_e32 v59, v59\n\t" \
    "v_mfma_f32_32x32x16_bf16 v[0:15], v[96:99], v[104:107], v[0:15]\n\t" \
    "v_exp_f32_e32 v60, v60\n\t" \
    "v_exp_f32_e32 v61, v61\n\t" \
    "v_exp_f32_e32 v62, v62\n\t" \
    "v_exp_f32_e32 v63, v63\n\t" \
    "v_cvt_pk_bf16_f32 v108, v56, v57\n\t" \
    "v_cvt_pk_bf16_f32 v109, v58, v59\n\t" \
    "v_cvt_pk_bf16_f32 v110, v60, v61\n\t" \
    "v_cvt_pk_bf16_f32 v111, v62, v63\n\t" \
    "s_waitcnt lgkmcnt(0)\n\t" \
    "s_nop 0\n\t" \
    "v_mfma_f32_32x32x16_bf16 v[0:15], v[92:95], v[108:111], v[0:15]\n\t" \
    "v_smfmac_f32_16x16x64_bf16 v[76:79], v[72:75], v[104:111], v174\n\t" \
    "s_nop 15\n\t" \
    "s_branch 99f\n\t" \
    "9:\n" \
    "s_mov_b32 s47, 0x40000\n\t" \
    "10:\n" \
    "s_sub_u32 s47, s47, 1\n\t" \
    "s_cbranch_scc1 12b\n\t" \
    "global_load_dwordx2 v[120:121], v170, %[ws] sc1\n\t" \
    "s_waitcnt vmcnt(0)\n\t" \
    "v_cmp_eq_u64_e64 s[42:43], v[120:121], v[140:141]\n\t" \
    "s_nop 4\n\t" \
    "s_bitcmp1_b64 s[42:43], s46\n\t" \
    "s_cbranch_scc1 12b\n\t" \
    "s_sleep 4\n\t" \
    "s_branch 10b\n\t" \
    "50:\n" \
    "global_store_dwordx4 v171, v[176:179], %[ws] offset:-3072 sc1\n\t" \
    "global_store_dwordx4 v171, v[180:183], %[ws] offset:-2944 sc1\n\t" \
    "global_store_dwordx4 v172, v[184:187], %[ws] offset:-1024 sc1\n\t" \
    "global_store_dwordx4 v172, v[188:191], %[ws] offset:-768 sc1\n\t" \
    "s_branch 24b\n\t" \
    "51:\n" \
    "global_store_dwordx4 v171, v[192:195], %[ws] offset:1024 sc1\n\t" \
    "global_store_dwordx4 v171, v[196:199], %[ws] offset:1152 sc1\n\t" \
    "global_store_dwordx4 v172, v[200:203], %[ws] offset:3072 sc1\n\t" \
    "global_store_dwordx4 v172, v[204:207], %[ws] offset:3328 sc1\n\t" \
    "s_branch 25b\n\t" \
    "40:\n" \
    "s_cmp_eq_u32 %[cnt], 7\n\t" \
    "s_cbranch_scc1 50b\n\t" \
    "s_add_u32 m0, %[wdst], 0\n\t" \
    "s_nop 0\n\t" \
    "global_load_lds_dwordx4 v116, %[wo01]\n\t" \
    "global_load_lds_dwordx4 v117, %[wo01] offset:1024\n\t" \
    "v_add_u32_e32 v116, 0x20000, v116\n\t" \
    "v_add_u32_e32 v117, 0x20000, v117\n\t" \
    "s_branch 24b\n\t" \
    "41:\n" \
    "s_cmp_eq_u32 %[cnt], 7\n\t" \
    "s_cbranch_scc1 51b\n\t" \
    "s_add_u32 m0, %[wdst], 16384\n\t" \
    "s_nop 0\n\t" \
    "global_load_lds_dwordx4 v116, %[wo01]\n\t" \
    "global_load_lds_dwordx4 v117, %[wo01] offset:1024\n\t" \
    "v_subrev_u32_e32 v116, 0x20000, v116\n\t" \
    "v_subrev_u32_e32 v117, 0x20000, v117\n\t" \
    "s_branch 25b\n\t" \
    "42:\n" \
    "s_cmp_eq_u32 %[cnt], 7\n\t" \
    "s_cbranch_scc1 26b\n\t" \
    "s_add_u32 m0, %[wdst], 32768\n\t" \
    "s_nop 0\n\t" \
    "global_load_lds_dwordx4 v116, %[wo23]\n\t" \
    "global_load_lds_dwordx4 v117, %[wo23] offset:1024\n\t" \
    "v_add_u32_e32 v116, 0x20000, v116\n\t" \
    "v_add_u32_e32 v117, 0x20000, v117\n\t" \
    "s_branch 26b\n\t" \
    "43:\n" \
    "s_cmp_eq_u32 %[cnt], 7\n\t" \
    "s_cbranch_scc1 27b\n\t" \
    "s_add_u32 m0, %[wdst], 49152\n\t" \
    "s_nop 0\n\t" \
    "global_load_lds_dwordx4 v116, %[wo23]\n\t" \
    "global_load_lds_dwordx4 v117, %[wo23] offset:1024\n\t" \
    "global_load_dword v168, v119, %[wo]\n\t" \
    "s_branch 27b\n\t" \
    "30:\n" \
    "s_cmp_eq_u32 %[cnt], 1\n\t" \
    "s_cbranch_scc0 31f\n\t" \
    "s_mov_b64 vcc, 0\n\t" \
    "s_bitcmp1_b32 %[mode], 1\n\t" \
    "s_cbranch_scc1 7b\n\t" \
    "s_branch 2b\n\t" \
    "31:\n" \
    "s_cmp_eq_u32 %[cnt], 6\n\t" \
    "s_cbranch_scc0 33f\n\t" \
    "global_store_dwordx2 v175, v[140:141], %[ws] sc1\n\t" \
    "s_branch 2b\n\t" \
    "33:\n" \
    "s_cmp_eq_u32 %[cnt], 5\n\t" \
    "s_cbranch_scc0 34f\n\t" \
    "global_store_dwordx2 v118, v[140:141], %[ws] sc1\n\t" \
    "s_branch 2b\n\t" \
    "34:\n" \
    "s_cmp_eq_u32 %[cnt], 3\n\t" \
    "s_cbranch_scc0 35f\n\t" \
    "global_load_dwordx2 v[168:169], v142, %[ws] sc1\n\t" \
    "s_branch 2b\n\t" \
    "35:\n" \
    "v_cmp_eq_u64_e64 s[42:43], v[168:169], v[140:141]\n\t" \
    "s_nop 4\n\t" \
    "s_cmp_eq_u64 s[42:43], -1\n\t" \
    "s_cbranch_scc0 2b\n\t" \
    "global_load_dwordx4 v[122:125], v143, %[ws] offset:-2048 sc1\n\t" \
    "global_load_dwordx4 v[126:129], v143, %[ws] offset:-1024 sc1\n\t" \
    "global_load_dwordx4 v[130:133], v143, %[ws] offset:0 sc1\n\t" \
    "global_load_dwordx4 v[134:137], v143, %[ws] offset:1024 sc1\n\t" \
    "global_load_dwordx2 v[138:139], v143, %[ws] offset:2048 sc1\n\t" \
    "s_bitset1_b32 %[mode], 2\n\t" \
    "s_branch 2b\n\t" \
    "99:\n"
#define ATT_TMP "v48", "v49", "v50", "v51", "v52", "v53", "v54", "v55", "v56", "v57", "v58", "v59", "v60", "v61", "v62", "v63", "v96", "v97", "v98", "v99", "v100", "v101", "v102", "v103"

constexpr int K1_X = 0, K1_W = 81920, K1_WB = 24576;
constexpr int K2_SLOT = 32768, K2_QX = 3 * K2_SLOT, K2_FL = 147456;
constexpr int LDS_BYTES = 163840;
constexpr int p3_wslot(int t) { return (t % 6) < 4 ? 98304 + (t % 6) * 16384 : 65536 + ((t % 6) - 4) * 16384; }
constexpr int p3_allow(int t) { return (((t < 2 || t - 2 >= 1) && t + 3 < 16) ? 4 : 0) + (t + 3 < 16 ? 4 : 0) + ((t >= 1 && t + 5 < 16) ? 4 : 0); }

__global__ __launch_bounds__(512, 2) void mk_fused(const u16* __restrict__ xA, const u16* __restrict__ wA, const u16* __restrict__ woA,
                                                   const u16* __restrict__ xB, const u16* __restrict__ wB, const u16* __restrict__ woB, char* ws0, u16* __restrict__ out) {
    char* ws = ws0;
    __shared__ __attribute__((aligned(1024))) char smem[LDS_BYTES];
    const unsigned lds0 = (unsigned)(uintptr_t)(LAS char*)smem;
    const int wid = __builtin_amdgcn_readfirstlane(threadIdx.x >> 6);
    int lane = threadIdx.x & 63;
#define BRX 1
    const int b = blockIdx.x, xcd = b & 7, j = b >> 3, br = (xcd >> 2) ^ BRX, qd = xcd & 3;
    const int mt = (qd & 1) * 4 + (j & 3), hp = (qd >> 1) * 8 + (j >> 2);
    const int mt3 = qd * 4 + (j & 3), nt3 = j >> 2;
    u64 tag;
    unsigned pst[8];
    u32x4 fq[2][2], fk[2][2], fv[2][2];
    f32x16 band;
    STAMP_AT(0, 0);

#undef SA6_
    {
        const int g = wid >> 2, wi = wid & 3, mw = wi >> 1, hd = wi & 1;
        if (wid == 7) {
            const char* pc; unsigned len;
            asm volatile("s_getpc_b64 %0\n.Lmk_code_begin:\n\ts_mov_b32 %1, .Lmk_code_end-.Lmk_code_begin" : "=s"(pc), "=s"(len));
            const unsigned off = (unsigned)(j * 64 + lane) * 64u;
            asm volatile("s_mov_b32 m0, %2\n\ts_nop 0\n\tglobal_load_lds_dword %0, %1" :: "v"(off < len ? off : 0u), "s"(pc), "s"(lds0 + 159744) : "memory");
        }
        unsigned vx[4], vw[3];
#pragma unroll
        for (int ii = 0; ii < 4; ++ii) {
            const int row = br ? ((wi >> 1) * 64 + (wi & 1) + ii * 16 + 2 * (lane >> 3)) : (wi * 32 + ii * 8 + (lane >> 3));
            vx[ii] = row * 2048 + (((lane & 7) ^ (((lane >> 4) & 1) | ((ii >> 1) << 1) | ((lane >> 5) << 2))) << 4) - ii * 1024;
        }
#pragma unroll
        for (int jj = 0; jj < 3; ++jj) {
            const int jw = 12 * g + 3 * wi + jj, n = jw >> 3, k0 = (jw & 7) * 8 + (lane >> 3);
            vw[jj] = k0 * 6144 + n * 2048 + (((lane & 7) ^ ((((lane >> 4) & 1) | ((jw & 1) << 1)) << 1)) << 4) - jj * 1024;
        }
        asm volatile("" : "+v"(vx[0]), "+v"(vx[1]), "+v"(vx[2]), "+v"(vx[3]), "+v"(vw[0]), "+v"(vw[1]), "+v"(vw[2]));
        SBAR();
        const char* x = (const char*)(br ? xB : xA) + (size_t)mt * 256 * 2048;
        const char* w = (const char*)(br ? wB : wA) + hp * 128;
        asm volatile("" :: "s"(x), "s"(w)); SBAR(); STAMP_AT(0, 6); SBAR();
        const unsigned xdst_o = lds0 + K1_X + wi * 4096;
        const unsigned wdst = lds0 + K1_W + (12 * g + 3 * wi) * 1024;
        const char* xo = x + (size_t)g * 128 * 2048;
#define ROTM 4
        const int kt0 = ROTM * (j & 3);
#define STAGE_X(kt, dst) do { const char* s_ = xo + (((kt) + kt0) & 15) * 128; glds_run<0>(s_, vx[0], dst); glds_run<1>(s_, vx[1], 0); glds_run<2>(s_, vx[2], 0); glds_run<3>(s_, vx[3], 0); } while (0)
#define STAGE_W(kt, buf) do { const char* s_ = w + (size_t)(((kt) + kt0) & 15) * 64 * 6144; glds_run<0>(s_, vw[0], wdst + (buf) * K1_WB); glds_run<1>(s_, vw[1], 0); glds_run<2>(s_, vw[2], 0); } while (0)
        JIT(14);
        STAGE_W(0, 0); STAGE_X(0, xdst_o + g * 16384); STAGE_W(1, 1); STAGE_X(1, xdst_o + (2 + g) * 16384);
        { u64 e; asm volatile("s_load_dwordx2 %0, %1, 0x0\n\ts_waitcnt lgkmcnt(0)" : "=s"(e) : "s"(ws + WS_EP) : "memory");
          e += 0x9E3779B97F4A7C15ull; e = (e ^ (e >> 30)) * 0xBF58476D1CE4E5B9ull; e = (e ^ (e >> 27)) * 0x94D049BB133111EBull; tag = e ^ (e >> 31); }
        const int c16 = lane & 15, g4 = lane >> 4;
        unsigned ax[2], aw[2];
        ax[0] = lds0 + K1_X + g * 16384 + (mw * 64 + 16 * (c16 >> 3) + (c16 & 7)) * 128 + ((g4 ^ (((c16 >> 1) & 1) | ((c16 >> 3) << 1) | (((c16 >> 2) & 1) << 2))) << 4);
        ax[1] = ax[0] ^ 64;
        { const int q4 = c16 >> 2, p = lane & 3;
          aw[0] = lds0 + K1_W + (8 * g4 + q4) * 128 + (((hd * 4 + (p >> 1)) ^ ((((q4 >> 1) & 1) | ((g4 & 1) << 1)) << 1)) << 4) + (p & 1) * 8; aw[1] = aw[0] ^ 32; }
        band = band_mask(lane); asm volatile("" : "+v"(band));
        if (g == 1) WAITBAR_(11); else WAITBAR_(7);
        SA_(1);
        if (g == 1) WAITBAR_(7);
        f32x4 acc[4][6];
#pragma unroll
        for (int m = 0; m < 4; ++m)
#pragma unroll
            for (int n = 0; n < 6; ++n) acc[m][n] = f32x4{};
        SBAR();
        unsigned wr_ = 0, xr_ = g * 16384, dw = wdst + 2 * K1_WB, dx = xdst_o + (g ? 0 : 4 * 16384);
        int tk = (2 + kt0) & 15;
        const char* sx = xo + tk * 128; const char* sw = w + (size_t)tk * 64 * 6144;
#define RDX(m) do { xf[m][0] = ldsrd128(ax[0] + ((m) >> 1) * 4096 + ((m) & 1) * 1024); xf[m][1] = ldsrd128(ax[1] + ((m) >> 1) * 4096 + ((m) & 1) * 1024); } while (0)
#define RDW(n, ks) wf[n][ks] = cat8(ldstr(aw[(n) & 1] + ((n) >> 1) * 8192 + (ks) * 4096), ldstr(aw[(n) & 1] + ((n) >> 1) * 8192 + (ks) * 4096 + 512))
#define P1_TILE(DO, WAIT1, WAIT2) do { \
            bf16x8 xf[4][2], wf[6][2]; \
            RDX(0); RDW(0, 0); if (DO) glds_run<0>(sw, vw[0], dw); \
            RDW(0, 1); RDW(1, 0); if (DO) glds_run<1>(sw, vw[1], 0); \
            RDX(1); RDW(1, 1); if (DO) glds_run<2>(sw, vw[2], 0); \
            RDW(2, 0); RDW(2, 1); if (DO) glds_run<0>(sx, vx[0], dx); \
            RDX(2); RDW(3, 0); if (DO) glds_run<1>(sx, vx[1], 0); \
            RDW(3, 1); RDW(4, 0); if (DO) glds_run<2>(sx, vx[2], 0); \
            RDX(3); RDW(4, 1); if (DO) glds_run<3>(sx, vx[3], 0); \
            RDW(5, 0); RDW(5, 1); \
            SBAR(); \
            {   \
              const unsigned d_ = (wr_ == 2 * K1_WB) ? (unsigned)(-2 * K1_WB) : (unsigned)K1_WB, e_ = (xr_ >= 3 * 16384) ? (unsigned)(-3 * 16384) : (unsigned)(2 * 16384); \
              dw = wdst + wr_; wr_ += d_; aw[0] += d_; aw[1] += d_; dx = xdst_o + ((xr_ == 4 * 16384) ? 0u : xr_ + 16384); xr_ += e_; ax[0] += e_; ax[1] += e_; \
              tk = (tk + 1) & 15; sx = xo + tk * 128; sw = w + (size_t)tk * 64 * 6144; \
              asm volatile("" : "+v"(aw[0]), "+v"(aw[1]), "+v"(ax[0]), "+v"(ax[1]), "+s"(sx), "+s"(sw)); } \
            WAIT1; \
            SBAR(); \
            __builtin_amdgcn_s_setprio(1); \
            _Pragma("unroll") for (int m = 0; m < 4; ++m) \
                _Pragma("unroll") for (int n = 0; n < 6; ++n) { \
                    if (n < 4) { acc[m][n] = MFMA16(wf[n][0], xf[m][0], acc[m][n]); acc[m][n] = MFMA16(wf[n][1], xf[m][1], acc[m][n]); } \
                    else       { acc[m][n] = MFMA16(xf[m][0], wf[n][0], acc[m][n]); acc[m][n] = MFMA16(xf[m][1], wf[n][1], acc[m][n]); } \
                    CHAIN(); \
                } \
            __builtin_amdgcn_s_setprio(0); \
            SBAR(); \
            WAIT2; \
            SBAR(); \
        } while (0)
#pragma unroll 1
        for (int t = 0; t < 14; ++t) P1_TILE(true, WAITBAR_(11), WAITBAR_(7));
        P1_TILE(false, WAITBAR_(4), WAITBAR_(0));
        P1_TILE(false, WAITBAR_(0), if (g == 0) asm volatile("s_barrier" ::: "memory"));
#undef P1_TILE
#undef RDW
#undef RDX
#undef STAGE_X
#undef STAGE_W
        SA_(2); SC_(1);
        JIT(1);
        FRESH();
#pragma unroll
        for (int mb = 0; mb < 2; ++mb)
#pragma unroll
            for (int u = 0; u < 2; ++u) {
                const f32x4 a = acc[2 * mb + u][0] * QSCALE, c = acc[2 * mb + u][1] * QSCALE;
                fq[mb][u] = u32x4{cvtpk(a[0], a[1]), cvtpk(a[2], a[3]), cvtpk(c[0], c[1]), cvtpk(c[2], c[3])};
                fk[mb][u] = pack44(acc[2 * mb + u][2], acc[2 * mb + u][3]);
                fv[mb][u] = pack44(acc[2 * mb][4 + u], acc[2 * mb + 1][4 + u]);
            }
    }
    const char* wo = (const char*)(br ? woB : woA) + nt3 * 256;
    const int k30 = 2 * nt3;
    {
        const int g = wid >> 2, wr = wid >> 1, hd = wid & 1;
        const int ph1 = 2 * hp + hd, pb1 = mt * 8 + wr * 2;
        const int head = 2 * hp + br, blk = mt * 8 + wid;
#define RBLK(e) ((((e) % 7) + (((e) % 7) >= mt)) * 8 + (e) / 7)
        const char* kv = ws + WS_KV + (size_t)head * 262144;
        unsigned l16 = lane * 16;
        const unsigned dbeg = lds0 + wid * 4096, lbase = lds0 + l16;
        const unsigned fk0 = (unsigned)WS_FK + head * 32 * FS;
        const unsigned fxo = (unsigned)WS_FX + ((hp * 8 + mt) * 8 + wid) * FS, xrec0 = (unsigned)WS_XP + ((hp * 8 + mt) * 8 + wid) * 5120 + 2048;
        const f32x16 z = {};
        f32x16 sa, negm, o;
        u32x16 qol, kvf, pub0, pub1; u32x8 ph;
        const unsigned sidx = (lane & 1) ? 0xEEEEEEEEu : 0x44444444u;

#define FIRST_TILE(base, q0, q1) do { const unsigned on_ = (lane & 2) ? 0u : 0x3F803F80u, e_ = (lane & 4) ? 0u : on_, f_ = (lane & 4) ? on_ : 0u; const u32x4 ones = {e_, e_, f_, f_}; \
            const bf16x8 k0_ = ldsrd128(base), k1_ = ldsrd128((base) + 1024); kvf = cat16(asu(ldsrd128((base) + 4096)), asu(ldsrd128((base) + 5120)), asu(ldsrd128((base) + 2048)), ones); \
            sa = MFMA32(k0_, q0, z); sa = MFMA32(k1_, q1, sa); float m_ = sa[0]; \
            _Pragma("unroll") for (int r = 1; r < 16; ++r) m_ = __builtin_fmaxf(m_, sa[r]); \
            m_ = -swap_max(m_); _Pragma("unroll") for (int r = 0; r < 16; ++r) { negm[r] = m_; sa[r] += m_; } \
            qol = cat16(asu(q0), asu(q1), ones, u32x4{0u, 0u, 0u, 0u}); } while (0)
#define L6_OUT "={v[0:15]}"(o), "+{v[32:47]}"(sa), "+{v[64:79]}"(qol), "+{v[80:95]}"(kvf), "={v[104:111]}"(ph)
#define L6C(HL, BASE) asm volatile(ATT_L6C : L6_OUT, "={v[144:159]}"(h0), "={v[160:167]}"(h1) \
            : "{v[16:31]}"(negm), "{v174}"(sidx), "{v112}"(BASE), "{v[140:143]}"(u32x4{0u, 0u, FLADDR, (unsigned)WS_HALO + (mt * 32 + ph1) * 6144 + 3072 + l16}), \
              [ws] "s"(ws), [hl] "s"(HL), [fld] "s"(fld) : ATT_TMP, "scc", "memory")
#define FLADDR (fk0 + ((((lane & 7) == 7) ? blk : RBLK(8 * (lane & 7) + wid)) >> 1) * FS)
        const unsigned own = (ph1 * 32 + mt * 4 + wr) * FS;
        const unsigned fld = lds0 + K2_FL + wid * 1024 + br * (2 * K2_SLOT + 4096 - K2_FL + wid * 11264);
        const unsigned pv = (lane >> 5) * 1024 + ((lane & 15) + 32 * ((lane >> 4) & 1)) * 16, pq = pv + (lane & 8) * 16;
        if (br == 0) {
            const char* kp = ws + WS_KV + (size_t)((ph1 * 64 + pb1) * 4) * 1024;
#define PUB_KV() do { st16_dev<0>(kp, pq, fk[0][0]); st16_dev<128>(kp, pq, fk[0][1]); st16_dev<2048>(kp, pv, fv[0][0]); st16_dev<2304>(kp, pv, fv[0][1]); \
            st16_dev<0>(kp + 4096, pq, fk[1][0]); st16_dev<128>(kp + 4096, pq, fk[1][1]); st16_dev<2048>(kp + 4096, pv, fv[1][0]); st16_dev<2304>(kp + 4096, pv, fv[1][1]); } while (0)
            if (hd == 1) {
                const char* qp = ws + WS_QF + (size_t)((ph1 * 64 + pb1) * 2) * 1024;
                st16_dev<0>(qp, pq, fq[0][0]); st16_dev<128>(qp, pq, fq[0][1]); st16_dev<2048>(qp, pq, fq[1][0]); st16_dev<2176>(qp, pq, fq[1][1]);
            }
            JIT(2);
            if (wr == 0) PUB_KV();
#pragma unroll
            for (int mb = 0; mb < 2; ++mb) {
                const unsigned a = lds0 + hd * 2 * K2_SLOT + (wr * 2 + mb) * 4096, aq = lds0 + K2_QX + hd * 16384 + (wr * 2 + mb) * 2048 + pq;
                ldswr128(a + pq, fk[mb][0]); ldswr128(a + pq + 128, fk[mb][1]); ldswr128(a + 2048 + pv, fv[mb][0]); ldswr128(a + 2304 + pv, fv[mb][1]);
                if (hd == 0 || g == 0) { ldswr128(aq, fq[mb][0]); ldswr128(aq + 128, fq[mb][1]); }
            }
            BARRIER_L();
            if (wr == 0 || hd == 1) {
                drain();
                if (lane == 0) { if (wr == 0) flag_set(ws + WS_FK, own, tag); if (hd == 1) flag_set(ws + WS_FQ, own, tag); }
            }
            JIT(3);
            if (wr == 1) PUB_KV();
            const unsigned qa = lbase + K2_QX + (1 - g) * 16384 + wid * 2048, cx = lbase + (1 - g) * 2 * K2_SLOT;
            SA_(3); SC_(2);
            pub0 = cat16(fk[0][0], fk[0][1], fv[0][0], fv[0][1]); pub1 = cat16(fk[1][0], fk[1][1], fv[1][0], fv[1][1]);
            { const bf16x8 q0 = ldsrd128(qa), q1 = ldsrd128(qa + 1024); FIRST_TILE(cx, q0, q1); }
            JIT(4);
            asm volatile(ATT_L4 : L6_OUT : "{v[16:31]}"(negm), "{v174}"(sidx), "{v112}"(cx) : ATT_TMP, "memory");
            SC_(3);
            if (g == 1) {
                const int b1 = RBLK(wid), b0 = RBLK(wid - 4);
                flag_wait(ws, fk0 + (((lane & 1) ? b1 : b0) >> 1) * FS, tag);
                glds4k_dev(kv + b1 * 4096, l16, dbeg + K2_SLOT);
                glds4k_dev(kv + b0 * 4096, l16, dbeg + K2_SLOT - 16384);
                glds16_dev(ws, FLADDR, fld);
            } else if (wr == 1) { drain(); if (lane == 0) flag_set(ws + WS_FK, own, tag); }
            SC_(4);
            JIT(5);
            asm volatile(ATT_L2 : "+{v[0:15]}"(o), "+{v[32:47]}"(sa), "+{v[64:79]}"(qol), "+{v[80:95]}"(kvf), "+{v[104:111]}"(ph) : "{v[16:31]}"(negm), "{v174}"(sidx), "{v112}"(cx) : ATT_TMP, "memory");
            if (g == 0) {
                asm volatile(ATT_XT : "+{v[0:15]}"(o), "+{v[32:47]}"(sa), "+{v[64:79]}"(qol), "+{v[80:95]}"(kvf), "+{v[104:111]}"(ph) : "{v[16:31]}"(negm), "{v174}"(sidx), "{v112}"(cx) : ATT_TMP, "memory");
                st16_dev<-2048>(ws, xrec0 + l16, quad(o, 0)); st16_dev<-1024>(ws, xrec0 + l16, quad(o, 1)); st16_dev<0>(ws, xrec0 + l16, quad(o, 2)); st16_dev<1024>(ws, xrec0 + l16, quad(o, 3));
                st16_dev<2048>(ws, xrec0 + l16, u32x4{__float_as_uint(__uint_as_float(qol[12]) + __uint_as_float(qol[13])), __float_as_uint(negm[0]), 0u, 0u});
                SC_(5);
                JIT(16);
                const bf16x8 q0 = ldsrd128(lbase + K2_QX + wid * 2048), q1 = ldsrd128(lbase + K2_QX + wid * 2048 + 1024);
                FIRST_TILE(lbase, q0, q1);
                u32x16 h0; u32x8 h1;
                L6C(0, lbase);
            }
#undef PUB_KV
        } else {
            if (wr == 3 && mt < 7) {
                const char* p = ws + WS_HALO + (size_t)(((mt + 1) * 32 + ph1) * 6) * 1024;
                st16_dev<0>(p, pq, fk[0][0]); st16_dev<128>(p, pq, fk[0][1]); st16_dev<3072>(p, pq, fk[1][0]); st16_dev<3200>(p, pq, fk[1][1]);
                if (lane >= 32) { st16_dev<1024>(p, pv, fv[0][0]); st16_dev<1280>(p, pv, fv[0][1]); st16_dev<1024>(p + 3072, pv, fv[1][0]); st16_dev<1280>(p + 3072, pv, fv[1][1]); }
            }
            JIT(15);
            const unsigned ex = lds0 + 2 * K2_SLOT + wid * 12288;
#pragma unroll
            for (int mb = 0; mb < 2; ++mb) {
                const unsigned a = ex + mb * 6144;
                ldswr128(a + pq, fq[mb][0]); ldswr128(a + pq + 128, fq[mb][1]); ldswr128(a + 2048 + pq, fk[mb][0]); ldswr128(a + 2176 + pq, fk[mb][1]);
                ldswr128(a + 4096 + pv, fv[mb][0]); ldswr128(a + 4352 + pv, fv[mb][1]);
            }
            BARRIER_L();
            if (wr == 3 && mt < 7) { drain(); if (lane == 0) flag_set(ws + WS_FH, (ph1 * 8 + mt + 1) * FS, tag); }
            SC_(2);
            LocalPart lp[2];
            bf16x8 bq[2][2];
#pragma unroll
            for (int mb = 0; mb < 2; ++mb) { bq[mb][0] = ldsrd128(ex + mb * 6144 + l16); bq[mb][1] = ldsrd128(ex + mb * 6144 + 1024 + l16); }
#define LOCAL_BLOCK(mb) do { const unsigned a_ = ex + mb * 6144 + l16; \
                const bf16x8 k0_ = ldsrd128(a_ + 2048), k1_ = ldsrd128(a_ + 3072), v0_ = ldsrd128(a_ + 4096), v1_ = ldsrd128(a_ + 5120); \
                if (wr > 0) local_own<true>(bq[mb][0], bq[mb][1], k0_, k1_, v0_, v1_, ldsrd128(a_ - 24576 + 2048), ldsrd128(a_ - 24576 + 3072), ldsrd128(a_ - 24576 + 5120), band, lane, lp[mb]); \
                else local_own<false>(bq[mb][0], bq[mb][1], k0_, k1_, v0_, v1_, k0_, k0_, k0_, band, lane, lp[mb]); \
                if (wr > 0 || mt == 0) local_fin(lp[mb]); } while (0)
            JIT(6);
            LOCAL_BLOCK(0);
            SC_(3);
            const int hl = (wid < 2) ? mt : 0;
            const unsigned fh = (unsigned)WS_FH + (ph1 * 8 + mt) * FS;
            const int b0 = RBLK(wid), b1 = RBLK(8 + wid);
            const int q = lane & 3;
            unsigned fa0 = fk0 - 1024 * FS + (blk >> 1) * FS, fa1 = fk0 + (b0 >> 1) * FS, fa2 = fk0 + (b1 >> 1) * FS, fa3 = fh;
            asm volatile("" : "+s"(fa0), "+s"(fa1), "+s"(fa2), "+s"(fa3));
            const unsigned lo_ = (q & 1) ? fa1 : fa0, hi_ = (q & 1) ? fa3 : fa2;
            const u64 fm = flag_wait_some(ws, (q & 2) ? hi_ : lo_, tag, 3);
            SC_(4);
            glds4k_dev(kv + b0 * 4096, l16, dbeg);
            const char* qp = ws + WS_QF + (size_t)((head * 64 + blk) * 2) * 1024;
            glds16_dev(qp, l16, ex); glds16_dev(qp + 1024, l16, ex + 1024);
            if (fm & 4) glds4k_dev(kv + b1 * 4096, l16, dbeg + K2_SLOT);
            JIT(7);
            LOCAL_BLOCK(1);
#undef LOCAL_BLOCK
            if (fm & 4) WAITBAR_(4);
            else {
                WAITBAR_(0);
                flag_wait(ws, fk0 + (b1 >> 1) * FS, tag);
                glds4k_dev(kv + b1 * 4096, l16, dbeg + K2_SLOT);
            }
            JIT(8);
            const bf16x8 q0 = ldsrd128(ex + l16), q1 = ldsrd128(ex + 1024 + l16);
            SA_(3); SC_(5);
            if (hl && !(fm & 8)) flag_wait(ws, fh, tag);
            FIRST_TILE(lbase, q0, q1);
            u32x16 h0; u32x8 h1;
            JIT(9);
            L6C(hl, lbase);
            if (hl) {
                local_prev(bq[0][0], bq[0][1], asbf(u32x4{h0[0], h0[1], h0[2], h0[3]}), asbf(u32x4{h0[4], h0[5], h0[6], h0[7]}), asbf(u32x4{h0[8], h0[9], h0[10], h0[11]}), lane, lp[0]);
                local_prev(bq[1][0], bq[1][1], asbf(u32x4{h0[12], h0[13], h0[14], h0[15]}), asbf(u32x4{h1[0], h1[1], h1[2], h1[3]}), asbf(u32x4{h1[4], h1[5], h1[6], h1[7]}), lane, lp[1]);
                local_fin(lp[0]); local_fin(lp[1]);
            }
            const char* op = ws + WS_OF + (size_t)(((64 + pb1) * 32 + ph1) * 2) * 1024;
            st16_dev<0>(op, l16, quad(lp[0].o, 0)); st16_dev<1024>(op, l16, quad(lp[0].o, 1)); st16_dev<0>(op + 65536, l16, quad(lp[1].o, 0)); st16_dev<1024>(op + 65536, l16, quad(lp[1].o, 1));
        }
#undef FIRST_TILE
#undef L6_OUT
#undef L6C
        FRESH();
        l16 = lane * 16;
        SA_(4); SC_(6);
        const int isx = br & (1 - g);
        unsigned cnt = 7, dst = dbeg + 2 * K2_SLOT, nx = K2_SLOT;
        unsigned mode = isx << 1;
        const unsigned late = (1 - br) & g;
        const unsigned act = 2u | (isx * 12u) | ((br | (1 - g)) << 5) | (late << 6), hk = 32u | (late << 7);
        u64 tab = (u64)blk << 42;
#pragma unroll
        for (int k = 1; k < 7; ++k) tab |= (u64)RBLK(8 * k + wid) << (6 * k);
        tab >>= 6 + 6 * br;
        u32x8 adr;
        { const int k = 8 * wid + (lane >> 4), c = lane & 15;
          adr = u32x8{lds0 + l16, lds0 + l16 + K2_SLOT, l16, lds0 + l16, (unsigned)(k * 2048 + ((c ^ ((k & 7) << 1)) << 4)), (unsigned)((k + 4) * 2048 + ((c ^ (((k + 4) & 7) << 1)) << 4) - 1024),
                      br ? (unsigned)WS_FO + ((64 + pb1 + (lane & 1)) * 32 + ph1) * FS : fxo, 0u};
          const int i = (mt3 * 8 + wid) * 12 + ((lane * 3) >> 4); adr[7] = ((((4 + (i >> 7) + k30) & 15) * 64 + ((i & 127) >> 1)) * 2048 + (i & 1) * 128); }
        const unsigned kvrec = (unsigned)WS_KV + (ph1 * 64 + pb1) * 4096 + 3072, pv2 = (lane >> 5) * 1024 + ((lane & 15) + 32 * ((lane >> 4) & 1)) * 16;
        f32x16 xo; f32x2 xl;
#define DBGOUT
        JIT(10);
        asm volatile(ATT_MAIN
                     : DBGOUT"+{v[0:15]}"(o), "+{v[32:47]}"(sa), "+{v[64:79]}"(qol), "+{v[80:95]}"(kvf), "+{v[104:111]}"(ph), "+{v[112:119]}"(adr), "={v[122:137]}"(xo), "={v[138:139]}"(xl),
                       [cnt] "+s"(cnt), [dst] "+s"(dst), [nx] "+s"(nx), [mode] "+s"(mode)
                     : "{v[16:31]}"(negm), "{v[174:175]}"(u32x2{sidx, (unsigned)WS_FK + own}), "{v[140:143]}"(u32x4{(unsigned)tag, (unsigned)(tag >> 32), fxo, xrec0 + l16}),
                       "{v[170:173]}"(u32x4{FLADDR, kvrec + pv2 + (lane & 8) * 16, kvrec + pv2, fld + l16}), "{v[176:191]}"(pub0), "{v[192:207]}"(pub1),
                       [srclo] "s"((unsigned)(uintptr_t)kv), [srchi] "s"((unsigned)((uintptr_t)kv >> 32)), [dbeg] "s"(dbeg), [dend] "s"(dbeg + 3 * K2_SLOT),
                       [wo01] "s"(wo + (size_t)k30 * 131072), [wo23] "s"(wo + (size_t)((k30 + 2) & 15) * 131072), [wo] "s"(wo), [wdst] "s"(lds0 + 98304 + wid * 2048), [act] "s"(act), [hk] "s"(hk), [tab] "s"(tab), [k0] "s"(br), [ws] "s"(ws)
                     : ATT_TMP, "v120", "v121", "v168", "v169", "s40", "s41", "s42", "s43", "s44", "s45", "s46", "s47", "s48", "s49", "s50", "s51", "vcc", "scc", "memory");
        FRESH();
        l16 = lane * 16;
        const unsigned xrec = xrec0 + l16;
        const bf16x8 q0 = asbf(u32x4{qol[0], qol[1], qol[2], qol[3]}), q1 = asbf(u32x4{qol[4], qol[5], qol[6], qol[7]});
        const unsigned one2 = (((lane >> 4) & 1) == ((lane >> 2) & 1)) ? 0x3F803F80u : 0u;
        const u32x4 ones = {one2, one2, one2, one2};
        SA_(5); SB_(1);
        JIT(11);
        float lsum = __uint_as_float(qol[12]) + __uint_as_float(qol[13]);
        bool ok = lsum < 1.0e18f;
        if (isx) {
            if (__builtin_expect(!(mode & 4), 0)) {
                flag_wait(ws, fxo, tag);
                f32x4 a0, a1, a2, a3;
                asm volatile("global_load_dwordx4 %0, %5, %6 offset:-2048 sc1\n\tglobal_load_dwordx4 %1, %5, %6 offset:-1024 sc1\n\tglobal_load_dwordx4 %2, %5, %6 sc1\n\t"
                             "global_load_dwordx4 %3, %5, %6 offset:1024 sc1\n\tglobal_load_dwordx2 %4, %5, %6 offset:2048 sc1\n\ts_waitcnt vmcnt(0)"
                             : "=&v"(a0), "=&v"(a1), "=&v"(a2), "=&v"(a3), "=&v"(xl) : "v"(xrec), "s"(ws) : "memory");
#pragma unroll
                for (int r = 0; r < 4; ++r) { xo[r] = a0[r]; xo[4 + r] = a1[r]; xo[8 + r] = a2[r]; xo[12 + r] = a3[r]; }
            }
            const float nm = __builtin_fminf(negm[0], xl[1]), fa = EXP2(nm - negm[0]), fx = EXP2(nm - xl[1]);
            ok = ok && (xl[0] < 1.0e18f);
            lsum = lsum * fa + xl[0] * fx;
#pragma unroll
            for (int r = 0; r < 16; ++r) o[r] = o[r] * fa + xo[r] * fx;
        }
#pragma unroll
        for (int r = 0; r < 16; ++r) ok = ok && (__builtin_fabsf(o[r]) < 3.0e38f);
        if (__builtin_expect(__any(!ok), 0)) {
            float m = -__builtin_inff();
            f32x4 l;
            for (int pass = 0; pass < 2; ++pass) {
                o = z; l = f32x4{};
                for (int t = 0; t < 64; ++t) {
                    bf16x8 f0, f1, f2, f3;
                    asm volatile("global_load_dwordx4 %0, %4, %5 sc1\n\tglobal_load_dwordx4 %1, %4, %5 offset:1024 sc1\n\tglobal_load_dwordx4 %2, %4, %5 offset:2048 sc1\n\t"
                                 "global_load_dwordx4 %3, %4, %5 offset:3072 sc1\n\ts_waitcnt vmcnt(0)" : "=&v"(f0), "=&v"(f1), "=&v"(f2), "=&v"(f3) : "v"(l16 + t * 4096), "s"(kv) : "memory");
                    f32x16 s = MFMA32(f0, q0, z); s = MFMA32(f1, q1, s);
                    if (pass == 0) {
#pragma unroll
                        for (int r = 0; r < 16; ++r) m = __builtin_fmaxf(m, s[r]);
                    } else {
#pragma unroll
                        for (int r = 0; r < 16; ++r) s[r] = EXP2(s[r] - m);
                        const bf16x8 p0 = asbf(pack8(s, 0)), p1 = asbf(pack8(s, 1));
                        o = MFMA32(f2, p0, o); o = MFMA32(f3, p1, o);
                        l = MFMA16(asbf(ones), p0, l); l = MFMA16(asbf(ones), p1, l);
                    }
                }
                if (pass == 0) m = swap_max(m);
            }
            lsum = l[0];
        }
        const float inv = __builtin_amdgcn_rcpf(lsum);
#pragma unroll
        for (int r = 0; r < 16; ++r) o[r] *= inv;
        const char* op = ws + WS_OF + (size_t)((blk * 32 + head) * 2) * 1024;
        st16_dev<0>(op, l16, pack8(o, 0)); st16_dev<1024>(op, l16, pack8(o, 1));
    }
    {
        FRESH();
        const int g = wid >> 2, wi = wid & 3, wm = wi >> 1, wn = wi & 1, g4 = lane >> 4;
        const char* of = ws + WS_OF + (size_t)(br * 64 + mt3 * 4 + wi) * 65536;
        unsigned vw4[4];
#pragma unroll
        for (int ii = 0; ii < 4; ++ii) { const int k = 16 * wi + 4 * ii + g4; vw4[ii] = k * 2048 + (((lane & 15) ^ ((k & 7) << 1)) << 4) - ii * 1024; }
        const unsigned l16 = lane * 16, sdst = lds0 + wi * 4096;
#define STAGE_O(t) glds4k_dev(of + (((t) + k30) & 15) * 4096, l16, sdst + ((t) & 3) * 16384)
#define STAGE_WO(t, slot) do { const char* s_ = wo + (size_t)(((t) + k30) & 15) * 131072; glds_run<0>(s_, vw4[0], sdst + (slot)); glds_run<1>(s_, vw4[1], 0); glds_run<2>(s_, vw4[2], 0); glds_run<3>(s_, vw4[3], 0); } while (0)
        const unsigned fo = (unsigned)WS_FO + ((br * 64 + mt3 * 4 + wi) * 32 + 4 * ((lane & 15) >> 1) + 2 * (1 - g) + (lane & 1)) * FS;
        drain();
        if (lane == 0) flag_set(ws + WS_FO, ((mt * 8 + wid) * 32 + 2 * hp + br) * FS, tag);
        SB_(2);
        asm volatile("s_barrier" ::: "memory");
        SB_(3);
        JIT(12);
        flag_wait(ws, fo, tag);
        STAGE_O(1 - g);
        if (g == 1) STAGE_O(2);
        STAGE_WO(5 - g, 65536 + (1 - g) * 16384);
        SB_(4);
        unsigned ao = lds0 + g * 16384 + wm * 8192 + (g4 >> 1) * 1024 + ((lane & 15) + 32 * (g4 & 1)) * 16;
        unsigned aw[4];
        { const int q4 = (lane & 15) >> 2, p = lane & 3, k = 16 * (g4 >> 1) + 4 * (g4 & 1) + q4;
          const unsigned a0 = lds0 + p3_wslot(0) + g * 16384 + k * 256 + (((8 * wn + (p >> 1)) ^ ((k & 7) << 1)) << 4) + (p & 1) * 8;
#pragma unroll
          for (int nb = 0; nb < 4; ++nb) aw[nb] = a0 ^ (nb << 5); }
        const char* so = of + ((3 + g + k30) & 15) * 4096; const char* sw = wo + (size_t)((5 + g + k30) & 15) * 131072;
        unsigned dob = sdst + ((3 + g) & 3) * 16384, dwb = sdst + p3_wslot(6);
        f32x4 acc[4][4];
#pragma unroll
        for (int mb = 0; mb < 4; ++mb)
#pragma unroll
            for (int nb = 0; nb < 4; ++nb) acc[mb][nb] = f32x4{};
        if (g == 1) { WAITBAR_(8); asm volatile("s_barrier" ::: "memory"); } else asm volatile("s_barrier" ::: "memory");
        SB_(5);
        SBAR();
#pragma unroll
        for (int u = 0; u < 8; ++u) {
            bf16x8 of_[4][2], wf[4][2];
            const bool doo = u < 6 || (u == 6 && g == 0), dow = (u >= 1 || g == 1) && (u < 5 || (u == 5 && g == 0));
#define P3RD(i) do { of_[(i) & 3][(i) >> 2] = ldsrd128(ao + (((i) & 3) >> 1) * 4096 + ((i) & 1) * 256 + ((i) >> 2) * 2048); \
                wf[(i) & 3][(i) >> 2] = cat8(ldstr(aw[(i) & 3] + ((i) >> 2) * 8192), ldstr(aw[(i) & 3] + ((i) >> 2) * 8192 + 2048)); } while (0)
            P3RD(0); if (u < 7 && doo) glds_run_dev<0>(so, l16, dob);
            P3RD(1); if (u < 7 && doo) glds_run_dev<1>(so, l16, 0);
            P3RD(2); if (u < 7 && doo) glds_run_dev<2>(so, l16, 0);
            P3RD(3); if (u < 7 && doo) glds_run_dev<3>(so, l16, 0);
            P3RD(4); if (u < 6 && dow) glds_run<0>(sw, vw4[0], dwb);
            P3RD(5); if (u < 6 && dow) glds_run<1>(sw, vw4[1], 0);
            P3RD(6); if (u < 6 && dow) glds_run<2>(sw, vw4[2], 0);
            P3RD(7); if (u < 6 && dow) glds_run<3>(sw, vw4[3], 0);
#undef P3RD
            SBAR();
            if (u < 7) {
                { const int to = 2 * u + 5 + g; so = of + ((to + k30) & 15) * 4096; sw = wo + (size_t)((to + 2 + k30) & 15) * 131072;
                  dob = sdst + (to & 3) * 16384; dwb = sdst + (g ? p3_wslot(2 * u + 8) : p3_wslot(2 * u + 7)); }
                ao ^= 32768;
#pragma unroll
                for (int q = 0; q < 4; ++q) aw[q] += p3_wslot(2 * u + 2) - p3_wslot(2 * u);
                asm volatile("" : "+v"(ao), "+v"(aw[0]), "+v"(aw[1]), "+v"(aw[2]), "+v"(aw[3]), "+s"(so), "+s"(sw), "+s"(dob), "+s"(dwb));
            }
            if (p3_allow(2 * u) == p3_allow(2 * u + 1)) WAITBAR(p3_allow(2 * u)); else if (g == 0) WAITBAR(p3_allow(2 * u)); else WAITBAR(p3_allow(2 * u + 1));
            SBAR();
            __builtin_amdgcn_s_setprio(1);
#pragma unroll
            for (int mb = 0; mb < 4; ++mb)
#pragma unroll
                for (int nb = 0; nb < 4; ++nb) { acc[mb][nb] = MFMA16(wf[nb][0], of_[mb][0], acc[mb][nb]); acc[mb][nb] = MFMA16(wf[nb][1], of_[mb][1], acc[mb][nb]); CHAIN();
                }
            __builtin_amdgcn_s_setprio(0);
            SBAR();
            if (u < 7 || g == 0) asm volatile("s_barrier" ::: "memory");
            SBAR();
        }
        SB_(6);
        FRESH();
        JIT(13);
        const unsigned xw = lds0 + wid * 8192 + l16, xr = lds0 + (wid ^ 4) * 8192 + l16;
#pragma unroll
        for (int i = 0; i < 8; ++i) { const f32x4 v = g ? acc[i >> 2][i & 3] : acc[2 + (i >> 2)][i & 3]; ldswr128(xw + i * 1024, __builtin_bit_cast(u32x4, v)); }
        BARRIER_L();
        const unsigned st = lds0 + 65536 + wid * 4096;
#pragma unroll
        for (int i = 0; i < 8; ++i) {
            const f32x4 v = (g ? acc[2 + (i >> 2)][i & 3] : acc[i >> 2][i & 3]) + __builtin_bit_cast(f32x4, ldsrd128(xr + i * 1024));
            const int row = 16 * (i >> 2) + (lane & 15), c = 2 * (i & 3) + (g4 >> 1);
            *(LAS u32x2*)(uintptr_t)(st + row * 128 + ((c ^ (row & 7)) << 4) + (g4 & 1) * 8) = u32x2{cvtpk(v[0], v[1]), cvtpk(v[2], v[3])};
        }
        const int blk = mt3 * 4 + 2 * wm + g;
        char* op = (char*)out + br * 2048 + nt3 * 256 + wn * 128 + (lane & 7) * 16;
#pragma unroll
        for (int i = 0; i < 4; ++i) {
            const int r = 8 * i + (lane >> 3), row = br ? ((blk >> 1) * 64 + 2 * r + (blk & 1)) : (blk * 32 + r);
            st16_dev<0>(out, (unsigned)(row * 4096 + br * 2048 + nt3 * 256 + wn * 128 + (lane & 7) * 16), __builtin_bit_cast(u32x4, ldsrd128(st + r * 128 + (((lane & 7) ^ (r & 7)) << 4))));
        }
#undef STAGE_O
#undef STAGE_WO
    }
    STAMP_AT(0, 7);
    if (wid == 4) {
        const unsigned rec = (100u << 20) + b * 128;
        if (lane == 0) { st16_dev<0>(ws0, rec, u32x4{pst[0], pst[1], pst[2], pst[3]}); st16_dev<16>(ws0, rec, u32x4{pst[4], pst[5], pst[6], pst[7]}); }
        drain();
        if (lane == 0) flag_set(ws0, rec + 64, tag);
    }
    if (b == 0 && wid == 1) {
        unsigned mn = 0xffffffffu, base = 0, mx = 0;
        for (int pass = 0; pass < 2; ++pass)
        for (int i = 0; i < 4; ++i) { const unsigned rec = (100u << 20) + (i * 64 + lane) * 128;
            flag_wait(ws0, rec + 64, tag);
            unsigned t0v, txv;
            asm volatile("global_load_dword %0, %2, %3 sc1\n\tglobal_load_dword %1, %2, %3 offset:%4 sc1\n\ts_waitcnt vmcnt(0)" : "=&v"(t0v), "=&v"(txv) : "v"(rec), "s"(ws0), "i"((PROBE & 7) * 4) : "memory");
            if (pass == 0) { if (i == 0) base = __builtin_amdgcn_readfirstlane(t0v) - 100000u; mn = min(mn, t0v - base); } else mx = max(mx, txv - base - mn);
            if (pass == 0 && i == 3) { for (int o = 32; o; o >>= 1) mn = min(mn, (unsigned)__shfl_xor((int)mn, o)); } }
        for (int o = 32; o; o >>= 1) mx = max(mx, (unsigned)__shfl_xor((int)mx, o));
        const unsigned until = base + mn + 15000u + ((PROBE & 8) ? 0u : mx);
        while ((int)((unsigned)__builtin_amdgcn_s_memrealtime() - until) < 0) __builtin_amdgcn_s_sleep(1);
    }
    if (b == 0 && wid == 0) {
#pragma unroll
        for (int i = 0; i < 2; ++i) { const int u = i * 64 + lane;
            flag_wait(ws0, (unsigned)WS_FK + ((2 * (u >> 3)) * 32 + (u & 7) * 4) * FS, tag);
            flag_wait(ws0, (unsigned)WS_FO + ((64 + (u & 7) * 8) * 32 + 2 * (u >> 3)) * FS, tag); }
        if (lane == 0) flag_set(ws0, (unsigned)WS_EP, tag);
    }
    asm volatile(".Lmk_code_end:");
}

extern "C" void kernel_launch(void* const* d_in, const int* in_sizes, int n_in, void* d_out, int out_size, void* d_ws, size_t ws_size, hipStream_t stream) {
    mk_fused<<<256, 512, 0, stream>>>((const u16*)d_in[0], (const u16*)d_in[1], (const u16*)d_in[2], (const u16*)d_in[3], (const u16*)d_in[4], (const u16*)d_in[5], (char*)d_ws, (u16*)d_out);
}
```

```cpp
#include <hip/hip_runtime.h>
#include <stdint.h>

typedef unsigned short u16;
typedef unsigned long long u64;
typedef __bf16 bf16x8 __attribute__((ext_vector_type(8)));
typedef __bf16 bf16x2 __attribute__((ext_vector_type(2)));
typedef short s16x4 __attribute__((ext_vector_type(4)));
typedef float f32x16 __attribute__((ext_vector_type(16)));
typedef float f32x4 __attribute__((ext_vector_type(4)));
typedef float f32x2 __attribute__((ext_vector_type(2)));
typedef unsigned u32x4 __attribute__((ext_vector_type(4)));
typedef unsigned u32x2 __attribute__((ext_vector_type(2)));
typedef unsigned u32x8 __attribute__((ext_vector_type(8)));
typedef unsigned u32x16 __attribute__((ext_vector_type(16)));
#define LAS __attribute__((address_space(3)))
#define DI __device__ __forceinline__
#define SBAR() __builtin_amdgcn_sched_barrier(0)
#define MFMA32(a, b, c) __builtin_amdgcn_mfma_f32_32x32x16_bf16((a), (b), (c), 0, 0, 0)
#define MFMA16(a, b, c) __builtin_amdgcn_mfma_f32_16x16x32_bf16((a), (b), (c), 0, 0, 0)
#define CHAIN() SBAR()
#define ORD 0
#define EXP2(x) __builtin_amdgcn_exp2f(x)

constexpr float QSCALE = 0.17677669529663687f * 1.4426950408889634f;

constexpr size_t WS_QF = 0;
constexpr size_t WS_KV = WS_QF + (4u << 20);
constexpr size_t WS_OF = WS_KV + (8u << 20);
constexpr size_t WS_HALO = WS_OF + (8u << 20);
constexpr size_t WS_XP = WS_HALO + (2u << 20);
constexpr size_t WS_FX = WS_XP + (5u << 20);
#define FS 64
constexpr size_t WS_FQ = WS_FX + 1024 * FS;
constexpr size_t WS_FK = WS_FQ + 1024 * FS;
constexpr size_t WS_FO = WS_FK + 1024 * FS;
constexpr size_t WS_FH = WS_FO + 4096 * FS;
constexpr size_t WS_EP = WS_FH + 256 * FS;

#define OUTST 1
#define SLP 1
#define STAMP_AT(K, I)
#define SA_(I)
#define SB_(I)

#define SC_(I)
#define JIT(p)
DI unsigned cvtpk(float lo, float hi) { f32x2 v = {lo, hi}; bf16x2 b = __builtin_convertvector(v, bf16x2); return __builtin_bit_cast(unsigned, b); }
DI u32x4 pack8(const f32x16& a, int s) { u32x4 r; r[0] = cvtpk(a[8 * s], a[8 * s + 1]); r[1] = cvtpk(a[8 * s + 2], a[8 * s + 3]); r[2] = cvtpk(a[8 * s + 4], a[8 * s + 5]); r[3] = cvtpk(a[8 * s + 6], a[8 * s + 7]); return r; }
DI u32x4 pack44(f32x4 a, f32x4 c) { return u32x4{cvtpk(a[0], a[1]), cvtpk(a[2], a[3]), cvtpk(c[0], c[1]), cvtpk(c[2], c[3])}; }
DI u32x4 asu(bf16x8 v) { return __builtin_bit_cast(u32x4, v); }
DI u32x16 cat16(u32x4 a, u32x4 b, u32x4 c, u32x4 d) { return u32x16{a[0], a[1], a[2], a[3], b[0], b[1], b[2], b[3], c[0], c[1], c[2], c[3], d[0], d[1], d[2], d[3]}; }
DI bf16x8 asbf(u32x4 v) { return __builtin_bit_cast(bf16x8, v); }
DI void glds16(const void* sbase, unsigned voff, unsigned lds) {
    asm volatile("s_mov_b32 m0, %2\n\ts_nop 0\n\tglobal_load_lds_dwordx4 %0, %1" :: "v"(voff), "s"(sbase), "s"(lds) : "memory");
}
template <int I> DI void glds_run(const void* sbase, unsigned voff, unsigned lds) {
    if (I == 0) asm volatile("s_mov_b32 m0, %2\n\ts_nop 0\n\tglobal_load_lds_dwordx4 %0, %1" :: "v"(voff), "s"(sbase), "s"(lds) : "memory");
    else asm volatile("global_load_lds_dwordx4 %0, %1 offset:%2" :: "v"(voff), "s"(sbase), "i"(I * 1024) : "memory");
}
template <int I> DI void glds_run_dev(const void* sbase, unsigned voff, unsigned lds) {
    if (I == 0) asm volatile("s_mov_b32 m0, %2\n\ts_nop 0\n\tglobal_load_lds_dwordx4 %0, %1 sc1" :: "v"(voff), "s"(sbase), "s"(lds) : "memory");
    else asm volatile("global_load_lds_dwordx4 %0, %1 offset:%2 sc1" :: "v"(voff), "s"(sbase), "i"(I * 1024) : "memory");
}
template <int I> DI void glds_sec(const void* sbase, unsigned voff, unsigned lds) {
    if (I < 2) asm volatile("s_add_u32 m0, %2, %4\n\ts_nop 0\n\tglobal_load_lds_dwordx4 %0, %1 offset:%3" :: "v"(voff), "s"(sbase), "s"(lds), "i"(I * 2048), "i"(I * 6144) : "memory", "scc");
    else asm volatile("s_add_u32 m0, %2, %4\n\ts_nop 0\n\tglobal_load_lds_dwordx4 %0, %1 offset:%3" :: "v"(voff), "s"((const char*)sbase + 2048), "s"(lds), "i"(2048), "i"(14336) : "memory", "scc");
}
DI void glds16_dev(const void* sbase, unsigned voff, unsigned lds) {
    asm volatile("s_mov_b32 m0, %2\n\ts_nop 0\n\tglobal_load_lds_dwordx4 %0, %1 sc1" :: "v"(voff), "s"(sbase), "s"(lds) : "memory");
}
DI void glds4k_dev(const void* sbase, unsigned voff, unsigned lds) {
    asm volatile("s_mov_b32 m0, %2\n\ts_nop 0\n\tglobal_load_lds_dwordx4 %0, %1 sc1\n\tglobal_load_lds_dwordx4 %0, %1 offset:1024 sc1\n\t"
                 "global_load_lds_dwordx4 %0, %1 offset:2048 sc1\n\tglobal_load_lds_dwordx4 %0, %1 offset:3072 sc1" :: "v"(voff), "s"(sbase), "s"(lds) : "memory");
}
template <int OFF> DI void st16_dev(const void* sbase, unsigned voff, u32x4 v) {
    asm volatile("global_store_dwordx4 %0, %1, %2 offset:%3 sc1\n\ts_nop 1" :: "v"(voff), "v"(v), "s"(sbase), "i"(OFF) : "memory");
}
DI void drain() { asm volatile("s_waitcnt vmcnt(0)" ::: "memory"); }
DI void flag_set(const void* sbase, unsigned voff, u64 tag) {
    asm volatile("global_store_dwordx2 %0, %1, %2 sc1" :: "v"(voff), "v"(tag), "s"(sbase) : "memory");
}
DI void flag_wait(const void* sbase, unsigned voff, u64 tag) {
    const u64 t0 = __builtin_amdgcn_s_memrealtime();
    for (;;) {
        u64 v;
        asm volatile("global_load_dwordx2 %0, %1, %2 sc1\n\ts_waitcnt vmcnt(0)" : "=v"(v) : "v"(voff), "s"(sbase) : "memory");
        if (__all(v == tag)) break;
        if (__builtin_amdgcn_s_memrealtime() - t0 > 2000000ull) break;
        __builtin_amdgcn_s_sleep(SLP);
    }
}
DI u64 flag_wait_some(const void* sbase, unsigned voff, u64 tag, u64 need) {
    const u64 t0 = __builtin_amdgcn_s_memrealtime();
    for (;;) {
        u64 v;
        asm volatile("global_load_dwordx2 %0, %1, %2 sc1\n\ts_waitcnt vmcnt(0)" : "=v"(v) : "v"(voff), "s"(sbase) : "memory");
        const u64 m = __ballot(v == tag);
        if ((m & need) == need || __builtin_amdgcn_s_memrealtime() - t0 > 2000000ull) return m;
        __builtin_amdgcn_s_sleep(SLP);
    }
}
DI bf16x8 ldsrd128(unsigned addr) { return *(const LAS bf16x8*)(uintptr_t)addr; }
DI void ldswr128(unsigned addr, u32x4 v) { *(LAS u32x4*)(uintptr_t)addr = v; }
DI s16x4 ldstr(unsigned addr) { return __builtin_amdgcn_ds_read_tr16_b64_v4i16((LAS s16x4*)(uintptr_t)addr); }
DI bf16x8 cat8(s16x4 lo, s16x4 hi) { typedef short s16x8 __attribute__((ext_vector_type(8))); s16x8 v = {lo[0], lo[1], lo[2], lo[3], hi[0], hi[1], hi[2], hi[3]}; return __builtin_bit_cast(bf16x8, v); }
DI float swap_max(float m) { auto r = __builtin_amdgcn_permlane32_swap(__float_as_uint(m), __float_as_uint(m), false, false); return __builtin_fmaxf(__uint_as_float(r[0]), __uint_as_float(r[1])); }
DI float swap_add(float m) { auto r = __builtin_amdgcn_permlane32_swap(__float_as_uint(m), __float_as_uint(m), false, false); return __uint_as_float(r[0]) + __uint_as_float(r[1]); }
#define FRESH() asm volatile("" : "+v"(lane), "+s"(ws))
#define BARRIER_L() asm volatile("s_waitcnt lgkmcnt(0)\n\ts_barrier" ::: "memory")
#define WAITBAR_(n) asm volatile("s_waitcnt vmcnt(%0) lgkmcnt(0)\n\ts_barrier" :: "i"(n) : "memory")
#define WAITBAR(n) do { switch (n) { case 0: WAITBAR_(0); break; case 2: WAITBAR_(2); break; case 3: WAITBAR_(3); break; case 4: WAITBAR_(4); break; case 6: WAITBAR_(6); break; \
    case 7: WAITBAR_(7); break; case 8: WAITBAR_(8); break; case 10: WAITBAR_(10); break; case 12: WAITBAR_(12); break; default: WAITBAR_(0); } } while (0)

struct LocalPart { f32x16 o; float m, l; };
DI f32x16 band_mask(int lane) {
    const int i = lane & 31, h = lane >> 5;
    f32x16 c;
#pragma unroll
    for (int r = 0; r < 16; ++r) { const int j = (r & 3) + 8 * (r >> 2) + 4 * h; c[r] = ((unsigned)(i - j) <= 7u) ? 0.f : -__builtin_inff(); }
    return c;
}
DI float max3(float a, float b, float c) { float r; asm("v_max3_f32 %0, %1, %2, %3" : "=v"(r) : "v"(a), "v"(b), "v"(c)); return r; }
template <bool PREV> DI void local_own(bf16x8 q0, bf16x8 q1, bf16x8 k0, bf16x8 k1, bf16x8 v0, bf16x8 v1, bf16x8 kp0, bf16x8 kp1, bf16x8 vp1, const f32x16& band, int lane, LocalPart& P) {
    const f32x16 z = {};
    f32x16 so = MFMA32(k0, q0, band), sp = z;
    if (PREV) sp = MFMA32(kp0, q0, z);
    so = MFMA32(k1, q1, so);
    if (PREV) sp = MFMA32(kp1, q1, sp);
    float m = __builtin_fmaxf(so[0], so[1]);
#pragma unroll
    for (int r = 2; r < 16; r += 2) m = __builtin_fmaxf(__builtin_fmaxf(m, so[r]), so[r + 1]);
    if (PREV) {
        const int d = 4 * (lane >> 5) - (lane & 31);
#pragma unroll
        for (int r = 12; r < 16; ++r) { sp[r] = (d + (r & 3) >= 1) ? sp[r] : -__builtin_inff(); m = __builtin_fmaxf(m, sp[r]); }
    }
    m = swap_max(m);
    float l = 0.f;
#pragma unroll
    for (int r = 0; r < 16; ++r) { so[r] = EXP2(so[r] - m); l += so[r]; }
    P.o = MFMA32(v0, asbf(pack8(so, 0)), z); P.o = MFMA32(v1, asbf(pack8(so, 1)), P.o);
    if (PREV) {
#pragma unroll
        for (int r = 12; r < 16; ++r) { sp[r] = EXP2(sp[r] - m); l += sp[r]; }
        P.o = MFMA32(vp1, asbf(u32x4{0u, 0u, cvtpk(sp[12], sp[13]), cvtpk(sp[14], sp[15])}), P.o);
    }
    P.m = m; P.l = l;
}
DI void local_prev(bf16x8 q0, bf16x8 q1, bf16x8 kp0, bf16x8 kp1, bf16x8 vp1, int lane, LocalPart& P) {
    const int i = lane & 31, h = lane >> 5;
    const f32x16 z = {};
    f32x16 sp = MFMA32(kp0, q0, z); sp = MFMA32(kp1, q1, sp);
    const float NEG = -__builtin_inff();
    float m = P.m;
#pragma unroll
    for (int r = 12; r < 16; ++r) { const int j = (r & 3) + 24 + 4 * h; sp[r] = (j >= i + 25) ? sp[r] : NEG; m = __builtin_fmaxf(m, sp[r]); }
    m = swap_max(m);
    const float f = EXP2(P.m - m);
    float l = P.l * f;
#pragma unroll
    for (int r = 12; r < 16; ++r) { sp[r] = EXP2(sp[r] - m); l += sp[r]; }
#pragma unroll
    for (int r = 0; r < 16; ++r) P.o[r] *= f;
    P.o = MFMA32(vp1, asbf(u32x4{0u, 0u, cvtpk(sp[12], sp[13]), cvtpk(sp[14], sp[15])}), P.o);
    P.m = m; P.l = l;
}
DI void local_fin(LocalPart& P) {
    const float inv = __builtin_amdgcn_rcpf(swap_add(P.l));
#pragma unroll
    for (int r = 0; r < 16; ++r) P.o[r] *= inv;
    const u32x4 o0 = pack8(P.o, 0), o1 = pack8(P.o, 1);
#pragma unroll
    for (int r = 0; r < 4; ++r) { P.o[r] = __uint_as_float(o0[r]); P.o[4 + r] = __uint_as_float(o1[r]); }
}
DI u32x4 quad(const f32x16& a, int i) { return __builtin_bit_cast(u32x4, f32x4{a[4 * i], a[4 * i + 1], a[4 * i + 2], a[4 * i + 3]}); }

#define ATT_L6C \
    "s_nop 1\n\t" \
    "s_cmp_eq_u32 %[hl], 0\n\t" \
    "s_cbranch_scc1 10f\n\t" \
    "global_load_dwordx4 v[144:147], v143, %[ws] offset:-3072 sc1\n\t" \
    "global_load_dwordx4 v[148:151], v143, %[ws] offset:-2048 sc1\n\t" \
    "global_load_dwordx4 v[152:155], v143, %[ws] offset:-1024 sc1\n\t" \
    "global_load_dwordx4 v[156:159], v143, %[ws] offset:0 sc1\n\t" \
    "global_load_dwordx4 v[160:163], v143, %[ws] offset:1024 sc1\n\t" \
    "global_load_dwordx4 v[164:167], v143, %[ws] offset:2048 sc1\n\t" \
    "10:\n" \
    "s_setprio 3\n\t" \
    "s_waitcnt lgkmcnt(0)\n\t" \
    "v_mfma_f32_32x32x16_bf16 v[48:63], v[80:83], v[64:67], v[16:31]\n\t" \
    "v_exp_f32_e32 v32, v32\n\t" \
    "v_exp_f32_e32 v33, v33\n\t" \
    "v_exp_f32_e32 v34, v34\n\t" \
    "v_exp_f32_e32 v35, v35\n\t" \
    "v_exp_f32_e32 v36, v36\n\t" \
    "v_mfma_f32_32x32x16_bf16 v[48:63], v[84:87], v[68:71], v[48:63]\n\t" \
    "ds_read_b128 v[80:83], v112 offset:8192\n\t" \
    "ds_read_b128 v[84:87], v112 offset:9216\n\t" \
    "v_cvt_pk_bf16_f32 v104, v32, v33\n\t" \
    "v_cvt_pk_bf16_f32 v105, v34, v35\n\t" \
    "v_exp_f32_e32 v37, v37\n\t" \
    "v_exp_f32_e32 v38, v38\n\t" \
    "v_exp_f32_e32 v39, v39\n\t" \
    "ds_read_b128 v[100:103], v112 offset:3072\n\t" \
    "ds_read_b128 v[96:99], v112 offset:6144\n\t" \
    "v_exp_f32_e32 v40, v40\n\t" \
    "v_cvt_pk_bf16_f32 v106, v36, v37\n\t" \
    "v_cvt_pk_bf16_f32 v107, v38, v39\n\t" \
    "v_exp_f32_e32 v41, v41\n\t" \
    "v_exp_f32_e32 v42, v42\n\t" \
    "v_exp_f32_e32 v43, v43\n\t" \
    "v_mfma_f32_32x32x16_bf16 v[0:15], v[88:91], v[104:107], 0\n\t" \
    "v_exp_f32_e32 v44, v44\n\t" \
    "v_exp_f32_e32 v45, v45\n\t" \
    "v_exp_f32_e32 v46, v46\n\t" \
    "v_exp_f32_e32 v47, v47\n\t" \
    "v_cvt_pk_bf16_f32 v108, v40, v41\n\t" \
    "v_cvt_pk_bf16_f32 v109, v42, v43\n\t" \
    "v_cvt_pk_bf16_f32 v110, v44, v45\n\t" \
    "v_cvt_pk_bf16_f32 v111, v46, v47\n\t" \
    "s_mov_b32 m0, %[fld]\n\t" \
    "s_nop 0\n\t" \
    "global_load_lds_dwordx4 v142, %[ws] sc1\n\t" \
    "s_waitcnt lgkmcnt(0)\n\t" \
    "v_mfma_f32_32x32x16_bf16 v[32:47], v[80:83], v[64:67], v[16:31]\n\t" \
    "v_exp_f32_e32 v48, v48\n\t" \
    "v_exp_f32_e32 v49, v49\n\t" \
    "v_exp_f32_e32 v50, v50\n\t" \
    "v_smfmac_f32_16x16x64_bf16 v[76:79], v[72:75], v[104:111], v174\n\t" \
    "v_exp_f32_e32 v51, v51\n\t" \
    "v_exp_f32_e32 v52, v52\n\t" \
    "v_mfma_f32_32x32x16_bf16 v[32:47], v[84:87], v[68:71], v[32:47]\n\t" \
    "ds_read_b128 v[80:83], v112 offset:12288\n\t" \
    "ds_read_b128 v[84:87], v112 offset:13312\n\t" \
    "v_cvt_pk_bf16_f32 v104, v48, v49\n\t" \
    "v_cvt_pk_bf16_f32 v105, v50, v51\n\t" \
    "v_exp_f32_e32 v53, v53\n\t" \
    "v_exp_f32_e32 v54, v54\n\t" \
    "v_exp_f32_e32 v55, v55\n\t" \
    "v_mfma_f32_32x32x16_bf16 v[0:15], v[100:103], v[108:111], v[0:15]\n\t" \
    "ds_read_b128 v[92:95], v112 offset:7168\n\t" \
    "ds_read_b128 v[88:91], v112 offset:10240\n\t" \
    "v_exp_f32_e32 v56, v56\n\t" \
    "v_cvt_pk_bf16_f32 v106, v52, v53\n\t" \
    "v_cvt_pk_bf16_f32 v107, v54, v55\n\t" \
    "v_exp_f32_e32 v57, v57\n\t" \
    "v_exp_f32_e32 v58, v58\n\t" \
    "v_exp_f32_e32 v59, v59\n\t" \
    "v_mfma_f32_32x32x16_bf16 v[0:15], v[96:99], v[104:107], v[0:15]\n\t" \
    "v_exp_f32_e32 v60, v60\n\t" \
    "v_exp_f32_e32 v61, v61\n\t" \
    "v_exp_f32_e32 v62, v62\n\t" \
    "v_exp_f32_e32 v63, v63\n\t" \
    "v_cvt_pk_bf16_f32 v108, v56, v57\n\t" \
    "v_cvt_pk_bf16_f32 v109, v58, v59\n\t" \
    "v_cvt_pk_bf16_f32 v110, v60, v61\n\t" \
    "v_cvt_pk_bf16_f32 v111, v62, v63\n\t" \
    "s_setprio 2\n\t" \
    "s_waitcnt lgkmcnt(0)\n\t" \
    "v_mfma_f32_32x32x16_bf16 v[48:63], v[80:83], v[64:67], v[16:31]\n\t" \
    "v_exp_f32_e32 v32, v32\n\t" \
    "v_exp_f32_e32 v33, v33\n\t" \
    "v_exp_f32_e32 v34, v34\n\t" \
    "v_smfmac_f32_16x16x64_bf16 v[76:79], v[72:75], v[104:111], v174\n\t" \
    "v_exp_f32_e32 v35, v35\n\t" \
    "v_exp_f32_e32 v36, v36\n\t" \
    "v_mfma_f32_32x32x16_bf16 v[48:63], v[84:87], v[68:71], v[48:63]\n\t" \
    "ds_read_b128 v[80:83], v112 offset:16384\n\t" \
    "ds_read_b128 v[84:87], v112 offset:17408\n\t" \
    "v_cvt_pk_bf16_f32 v104, v32, v33\n\t" \
    "v_cvt_pk_bf16_f32 v105, v34, v35\n\t" \
    "v_exp_f32_e32 v37, v37\n\t" \
    "v_exp_f32_e32 v38, v38\n\t" \
    "v_exp_f32_e32 v39, v39\n\t" \
    "v_mfma_f32_32x32x16_bf16 v[0:15], v[92:95], v[108:111], v[0:15]\n\t" \
    "ds_read_b128 v[100:103], v112 offset:11264\n\t" \
    "ds_read_b128 v[96:99], v112 offset:14336\n\t" \
    "v_exp_f32_e32 v40, v40\n\t" \
    "v_cvt_pk_bf16_f32 v106, v36, v37\n\t" \
    "v_cvt_pk_bf16_f32 v107, v38, v39\n\t" \
    "v_exp_f32_e32 v41, v41\n\t" \
    "v_exp_f32_e32 v42, v42\n\t" \
    "v_exp_f32_e32 v43, v43\n\t" \
    "v_mfma_f32_32x32x16_bf16 v[0:15], v[88:91], v[104:107], v[0:15]\n\t" \
    "v_exp_f32_e32 v44, v44\n\t" \
    "v_exp_f32_e32 v45, v45\n\t" \
    "v_exp_f32_e32 v46, v46\n\t" \
    "v_exp_f32_e32 v47, v47\n\t" \
    "v_cvt_pk_bf16_f32 v108, v40, v41\n\t" \
    "v_cvt_pk_bf16_f32 v109, v42, v43\n\t" \
    "v_cvt_pk_bf16_f32 v110, v44, v45\n\t" \
    "v_cvt_pk_bf16_f32 v111, v46, v47\n\t" \
    "s_waitcnt lgkmcnt(0)\n\t" \
    "v_mfma_f32_32x32x16_bf16 v[32:47], v[80:83], v[64:67], v[16:31]\n\t" \
    "v_exp_f32_e32 v48, v48\n\t" \
    "v_exp_f32_e32 v49, v49\n\t" \
    "v_exp_f32_e32 v50, v50\n\t" \
    "v_smfmac_f32_16x16x64_bf16 v[76:79], v[72:75], v[104:111], v174\n\t" \
    "v_exp_f32_e32 v51, v51\n\t" \
    "v_exp_f32_e32 v52, v52\n\t" \
    "v_mfma_f32_32x32x16_bf16 v[32:47], v[84:87], v[68:71], v[32:47]\n\t" \
    "ds_read_b128 v[80:83], v112 offset:20480\n\t" \
    "ds_read_b128 v[84:87], v112 offset:21504\n\t" \
    "v_cvt_pk_bf16_f32 v104, v48, v49\n\t" \
    "v_cvt_pk_bf16_f32 v105, v50, v51\n\t" \
    "v_exp_f32_e32 v53, v53\n\t" \
    "v_exp_f32_e32 v54, v54\n\t" \
    "v_exp_f32_e32 v55, v55\n\t" \
    "v_mfma_f32_32x32x16_bf16 v[0:15], v[100:103], v[108:111], v[0:15]\n\t" \
    "ds_read_b128 v[92:95], v112 offset:15360\n\t" \
    "ds_read_b128 v[88:91], v112 offset:18432\n\t" \
    "v_exp_f32_e32 v56, v56\n\t" \
    "v_cvt_pk_bf16_f32 v106, v52, v53\n\t" \
    "v_cvt_pk_bf16_f32 v107, v54, v55\n\t" \
    "v_exp_f32_e32 v57, v57\n\t" \
    "v_exp_f32_e32 v58, v58\n\t" \
    "v_exp_f32_e32 v59, v59\n\t" \
    "v_mfma_f32_32x32x16_bf16 v[0:15], v[96:99], v[104:107], v[0:15]\n\t" \
    "v_exp_f32_e32 v60, v60\n\t" \
    "v_exp_f32_e32 v61, v61\n\t" \
    "v_exp_f32_e32 v62, v62\n\t" \
    "v_exp_f32_e32 v63, v63\n\t" \
    "v_cvt_pk_bf16_f32 v108, v56, v57\n\t" \
    "v_cvt_pk_bf16_f32 v109, v58, v59\n\t" \
    "v_cvt_pk_bf16_f32 v110, v60, v61\n\t" \
    "v_cvt_pk_bf16_f32 v111, v62, v63\n\t" \
    "s_setprio 1\n\t" \
    "s_waitcnt lgkmcnt(0)\n\t" \
    "v_mfma_f32_32x32x16_bf16 v[48:63], v[80:83], v[64:67], v[16:31]\n\t" \
    "v_exp_f32_e32 v32, v32\n\t" \
    "v_exp_f32_e32 v33, v33\n\t" \
    "v_exp_f32_e32 v34, v34\n\t" \
    "v_smfmac_f32_16x16x64_bf16 v[76:79], v[72:75], v[104:111], v174\n\t" \
    "v_exp_f32_e32 v35, v35\n\t" \
    "v_exp_f32_e32 v36, v36\n\t" \
    "v_mfma_f32_32x32x16_bf16 v[48:63], v[84:87], v[68:71], v[48:63]\n\t" \
    "ds_read_b128 v[80:83], v112 offset:24576\n\t" \
    "ds_read_b128 v[84:87], v112 offset:25600\n\t" \
    "v_cvt_pk_bf16_f32 v104, v32, v33\n\t" \
    "v_cvt_pk_bf16_f32 v105, v34, v35\n\t" \
    "v_exp_f32_e32 v37, v37\n\t" \
    "v_exp_f32_e32 v38, v38\n\t" \
    "v_exp_f32_e32 v39, v39\n\t" \
    "v_mfma_f32_32x32x16_bf16 v[0:15], v[92:95], v[108:111], v[0:15]\n\t" \
    "ds_read_b128 v[100:103], v112 offset:19456\n\t" \
    "ds_read_b128 v[96:99], v112 offset:22528\n\t" \
    "v_exp_f32_e32 v40, v40\n\t" \
    "v_cvt_pk_bf16_f32 v106, v36, v37\n\t" \
    "v_cvt_pk_bf16_f32 v107, v38, v39\n\t" \
    "v_exp_f32_e32 v41, v41\n\t" \
    "v_exp_f32_e32 v42, v42\n\t" \
    "v_exp_f32_e32 v43, v43\n\t" \
    "v_mfma_f32_32x32x16_bf16 v[0:15], v[88:91], v[104:107], v[0:15]\n\t" \
    "v_exp_f32_e32 v44, v44\n\t" \
    "v_exp_f32_e32 v45, v45\n\t" \
    "v_exp_f32_e32 v46, v46\n\t" \
    "v_exp_f32_e32 v47, v47\n\t" \
    "v_cvt_pk_bf16_f32 v108, v40, v41\n\t" \
    "v_cvt_pk_bf16_f32 v109, v42, v43\n\t" \
    "v_cvt_pk_bf16_f32 v110, v44, v45\n\t" \
    "v_cvt_pk_bf16_f32 v111, v46, v47\n\t" \
    "s_waitcnt lgkmcnt(0)\n\t" \
    "v_mfma_f32_32x32x16_bf16 v[32:47], v[80:83], v[64:67], v[16:31]\n\t" \
    "v_exp_f32_e32 v48, v48\n\t" \
    "v_exp_f32_e32 v49, v49\n\t" \
    "v_exp_f32_e32 v50, v50\n\t" \
    "v_smfmac_f32_16x16x64_bf16 v[76:79], v[72:75], v[104:111], v174\n\t" \
    "v_exp_f32_e32 v51, v51\n\t" \
    "v_exp_f32_e32 v52, v52\n\t" \
    "v_mfma_f32_32x32x16_bf16 v[32:47], v[84:87], v[68:71], v[32:47]\n\t" \
    "ds_read_b128 v[80:83], v112 offset:28672\n\t" \
    "ds_read_b128 v[84:87], v112 offset:29696\n\t" \
    "v_cvt_pk_bf16_f32 v104, v48, v49\n\t" \
    "v_cvt_pk_bf16_f32 v105, v50, v51\n\t" \
    "v_exp_f32_e32 v53, v53\n\t" \
    "v_exp_f32_e32 v54, v54\n\t" \
    "v_exp_f32_e32 v55, v55\n\t" \
    "v_mfma_f32_32x32x16_bf16 v[0:15], v[100:103], v[108:111], v[0:15]\n\t" \
    "ds_read_b128 v[92:95], v112 offset:23552\n\t" \
    "ds_read_b128 v[88:91], v112 offset:26624\n\t" \
    "v_exp_f32_e32 v56, v56\n\t" \
    "v_cvt_pk_bf16_f32 v106, v52, v53\n\t" \
    "v_cvt_pk_bf16_f32 v107, v54, v55\n\t" \
    "v_exp_f32_e32 v57, v57\n\t" \
    "v_exp_f32_e32 v58, v58\n\t" \
    "v_exp_f32_e32 v59, v59\n\t" \
    "v_mfma_f32_32x32x16_bf16 v[0:15], v[96:99], v[104:107], v[0:15]\n\t" \
    "v_exp_f32_e32 v60, v60\n\t" \
    "v_exp_f32_e32 v61, v61\n\t" \
    "v_exp_f32_e32 v62, v62\n\t" \
    "v_exp_f32_e32 v63, v63\n\t" \
    "v_cvt_pk_bf16_f32 v108, v56, v57\n\t" \
    "v_cvt_pk_bf16_f32 v109, v58, v59\n\t" \
    "v_cvt_pk_bf16_f32 v110, v60, v61\n\t" \
    "v_cvt_pk_bf16_f32 v111, v62, v63\n\t" \
    "s_setprio 0\n\t" \
    "s_cmp_eq_u32 %[hl], 0\n\t" \
    "s_cbranch_scc1 11f\n\t" \
    "s_waitcnt vmcnt(1)\n\t" \
    "11:\n" \
    "s_waitcnt lgkmcnt(0)\n\t" \
    "s_nop 15\n\t"
#define ATT_L4 \
    "s_nop 1\n\t" \
    "s_setprio 3\n\t" \
    "s_waitcnt lgkmcnt(0)\n\t" \
    "v_mfma_f32_32x32x16_bf16 v[48:63], v[80:83], v[64:67], v[16:31]\n\t" \
    "v_exp_f32_e32 v32, v32\n\t" \
    "v_exp_f32_e32 v33, v33\n\t" \
    "v_exp_f32_e32 v34, v34\n\t" \
    "v_exp_f32_e32 v35, v35\n\t" \
    "v_exp_f32_e32 v36, v36\n\t" \
    "v_mfma_f32_32x32x16_bf16 v[48:63], v[84:87], v[68:71], v[48:63]\n\t" \
    "ds_read_b128 v[80:83], v112 offset:8192\n\t" \
    "ds_read_b128 v[84:87], v112 offset:9216\n\t" \
    "v_cvt_pk_bf16_f32 v104, v32, v33\n\t" \
    "v_cvt_pk_bf16_f32 v105, v34, v35\n\t" \
    "v_exp_f32_e32 v37, v37\n\t" \
    "v_exp_f32_e32 v38, v38\n\t" \
    "v_exp_f32_e32 v39, v39\n\t" \
    "ds_read_b128 v[100:103], v112 offset:3072\n\t" \
    "ds_read_b128 v[96:99], v112 offset:6144\n\t" \
    "v_exp_f32_e32 v40, v40\n\t" \
    "v_cvt_pk_bf16_f32 v106, v36, v37\n\t" \
    "v_cvt_pk_bf16_f32 v107, v38, v39\n\t" \
    "v_exp_f32_e32 v41, v41\n\t" \
    "v_exp_f32_e32 v42, v42\n\t" \
    "v_exp_f32_e32 v43, v43\n\t" \
    "v_mfma_f32_32x32x16_bf16 v[0:15], v[88:91], v[104:107], 0\n\t" \
    "v_exp_f32_e32 v44, v44\n\t" \
    "v_exp_f32_e32 v45, v45\n\t" \
    "v_exp_f32_e32 v46, v46\n\t" \
    "v_exp_f32_e32 v47, v47\n\t" \
    "v_cvt_pk_bf16_f32 v108, v40, v41\n\t" \
    "v_cvt_pk_bf16_f32 v109, v42, v43\n\t" \
    "v_cvt_pk_bf16_f32 v110, v44, v45\n\t" \
    "v_cvt_pk_bf16_f32 v111, v46, v47\n\t" \
    "s_waitcnt lgkmcnt(0)\n\t" \
    "v_mfma_f32_32x32x16_bf16 v[32:47], v[80:83], v[64:67], v[16:31]\n\t" \
    "v_exp_f32_e32 v48, v48\n\t" \
    "v_exp_f32_e32 v49, v49\n\t" \
    "v_exp_f32_e32 v50, v50\n\t" \
    "v_smfmac_f32_16x16x64_bf16 v[76:79], v[72:75], v[104:111], v174\n\t" \
    "v_exp_f32_e32 v51, v51\n\t" \
    "v_exp_f32_e32 v52, v52\n\t" \
    "v_mfma_f32_32x32x16_bf16 v[32:47], v[84:87], v[68:71], v[32:47]\n\t" \
    "ds_read_b128 v[80:83], v112 offset:12288\n\t" \
    "ds_read_b128 v[84:87], v112 offset:13312\n\t" \
    "v_cvt_pk_bf16_f32 v104, v48, v49\n\t" \
    "v_cvt_pk_bf16_f32 v105, v50, v51\n\t" \
    "v_exp_f32_e32 v53, v53\n\t" \
    "v_exp_f32_e32 v54, v54\n\t" \
    "v_exp_f32_e32 v55, v55\n\t" \
    "v_mfma_f32_32x32x16_bf16 v[0:15], v[100:103], v[108:111], v[0:15]\n\t" \
    "ds_read_b128 v[92:95], v112 offset:7168\n\t" \
    "ds_read_b128 v[88:91], v112 offset:10240\n\t" \
    "v_exp_f32_e32 v56, v56\n\t" \
    "v_cvt_pk_bf16_f32 v106, v52, v53\n\t" \
    "v_cvt_pk_bf16_f32 v107, v54, v55\n\t" \
    "v_exp_f32_e32 v57, v57\n\t" \
    "v_exp_f32_e32 v58, v58\n\t" \
    "v_exp_f32_e32 v59, v59\n\t" \
    "v_mfma_f32_32x32x16_bf16 v[0:15], v[96:99], v[104:107], v[0:15]\n\t" \
    "v_exp_f32_e32 v60, v60\n\t" \
    "v_exp_f32_e32 v61, v61\n\t" \
    "v_exp_f32_e32 v62, v62\n\t" \
    "v_exp_f32_e32 v63, v63\n\t" \
    "v_cvt_pk_bf16_f32 v108, v56, v57\n\t" \
    "v_cvt_pk_bf16_f32 v109, v58, v59\n\t" \
    "v_cvt_pk_bf16_f32 v110, v60, v61\n\t" \
    "v_cvt_pk_bf16_f32 v111, v62, v63\n\t" \
    "s_setprio 2\n\t" \
    "s_waitcnt lgkmcnt(0)\n\t" \
    "v_mfma_f32_32x32x16_bf16 v[48:63], v[80:83], v[64:67], v[16:31]\n\t" \
    "v_exp_f32_e32 v32, v32\n\t" \
    "v_exp_f32_e32 v33, v33\n\t" \
    "v_exp_f32_e32 v34, v34\n\t" \
    "v_smfmac_f32_16x16x64_bf16 v[76:79], v[72:75], v[104:111], v174\n\t" \
    "v_exp_f32_e32 v35, v35\n\t" \
    "v_exp_f32_e32 v36, v36\n\t" \
    "v_mfma_f32_32x32x16_bf16 v[48:63], v[84:87], v[68:71], v[48:63]\n\t" \
    "ds_read_b128 v[80:83], v112 offset:16384\n\t" \
    "ds_read_b128 v[84:87], v112 offset:17408\n\t" \
    "v_cvt_pk_bf16_f32 v104, v32, v33\n\t" \
    "v_cvt_pk_bf16_f32 v105, v34, v35\n\t" \
    "v_exp_f32_e32 v37, v37\n\t" \
    "v_exp_f32_e32 v38, v38\n\t" \
    "v_exp_f32_e32 v39, v39\n\t" \
    "v_mfma_f32_32x32x16_bf16 v[0:15], v[92:95], v[108:111], v[0:15]\n\t" \
    "ds_read_b128 v[100:103], v112 offset:11264\n\t" \
    "ds_read_b128 v[96:99], v112 offset:14336\n\t" \
    "v_exp_f32_e32 v40, v40\n\t" \
    "v_cvt_pk_bf16_f32 v106, v36, v37\n\t" \
    "v_cvt_pk_bf16_f32 v107, v38, v39\n\t" \
    "v_exp_f32_e32 v41, v41\n\t" \
    "v_exp_f32_e32 v42, v42\n\t" \
    "v_exp_f32_e32 v43, v43\n\t" \
    "v_mfma_f32_32x32x16_bf16 v[0:15], v[88:91], v[104:107], v[0:15]\n\t" \
    "v_exp_f32_e32 v44, v44\n\t" \
    "v_exp_f32_e32 v45, v45\n\t" \
    "v_exp_f32_e32 v46, v46\n\t" \
    "v_exp_f32_e32 v47, v47\n\t" \
    "v_cvt_pk_bf16_f32 v108, v40, v41\n\t" \
    "v_cvt_pk_bf16_f32 v109, v42, v43\n\t" \
    "v_cvt_pk_bf16_f32 v110, v44, v45\n\t" \
    "v_cvt_pk_bf16_f32 v111, v46, v47\n\t" \
    "s_waitcnt lgkmcnt(0)\n\t" \
    "v_mfma_f32_32x32x16_bf16 v[32:47], v[80:83], v[64:67], v[16:31]\n\t" \
    "v_exp_f32_e32 v48, v48\n\t" \
    "v_exp_f32_e32 v49, v49\n\t" \
    "v_exp_f32_e32 v50, v50\n\t" \
    "v_smfmac_f32_16x16x64_bf16 v[76:79], v[72:75], v[104:111], v174\n\t" \
    "v_exp_f32_e32 v51, v51\n\t" \
    "v_exp_f32_e32 v52, v52\n\t" \
    "v_mfma_f32_32x32x16_bf16 v[32:47], v[84:87], v[68:71], v[32:47]\n\t" \
    "ds_read_b128 v[80:83], v112 offset:20480\n\t" \
    "ds_read_b128 v[84:87], v112 offset:21504\n\t" \
    "v_cvt_pk_bf16_f32 v104, v48, v49\n\t" \
    "v_cvt_pk_bf16_f32 v105, v50, v51\n\t" \
    "v_exp_f32_e32 v53, v53\n\t" \
    "v_exp_f32_e32 v54, v54\n\t" \
    "v_exp_f32_e32 v55, v55\n\t" \
    "v_mfma_f32_32x32x16_bf16 v[0:15], v[100:103], v[108:111], v[0:15]\n\t" \
    "ds_read_b128 v[92:95], v112 offset:15360\n\t" \
    "ds_read_b128 v[88:91], v112 offset:18432\n\t" \
    "v_exp_f32_e32 v56, v56\n\t" \
    "v_cvt_pk_bf16_f32 v106, v52, v53\n\t" \
    "v_cvt_pk_bf16_f32 v107, v54, v55\n\t" \
    "v_exp_f32_e32 v57, v57\n\t" \
    "v_exp_f32_e32 v58, v58\n\t" \
    "v_exp_f32_e32 v59, v59\n\t" \
    "v_mfma_f32_32x32x16_bf16 v[0:15], v[96:99], v[104:107], v[0:15]\n\t" \
    "v_exp_f32_e32 v60, v60\n\t" \
    "v_exp_f32_e32 v61, v61\n\t" \
    "v_exp_f32_e32 v62, v62\n\t" \
    "v_exp_f32_e32 v63, v63\n\t" \
    "v_cvt_pk_bf16_f32 v108, v56, v57\n\t" \
    "v_cvt_pk_bf16_f32 v109, v58, v59\n\t" \
    "v_cvt_pk_bf16_f32 v110, v60, v61\n\t" \
    "v_cvt_pk_bf16_f32 v111, v62, v63\n\t" \
    "s_setprio 0\n\t" \
    "s_waitcnt lgkmcnt(0)\n\t" \
    "s_nop 15\n\t"
#define ATT_L2 \
    "s_nop 1\n\t" \
    "s_setprio 1\n\t" \
    "s_waitcnt lgkmcnt(0)\n\t" \
    "v_mfma_f32_32x32x16_bf16 v[48:63], v[80:83], v[64:67], v[16:31]\n\t" \
    "v_exp_f32_e32 v32, v32\n\t" \
    "v_exp_f32_e32 v33, v33\n\t" \
    "v_exp_f32_e32 v34, v34\n\t" \
    "v_smfmac_f32_16x16x64_bf16 v[76:79], v[72:75], v[104:111], v174\n\t" \
    "v_exp_f32_e32 v35, v35\n\t" \
    "v_exp_f32_e32 v36, v36\n\t" \
    "v_mfma_f32_32x32x16_bf16 v[48:63], v[84:87], v[68:71], v[48:63]\n\t" \
    "ds_read_b128 v[80:83], v112 offset:24576\n\t" \
    "ds_read_b128 v[84:87], v112 offset:25600\n\t" \
    "v_cvt_pk_bf16_f32 v104, v32, v33\n\t" \
    "v_cvt_pk_bf16_f32 v105, v34, v35\n\t" \
    "v_exp_f32_e32 v37, v37\n\t" \
    "v_exp_f32_e32 v38, v38\n\t" \
    "v_exp_f32_e32 v39, v39\n\t" \
    "v_mfma_f32_32x32x16_bf16 v[0:15], v[92:95], v[108:111], v[0:15]\n\t" \
    "ds_read_b128 v[100:103], v112 offset:19456\n\t" \
    "ds_read_b128 v[96:99], v112 offset:22528\n\t" \
    "v_exp_f32_e32 v40, v40\n\t" \
    "v_cvt_pk_bf16_f32 v106, v36, v37\n\t" \
    "v_cvt_pk_bf16_f32 v107, v38, v39\n\t" \
    "v_exp_f32_e32 v41, v41\n\t" \
    "v_exp_f32_e32 v42, v42\n\t" \
    "v_exp_f32_e32 v43, v43\n\t" \
    "v_mfma_f32_32x32x16_bf16 v[0:15], v[88:91], v[104:107], v[0:15]\n\t" \
    "v_exp_f32_e32 v44, v44\n\t" \
    "v_exp_f32_e32 v45, v45\n\t" \
    "v_exp_f32_e32 v46, v46\n\t" \
    "v_exp_f32_e32 v47, v47\n\t" \
    "v_cvt_pk_bf16_f32 v108, v40, v41\n\t" \
    "v_cvt_pk_bf16_f32 v109, v42, v43\n\t" \
    "v_cvt_pk_bf16_f32 v110, v44, v45\n\t" \
    "v_cvt_pk_bf16_f32 v111, v46, v47\n\t" \
    "s_waitcnt lgkmcnt(0)\n\t" \
    "v_mfma_f32_32x32x16_bf16 v[32:47], v[80:83], v[64:67], v[16:31]\n\t" \
    "v_exp_f32_e32 v48, v48\n\t" \
    "v_exp_f32_e32 v49, v49\n\t" \
    "v_exp_f32_e32 v50, v50\n\t" \
    "v_smfmac_f32_16x16x64_bf16 v[76:79], v[72:75], v[104:111], v174\n\t" \
    "v_exp_f32_e32 v51, v51\n\t" \
    "v_exp_f32_e32 v52, v52\n\t" \
    "v_mfma_f32_32x32x16_bf16 v[32:47], v[84:87], v[68:71], v[32:47]\n\t" \
    "ds_read_b128 v[80:83], v112 offset:28672\n\t" \
    "ds_read_b128 v[84:87], v112 offset:29696\n\t" \
    "v_cvt_pk_bf16_f32 v104, v48, v49\n\t" \
    "v_cvt_pk_bf16_f32 v105, v50, v51\n\t" \
    "v_exp_f32_e32 v53, v53\n\t" \
    "v_exp_f32_e32 v54, v54\n\t" \
    "v_exp_f32_e32 v55, v55\n\t" \
    "v_mfma_f32_32x32x16_bf16 v[0:15], v[100:103], v[108:111], v[0:15]\n\t" \
    "ds_read_b128 v[92:95], v112 offset:23552\n\t" \
    "ds_read_b128 v[88:91], v112 offset:26624\n\t" \
    "v_exp_f32_e32 v56, v56\n\t" \
    "v_cvt_pk_bf16_f32 v106, v52, v53\n\t" \
    "v_cvt_pk_bf16_f32 v107, v54, v55\n\t" \
    "v_exp_f32_e32 v57, v57\n\t" \
    "v_exp_f32_e32 v58, v58\n\t" \
    "v_exp_f32_e32 v59, v59\n\t" \
    "v_mfma_f32_32x32x16_bf16 v[0:15], v[96:99], v[104:107], v[0:15]\n\t" \
    "v_exp_f32_e32 v60, v60\n\t" \
    "v_exp_f32_e32 v61, v61\n\t" \
    "v_exp_f32_e32 v62, v62\n\t" \
    "v_exp_f32_e32 v63, v63\n\t" \
    "v_cvt_pk_bf16_f32 v108, v56, v57\n\t" \
    "v_cvt_pk_bf16_f32 v109, v58, v59\n\t" \
    "v_cvt_pk_bf16_f32 v110, v60, v61\n\t" \
    "v_cvt_pk_bf16_f32 v111, v62, v63\n\t" \
    "s_setprio 0\n\t" \
    "s_waitcnt lgkmcnt(0)\n\t" \
    "s_nop 15\n\t"
#define ATT_XT \
    "s_nop 1\n\t" \
    "s_waitcnt lgkmcnt(0)\n\t" \
    "v_mfma_f32_32x32x16_bf16 v[48:63], v[80:83], v[64:67], v[16:31]\n\t" \
    "v_exp_f32_e32 v32, v32\n\t" \
    "v_exp_f32_e32 v33, v33\n\t" \
    "v_exp_f32_e32 v34, v34\n\t" \
    "v_smfmac_f32_16x16x64_bf16 v[76:79], v[72:75], v[104:111], v174\n\t" \
    "v_exp_f32_e32 v35, v35\n\t" \
    "v_exp_f32_e32 v36, v36\n\t" \
    "v_mfma_f32_32x32x16_bf16 v[48:63], v[84:87], v[68:71], v[48:63]\n\t" \
    "v_cvt_pk_bf16_f32 v104, v32, v33\n\t" \
    "v_cvt_pk_bf16_f32 v105, v34, v35\n\t" \
    "v_exp_f32_e32 v37, v37\n\t" \
    "v_exp_f32_e32 v38, v38\n\t" \
    "v_exp_f32_e32 v39, v39\n\t" \
    "v_mfma_f32_32x32x16_bf16 v[0:15], v[92:95], v[108:111], v[0:15]\n\t" \
    "ds_read_b128 v[100:103], v112 offset:27648\n\t" \
    "ds_read_b128 v[96:99], v112 offset:30720\n\t" \
    "v_exp_f32_e32 v40, v40\n\t" \
    "v_cvt_pk_bf16_f32 v106, v36, v37\n\t" \
    "v_cvt_pk_bf16_f32 v107, v38, v39\n\t" \
    "v_exp_f32_e32 v41, v41\n\t" \
    "v_exp_f32_e32 v42, v42\n\t" \
    "v_exp_f32_e32 v43, v43\n\t" \
    "v_mfma_f32_32x32x16_bf16 v[0:15], v[88:91], v[104:107], v[0:15]\n\t" \
    "v_exp_f32_e32 v44, v44\n\t" \
    "v_exp_f32_e32 v45, v45\n\t" \
    "v_exp_f32_e32 v46, v46\n\t" \
    "v_exp_f32_e32 v47, v47\n\t" \
    "v_cvt_pk_bf16_f32 v108, v40, v41\n\t" \
    "v_cvt_pk_bf16_f32 v109, v42, v43\n\t" \
    "v_cvt_pk_bf16_f32 v110, v44, v45\n\t" \
    "v_cvt_pk_bf16_f32 v111, v46, v47\n\t" \
    "s_waitcnt lgkmcnt(0)\n\t" \
    "v_exp_f32_e32 v48, v48\n\t" \
    "v_exp_f32_e32 v49, v49\n\t" \
    "v_exp_f32_e32 v50, v50\n\t" \
    "v_smfmac_f32_16x16x64_bf16 v[76:79], v[72:75], v[104:111], v174\n\t" \
    "v_exp_f32_e32 v51, v51\n\t" \
    "v_exp_f32_e32 v52, v52\n\t" \
    "v_cvt_pk_bf16_f32 v104, v48, v49\n\t" \
    "v_cvt_pk_bf16_f32 v105, v50, v51\n\t" \
    "v_exp_f32_e32 v53, v53\n\t" \
    "v_exp_f32_e32 v54, v54\n\t" \
    "v_exp_f32_e32 v55, v55\n\t" \
    "v_mfma_f32_32x32x16_bf16 v[0:15], v[100:103], v[108:111], v[0:15]\n\t" \
    "ds_read_b128 v[92:95], v112 offset:31744\n\t" \
    "v_exp_f32_e32 v56, v56\n\t" \
    "v_cvt_pk_bf16_f32 v106, v52, v53\n\t" \
    "v_cvt_pk_bf16_f32 v107, v54, v55\n\t" \
    "v_exp_f32_e32 v57, v57\n\t" \
    "v_exp_f32_e32 v58, v58\n\t" \
    "v_exp_f32_e32 v59, v59\n\t" \
    "v_mfma_f32_32x32x16_bf16 v[0:15], v[96:99], v[104:107], v[0:15]\n\t" \
    "v_exp_f32_e32 v60, v60\n\t" \
    "v_exp_f32_e32 v61, v61\n\t" \
    "v_exp_f32_e32 v62, v62\n\t" \
    "v_exp_f32_e32 v63, v63\n\t" \
    "v_cvt_pk_bf16_f32 v108, v56, v57\n\t" \
    "v_cvt_pk_bf16_f32 v109, v58, v59\n\t" \
    "v_cvt_pk_bf16_f32 v110, v60, v61\n\t" \
    "v_cvt_pk_bf16_f32 v111, v62, v63\n\t" \
    "s_waitcnt lgkmcnt(0)\n\t" \
    "s_nop 0\n\t" \
    "v_mfma_f32_32x32x16_bf16 v[0:15], v[92:95], v[108:111], v[0:15]\n\t" \
    "v_smfmac_f32_16x16x64_bf16 v[76:79], v[72:75], v[104:111], v174\n\t" \
    "s_nop 15\n\t"
#define ATT_MAIN \
    "s_mov_b64 s[48:49], %[tab]\n\t" \
    "s_mov_b32 s46, %[k0]\n\t" \
    "s_and_b32 s40, s48, 63\n\t" \
    "s_lshl_b32 s40, s40, 12\n\t" \
    "s_add_u32 s44, %[srclo], s40\n\t" \
    "s_addc_u32 s45, %[srchi], 0\n\t" \
    "s_lshr_b64 s[48:49], s[48:49], 6\n\t" \
    "s_add_u32 s46, s46, 1\n\t" \
    "s_mov_b64 vcc, -1\n\t" \
    "s_waitcnt vmcnt(0)\n\t" \
    "ds_read_b64 v[120:121], v173\n\t" \
    "s_waitcnt lgkmcnt(0)\n\t" \
    "1:\n" \
    "s_cmp_eq_u32 %[cnt], 4\n\t" \
    "s_cbranch_scc1 5f\n\t" \
    "s_waitcnt vmcnt(0)\n\t" \
    "5:\n" \
    "s_waitcnt vmcnt(9)\n\t" \
    "s_barrier\n\t" \
    "s_bitcmp1_b32 %[act], %[cnt]\n\t" \
    "s_cbranch_scc1 30f\n\t" \
    "2:\n" \
    "s_setprio 3\n\t" \
    "s_waitcnt lgkmcnt(0)\n\t" \
    "v_mfma_f32_32x32x16_bf16 v[48:63], v[80:83], v[64:67], v[16:31]\n\t" \
    "v_exp_f32_e32 v32, v32\n\t" \
    "v_exp_f32_e32 v33, v33\n\t" \
    "v_exp_f32_e32 v34, v34\n\t" \
    "v_smfmac_f32_16x16x64_bf16 v[76:79], v[72:75], v[104:111], v174\n\t" \
    "v_exp_f32_e32 v35, v35\n\t" \
    "v_exp_f32_e32 v36, v36\n\t" \
    "v_mfma_f32_32x32x16_bf16 v[48:63], v[84:87], v[68:71], v[48:63]\n\t" \
    "ds_read_b128 v[80:83], v113 offset:0\n\t" \
    "ds_read_b128 v[84:87], v113 offset:1024\n\t" \
    "v_cvt_pk_bf16_f32 v104, v32, v33\n\t" \
    "v_cvt_pk_bf16_f32 v105, v34, v35\n\t" \
    "v_exp_f32_e32 v37, v37\n\t" \
    "v_exp_f32_e32 v38, v38\n\t" \
    "v_exp_f32_e32 v39, v39\n\t" \
    "v_mfma_f32_32x32x16_bf16 v[0:15], v[92:95], v[108:111], v[0:15]\n\t" \
    "ds_read_b128 v[100:103], v112 offset:27648\n\t" \
    "ds_read_b128 v[96:99], v112 offset:30720\n\t" \
    "v_exp_f32_e32 v40, v40\n\t" \
    "v_cvt_pk_bf16_f32 v106, v36, v37\n\t" \
    "v_cvt_pk_bf16_f32 v107, v38, v39\n\t" \
    "v_exp_f32_e32 v41, v41\n\t" \
    "v_exp_f32_e32 v42, v42\n\t" \
    "v_exp_f32_e32 v43, v43\n\t" \
    "v_mfma_f32_32x32x16_bf16 v[0:15], v[88:91], v[104:107], v[0:15]\n\t" \
    "s_cbranch_vccz 20f\n\t" \
    "v_cmp_eq_u64_e64 s[42:43], v[120:121], v[140:141]\n\t" \
    "s_mov_b32 m0, %[dst]\n\t" \
    "s_nop 3\n\t" \
    "s_bitcmp1_b64 s[42:43], s46\n\t" \
    "s_cbranch_scc0 9f\n\t" \
    "12:\n" \
    "global_load_lds_dwordx4 v114, s[44:45] offset:0 sc1\n\t" \
    "20:\n" \
    "v_exp_f32_e32 v44, v44\n\t" \
    "v_exp_f32_e32 v45, v45\n\t" \
    "v_exp_f32_e32 v46, v46\n\t" \
    "v_exp_f32_e32 v47, v47\n\t" \
    "v_cvt_pk_bf16_f32 v108, v40, v41\n\t" \
    "v_cvt_pk_bf16_f32 v109, v42, v43\n\t" \
    "v_cvt_pk_bf16_f32 v110, v44, v45\n\t" \
    "v_cvt_pk_bf16_f32 v111, v46, v47\n\t" \
    "s_waitcnt lgkmcnt(0)\n\t" \
    "v_mfma_f32_32x32x16_bf16 v[32:47], v[80:83], v[64:67], v[16:31]\n\t" \
    "v_exp_f32_e32 v48, v48\n\t" \
    "v_exp_f32_e32 v49, v49\n\t" \
    "v_exp_f32_e32 v50, v50\n\t" \
    "v_smfmac_f32_16x16x64_bf16 v[76:79], v[72:75], v[104:111], v174\n\t" \
    "v_exp_f32_e32 v51, v51\n\t" \
    "v_exp_f32_e32 v52, v52\n\t" \
    "v_mfma_f32_32x32x16_bf16 v[32:47], v[84:87], v[68:71], v[32:47]\n\t" \
    "ds_read_b128 v[80:83], v113 offset:4096\n\t" \
    "ds_read_b128 v[84:87], v113 offset:5120\n\t" \
    "v_cvt_pk_bf16_f32 v104, v48, v49\n\t" \
    "v_cvt_pk_bf16_f32 v105, v50, v51\n\t" \
    "v_exp_f32_e32 v53, v53\n\t" \
    "v_exp_f32_e32 v54, v54\n\t" \
    "v_exp_f32_e32 v55, v55\n\t" \
    "v_mfma_f32_32x32x16_bf16 v[0:15], v[100:103], v[108:111], v[0:15]\n\t" \
    "ds_read_b128 v[92:95], v112 offset:31744\n\t" \
    "ds_read_b128 v[88:91], v113 offset:2048\n\t" \
    "v_exp_f32_e32 v56, v56\n\t" \
    "v_cvt_pk_bf16_f32 v106, v52, v53\n\t" \
    "v_cvt_pk_bf16_f32 v107, v54, v55\n\t" \
    "v_exp_f32_e32 v57, v57\n\t" \
    "v_exp_f32_e32 v58, v58\n\t" \
    "v_exp_f32_e32 v59, v59\n\t" \
    "v_mfma_f32_32x32x16_bf16 v[0:15], v[96:99], v[104:107], v[0:15]\n\t" \
    "s_cbranch_vccz 21f\n\t" \
    "global_load_lds_dwordx4 v114, s[44:45] offset:1024 sc1\n\t" \
    "21:\n" \
    "v_exp_f32_e32 v60, v60\n\t" \
    "v_exp_f32_e32 v61, v61\n\t" \
    "v_exp_f32_e32 v62, v62\n\t" \
    "v_exp_f32_e32 v63, v63\n\t" \
    "v_cvt_pk_bf16_f32 v108, v56, v57\n\t" \
    "v_cvt_pk_bf16_f32 v109, v58, v59\n\t" \
    "v_cvt_pk_bf16_f32 v110, v60, v61\n\t" \
    "v_cvt_pk_bf16_f32 v111, v62, v63\n\t" \
    "s_setprio 2\n\t" \
    "s_waitcnt lgkmcnt(0)\n\t" \
    "v_mfma_f32_32x32x16_bf16 v[48:63], v[80:83], v[64:67], v[16:31]\n\t" \
    "v_exp_f32_e32 v32, v32\n\t" \
    "v_exp_f32_e32 v33, v33\n\t" \
    "v_exp_f32_e32 v34, v34\n\t" \
    "v_smfmac_f32_16x16x64_bf16 v[76:79], v[72:75], v[104:111], v174\n\t" \
    "v_exp_f32_e32 v35, v35\n\t" \
    "v_exp_f32_e32 v36, v36\n\t" \
    "v_mfma_f32_32x32x16_bf16 v[48:63], v[84:87], v[68:71], v[48:63]\n\t" \
    "ds_read_b128 v[80:83], v113 offset:8192\n\t" \
    "ds_read_b128 v[84:87], v113 offset:9216\n\t" \
    "v_cvt_pk_bf16_f32 v104, v32, v33\n\t" \
    "v_cvt_pk_bf16_f32 v105, v34, v35\n\t" \
    "v_exp_f32_e32 v37, v37\n\t" \
    "v_exp_f32_e32 v38, v38\n\t" \
    "v_exp_f32_e32 v39, v39\n\t" \
    "v_mfma_f32_32x32x16_bf16 v[0:15], v[92:95], v[108:111], v[0:15]\n\t" \
    "ds_read_b128 v[100:103], v113 offset:3072\n\t" \
    "ds_read_b128 v[96:99], v113 offset:6144\n\t" \
    "v_exp_f32_e32 v40, v40\n\t" \
    "v_cvt_pk_bf16_f32 v106, v36, v37\n\t" \
    "v_cvt_pk_bf16_f32 v107, v38, v39\n\t" \
    "v_exp_f32_e32 v41, v41\n\t" \
    "v_exp_f32_e32 v42, v42\n\t" \
    "v_exp_f32_e32 v43, v43\n\t" \
    "v_mfma_f32_32x32x16_bf16 v[0:15], v[88:91], v[104:107], v[0:15]\n\t" \
    "s_cbranch_vccz 22f\n\t" \
    "global_load_lds_dwordx4 v114, s[44:45] offset:2048 sc1\n\t" \
    "22:\n" \
    "v_exp_f32_e32 v44, v44\n\t" \
    "v_exp_f32_e32 v45, v45\n\t" \
    "v_exp_f32_e32 v46, v46\n\t" \
    "v_exp_f32_e32 v47, v47\n\t" \
    "v_cvt_pk_bf16_f32 v108, v40, v41\n\t" \
    "v_cvt_pk_bf16_f32 v109, v42, v43\n\t" \
    "v_cvt_pk_bf16_f32 v110, v44, v45\n\t" \
    "v_cvt_pk_bf16_f32 v111, v46, v47\n\t" \
    "s_waitcnt lgkmcnt(0)\n\t" \
    "v_mfma_f32_32x32x16_bf16 v[32:47], v[80:83], v[64:67], v[16:31]\n\t" \
    "v_exp_f32_e32 v48, v48\n\t" \
    "v_exp_f32_e32 v49, v49\n\t" \
    "v_exp_f32_e32 v50, v50\n\t" \
    "v_smfmac_f32_16x16x64_bf16 v[76:79], v[72:75], v[104:111], v174\n\t" \
    "v_exp_f32_e32 v51, v51\n\t" \
    "v_exp_f32_e32 v52, v52\n\t" \
    "v_mfma_f32_32x32x16_bf16 v[32:47], v[84:87], v[68:71], v[32:47]\n\t" \
    "ds_read_b128 v[80:83], v113 offset:12288\n\t" \
    "ds_read_b128 v[84:87], v113 offset:13312\n\t" \
    "v_cvt_pk_bf16_f32 v104, v48, v49\n\t" \
    "v_cvt_pk_bf16_f32 v105, v50, v51\n\t" \
    "v_exp_f32_e32 v53, v53\n\t" \
    "v_exp_f32_e32 v54, v54\n\t" \
    "v_exp_f32_e32 v55, v55\n\t" \
    "v_mfma_f32_32x32x16_bf16 v[0:15], v[100:103], v[108:111], v[0:15]\n\t" \
    "ds_read_b128 v[92:95], v113 offset:7168\n\t" \
    "ds_read_b128 v[88:91], v113 offset:10240\n\t" \
    "v_exp_f32_e32 v56, v56\n\t" \
    "v_cvt_pk_bf16_f32 v106, v52, v53\n\t" \
    "v_cvt_pk_bf16_f32 v107, v54, v55\n\t" \
    "v_exp_f32_e32 v57, v57\n\t" \
    "v_exp_f32_e32 v58, v58\n\t" \
    "v_exp_f32_e32 v59, v59\n\t" \
    "v_mfma_f32_32x32x16_bf16 v[0:15], v[96:99], v[104:107], v[0:15]\n\t" \
    "s_cbranch_vccz 23f\n\t" \
    "global_load_lds_dwordx4 v114, s[44:45] offset:3072 sc1\n\t" \
    "global_load_dwordx2 v[120:121], v170, %[ws] sc1\n\t" \
    "23:\n" \
    "v_exp_f32_e32 v60, v60\n\t" \
    "v_exp_f32_e32 v61, v61\n\t" \
    "v_exp_f32_e32 v62, v62\n\t" \
    "v_exp_f32_e32 v63, v63\n\t" \
    "v_cvt_pk_bf16_f32 v108, v56, v57\n\t" \
    "v_cvt_pk_bf16_f32 v109, v58, v59\n\t" \
    "v_cvt_pk_bf16_f32 v110, v60, v61\n\t" \
    "v_cvt_pk_bf16_f32 v111, v62, v63\n\t" \
    "s_setprio 1\n\t" \
    "s_waitcnt lgkmcnt(0)\n\t" \
    "v_mfma_f32_32x32x16_bf16 v[48:63], v[80:83], v[64:67], v[16:31]\n\t" \
    "v_exp_f32_e32 v32, v32\n\t" \
    "v_exp_f32_e32 v33, v33\n\t" \
    "v_exp_f32_e32 v34, v34\n\t" \
    "v_smfmac_f32_16x16x64_bf16 v[76:79], v[72:75], v[104:111], v174\n\t" \
    "v_exp_f32_e32 v35, v35\n\t" \
    "v_exp_f32_e32 v36, v36\n\t" \
    "v_mfma_f32_32x32x16_bf16 v[48:63], v[84:87], v[68:71], v[48:63]\n\t" \
    "ds_read_b128 v[80:83], v113 offset:16384\n\t" \
    "ds_read_b128 v[84:87], v113 offset:17408\n\t" \
    "v_cvt_pk_bf16_f32 v104, v32, v33\n\t" \
    "v_cvt_pk_bf16_f32 v105, v34, v35\n\t" \
    "v_exp_f32_e32 v37, v37\n\t" \
    "v_exp_f32_e32 v38, v38\n\t" \
    "v_exp_f32_e32 v39, v39\n\t" \
    "v_mfma_f32_32x32x16_bf16 v[0:15], v[92:95], v[108:111], v[0:15]\n\t" \
    "ds_read_b128 v[100:103], v113 offset:11264\n\t" \
    "ds_read_b128 v[96:99], v113 offset:14336\n\t" \
    "v_exp_f32_e32 v40, v40\n\t" \
    "v_cvt_pk_bf16_f32 v106, v36, v37\n\t" \
    "v_cvt_pk_bf16_f32 v107, v38, v39\n\t" \
    "v_exp_f32_e32 v41, v41\n\t" \
    "v_exp_f32_e32 v42, v42\n\t" \
    "v_exp_f32_e32 v43, v43\n\t" \
    "v_mfma_f32_32x32x16_bf16 v[0:15], v[88:91], v[104:107], v[0:15]\n\t" \
    "s_add_u32 %[dst], %[dst], 0x8000\n\t" \
    "s_cmp_eq_u32 %[dst], %[dend]\n\t" \
    "s_cselect_b32 %[dst], %[dbeg], %[dst]\n\t" \
    "s_add_u32 %[nx], %[nx], 0x8000\n\t" \
    "s_cmp_eq_u32 %[nx], 0x18000\n\t" \
    "s_cselect_b32 %[nx], 0, %[nx]\n\t" \
    "s_bitcmp1_b32 %[hk], %[cnt]\n\t" \
    "s_cbranch_scc1 40f\n\t" \
    "24:\n" \
    "v_exp_f32_e32 v44, v44\n\t" \
    "v_exp_f32_e32 v45, v45\n\t" \
    "v_exp_f32_e32 v46, v46\n\t" \
    "v_exp_f32_e32 v47, v47\n\t" \
    "v_cvt_pk_bf16_f32 v108, v40, v41\n\t" \
    "v_cvt_pk_bf16_f32 v109, v42, v43\n\t" \
    "v_cvt_pk_bf16_f32 v110, v44, v45\n\t" \
    "v_cvt_pk_bf16_f32 v111, v46, v47\n\t" \
    "s_waitcnt lgkmcnt(0)\n\t" \
    "v_mfma_f32_32x32x16_bf16 v[32:47], v[80:83], v[64:67], v[16:31]\n\t" \
    "v_exp_f32_e32 v48, v48\n\t" \
    "v_exp_f32_e32 v49, v49\n\t" \
    "v_exp_f32_e32 v50, v50\n\t" \
    "v_smfmac_f32_16x16x64_bf16 v[76:79], v[72:75], v[104:111], v174\n\t" \
    "v_exp_f32_e32 v51, v51\n\t" \
    "v_exp_f32_e32 v52, v52\n\t" \
    "v_mfma_f32_32x32x16_bf16 v[32:47], v[84:87], v[68:71], v[32:47]\n\t" \
    "ds_read_b128 v[80:83], v113 offset:20480\n\t" \
    "ds_read_b128 v[84:87], v113 offset:21504\n\t" \
    "v_cvt_pk_bf16_f32 v104, v48, v49\n\t" \
    "v_cvt_pk_bf16_f32 v105, v50, v51\n\t" \
    "v_exp_f32_e32 v53, v53\n\t" \
    "v_exp_f32_e32 v54, v54\n\t" \
    "v_exp_f32_e32 v55, v55\n\t" \
    "v_mfma_f32_32x32x16_bf16 v[0:15], v[100:103], v[108:111], v[0:15]\n\t" \
    "ds_read_b128 v[92:95], v113 offset:15360\n\t" \
    "ds_read_b128 v[88:91], v113 offset:18432\n\t" \
    "v_exp_f32_e32 v56, v56\n\t" \
    "v_cvt_pk_bf16_f32 v106, v52, v53\n\t" \
    "v_cvt_pk_bf16_f32 v107, v54, v55\n\t" \
    "v_exp_f32_e32 v57, v57\n\t" \
    "v_exp_f32_e32 v58, v58\n\t" \
    "v_exp_f32_e32 v59, v59\n\t" \
    "v_mfma_f32_32x32x16_bf16 v[0:15], v[96:99], v[104:107], v[0:15]\n\t" \
    "s_bitcmp1_b32 %[hk], %[cnt]\n\t" \
    "s_cbranch_scc1 41f\n\t" \
    "25:\n" \
    "v_exp_f32_e32 v60, v60\n\t" \
    "v_exp_f32_e32 v61, v61\n\t" \
    "v_exp_f32_e32 v62, v62\n\t" \
    "v_exp_f32_e32 v63, v63\n\t" \
    "v_cvt_pk_bf16_f32 v108, v56, v57\n\t" \
    "v_cvt_pk_bf16_f32 v109, v58, v59\n\t" \
    "v_cvt_pk_bf16_f32 v110, v60, v61\n\t" \
    "v_cvt_pk_bf16_f32 v111, v62, v63\n\t" \
    "s_setprio 0\n\t" \
    "s_waitcnt lgkmcnt(0)\n\t" \
    "v_mfma_f32_32x32x16_bf16 v[48:63], v[80:83], v[64:67], v[16:31]\n\t" \
    "v_exp_f32_e32 v32, v32\n\t" \
    "v_exp_f32_e32 v33, v33\n\t" \
    "v_exp_f32_e32 v34, v34\n\t" \
    "v_smfmac_f32_16x16x64_bf16 v[76:79], v[72:75], v[104:111], v174\n\t" \
    "v_exp_f32_e32 v35, v35\n\t" \
    "v_exp_f32_e32 v36, v36\n\t" \
    "v_mfma_f32_32x32x16_bf16 v[48:63], v[84:87], v[68:71], v[48:63]\n\t" \
    "ds_read_b128 v[80:83], v113 offset:24576\n\t" \
    "ds_read_b128 v[84:87], v113 offset:25600\n\t" \
    "v_cvt_pk_bf16_f32 v104, v32, v33\n\t" \
    "v_cvt_pk_bf16_f32 v105, v34, v35\n\t" \
    "v_exp_f32_e32 v37, v37\n\t" \
    "v_exp_f32_e32 v38, v38\n\t" \
    "v_exp_f32_e32 v39, v39\n\t" \
    "v_mfma_f32_32x32x16_bf16 v[0:15], v[92:95], v[108:111], v[0:15]\n\t" \
    "ds_read_b128 v[100:103], v113 offset:19456\n\t" \
    "ds_read_b128 v[96:99], v113 offset:22528\n\t" \
    "v_exp_f32_e32 v40, v40\n\t" \
    "v_cvt_pk_bf16_f32 v106, v36, v37\n\t" \
    "v_cvt_pk_bf16_f32 v107, v38, v39\n\t" \
    "v_exp_f32_e32 v41, v41\n\t" \
    "v_exp_f32_e32 v42, v42\n\t" \
    "v_exp_f32_e32 v43, v43\n\t" \
    "v_mfma_f32_32x32x16_bf16 v[0:15], v[88:91], v[104:107], v[0:15]\n\t" \
    "s_and_b32 s40, s48, 63\n\t" \
    "s_lshl_b32 s40, s40, 12\n\t" \
    "s_add_u32 s44, %[srclo], s40\n\t" \
    "s_addc_u32 s45, %[srchi], 0\n\t" \
    "s_lshr_b64 s[48:49], s[48:49], 6\n\t" \
    "s_add_u32 s46, s46, 1\n\t" \
    "s_bitcmp1_b32 %[hk], %[cnt]\n\t" \
    "s_cbranch_scc1 42f\n\t" \
    "26:\n" \
    "v_exp_f32_e32 v44, v44\n\t" \
    "v_exp_f32_e32 v45, v45\n\t" \
    "v_exp_f32_e32 v46, v46\n\t" \
    "v_exp_f32_e32 v47, v47\n\t" \
    "v_cvt_pk_bf16_f32 v108, v40, v41\n\t" \
    "v_cvt_pk_bf16_f32 v109, v42, v43\n\t" \
    "v_cvt_pk_bf16_f32 v110, v44, v45\n\t" \
    "v_cvt_pk_bf16_f32 v111, v46, v47\n\t" \
    "s_waitcnt lgkmcnt(0)\n\t" \
    "v_mfma_f32_32x32x16_bf16 v[32:47], v[80:83], v[64:67], v[16:31]\n\t" \
    "v_exp_f32_e32 v48, v48\n\t" \
    "v_exp_f32_e32 v49, v49\n\t" \
    "v_exp_f32_e32 v50, v50\n\t" \
    "v_smfmac_f32_16x16x64_bf16 v[76:79], v[72:75], v[104:111], v174\n\t" \
    "v_exp_f32_e32 v51, v51\n\t" \
    "v_exp_f32_e32 v52, v52\n\t" \
    "v_mfma_f32_32x32x16_bf16 v[32:47], v[84:87], v[68:71], v[32:47]\n\t" \
    "ds_read_b128 v[80:83], v113 offset:28672\n\t" \
    "ds_read_b128 v[84:87], v113 offset:29696\n\t" \
    "v_cvt_pk_bf16_f32 v104, v48, v49\n\t" \
    "v_cvt_pk_bf16_f32 v105, v50, v51\n\t" \
    "v_exp_f32_e32 v53, v53\n\t" \
    "v_exp_f32_e32 v54, v54\n\t" \
    "v_exp_f32_e32 v55, v55\n\t" \
    "v_mfma_f32_32x32x16_bf16 v[0:15], v[100:103], v[108:111], v[0:15]\n\t" \
    "ds_read_b128 v[92:95], v113 offset:23552\n\t" \
    "ds_read_b128 v[88:91], v113 offset:26624\n\t" \
    "v_exp_f32_e32 v56, v56\n\t" \
    "v_cvt_pk_bf16_f32 v106, v52, v53\n\t" \
    "v_cvt_pk_bf16_f32 v107, v54, v55\n\t" \
    "v_exp_f32_e32 v57, v57\n\t" \
    "v_exp_f32_e32 v58, v58\n\t" \
    "v_exp_f32_e32 v59, v59\n\t" \
    "v_mfma_f32_32x32x16_bf16 v[0:15], v[96:99], v[104:107], v[0:15]\n\t" \
    "s_bitcmp1_b32 %[hk], %[cnt]\n\t" \
    "s_cbranch_scc1 43f\n\t" \
    "27:\n" \
    "v_mov_b32_e32 v112, v113\n\t" \
    "v_add_u32_e32 v113, %[nx], v115\n\t" \
    "s_sub_u32 %[cnt], %[cnt], 1\n\t" \
    "s_cmp_lg_u32 %[cnt], 0\n\t" \
    "v_exp_f32_e32 v60, v60\n\t" \
    "v_exp_f32_e32 v61, v61\n\t" \
    "v_exp_f32_e32 v62, v62\n\t" \
    "v_exp_f32_e32 v63, v63\n\t" \
    "v_cvt_pk_bf16_f32 v108, v56, v57\n\t" \
    "v_cvt_pk_bf16_f32 v109, v58, v59\n\t" \
    "v_cvt_pk_bf16_f32 v110, v60, v61\n\t" \
    "v_cvt_pk_bf16_f32 v111, v62, v63\n\t" \
    "s_cbranch_scc1 1b\n\t" \
    "7:\n" \
    "s_setprio 0\n\t" \
    "s_waitcnt lgkmcnt(0)\n\t" \
    "v_mfma_f32_32x32x16_bf16 v[48:63], v[80:83], v[64:67], v[16:31]\n\t" \
    "v_exp_f32_e32 v32, v32\n\t" \
    "v_exp_f32_e32 v33, v33\n\t" \
    "v_exp_f32_e32 v34, v34\n\t" \
    "v_smfmac_f32_16x16x64_bf16 v[76:79], v[72:75], v[104:111], v174\n\t" \
    "v_exp_f32_e32 v35, v35\n\t" \
    "v_exp_f32_e32 v36, v36\n\t" \
    "v_mfma_f32_32x32x16_bf16 v[48:63], v[84:87], v[68:71], v[48:63]\n\t" \
    "v_cvt_pk_bf16_f32 v104, v32, v33\n\t" \
    "v_cvt_pk_bf16_f32 v105, v34, v35\n\t" \
    "v_exp_f32_e32 v37, v37\n\t" \
    "v_exp_f32_e32 v38, v38\n\t" \
    "v_exp_f32_e32 v39, v39\n\t" \
    "v_mfma_f32_32x32x16_bf16 v[0:15], v[92:95], v[108:111], v[0:15]\n\t" \
    "ds_read_b128 v[100:103], v112 offset:27648\n\t" \
    "ds_read_b128 v[96:99], v112 offset:30720\n\t" \
    "v_exp_f32_e32 v40, v40\n\t" \
    "v_cvt_pk_bf16_f32 v106, v36, v37\n\t" \
    "v_cvt_pk_bf16_f32 v107, v38, v39\n\t" \
    "v_exp_f32_e32 v41, v41\n\t" \
    "v_exp_f32_e32 v42, v42\n\t" \
    "v_exp_f32_e32 v43, v43\n\t" \
    "v_mfma_f32_32x32x16_bf16 v[0:15], v[88:91], v[104:107], v[0:15]\n\t" \
    "v_exp_f32_e32 v44, v44\n\t" \
    "v_exp_f32_e32 v45, v45\n\t" \
    "v_exp_f32_e32 v46, v46\n\t" \
    "v_exp_f32_e32 v47, v47\n\t" \
    "v_cvt_pk_bf16_f32 v108, v40, v41\n\t" \
    "v_cvt_pk_bf16_f32 v109, v42, v43\n\t" \
    "v_cvt_pk_bf16_f32 v110, v44, v45\n\t" \
    "v_cvt_pk_bf16_f32 v111, v46, v47\n\t" \
    "s_waitcnt lgkmcnt(0)\n\t" \
    "v_exp_f32_e32 v48, v48\n\t" \
    "v_exp_f32_e32 v49, v49\n\t" \
    "v_exp_f32_e32 v50, v50\n\t" \
    "v_smfmac_f32_16x16x64_bf16 v[76:79], v[72:75], v[104:111], v174\n\t" \
    "v_exp_f32_e32 v51, v51\n\t" \
    "v_exp_f32_e32 v52, v52\n\t" \
    "v_cvt_pk_bf16_f32 v104, v48, v49\n\t" \
    "v_cvt_pk_bf16_f32 v105, v50, v51\n\t" \
    "v_exp_f32_e32 v53, v53\n\t" \
    "v_exp_f32_e32 v54, v54\n\t" \
    "v_exp_f32_e32 v55, v55\n\t" \
    "v_mfma_f32_32x32x16_bf16 v[0:15], v[100:103], v[108:111], v[0:15]\n\t" \
    "ds_read_b128 v[92:95], v112 offset:31744\n\t" \
    "v_exp_f32_e32 v56, v56\n\t" \
    "v_cvt_pk_bf16_f32 v106, v52, v53\n\t" \
    "v_cvt_pk_bf16_f32 v107, v54, v55\n\t" \
    "v_exp_f32_e32 v57, v57\n\t" \
    "v_exp_f32_e32 v58, v58\n\t" \
    "v_exp_f32_e32 v59, v59\n\t" \
    "v_mfma_f32_32x32x16_bf16 v[0:15], v[96:99], v[104:107], v[0:15]\n\t" \
    "v_exp_f32_e32 v60, v60\n\t" \
    "v_exp_f32_e32 v61, v61\n\t" \
    "v_exp_f32_e32 v62, v62\n\t" \
    "v_exp_f32_e32 v63, v63\n\t" \
    "v_cvt_pk_bf16_f32 v108, v56, v57\n\t" \
    "v_cvt_pk_bf16_f32 v109, v58, v59\n\t" \
    "v_cvt_pk_bf16_f32 v110, v60, v61\n\t" \
    "v_cvt_pk_bf16_f32 v111, v62, v63\n\t" \
    "s_waitcnt lgkmcnt(0)\n\t" \
    "s_nop 0\n\t" \
    "v_mfma_f32_32x32x16_bf16 v[0:15], v[92:95], v[108:111], v[0:15]\n\t" \
    "v_smfmac_f32_16x16x64_bf16 v[76:79], v[72:75], v[104:111], v174\n\t" \
    "s_nop 15\n\t" \
    "s_branch 99f\n\t" \
    "9:\n" \
    "s_mov_b32 s47, 0x40000\n\t" \
    "10:\n" \
    "s_sub_u32 s47, s47, 1\n\t" \
    "s_cbranch_scc1 12b\n\t" \
    "global_load_dwordx2 v[120:121], v170, %[ws] sc1\n\t" \
    "s_waitcnt vmcnt(0)\n\t" \
    "v_cmp_eq_u64_e64 s[42:43], v[120:121], v[140:141]\n\t" \
    "s_nop 4\n\t" \
    "s_bitcmp1_b64 s[42:43], s46\n\t" \
    "s_cbranch_scc1 12b\n\t" \
    "s_sleep 4\n\t" \
    "s_branch 10b\n\t" \
    "50:\n" \
    "global_store_dwordx4 v171, v[176:179], %[ws] offset:-3072 sc1\n\t" \
    "global_store_dwordx4 v171, v[180:183], %[ws] offset:-2944 sc1\n\t" \
    "global_store_dwordx4 v172, v[184:187], %[ws] offset:-1024 sc1\n\t" \
    "global_store_dwordx4 v172, v[188:191], %[ws] offset:-768 sc1\n\t" \
    "s_branch 24b\n\t" \
    "51:\n" \
    "global_store_dwordx4 v171, v[192:195], %[ws] offset:1024 sc1\n\t" \
    "global_store_dwordx4 v171, v[196:199], %[ws] offset:1152 sc1\n\t" \
    "global_store_dwordx4 v172, v[200:203], %[ws] offset:3072 sc1\n\t" \
    "global_store_dwordx4 v172, v[204:207], %[ws] offset:3328 sc1\n\t" \
    "s_branch 25b\n\t" \
    "40:\n" \
    "s_cmp_eq_u32 %[cnt], 7\n\t" \
    "s_cbranch_scc1 50b\n\t" \
    "s_add_u32 m0, %[wdst], 0\n\t" \
    "s_nop 0\n\t" \
    "global_load_lds_dwordx4 v116, %[wo01]\n\t" \
    "global_load_lds_dwordx4 v117, %[wo01] offset:1024\n\t" \
    "v_add_u32_e32 v116, 0x20000, v116\n\t" \
    "v_add_u32_e32 v117, 0x20000, v117\n\t" \
    "s_branch 24b\n\t" \
    "41:\n" \
    "s_cmp_eq_u32 %[cnt], 7\n\t" \
    "s_cbranch_scc1 51b\n\t" \
    "s_add_u32 m0, %[wdst], 16384\n\t" \
    "s_nop 0\n\t" \
    "global_load_lds_dwordx4 v116, %[wo01]\n\t" \
    "global_load_lds_dwordx4 v117, %[wo01] offset:1024\n\t" \
    "v_subrev_u32_e32 v116, 0x20000, v116\n\t" \
    "v_subrev_u32_e32 v117, 0x20000, v117\n\t" \
    "s_branch 25b\n\t" \
    "42:\n" \
    "s_cmp_eq_u32 %[cnt], 7\n\t" \
    "s_cbranch_scc1 26b\n\t" \
    "s_add_u32 m0, %[wdst], 32768\n\t" \
    "s_nop 0\n\t" \
    "global_load_lds_dwordx4 v116, %[wo23]\n\t" \
    "global_load_lds_dwordx4 v117, %[wo23] offset:1024\n\t" \
    "v_add_u32_e32 v116, 0x20000, v116\n\t" \
    "v_add_u32_e32 v117, 0x20000, v117\n\t" \
    "s_branch 26b\n\t" \
    "43:\n" \
    "s_cmp_eq_u32 %[cnt], 7\n\t" \
    "s_cbranch_scc1 27b\n\t" \
    "s_add_u32 m0, %[wdst], 49152\n\t" \
    "s_nop 0\n\t" \
    "global_load_lds_dwordx4 v116, %[wo23]\n\t" \
    "global_load_lds_dwordx4 v117, %[wo23] offset:1024\n\t" \
    "global_load_dword v168, v119, %[wo]\n\t" \
    "s_branch 27b\n\t" \
    "30:\n" \
    "s_cmp_eq_u32 %[cnt], 1\n\t" \
    "s_cbranch_scc0 31f\n\t" \
    "s_mov_b64 vcc, 0\n\t" \
    "s_bitcmp1_b32 %[mode], 1\n\t" \
    "s_cbranch_scc1 7b\n\t" \
    "s_branch 2b\n\t" \
    "31:\n" \
    "s_cmp_eq_u32 %[cnt], 6\n\t" \
    "s_cbranch_scc0 33f\n\t" \
    "global_store_dwordx2 v175, v[140:141], %[ws] sc1\n\t" \
    "s_branch 2b\n\t" \
    "33:\n" \
    "s_cmp_eq_u32 %[cnt], 5\n\t" \
    "s_cbranch_scc0 34f\n\t" \
    "global_store_dwordx2 v118, v[140:141], %[ws] sc1\n\t" \
    "s_branch 2b\n\t" \
    "34:\n" \
    "s_cmp_eq_u32 %[cnt], 3\n\t" \
    "s_cbranch_scc0 35f\n\t" \
    "global_load_dwordx2 v[168:169], v142, %[ws] sc1\n\t" \
    "s_branch 2b\n\t" \
    "35:\n" \
    "v_cmp_eq_u64_e64 s[42:43], v[168:169], v[140:141]\n\t" \
    "s_nop 4\n\t" \
    "s_cmp_eq_u64 s[42:43], -1\n\t" \
    "s_cbranch_scc0 2b\n\t" \
    "global_load_dwordx4 v[122:125], v143, %[ws] offset:-2048 sc1\n\t" \
    "global_load_dwordx4 v[126:129], v143, %[ws] offset:-1024 sc1\n\t" \
    "global_load_dwordx4 v[130:133], v143, %[ws] offset:0 sc1\n\t" \
    "global_load_dwordx4 v[134:137], v143, %[ws] offset:1024 sc1\n\t" \
    "global_load_dwordx2 v[138:139], v143, %[ws] offset:2048 sc1\n\t" \
    "s_bitset1_b32 %[mode], 2\n\t" \
    "s_branch 2b\n\t" \
    "99:\n"
#define ATT_TMP "v48", "v49", "v50", "v51", "v52", "v53", "v54", "v55", "v56", "v57", "v58", "v59", "v60", "v61", "v62", "v63", "v96", "v97", "v98", "v99", "v100", "v101", "v102", "v103"

constexpr int K1_X = 0, K1_W = 81920, K1_WB = 24576;
constexpr int K2_SLOT = 32768, K2_QX = 3 * K2_SLOT, K2_FL = 147456;
constexpr int LDS_BYTES = 163840;
constexpr int p3_wslot(int t) { return (t % 6) < 4 ? 98304 + (t % 6) * 16384 : 65536 + ((t % 6) - 4) * 16384; }
constexpr int p3_allow(int t) { return (((t < 2 || t - 2 >= 1) && t + 3 < 16) ? 4 : 0) + (t + 3 < 16 ? 4 : 0) + ((t >= 1 && t + 5 < 16) ? 4 : 0); }

__global__ __launch_bounds__(512, 2) void mk_fused(const u16* __restrict__ xA, const u16* __restrict__ wA, const u16* __restrict__ woA,
                                                   const u16* __restrict__ xB, const u16* __restrict__ wB, const u16* __restrict__ woB, char* ws0, u16* __restrict__ out) {
    char* ws = ws0;
    __shared__ __attribute__((aligned(1024))) char smem[LDS_BYTES];
    const unsigned lds0 = (unsigned)(uintptr_t)(LAS char*)smem;
    const int wid = __builtin_amdgcn_readfirstlane(threadIdx.x >> 6);
    int lane = threadIdx.x & 63;
#define BRX 1
    const int b = blockIdx.x, xcd = b & 7, j = b >> 3, br = (xcd >> 2) ^ BRX, qd = xcd & 3;
    const int mt = (qd & 1) * 4 + (j & 3), hp = (qd >> 1) * 8 + (j >> 2);
    const int mt3 = qd * 4 + (j & 3), nt3 = j >> 2;
    u64 tag;
    u32x4 fq[2][2], fk[2][2], fv[2][2];
    f32x16 band;
    STAMP_AT(0, 0);
    {
        const int g = wid >> 2, wi = wid & 3, mw = wi >> 1, hd = wi & 1;
        if (wid == 7) {
            const char* pc; unsigned len;
            asm volatile("s_getpc_b64 %0\n.Lmk_code_begin:\n\ts_mov_b32 %1, .Lmk_code_end-.Lmk_code_begin" : "=s"(pc), "=s"(len));
            const unsigned off = (unsigned)(j * 64 + lane) * 64u;
            asm volatile("s_mov_b32 m0, %2\n\ts_nop 0\n\tglobal_load_lds_dword %0, %1" :: "v"(off < len ? off : 0u), "s"(pc), "s"(lds0 + 159744) : "memory");
        }
        unsigned vx[4], vw;
        { const unsigned r8 = lane >> 3, c8 = lane & 7, b1 = (lane >> 4) & 1;
          vw = (wid * 8 + r8) * 6144 + ((c8 ^ ((b1 | ((wid & 1) << 1)) << 1)) << 4);
          const unsigned st = (16384u << br) - 1024u;
          vx[0] = (((r8 << br) + (br ? (wi >> 1) * 64 + (wi & 1) : wi * 32)) << 11) + ((c8 ^ (b1 | ((lane >> 5) << 2))) << 4);
          vx[1] = vx[0] + st; vx[2] = (vx[0] ^ 32) + 2 * st; vx[3] = vx[2] + st; }
        asm volatile("" : "+v"(vx[0]), "+v"(vx[1]), "+v"(vx[2]), "+v"(vx[3]), "+v"(vw));
        SBAR();
        const char* x = (const char*)(br ? xB : xA) + (size_t)mt * 256 * 2048;
        const char* w = (const char*)(br ? wB : wA) + hp * 128;
        const unsigned xdst_o = lds0 + K1_X + wi * 4096;
        const unsigned wdst = lds0 + K1_W + wid * 1024;
        const char* xo = x + (size_t)g * 128 * 2048;
#define ROTM 4
        const int kt0 = ROTM * (j & 3);
#define STAGE_X(kt, dst) do { const char* s_ = xo + (((kt) + kt0) & 15) * 128; glds_run<0>(s_, vx[0], dst); glds_run<1>(s_, vx[1], 0); glds_run<2>(s_, vx[2], 0); glds_run<3>(s_, vx[3], 0); } while (0)
#define STAGE_W(kt, buf) do { const char* s_ = w + (size_t)(((kt) + kt0) & 15) * 64 * 6144; glds_sec<0>(s_, vw, wdst + (buf) * K1_WB); glds_sec<1>(s_, vw, wdst + (buf) * K1_WB); glds_sec<2>(s_, vw, wdst + (buf) * K1_WB); } while (0)
        JIT(14);
        STAGE_W(0, 0); STAGE_X(0, xdst_o + g * 16384); STAGE_W(1, 1); STAGE_X(1, xdst_o + (2 + g) * 16384);
        { u64 e; asm volatile("s_load_dwordx2 %0, %1, 0x0\n\ts_waitcnt lgkmcnt(0)" : "=s"(e) : "s"(ws + WS_EP) : "memory");
          e += 0x9E3779B97F4A7C15ull; e = (e ^ (e >> 30)) * 0xBF58476D1CE4E5B9ull; e = (e ^ (e >> 27)) * 0x94D049BB133111EBull; tag = e ^ (e >> 31); }
        const int c16 = lane & 15, g4 = lane >> 4;
        unsigned ax[2], aw[2];
        ax[0] = lds0 + K1_X + g * 16384 + (mw * 64 + 16 * (c16 >> 3) + (c16 & 7)) * 128 + ((g4 ^ (((c16 >> 1) & 1) | ((c16 >> 3) << 1) | (((c16 >> 2) & 1) << 2))) << 4);
        ax[1] = ax[0] ^ 64;
        { const int q4 = c16 >> 2, p = lane & 3;
          aw[0] = lds0 + K1_W + (8 * g4 + q4) * 128 + (((hd * 4 + (p >> 1)) ^ ((((q4 >> 1) & 1) | ((g4 & 1) << 1)) << 1)) << 4) + (p & 1) * 8; aw[1] = aw[0] ^ 32; }
        band = band_mask(lane); asm volatile("" : "+v"(band));
        if (g == 1) WAITBAR_(11); else WAITBAR_(7);
        SA_(1);
        if (g == 1) WAITBAR_(7);
        f32x4 acc[4][6];
#pragma unroll
        for (int m = 0; m < 4; ++m)
#pragma unroll
            for (int n = 0; n < 6; ++n) acc[m][n] = f32x4{};
        SBAR();
        unsigned wr_ = 0, xr_ = g * 16384, dw = wdst + 2 * K1_WB, dx = xdst_o + (g ? 0 : 4 * 16384);
        int tk = (2 + kt0) & 15;
        const char* sx = xo + tk * 128; const char* sw = w + (size_t)tk * 64 * 6144;
#define RDX(m) do { xf[m][0] = ldsrd128(ax[0] + ((m) >> 1) * 4096 + ((m) & 1) * 1024); xf[m][1] = ldsrd128(ax[1] + ((m) >> 1) * 4096 + ((m) & 1) * 1024); } while (0)
#define RDW(n, ks) wf[n][ks] = cat8(ldstr(aw[(n) & 1] + ((n) >> 1) * 8192 + (ks) * 4096), ldstr(aw[(n) & 1] + ((n) >> 1) * 8192 + (ks) * 4096 + 512))
#define MM(m, n, k) do { if ((n) < 4) acc[m][n] = MFMA16(wf[n][k], xf[m][k], acc[m][n]); else acc[m][n] = MFMA16(xf[m][k], wf[n][k], acc[m][n]); } while (0)
#define P1_MMA() _Pragma("unroll") for (int m = 0; m < 4; ++m) _Pragma("unroll") for (int n = 0; n < 6; ++n) { MM(m, n, 0); MM(m, n, 1); CHAIN(); }
#define P1_TILE(DO, WAIT1, WAIT2) do { \
            bf16x8 xf[4][2], wf[6][2]; \
            RDX(0); RDW(0, 0); if (DO) glds_sec<0>(sw, vw, dw); \
            RDW(0, 1); RDW(1, 0); if (DO) glds_sec<1>(sw, vw, dw); \
            RDX(1); RDW(1, 1); if (DO) glds_sec<2>(sw, vw, dw); \
            RDW(2, 0); RDW(2, 1); if (DO) glds_run<0>(sx, vx[0], dx); \
            RDX(2); RDW(3, 0); if (DO) glds_run<1>(sx, vx[1], 0); \
            RDW(3, 1); RDW(4, 0); if (DO) glds_run<2>(sx, vx[2], 0); \
            RDX(3); RDW(4, 1); if (DO) glds_run<3>(sx, vx[3], 0); \
            RDW(5, 0); RDW(5, 1); \
            SBAR(); \
            {   \
              const unsigned d_ = (wr_ == 2 * K1_WB) ? (unsigned)(-2 * K1_WB) : (unsigned)K1_WB, e_ = (xr_ >= 3 * 16384) ? (unsigned)(-3 * 16384) : (unsigned)(2 * 16384); \
              dw = wdst + wr_; wr_ += d_; aw[0] += d_; aw[1] += d_; dx = xdst_o + ((xr_ == 4 * 16384) ? 0u : xr_ + 16384); xr_ += e_; ax[0] += e_; ax[1] += e_; \
              tk = (tk + 1) & 15; sx = xo + tk * 128; sw = w + (size_t)tk * 64 * 6144; \
              asm volatile("" : "+v"(aw[0]), "+v"(aw[1]), "+v"(ax[0]), "+v"(ax[1]), "+s"(sx), "+s"(sw)); } \
            WAIT1; \
            SBAR(); \
            __builtin_amdgcn_s_setprio(1); \
            P1_MMA(); \
            __builtin_amdgcn_s_setprio(0); \
            SBAR(); \
            WAIT2; \
            SBAR(); \
        } while (0)
#pragma unroll 1
        for (int t = 0; t < 14; ++t) P1_TILE(true, WAITBAR_(11), WAITBAR_(7));
        P1_TILE(false, WAITBAR_(4), WAITBAR_(0));
        P1_TILE(false, WAITBAR_(0), if (g == 0) asm volatile("s_barrier" ::: "memory"));
#undef P1_TILE
#undef RDW
#undef RDX
#undef STAGE_X
#undef STAGE_W
        SA_(2); SC_(1);
        JIT(1);
        FRESH();
#pragma unroll
        for (int mb = 0; mb < 2; ++mb)
#pragma unroll
            for (int u = 0; u < 2; ++u) {
                const f32x4 a = acc[2 * mb + u][0] * QSCALE, c = acc[2 * mb + u][1] * QSCALE;
                fq[mb][u] = u32x4{cvtpk(a[0], a[1]), cvtpk(a[2], a[3]), cvtpk(c[0], c[1]), cvtpk(c[2], c[3])};
                fk[mb][u] = pack44(acc[2 * mb + u][2], acc[2 * mb + u][3]);
                fv[mb][u] = pack44(acc[2 * mb][4 + u], acc[2 * mb + 1][4 + u]);
            }
    }
    const char* wo = (const char*)(br ? woB : woA) + nt3 * 256;
    const int k30 = 2 * nt3;
    {
        const int g = wid >> 2, wr = wid >> 1, hd = wid & 1;
        const int ph1 = 2 * hp + hd, pb1 = mt * 8 + wr * 2;
        const int head = 2 * hp + br, blk = mt * 8 + wid;
#define RBLK(e) ((((e) % 7) + (((e) % 7) >= mt)) * 8 + (e) / 7)
        const char* kv = ws + WS_KV + (size_t)head * 262144;
        unsigned l16 = lane * 16;
        const unsigned dbeg = lds0 + wid * 4096, lbase = lds0 + l16;
        const unsigned fk0 = (unsigned)WS_FK + head * 32 * FS;
        const unsigned fxo = (unsigned)WS_FX + ((hp * 8 + mt) * 8 + wid) * FS, xrec0 = (unsigned)WS_XP + ((hp * 8 + mt) * 8 + wid) * 5120 + 2048;
        const f32x16 z = {};
        f32x16 sa, negm, o;
        u32x16 qol, kvf, pub0, pub1; u32x8 ph;
        const unsigned sidx = (lane & 1) ? 0xEEEEEEEEu : 0x44444444u;

#define FIRST_TILE(base, q0, q1) do { const unsigned on_ = (lane & 2) ? 0u : 0x3F803F80u, e_ = (lane & 4) ? 0u : on_, f_ = (lane & 4) ? on_ : 0u; const u32x4 ones = {e_, e_, f_, f_}; \
            const bf16x8 k0_ = ldsrd128(base), k1_ = ldsrd128((base) + 1024); kvf = cat16(asu(ldsrd128((base) + 4096)), asu(ldsrd128((base) + 5120)), asu(ldsrd128((base) + 2048)), ones); \
            sa = MFMA32(k0_, q0, z); sa = MFMA32(k1_, q1, sa); float m_ = sa[0]; \
            _Pragma("unroll") for (int r = 1; r < 16; ++r) m_ = __builtin_fmaxf(m_, sa[r]); \
            m_ = -swap_max(m_); _Pragma("unroll") for (int r = 0; r < 16; ++r) { negm[r] = m_; sa[r] += m_; } \
            qol = cat16(asu(q0), asu(q1), ones, u32x4{0u, 0u, 0u, 0u}); } while (0)
#define L6_OUT "={v[0:15]}"(o), "+{v[32:47]}"(sa), "+{v[64:79]}"(qol), "+{v[80:95]}"(kvf), "={v[104:111]}"(ph)
#define L6C(HL, BASE) asm volatile(ATT_L6C : L6_OUT, "={v[144:159]}"(h0), "={v[160:167]}"(h1) \
            : "{v[16:31]}"(negm), "{v174}"(sidx), "{v112}"(BASE), "{v[140:143]}"(u32x4{0u, 0u, FLADDR, (unsigned)WS_HALO + (mt * 32 + ph1) * 6144 + 3072 + l16}), \
              [ws] "s"(ws), [hl] "s"(HL), [fld] "s"(fld) : ATT_TMP, "scc", "memory")
#define FLADDR (fk0 + ((((lane & 7) == 7) ? blk : RBLK(8 * (lane & 7) + wid)) >> 1) * FS)
        const unsigned own = (ph1 * 32 + mt * 4 + wr) * FS;
        const unsigned fld = lds0 + K2_FL + wid * 1024 + br * (2 * K2_SLOT + 4096 - K2_FL + wid * 11264);
        const unsigned pv = (lane >> 5) * 1024 + ((lane & 15) + 32 * ((lane >> 4) & 1)) * 16, pq = pv + (lane & 8) * 16;
        if (br == 0) {
            const char* kp = ws + WS_KV + (size_t)((ph1 * 64 + pb1) * 4) * 1024;
#define PUB_KV() do { st16_dev<0>(kp, pq, fk[0][0]); st16_dev<128>(kp, pq, fk[0][1]); st16_dev<2048>(kp, pv, fv[0][0]); st16_dev<2304>(kp, pv, fv[0][1]); \
            st16_dev<0>(kp + 4096, pq, fk[1][0]); st16_dev<128>(kp + 4096, pq, fk[1][1]); st16_dev<2048>(kp + 4096, pv, fv[1][0]); st16_dev<2304>(kp + 4096, pv, fv[1][1]); } while (0)
            if (hd == 1) {
                const char* qp = ws + WS_QF + (size_t)((ph1 * 64 + pb1) * 2) * 1024;
                st16_dev<0>(qp, pq, fq[0][0]); st16_dev<128>(qp, pq, fq[0][1]); st16_dev<2048>(qp, pq, fq[1][0]); st16_dev<2176>(qp, pq, fq[1][1]);
            }
            JIT(2);
            if (wr == 0) PUB_KV();
#pragma unroll
            for (int mb = 0; mb < 2; ++mb) {
                const unsigned a = lds0 + hd * 2 * K2_SLOT + (wr * 2 + mb) * 4096, aq = lds0 + K2_QX + hd * 16384 + (wr * 2 + mb) * 2048 + pq;
                ldswr128(a + pq, fk[mb][0]); ldswr128(a + pq + 128, fk[mb][1]); ldswr128(a + 2048 + pv, fv[mb][0]); ldswr128(a + 2304 + pv, fv[mb][1]);
                if (hd == 0 || g == 0) { ldswr128(aq, fq[mb][0]); ldswr128(aq + 128, fq[mb][1]); }
            }
            BARRIER_L();
            if (wr == 0 || hd == 1) {
                drain();
                if (lane == 0) { if (wr == 0) flag_set(ws + WS_FK, own, tag); if (hd == 1) flag_set(ws + WS_FQ, own, tag); }
            }
            JIT(3);
            if (wr == 1) PUB_KV();
            const unsigned qa = lbase + K2_QX + (1 - g) * 16384 + wid * 2048, cx = lbase + (1 - g) * 2 * K2_SLOT;
            SA_(3); SC_(2);
            pub0 = cat16(fk[0][0], fk[0][1], fv[0][0], fv[0][1]); pub1 = cat16(fk[1][0], fk[1][1], fv[1][0], fv[1][1]);
            { const bf16x8 q0 = ldsrd128(qa), q1 = ldsrd128(qa + 1024); FIRST_TILE(cx, q0, q1); }
            JIT(4);
            asm volatile(ATT_L4 : L6_OUT : "{v[16:31]}"(negm), "{v174}"(sidx), "{v112}"(cx) : ATT_TMP, "memory");
            SC_(3);
            if (g == 1) {
                const int b1 = RBLK(wid), b0 = RBLK(wid - 4);
                flag_wait(ws, fk0 + (((lane & 1) ? b1 : b0) >> 1) * FS, tag);
                glds4k_dev(kv + b1 * 4096, l16, dbeg + K2_SLOT);
                glds4k_dev(kv + b0 * 4096, l16, dbeg + K2_SLOT - 16384);
                glds16_dev(ws, FLADDR, fld);
            } else if (wr == 1) { drain(); if (lane == 0) flag_set(ws + WS_FK, own, tag); }
            SC_(4);
            JIT(5);
            asm volatile(ATT_L2 : "+{v[0:15]}"(o), "+{v[32:47]}"(sa), "+{v[64:79]}"(qol), "+{v[80:95]}"(kvf), "+{v[104:111]}"(ph) : "{v[16:31]}"(negm), "{v174}"(sidx), "{v112}"(cx) : ATT_TMP, "memory");
            if (g == 0) {
                asm volatile(ATT_XT : "+{v[0:15]}"(o), "+{v[32:47]}"(sa), "+{v[64:79]}"(qol), "+{v[80:95]}"(kvf), "+{v[104:111]}"(ph) : "{v[16:31]}"(negm), "{v174}"(sidx), "{v112}"(cx) : ATT_TMP, "memory");
                st16_dev<-2048>(ws, xrec0 + l16, quad(o, 0)); st16_dev<-1024>(ws, xrec0 + l16, quad(o, 1)); st16_dev<0>(ws, xrec0 + l16, quad(o, 2)); st16_dev<1024>(ws, xrec0 + l16, quad(o, 3));
                st16_dev<2048>(ws, xrec0 + l16, u32x4{__float_as_uint(__uint_as_float(qol[12]) + __uint_as_float(qol[13])), __float_as_uint(negm[0]), 0u, 0u});
                SC_(5);
                JIT(16);
                const bf16x8 q0 = ldsrd128(lbase + K2_QX + wid * 2048), q1 = ldsrd128(lbase + K2_QX + wid * 2048 + 1024);
                FIRST_TILE(lbase, q0, q1);
                u32x16 h0; u32x8 h1;
                L6C(0, lbase);
            }
#undef PUB_KV
        } else {
            if (wr == 3 && mt < 7) {
                const char* p = ws + WS_HALO + (size_t)(((mt + 1) * 32 + ph1) * 6) * 1024;
                st16_dev<0>(p, pq, fk[0][0]); st16_dev<128>(p, pq, fk[0][1]); st16_dev<3072>(p, pq, fk[1][0]); st16_dev<3200>(p, pq, fk[1][1]);
                if (lane >= 32) { st16_dev<1024>(p, pv, fv[0][0]); st16_dev<1280>(p, pv, fv[0][1]); st16_dev<1024>(p + 3072, pv, fv[1][0]); st16_dev<1280>(p + 3072, pv, fv[1][1]); }
            }
            JIT(15);
            const unsigned ex = lds0 + 2 * K2_SLOT + wid * 12288;
#pragma unroll
            for (int mb = 0; mb < 2; ++mb) {
                const unsigned a = ex + mb * 6144;
                ldswr128(a + pq, fq[mb][0]); ldswr128(a + pq + 128, fq[mb][1]); ldswr128(a + 2048 + pq, fk[mb][0]); ldswr128(a + 2176 + pq, fk[mb][1]);
                ldswr128(a + 4096 + pv, fv[mb][0]); ldswr128(a + 4352 + pv, fv[mb][1]);
            }
            BARRIER_L();
            if (wr == 3 && mt < 7) { drain(); if (lane == 0) flag_set(ws + WS_FH, (ph1 * 8 + mt + 1) * FS, tag); }
            SC_(2);
            LocalPart lp[2];
            bf16x8 bq[2][2];
#pragma unroll
            for (int mb = 0; mb < 2; ++mb) { bq[mb][0] = ldsrd128(ex + mb * 6144 + l16); bq[mb][1] = ldsrd128(ex + mb * 6144 + 1024 + l16); }
#define LOCAL_BLOCK(mb) do { const unsigned a_ = ex + mb * 6144 + l16; \
                const bf16x8 k0_ = ldsrd128(a_ + 2048), k1_ = ldsrd128(a_ + 3072), v0_ = ldsrd128(a_ + 4096), v1_ = ldsrd128(a_ + 5120); \
                if (wr > 0) local_own<true>(bq[mb][0], bq[mb][1], k0_, k1_, v0_, v1_, ldsrd128(a_ - 24576 + 2048), ldsrd128(a_ - 24576 + 3072), ldsrd128(a_ - 24576 + 5120), band, lane, lp[mb]); \
                else local_own<false>(bq[mb][0], bq[mb][1], k0_, k1_, v0_, v1_, k0_, k0_, k0_, band, lane, lp[mb]); \
                if (wr > 0 || mt == 0) local_fin(lp[mb]); } while (0)
            JIT(6);
            LOCAL_BLOCK(0);
            SC_(3);
            const int hl = (wid < 2) ? mt : 0;
            const unsigned fh = (unsigned)WS_FH + (ph1 * 8 + mt) * FS;
            const int b0 = RBLK(wid), b1 = RBLK(8 + wid);
            const int q = lane & 3;
            unsigned fa0 = fk0 - 1024 * FS + (blk >> 1) * FS, fa1 = fk0 + (b0 >> 1) * FS, fa2 = fk0 + (b1 >> 1) * FS, fa3 = fh;
            asm volatile("" : "+s"(fa0), "+s"(fa1), "+s"(fa2), "+s"(fa3));
            const unsigned lo_ = (q & 1) ? fa1 : fa0, hi_ = (q & 1) ? fa3 : fa2;
            const u64 fm = flag_wait_some(ws, (q & 2) ? hi_ : lo_, tag, 3);
            SC_(4);
            glds4k_dev(kv + b0 * 4096, l16, dbeg);
            const char* qp = ws + WS_QF + (size_t)((head * 64 + blk) * 2) * 1024;
            glds16_dev(qp, l16, ex); glds16_dev(qp + 1024, l16, ex + 1024);
            if (fm & 4) glds4k_dev(kv + b1 * 4096, l16, dbeg + K2_SLOT);
            JIT(7);
            LOCAL_BLOCK(1);
#undef LOCAL_BLOCK
            if (fm & 4) WAITBAR_(4);
            else {
                WAITBAR_(0);
                flag_wait(ws, fk0 + (b1 >> 1) * FS, tag);
                glds4k_dev(kv + b1 * 4096, l16, dbeg + K2_SLOT);
            }
            JIT(8);
            const bf16x8 q0 = ldsrd128(ex + l16), q1 = ldsrd128(ex + 1024 + l16);
            SA_(3); SC_(5);
            if (hl && !(fm & 8)) flag_wait(ws, fh, tag);
            FIRST_TILE(lbase, q0, q1);
            u32x16 h0; u32x8 h1;
            JIT(9);
            L6C(hl, lbase);
            if (hl) {
                local_prev(bq[0][0], bq[0][1], asbf(u32x4{h0[0], h0[1], h0[2], h0[3]}), asbf(u32x4{h0[4], h0[5], h0[6], h0[7]}), asbf(u32x4{h0[8], h0[9], h0[10], h0[11]}), lane, lp[0]);
                local_prev(bq[1][0], bq[1][1], asbf(u32x4{h0[12], h0[13], h0[14], h0[15]}), asbf(u32x4{h1[0], h1[1], h1[2], h1[3]}), asbf(u32x4{h1[4], h1[5], h1[6], h1[7]}), lane, lp[1]);
                local_fin(lp[0]); local_fin(lp[1]);
            }
            const char* op = ws + WS_OF + (size_t)(((64 + pb1) * 32 + ph1) * 2) * 1024;
            st16_dev<0>(op, l16, quad(lp[0].o, 0)); st16_dev<1024>(op, l16, quad(lp[0].o, 1)); st16_dev<0>(op + 65536, l16, quad(lp[1].o, 0)); st16_dev<1024>(op + 65536, l16, quad(lp[1].o, 1));
        }
#undef FIRST_TILE
#undef L6_OUT
#undef L6C
        FRESH();
        l16 = lane * 16;
        SA_(4); SC_(6);
        const int isx = br & (1 - g);
        unsigned cnt = 7, dst = dbeg + 2 * K2_SLOT, nx = K2_SLOT;
        unsigned mode = isx << 1;
        const unsigned late = (1 - br) & g;
        const unsigned act = 2u | (isx * 12u) | ((br | (1 - g)) << 5) | (late << 6), hk = 32u | (late << 7);
        u64 tab = (u64)blk << 42;
#pragma unroll
        for (int k = 1; k < 7; ++k) tab |= (u64)RBLK(8 * k + wid) << (6 * k);
        tab >>= 6 + 6 * br;
        u32x8 adr;
        { const int k = 8 * wid + (lane >> 4), c = lane & 15;
          adr = u32x8{lds0 + l16, lds0 + l16 + K2_SLOT, l16, lds0 + l16, (unsigned)(k * 2048 + ((c ^ ((k & 7) << 1)) << 4)), (unsigned)((k + 4) * 2048 + ((c ^ (((k + 4) & 7) << 1)) << 4) - 1024),
                      br ? (unsigned)WS_FO + ((64 + pb1 + (lane & 1)) * 32 + ph1) * FS : fxo, 0u};
          const int i = (mt3 * 8 + wid) * 12 + ((lane * 3) >> 4); adr[7] = ((((4 + (i >> 7) + k30) & 15) * 64 + ((i & 127) >> 1)) * 2048 + (i & 1) * 128); }
        const unsigned kvrec = (unsigned)WS_KV + (ph1 * 64 + pb1) * 4096 + 3072, pv2 = (lane >> 5) * 1024 + ((lane & 15) + 32 * ((lane >> 4) & 1)) * 16;
        f32x16 xo; f32x2 xl;
#define DBGOUT
        JIT(10);
        asm volatile(ATT_MAIN
                     : DBGOUT"+{v[0:15]}"(o), "+{v[32:47]}"(sa), "+{v[64:79]}"(qol), "+{v[80:95]}"(kvf), "+{v[104:111]}"(ph), "+{v[112:119]}"(adr), "={v[122:137]}"(xo), "={v[138:139]}"(xl),
                       [cnt] "+s"(cnt), [dst] "+s"(dst), [nx] "+s"(nx), [mode] "+s"(mode)
                     : "{v[16:31]}"(negm), "{v[174:175]}"(u32x2{sidx, (unsigned)WS_FK + own}), "{v[140:143]}"(u32x4{(unsigned)tag, (unsigned)(tag >> 32), fxo, xrec0 + l16}),
                       "{v[170:173]}"(u32x4{FLADDR, kvrec + pv2 + (lane & 8) * 16, kvrec + pv2, fld + l16}), "{v[176:191]}"(pub0), "{v[192:207]}"(pub1),
                       [srclo] "s"((unsigned)(uintptr_t)kv), [srchi] "s"((unsigned)((uintptr_t)kv >> 32)), [dbeg] "s"(dbeg), [dend] "s"(dbeg + 3 * K2_SLOT),
                       [wo01] "s"(wo + (size_t)k30 * 131072), [wo23] "s"(wo + (size_t)((k30 + 2) & 15) * 131072), [wo] "s"(wo), [wdst] "s"(lds0 + 98304 + wid * 2048), [act] "s"(act), [hk] "s"(hk), [tab] "s"(tab), [k0] "s"(br), [ws] "s"(ws)
                     : ATT_TMP, "v120", "v121", "v168", "v169", "s40", "s41", "s42", "s43", "s44", "s45", "s46", "s47", "s48", "s49", "s50", "s51", "vcc", "scc", "memory");
        FRESH();
        l16 = lane * 16;
        const unsigned xrec = xrec0 + l16;
        const bf16x8 q0 = asbf(u32x4{qol[0], qol[1], qol[2], qol[3]}), q1 = asbf(u32x4{qol[4], qol[5], qol[6], qol[7]});
        const unsigned one2 = (((lane >> 4) & 1) == ((lane >> 2) & 1)) ? 0x3F803F80u : 0u;
        const u32x4 ones = {one2, one2, one2, one2};
        SA_(5); SB_(1);
        JIT(11);
        float lsum = __uint_as_float(qol[12]) + __uint_as_float(qol[13]);
        bool ok = lsum < 1.0e18f;
        if (isx) {
            if (__builtin_expect(!(mode & 4), 0)) {
                flag_wait(ws, fxo, tag);
                f32x4 a0, a1, a2, a3;
                asm volatile("global_load_dwordx4 %0, %5, %6 offset:-2048 sc1\n\tglobal_load_dwordx4 %1, %5, %6 offset:-1024 sc1\n\tglobal_load_dwordx4 %2, %5, %6 sc1\n\t"
                             "global_load_dwordx4 %3, %5, %6 offset:1024 sc1\n\tglobal_load_dwordx2 %4, %5, %6 offset:2048 sc1\n\ts_waitcnt vmcnt(0)"
                             : "=&v"(a0), "=&v"(a1), "=&v"(a2), "=&v"(a3), "=&v"(xl) : "v"(xrec), "s"(ws) : "memory");
#pragma unroll
                for (int r = 0; r < 4; ++r) { xo[r] = a0[r]; xo[4 + r] = a1[r]; xo[8 + r] = a2[r]; xo[12 + r] = a3[r]; }
            }
            const float nm = __builtin_fminf(negm[0], xl[1]), fa = EXP2(nm - negm[0]), fx = EXP2(nm - xl[1]);
            ok = ok && (xl[0] < 1.0e18f);
            lsum = lsum * fa + xl[0] * fx;
#pragma unroll
            for (int r = 0; r < 16; ++r) o[r] = o[r] * fa + xo[r] * fx;
        }
#pragma unroll
        for (int r = 0; r < 16; ++r) ok = ok && (__builtin_fabsf(o[r]) < 3.0e38f);
        if (__builtin_expect(__any(!ok), 0)) {
            float m = -__builtin_inff();
            f32x4 l;
            for (int pass = 0; pass < 2; ++pass) {
                o = z; l = f32x4{};
                for (int t = 0; t < 64; ++t) {
                    bf16x8 f0, f1, f2, f3;
                    asm volatile("global_load_dwordx4 %0, %4, %5 sc1\n\tglobal_load_dwordx4 %1, %4, %5 offset:1024 sc1\n\tglobal_load_dwordx4 %2, %4, %5 offset:2048 sc1\n\t"
                                 "global_load_dwordx4 %3, %4, %5 offset:3072 sc1\n\ts_waitcnt vmcnt(0)" : "=&v"(f0), "=&v"(f1), "=&v"(f2), "=&v"(f3) : "v"(l16 + t * 4096), "s"(kv) : "memory");
                    f32x16 s = MFMA32(f0, q0, z); s = MFMA32(f1, q1, s);
                    if (pass == 0) {
#pragma unroll
                        for (int r = 0; r < 16; ++r) m = __builtin_fmaxf(m, s[r]);
                    } else {
#pragma unroll
                        for (int r = 0; r < 16; ++r) s[r] = EXP2(s[r] - m);
                        const bf16x8 p0 = asbf(pack8(s, 0)), p1 = asbf(pack8(s, 1));
                        o = MFMA32(f2, p0, o); o = MFMA32(f3, p1, o);
                        l = MFMA16(asbf(ones), p0, l); l = MFMA16(asbf(ones), p1, l);
                    }
                }
                if (pass == 0) m = swap_max(m);
            }
            lsum = l[0];
        }
        const float inv = __builtin_amdgcn_rcpf(lsum);
#pragma unroll
        for (int r = 0; r < 16; ++r) o[r] *= inv;
        const char* op = ws + WS_OF + (size_t)((blk * 32 + head) * 2) * 1024;
        st16_dev<0>(op, l16, pack8(o, 0)); st16_dev<1024>(op, l16, pack8(o, 1));
    }
    {
        FRESH();
        const int g = wid >> 2, wi = wid & 3, wm = wi >> 1, wn = wi & 1, g4 = lane >> 4;
        const char* of = ws + WS_OF + (size_t)(br * 64 + mt3 * 4 + wi) * 65536;
        unsigned vw4[4];
#pragma unroll
        for (int ii = 0; ii < 4; ++ii) { const int k = 16 * wi + 4 * ii + g4; vw4[ii] = k * 2048 + (((lane & 15) ^ ((k & 7) << 1)) << 4) - ii * 1024; }
        const unsigned l16 = lane * 16, sdst = lds0 + wi * 4096;
#define STAGE_O(t) glds4k_dev(of + (((t) + k30) & 15) * 4096, l16, sdst + ((t) & 3) * 16384)
#define STAGE_WO(t, slot) do { const char* s_ = wo + (size_t)(((t) + k30) & 15) * 131072; glds_run<0>(s_, vw4[0], sdst + (slot)); glds_run<1>(s_, vw4[1], 0); glds_run<2>(s_, vw4[2], 0); glds_run<3>(s_, vw4[3], 0); } while (0)
        const unsigned fo = (unsigned)WS_FO + ((br * 64 + mt3 * 4 + wi) * 32 + 4 * ((lane & 15) >> 1) + 2 * (1 - g) + (lane & 1)) * FS;
        drain();
        if (lane == 0) flag_set(ws + WS_FO, ((mt * 8 + wid) * 32 + 2 * hp + br) * FS, tag);
        SB_(2);
        asm volatile("s_barrier" ::: "memory");
        SB_(3);
        JIT(12);
        const u64 fm = flag_wait_some(ws, fo, tag, (3ull << (2 * nt3)) | (g ? 3ull << (2 * ((nt3 + 1) & 7)) : 0ull));
        STAGE_O(1 - g);
        if (g == 1) STAGE_O(2);
        STAGE_WO(5 - g, 65536 + (1 - g) * 16384);
        if (fm != ~0ull) flag_wait(ws, fo, tag);
        SB_(4);
        SA_(6);
        unsigned ao = lds0 + g * 16384 + wm * 8192 + (g4 >> 1) * 1024 + ((lane & 15) + 32 * (g4 & 1)) * 16;
        unsigned aw[4];
        { const int q4 = (lane & 15) >> 2, p = lane & 3, k = 16 * (g4 >> 1) + 4 * (g4 & 1) + q4;
          const unsigned a0 = lds0 + p3_wslot(0) + g * 16384 + k * 256 + (((8 * wn + (p >> 1)) ^ ((k & 7) << 1)) << 4) + (p & 1) * 8;
#pragma unroll
          for (int nb = 0; nb < 4; ++nb) aw[nb] = a0 ^ (nb << 5); }
        const char* so = of + ((3 + g + k30) & 15) * 4096; const char* sw = wo + (size_t)((5 + g + k30) & 15) * 131072;
        unsigned dob = sdst + ((3 + g) & 3) * 16384, dwb = sdst + p3_wslot(6);
        f32x4 acc[4][4];
#pragma unroll
        for (int mb = 0; mb < 4; ++mb)
#pragma unroll
            for (int nb = 0; nb < 4; ++nb) acc[mb][nb] = f32x4{};
        if (g == 1) { WAITBAR_(8); asm volatile("s_barrier" ::: "memory"); } else asm volatile("s_barrier" ::: "memory");
        SB_(5);
        SBAR();
#pragma unroll
        for (int u = 0; u < 8; ++u) {
            bf16x8 of_[4][2], wf[4][2];
            const bool doo = u < 6 || (u == 6 && g == 0), dow = (u >= 1 || g == 1) && (u < 5 || (u == 5 && g == 0));
#define P3RD(i) do { of_[(i) & 3][(i) >> 2] = ldsrd128(ao + (((i) & 3) >> 1) * 4096 + ((i) & 1) * 256 + ((i) >> 2) * 2048); \
                wf[(i) & 3][(i) >> 2] = cat8(ldstr(aw[(i) & 3] + ((i) >> 2) * 8192), ldstr(aw[(i) & 3] + ((i) >> 2) * 8192 + 2048)); } while (0)
            P3RD(0); if (u < 7 && doo) glds_run_dev<0>(so, l16, dob);
            P3RD(1); if (u < 7 && doo) glds_run_dev<1>(so, l16, 0);
            P3RD(2); if (u < 7 && doo) glds_run_dev<2>(so, l16, 0);
            P3RD(3); if (u < 7 && doo) glds_run_dev<3>(so, l16, 0);
            P3RD(4); if (u < 6 && dow) glds_run<0>(sw, vw4[0], dwb);
            P3RD(5); if (u < 6 && dow) glds_run<1>(sw, vw4[1], 0);
            P3RD(6); if (u < 6 && dow) glds_run<2>(sw, vw4[2], 0);
            P3RD(7); if (u < 6 && dow) glds_run<3>(sw, vw4[3], 0);
#undef P3RD
            SBAR();
            if (u < 7) {
                { const int to = 2 * u + 5 + g; so = of + ((to + k30) & 15) * 4096; sw = wo + (size_t)((to + 2 + k30) & 15) * 131072;
                  dob = sdst + (to & 3) * 16384; dwb = sdst + (g ? p3_wslot(2 * u + 8) : p3_wslot(2 * u + 7)); }
                ao ^= 32768;
#pragma unroll
                for (int q = 0; q < 4; ++q) aw[q] += p3_wslot(2 * u + 2) - p3_wslot(2 * u);
                asm volatile("" : "+v"(ao), "+v"(aw[0]), "+v"(aw[1]), "+v"(aw[2]), "+v"(aw[3]), "+s"(so), "+s"(sw), "+s"(dob), "+s"(dwb));
            }
            if (p3_allow(2 * u) == p3_allow(2 * u + 1)) WAITBAR(p3_allow(2 * u)); else if (g == 0) WAITBAR(p3_allow(2 * u)); else WAITBAR(p3_allow(2 * u + 1));
            SBAR();
            __builtin_amdgcn_s_setprio(1);
#pragma unroll
            for (int mb = 0; mb < 4; ++mb)
#pragma unroll
                for (int nb = 0; nb < 4; ++nb) { acc[mb][nb] = MFMA16(wf[nb][0], of_[mb][0], acc[mb][nb]); acc[mb][nb] = MFMA16(wf[nb][1], of_[mb][1], acc[mb][nb]); CHAIN();
                }
            __builtin_amdgcn_s_setprio(0);
            SBAR();
            if (u < 7 || g == 0) asm volatile("s_barrier" ::: "memory");
            SBAR();
        }
        SB_(6);
        FRESH();
        JIT(13);
        const unsigned xw = lds0 + wid * 8192 + l16, xr = lds0 + (wid ^ 4) * 8192 + l16;
#pragma unroll
        for (int i = 0; i < 8; ++i) { const f32x4 v = g ? acc[i >> 2][i & 3] : acc[2 + (i >> 2)][i & 3]; ldswr128(xw + i * 1024, __builtin_bit_cast(u32x4, v)); }
        BARRIER_L();
        const unsigned st = lds0 + 65536 + wid * 4096;
#pragma unroll
        for (int i = 0; i < 8; ++i) {
            const f32x4 v = (g ? acc[2 + (i >> 2)][i & 3] : acc[i >> 2][i & 3]) + __builtin_bit_cast(f32x4, ldsrd128(xr + i * 1024));
            const int row = 16 * (i >> 2) + (lane & 15), c = 2 * (i & 3) + (g4 >> 1);
            *(LAS u32x2*)(uintptr_t)(st + row * 128 + ((c ^ (row & 7)) << 4) + (g4 & 1) * 8) = u32x2{cvtpk(v[0], v[1]), cvtpk(v[2], v[3])};
        }
        const int blk = mt3 * 4 + 2 * wm + g;
        char* op = (char*)out + br * 2048 + nt3 * 256 + wn * 128 + (lane & 7) * 16;
#pragma unroll
        for (int i = 0; i < 4; ++i) {
            const int r = 8 * i + (lane >> 3), row = br ? ((blk >> 1) * 64 + 2 * r + (blk & 1)) : (blk * 32 + r);
            st16_dev<0>(out, (unsigned)(row * 4096 + br * 2048 + nt3 * 256 + wn * 128 + (lane & 7) * 16), __builtin_bit_cast(u32x4, ldsrd128(st + r * 128 + (((lane & 7) ^ (r & 7)) << 4))));
        }
#undef STAGE_O
#undef STAGE_WO
    }
    STAMP_AT(0, 7);
    if (b == 0 && wid == 0) {
#pragma unroll
        for (int i = 0; i < 2; ++i) { const int u = i * 64 + lane;
            flag_wait(ws0, (unsigned)WS_FK + ((2 * (u >> 3)) * 32 + (u & 7) * 4) * FS, tag);
            flag_wait(ws0, (unsigned)WS_FO + ((64 + (u & 7) * 8) * 32 + 2 * (u >> 3)) * FS, tag); }
        if (lane == 0) flag_set(ws0, (unsigned)WS_EP, tag);
    }
    asm volatile(".Lmk_code_end:");
}

extern "C" void kernel_launch(void* const* d_in, const int* in_sizes, int n_in, void* d_out, int out_size, void* d_ws, size_t ws_size, hipStream_t stream) {
    mk_fused<<<256, 512, 0, stream>>>((const u16*)d_in[0], (const u16*)d_in[1], (const u16*)d_in[2], (const u16*)d_in[3], (const u16*)d_in[4], (const u16*)d_in[5], (char*)d_ws, (u16*)d_out);
}
```

```cpp
#define PROBE 8
#include <hip/hip_runtime.h>
#include <stdint.h>

typedef unsigned short u16;
typedef unsigned long long u64;
typedef __bf16 bf16x8 __attribute__((ext_vector_type(8)));
typedef __bf16 bf16x2 __attribute__((ext_vector_type(2)));
typedef short s16x4 __attribute__((ext_vector_type(4)));
typedef float f32x16 __attribute__((ext_vector_type(16)));
typedef float f32x4 __attribute__((ext_vector_type(4)));
typedef float f32x2 __attribute__((ext_vector_type(2)));
typedef unsigned u32x4 __attribute__((ext_vector_type(4)));
typedef unsigned u32x2 __attribute__((ext_vector_type(2)));
typedef unsigned u32x8 __attribute__((ext_vector_type(8)));
typedef unsigned u32x16 __attribute__((ext_vector_type(16)));
#define LAS __attribute__((address_space(3)))
#define DI __device__ __forceinline__
#define SBAR() __builtin_amdgcn_sched_barrier(0)
#define MFMA32(a, b, c) __builtin_amdgcn_mfma_f32_32x32x16_bf16((a), (b), (c), 0, 0, 0)
#define MFMA16(a, b, c) __builtin_amdgcn_mfma_f32_16x16x32_bf16((a), (b), (c), 0, 0, 0)
#define CHAIN() SBAR()
#define ORD 0
#define EXP2(x) __builtin_amdgcn_exp2f(x)

constexpr float QSCALE = 0.17677669529663687f * 1.4426950408889634f;

constexpr size_t WS_QF = 0;
constexpr size_t WS_KV = WS_QF + (4u << 20);
constexpr size_t WS_OF = WS_KV + (8u << 20);
constexpr size_t WS_HALO = WS_OF + (8u << 20);
constexpr size_t WS_XP = WS_HALO + (2u << 20);
constexpr size_t WS_FX = WS_XP + (5u << 20);
#define FS 64
constexpr size_t WS_FQ = WS_FX + 1024 * FS;
constexpr size_t WS_FK = WS_FQ + 1024 * FS;
constexpr size_t WS_FO = WS_FK + 1024 * FS;
constexpr size_t WS_FH = WS_FO + 4096 * FS;
constexpr size_t WS_EP = WS_FH + 256 * FS;

#define OUTST 1
#define SLP 1
#define STAMP_AT(K, I)
#define SA_(I)
#define SB_(I)

#define SC_(I)
#undef STAMP_AT
#undef SA_
#define STAMP_AT(K, I) do { pst[I] = (unsigned)__builtin_amdgcn_s_memrealtime(); } while (0)
#define SA_(I) STAMP_AT(0, I)
#define JIT(p)
DI unsigned cvtpk(float lo, float hi) { f32x2 v = {lo, hi}; bf16x2 b = __builtin_convertvector(v, bf16x2); return __builtin_bit_cast(unsigned, b); }
DI u32x4 pack8(const f32x16& a, int s) { u32x4 r; r[0] = cvtpk(a[8 * s], a[8 * s + 1]); r[1] = cvtpk(a[8 * s + 2], a[8 * s + 3]); r[2] = cvtpk(a[8 * s + 4], a[8 * s + 5]); r[3] = cvtpk(a[8 * s + 6], a[8 * s + 7]); return r; }
DI u32x4 pack44(f32x4 a, f32x4 c) { return u32x4{cvtpk(a[0], a[1]), cvtpk(a[2], a[3]), cvtpk(c[0], c[1]), cvtpk(c[2], c[3])}; }
DI u32x4 asu(bf16x8 v) { return __builtin_bit_cast(u32x4, v); }
DI u32x16 cat16(u32x4 a, u32x4 b, u32x4 c, u32x4 d) { return u32x16{a[0], a[1], a[2], a[3], b[0], b[1], b[2], b[3], c[0], c[1], c[2], c[3], d[0], d[1], d[2], d[3]}; }
DI bf16x8 asbf(u32x4 v) { return __builtin_bit_cast(bf16x8, v); }
DI void glds16(const void* sbase, unsigned voff, unsigned lds) {
    asm volatile("s_mov_b32 m0, %2\n\ts_nop 0\n\tglobal_load_lds_dwordx4 %0, %1" :: "v"(voff), "s"(sbase), "s"(lds) : "memory");
}
template <int I> DI void glds_run(const void* sbase, unsigned voff, unsigned lds) {
    if (I == 0) asm volatile("s_mov_b32 m0, %2\n\ts_nop 0\n\tglobal_load_lds_dwordx4 %0, %1" :: "v"(voff), "s"(sbase), "s"(lds) : "memory");
    else asm volatile("global_load_lds_dwordx4 %0, %1 offset:%2" :: "v"(voff), "s"(sbase), "i"(I * 1024) : "memory");
}
template <int I> DI void glds_run_dev(const void* sbase, unsigned voff, unsigned lds) {
    if (I == 0) asm volatile("s_mov_b32 m0, %2\n\ts_nop 0\n\tglobal_load_lds_dwordx4 %0, %1 sc1" :: "v"(voff), "s"(sbase), "s"(lds) : "memory");
    else asm volatile("global_load_lds_dwordx4 %0, %1 offset:%2 sc1" :: "v"(voff), "s"(sbase), "i"(I * 1024) : "memory");
}
template <int I> DI void glds_sec(const void* sbase, unsigned voff, unsigned lds) {
    if (I < 2) asm volatile("s_add_u32 m0, %2, %4\n\ts_nop 0\n\tglobal_load_lds_dwordx4 %0, %1 offset:%3" :: "v"(voff), "s"(sbase), "s"(lds), "i"(I * 2048), "i"(I * 6144) : "memory", "scc");
    else asm volatile("s_add_u32 m0, %2, %4\n\ts_nop 0\n\tglobal_load_lds_dwordx4 %0, %1 offset:%3" :: "v"(voff), "s"((const char*)sbase + 2048), "s"(lds), "i"(2048), "i"(14336) : "memory", "scc");
}
DI void glds16_dev(const void* sbase, unsigned voff, unsigned lds) {
    asm volatile("s_mov_b32 m0, %2\n\ts_nop 0\n\tglobal_load_lds_dwordx4 %0, %1 sc1" :: "v"(voff), "s"(sbase), "s"(lds) : "memory");
}
DI void glds4k_dev(const void* sbase, unsigned voff, unsigned lds) {
    asm volatile("s_mov_b32 m0, %2\n\ts_nop 0\n\tglobal_load_lds_dwordx4 %0, %1 sc1\n\tglobal_load_lds_dwordx4 %0, %1 offset:1024 sc1\n\t"
                 "global_load_lds_dwordx4 %0, %1 offset:2048 sc1\n\tglobal_load_lds_dwordx4 %0, %1 offset:3072 sc1" :: "v"(voff), "s"(sbase), "s"(lds) : "memory");
}
template <int OFF> DI void st16_dev(const void* sbase, unsigned voff, u32x4 v) {
    asm volatile("global_store_dwordx4 %0, %1, %2 offset:%3 sc1\n\ts_nop 1" :: "v"(voff), "v"(v), "s"(sbase), "i"(OFF) : "memory");
}
DI void drain() { asm volatile("s_waitcnt vmcnt(0)" ::: "memory"); }
DI void flag_set(const void* sbase, unsigned voff, u64 tag) {
    asm volatile("global_store_dwordx2 %0, %1, %2 sc1" :: "v"(voff), "v"(tag), "s"(sbase) : "memory");
}
DI void flag_wait(const void* sbase, unsigned voff, u64 tag) {
    const u64 t0 = __builtin_amdgcn_s_memrealtime();
    for (;;) {
        u64 v;
        asm volatile("global_load_dwordx2 %0, %1, %2 sc1\n\ts_waitcnt vmcnt(0)" : "=v"(v) : "v"(voff), "s"(sbase) : "memory");
        if (__all(v == tag)) break;
        if (__builtin_amdgcn_s_memrealtime() - t0 > 2000000ull) break;
        __builtin_amdgcn_s_sleep(SLP);
    }
}
DI u64 flag_wait_some(const void* sbase, unsigned voff, u64 tag, u64 need) {
    const u64 t0 = __builtin_amdgcn_s_memrealtime();
    for (;;) {
        u64 v;
        asm volatile("global_load_dwordx2 %0, %1, %2 sc1\n\ts_waitcnt vmcnt(0)" : "=v"(v) : "v"(voff), "s"(sbase) : "memory");
        const u64 m = __ballot(v == tag);
        if ((m & need) == need || __builtin_amdgcn_s_memrealtime() - t0 > 2000000ull) return m;
        __builtin_amdgcn_s_sleep(SLP);
    }
}
DI bf16x8 ldsrd128(unsigned addr) { return *(const LAS bf16x8*)(uintptr_t)addr; }
DI void ldswr128(unsigned addr, u32x4 v) { *(LAS u32x4*)(uintptr_t)addr = v; }
DI s16x4 ldstr(unsigned addr) { return __builtin_amdgcn_ds_read_tr16_b64_v4i16((LAS s16x4*)(uintptr_t)addr); }
DI bf16x8 cat8(s16x4 lo, s16x4 hi) { typedef short s16x8 __attribute__((ext_vector_type(8))); s16x8 v = {lo[0], lo[1], lo[2], lo[3], hi[0], hi[1], hi[2], hi[3]}; return __builtin_bit_cast(bf16x8, v); }
DI float swap_max(float m) { auto r = __builtin_amdgcn_permlane32_swap(__float_as_uint(m), __float_as_uint(m), false, false); return __builtin_fmaxf(__uint_as_float(r[0]), __uint_as_float(r[1])); }
DI float swap_add(float m) { auto r = __builtin_amdgcn_permlane32_swap(__float_as_uint(m), __float_as_uint(m), false, false); return __uint_as_float(r[0]) + __uint_as_float(r[1]); }
#define FRESH() asm volatile("" : "+v"(lane), "+s"(ws))
#define BARRIER_L() asm volatile("s_waitcnt lgkmcnt(0)\n\ts_barrier" ::: "memory")
#define WAITBAR_(n) asm volatile("s_waitcnt vmcnt(%0) lgkmcnt(0)\n\ts_barrier" :: "i"(n) : "memory")
#define WAITBAR(n) do { switch (n) { case 0: WAITBAR_(0); break; case 2: WAITBAR_(2); break; case 3: WAITBAR_(3); break; case 4: WAITBAR_(4); break; case 6: WAITBAR_(6); break; \
    case 7: WAITBAR_(7); break; case 8: WAITBAR_(8); break; case 10: WAITBAR_(10); break; case 12: WAITBAR_(12); break; default: WAITBAR_(0); } } while (0)

struct LocalPart { f32x16 o; float m, l; };
DI f32x16 band_mask(int lane) {
    const int i = lane & 31, h = lane >> 5;
    f32x16 c;
#pragma unroll
    for (int r = 0; r < 16; ++r) { const int j = (r & 3) + 8 * (r >> 2) + 4 * h; c[r] = ((unsigned)(i - j) <= 7u) ? 0.f : -__builtin_inff(); }
    return c;
}
DI float max3(float a, float b, float c) { float r; asm("v_max3_f32 %0, %1, %2, %3" : "=v"(r) : "v"(a), "v"(b), "v"(c)); return r; }
template <bool PREV> DI void local_own(bf16x8 q0, bf16x8 q1, bf16x8 k0, bf16x8 k1, bf16x8 v0, bf16x8 v1, bf16x8 kp0, bf16x8 kp1, bf16x8 vp1, const f32x16& band, int lane, LocalPart& P) {
    const f32x16 z = {};
    f32x16 so = MFMA32(k0, q0, band), sp = z;
    if (PREV) sp = MFMA32(kp0, q0, z);
    so = MFMA32(k1, q1, so);
    if (PREV) sp = MFMA32(kp1, q1, sp);
    float m = __builtin_fmaxf(so[0], so[1]);
#pragma unroll
    for (int r = 2; r < 16; r += 2) m = __builtin_fmaxf(__builtin_fmaxf(m, so[r]), so[r + 1]);
    if (PREV) {
        const int d = 4 * (lane >> 5) - (lane & 31);
#pragma unroll
        for (int r = 12; r < 16; ++r) { sp[r] = (d + (r & 3) >= 1) ? sp[r] : -__builtin_inff(); m = __builtin_fmaxf(m, sp[r]); }
    }
    m = swap_max(m);
    float l = 0.f;
#pragma unroll
    for (int r = 0; r < 16; ++r) { so[r] = EXP2(so[r] - m); l += so[r]; }
    P.o = MFMA32(v0, asbf(pack8(so, 0)), z); P.o = MFMA32(v1, asbf(pack8(so, 1)), P.o);
    if (PREV) {
#pragma unroll
        for (int r = 12; r < 16; ++r) { sp[r] = EXP2(sp[r] - m); l += sp[r]; }
        P.o = MFMA32(vp1, asbf(u32x4{0u, 0u, cvtpk(sp[12], sp[13]), cvtpk(sp[14], sp[15])}), P.o);
    }
    P.m = m; P.l = l;
}
DI void local_prev(bf16x8 q0, bf16x8 q1, bf16x8 kp0, bf16x8 kp1, bf16x8 vp1, int lane, LocalPart& P) {
    const int i = lane & 31, h = lane >> 5;
    const f32x16 z = {};
    f32x16 sp = MFMA32(kp0, q0, z); sp = MFMA32(kp1, q1, sp);
    const float NEG = -__builtin_inff();
    float m = P.m;
#pragma unroll
    for (int r = 12; r < 16; ++r) { const int j = (r & 3) + 24 + 4 * h; sp[r] = (j >= i + 25) ? sp[r] : NEG; m = __builtin_fmaxf(m, sp[r]); }
    m = swap_max(m);
    const float f = EXP2(P.m - m);
    float l = P.l * f;
#pragma unroll
    for (int r = 12; r < 16; ++r) { sp[r] = EXP2(sp[r] - m); l += sp[r]; }
#pragma unroll
    for (int r = 0; r < 16; ++r) P.o[r] *= f;
    P.o = MFMA32(vp1, asbf(u32x4{0u, 0u, cvtpk(sp[12], sp[13]), cvtpk(sp[14], sp[15])}), P.o);
    P.m = m; P.l = l;
}
DI void local_fin(LocalPart& P) {
    const float inv = __builtin_amdgcn_rcpf(swap_add(P.l));
#pragma unroll
    for (int r = 0; r < 16; ++r) P.o[r] *= inv;
    const u32x4 o0 = pack8(P.o, 0), o1 = pack8(P.o, 1);
#pragma unroll
    for (int r = 0; r < 4; ++r) { P.o[r] = __uint_as_float(o0[r]); P.o[4 + r] = __uint_as_float(o1[r]); }
}
DI u32x4 quad(const f32x16& a, int i) { return __builtin_bit_cast(u32x4, f32x4{a[4 * i], a[4 * i + 1], a[4 * i + 2], a[4 * i + 3]}); }

#define ATT_L6C \
    "s_nop 1\n\t" \
    "s_cmp_eq_u32 %[hl], 0\n\t" \
    "s_cbranch_scc1 10f\n\t" \
    "global_load_dwordx4 v[144:147], v143, %[ws] offset:-3072 sc1\n\t" \
    "global_load_dwordx4 v[148:151], v143, %[ws] offset:-2048 sc1\n\t" \
    "global_load_dwordx4 v[152:155], v143, %[ws] offset:-1024 sc1\n\t" \
    "global_load_dwordx4 v[156:159], v143, %[ws] offset:0 sc1\n\t" \
    "global_load_dwordx4 v[160:163], v143, %[ws] offset:1024 sc1\n\t" \
    "global_load_dwordx4 v[164:167], v143, %[ws] offset:2048 sc1\n\t" \
    "10:\n" \
    "s_setprio 3\n\t" \
    "s_waitcnt lgkmcnt(0)\n\t" \
    "v_mfma_f32_32x32x16_bf16 v[48:63], v[80:83], v[64:67], v[16:31]\n\t" \
    "v_exp_f32_e32 v32, v32\n\t" \
    "v_exp_f32_e32 v33, v33\n\t" \
    "v_exp_f32_e32 v34, v34\n\t" \
    "v_exp_f32_e32 v35, v35\n\t" \
    "v_exp_f32_e32 v36, v36\n\t" \
    "v_mfma_f32_32x32x16_bf16 v[48:63], v[84:87], v[68:71], v[48:63]\n\t" \
    "ds_read_b128 v[80:83], v112 offset:8192\n\t" \
    "ds_read_b128 v[84:87], v112 offset:9216\n\t" \
    "v_cvt_pk_bf16_f32 v104, v32, v33\n\t" \
    "v_cvt_pk_bf16_f32 v105, v34, v35\n\t" \
    "v_exp_f32_e32 v37, v37\n\t" \
    "v_exp_f32_e32 v38, v38\n\t" \
    "v_exp_f32_e32 v39, v39\n\t" \
    "ds_read_b128 v[100:103], v112 offset:3072\n\t" \
    "ds_read_b128 v[96:99], v112 offset:6144\n\t" \
    "v_exp_f32_e32 v40, v40\n\t" \
    "v_cvt_pk_bf16_f32 v106, v36, v37\n\t" \
    "v_cvt_pk_bf16_f32 v107, v38, v39\n\t" \
    "v_exp_f32_e32 v41, v41\n\t" \
    "v_exp_f32_e32 v42, v42\n\t" \
    "v_exp_f32_e32 v43, v43\n\t" \
    "v_mfma_f32_32x32x16_bf16 v[0:15], v[88:91], v[104:107], 0\n\t" \
    "v_exp_f32_e32 v44, v44\n\t" \
    "v_exp_f32_e32 v45, v45\n\t" \
    "v_exp_f32_e32 v46, v46\n\t" \
    "v_exp_f32_e32 v47, v47\n\t" \
    "v_cvt_pk_bf16_f32 v108, v40, v41\n\t" \
    "v_cvt_pk_bf16_f32 v109, v42, v43\n\t" \
    "v_cvt_pk_bf16_f32 v110, v44, v45\n\t" \
    "v_cvt_pk_bf16_f32 v111, v46, v47\n\t" \
    "s_mov_b32 m0, %[fld]\n\t" \
    "s_nop 0\n\t" \
    "global_load_lds_dwordx4 v142, %[ws] sc1\n\t" \
    "s_waitcnt lgkmcnt(0)\n\t" \
    "v_mfma_f32_32x32x16_bf16 v[32:47], v[80:83], v[64:67], v[16:31]\n\t" \
    "v_exp_f32_e32 v48, v48\n\t" \
    "v_exp_f32_e32 v49, v49\n\t" \
    "v_exp_f32_e32 v50, v50\n\t" \
    "v_smfmac_f32_16x16x64_bf16 v[76:79], v[72:75], v[104:111], v174\n\t" \
    "v_exp_f32_e32 v51, v51\n\t" \
    "v_exp_f32_e32 v52, v52\n\t" \
    "v_mfma_f32_32x32x16_bf16 v[32:47], v[84:87], v[68:71], v[32:47]\n\t" \
    "ds_read_b128 v[80:83], v112 offset:12288\n\t" \
    "ds_read_b128 v[84:87], v112 offset:13312\n\t" \
    "v_cvt_pk_bf16_f32 v104, v48, v49\n\t" \
    "v_cvt_pk_bf16_f32 v105, v50, v51\n\t" \
    "v_exp_f32_e32 v53, v53\n\t" \
    "v_exp_f32_e32 v54, v54\n\t" \
    "v_exp_f32_e32 v55, v55\n\t" \
    "v_mfma_f32_32x32x16_bf16 v[0:15], v[100:103], v[108:111], v[0:15]\n\t" \
    "ds_read_b128 v[92:95], v112 offset:7168\n\t" \
    "ds_read_b128 v[88:91], v112 offset:10240\n\t" \
    "v_exp_f32_e32 v56, v56\n\t" \
    "v_cvt_pk_bf16_f32 v106, v52, v53\n\t" \
    "v_cvt_pk_bf16_f32 v107, v54, v55\n\t" \
    "v_exp_f32_e32 v57, v57\n\t" \
    "v_exp_f32_e32 v58, v58\n\t" \
    "v_exp_f32_e32 v59, v59\n\t" \
    "v_mfma_f32_32x32x16_bf16 v[0:15], v[96:99], v[104:107], v[0:15]\n\t" \
    "v_exp_f32_e32 v60, v60\n\t" \
    "v_exp_f32_e32 v61, v61\n\t" \
    "v_exp_f32_e32 v62, v62\n\t" \
    "v_exp_f32_e32 v63, v63\n\t" \
    "v_cvt_pk_bf16_f32 v108, v56, v57\n\t" \
    "v_cvt_pk_bf16_f32 v109, v58, v59\n\t" \
    "v_cvt_pk_bf16_f32 v110, v60, v61\n\t" \
    "v_cvt_pk_bf16_f32 v111, v62, v63\n\t" \
    "s_setprio 2\n\t" \
    "s_waitcnt lgkmcnt(0)\n\t" \
    "v_mfma_f32_32x32x16_bf16 v[48:63], v[80:83], v[64:67], v[16:31]\n\t" \
    "v_exp_f32_e32 v32, v32\n\t" \
    "v_exp_f32_e32 v33, v33\n\t" \
    "v_exp_f32_e32 v34, v34\n\t" \
    "v_smfmac_f32_16x16x64_bf16 v[76:79], v[72:75], v[104:111], v174\n\t" \
    "v_exp_f32_e32 v35, v35\n\t" \
    "v_exp_f32_e32 v36, v36\n\t" \
    "v_mfma_f32_32x32x16_bf16 v[48:63], v[84:87], v[68:71], v[48:63]\n\t" \
    "ds_read_b128 v[80:83], v112 offset:16384\n\t" \
    "ds_read_b128 v[84:87], v112 offset:17408\n\t" \
    "v_cvt_pk_bf16_f32 v104, v32, v33\n\t" \
    "v_cvt_pk_bf16_f32 v105, v34, v35\n\t" \
    "v_exp_f32_e32 v37, v37\n\t" \
    "v_exp_f32_e32 v38, v38\n\t" \
    "v_exp_f32_e32 v39, v39\n\t" \
    "v_mfma_f32_32x32x16_bf16 v[0:15], v[92:95], v[108:111], v[0:15]\n\t" \
    "ds_read_b128 v[100:103], v112 offset:11264\n\t" \
    "ds_read_b128 v[96:99], v112 offset:14336\n\t" \
    "v_exp_f32_e32 v40, v40\n\t" \
    "v_cvt_pk_bf16_f32 v106, v36, v37\n\t" \
    "v_cvt_pk_bf16_f32 v107, v38, v39\n\t" \
    "v_exp_f32_e32 v41, v41\n\t" \
    "v_exp_f32_e32 v42, v42\n\t" \
    "v_exp_f32_e32 v43, v43\n\t" \
    "v_mfma_f32_32x32x16_bf16 v[0:15], v[88:91], v[104:107], v[0:15]\n\t" \
    "v_exp_f32_e32 v44, v44\n\t" \
    "v_exp_f32_e32 v45, v45\n\t" \
    "v_exp_f32_e32 v46, v46\n\t" \
    "v_exp_f32_e32 v47, v47\n\t" \
    "v_cvt_pk_bf16_f32 v108, v40, v41\n\t" \
    "v_cvt_pk_bf16_f32 v109, v42, v43\n\t" \
    "v_cvt_pk_bf16_f32 v110, v44, v45\n\t" \
    "v_cvt_pk_bf16_f32 v111, v46, v47\n\t" \
    "s_waitcnt lgkmcnt(0)\n\t" \
    "v_mfma_f32_32x32x16_bf16 v[32:47], v[80:83], v[64:67], v[16:31]\n\t" \
    "v_exp_f32_e32 v48, v48\n\t" \
    "v_exp_f32_e32 v49, v49\n\t" \
    "v_exp_f32_e32 v50, v50\n\t" \
    "v_smfmac_f32_16x16x64_bf16 v[76:79], v[72:75], v[104:111], v174\n\t" \
    "v_exp_f32_e32 v51, v51\n\t" \
    "v_exp_f32_e32 v52, v52\n\t" \
    "v_mfma_f32_32x32x16_bf16 v[32:47], v[84:87], v[68:71], v[32:47]\n\t" \
    "ds_read_b128 v[80:83], v112 offset:20480\n\t" \
    "ds_read_b128 v[84:87], v112 offset:21504\n\t" \
    "v_cvt_pk_bf16_f32 v104, v48, v49\n\t" \
    "v_cvt_pk_bf16_f32 v105, v50, v51\n\t" \
    "v_exp_f32_e32 v53, v53\n\t" \
    "v_exp_f32_e32 v54, v54\n\t" \
    "v_exp_f32_e32 v55, v55\n\t" \
    "v_mfma_f32_32x32x16_bf16 v[0:15], v[100:103], v[108:111], v[0:15]\n\t" \
    "ds_read_b128 v[92:95], v112 offset:15360\n\t" \
    "ds_read_b128 v[88:91], v112 offset:18432\n\t" \
    "v_exp_f32_e32 v56, v56\n\t" \
    "v_cvt_pk_bf16_f32 v106, v52, v53\n\t" \
    "v_cvt_pk_bf16_f32 v107, v54, v55\n\t" \
    "v_exp_f32_e32 v57, v57\n\t" \
    "v_exp_f32_e32 v58, v58\n\t" \
    "v_exp_f32_e32 v59, v59\n\t" \
    "v_mfma_f32_32x32x16_bf16 v[0:15], v[96:99], v[104:107], v[0:15]\n\t" \
    "v_exp_f32_e32 v60, v60\n\t" \
    "v_exp_f32_e32 v61, v61\n\t" \
    "v_exp_f32_e32 v62, v62\n\t" \
    "v_exp_f32_e32 v63, v63\n\t" \
    "v_cvt_pk_bf16_f32 v108, v56, v57\n\t" \
    "v_cvt_pk_bf16_f32 v109, v58, v59\n\t" \
    "v_cvt_pk_bf16_f32 v110, v60, v61\n\t" \
    "v_cvt_pk_bf16_f32 v111, v62, v63\n\t" \
    "s_setprio 1\n\t" \
    "s_waitcnt lgkmcnt(0)\n\t" \
    "v_mfma_f32_32x32x16_bf16 v[48:63], v[80:83], v[64:67], v[16:31]\n\t" \
    "v_exp_f32_e32 v32, v32\n\t" \
    "v_exp_f32_e32 v33, v33\n\t" \
    "v_exp_f32_e32 v34, v34\n\t" \
    "v_smfmac_f32_16x16x64_bf16 v[76:79], v[72:75], v[104:111], v174\n\t" \
    "v_exp_f32_e32 v35, v35\n\t" \
    "v_exp_f32_e32 v36, v36\n\t" \
    "v_mfma_f32_32x32x16_bf16 v[48:63], v[84:87], v[68:71], v[48:63]\n\t" \
    "ds_read_b128 v[80:83], v112 offset:24576\n\t" \
    "ds_read_b128 v[84:87], v112 offset:25600\n\t" \
    "v_cvt_pk_bf16_f32 v104, v32, v33\n\t" \
    "v_cvt_pk_bf16_f32 v105, v34, v35\n\t" \
    "v_exp_f32_e32 v37, v37\n\t" \
    "v_exp_f32_e32 v38, v38\n\t" \
    "v_exp_f32_e32 v39, v39\n\t" \
    "v_mfma_f32_32x32x16_bf16 v[0:15], v[92:95], v[108:111], v[0:15]\n\t" \
    "ds_read_b128 v[100:103], v112 offset:19456\n\t" \
    "ds_read_b128 v[96:99], v112 offset:22528\n\t" \
    "v_exp_f32_e32 v40, v40\n\t" \
    "v_cvt_pk_bf16_f32 v106, v36, v37\n\t" \
    "v_cvt_pk_bf16_f32 v107, v38, v39\n\t" \
    "v_exp_f32_e32 v41, v41\n\t" \
    "v_exp_f32_e32 v42, v42\n\t" \
    "v_exp_f32_e32 v43, v43\n\t" \
    "v_mfma_f32_32x32x16_bf16 v[0:15], v[88:91], v[104:107], v[0:15]\n\t" \
    "v_exp_f32_e32 v44, v44\n\t" \
    "v_exp_f32_e32 v45, v45\n\t" \
    "v_exp_f32_e32 v46, v46\n\t" \
    "v_exp_f32_e32 v47, v47\n\t" \
    "v_cvt_pk_bf16_f32 v108, v40, v41\n\t" \
    "v_cvt_pk_bf16_f32 v109, v42, v43\n\t" \
    "v_cvt_pk_bf16_f32 v110, v44, v45\n\t" \
    "v_cvt_pk_bf16_f32 v111, v46, v47\n\t" \
    "s_waitcnt lgkmcnt(0)\n\t" \
    "v_mfma_f32_32x32x16_bf16 v[32:47], v[80:83], v[64:67], v[16:31]\n\t" \
    "v_exp_f32_e32 v48, v48\n\t" \
    "v_exp_f32_e32 v49, v49\n\t" \
    "v_exp_f32_e32 v50, v50\n\t" \
    "v_smfmac_f32_16x16x64_bf16 v[76:79], v[72:75], v[104:111], v174\n\t" \
    "v_exp_f32_e32 v51, v51\n\t" \
    "v_exp_f32_e32 v52, v52\n\t" \
    "v_mfma_f32_32x32x16_bf16 v[32:47], v[84:87], v[68:71], v[32:47]\n\t" \
    "ds_read_b128 v[80:83], v112 offset:28672\n\t" \
    "ds_read_b128 v[84:87], v112 offset:29696\n\t" \
    "v_cvt_pk_bf16_f32 v104, v48, v49\n\t" \
    "v_cvt_pk_bf16_f32 v105, v50, v51\n\t" \
    "v_exp_f32_e32 v53, v53\n\t" \
    "v_exp_f32_e32 v54, v54\n\t" \
    "v_exp_f32_e32 v55, v55\n\t" \
    "v_mfma_f32_32x32x16_bf16 v[0:15], v[100:103], v[108:111], v[0:15]\n\t" \
    "ds_read_b128 v[92:95], v112 offset:23552\n\t" \
    "ds_read_b128 v[88:91], v112 offset:26624\n\t" \
    "v_exp_f32_e32 v56, v56\n\t" \
    "v_cvt_pk_bf16_f32 v106, v52, v53\n\t" \
    "v_cvt_pk_bf16_f32 v107, v54, v55\n\t" \
    "v_exp_f32_e32 v57, v57\n\t" \
    "v_exp_f32_e32 v58, v58\n\t" \
    "v_exp_f32_e32 v59, v59\n\t" \
    "v_mfma_f32_32x32x16_bf16 v[0:15], v[96:99], v[104:107], v[0:15]\n\t" \
    "v_exp_f32_e32 v60, v60\n\t" \
    "v_exp_f32_e32 v61, v61\n\t" \
    "v_exp_f32_e32 v62, v62\n\t" \
    "v_exp_f32_e32 v63, v63\n\t" \
    "v_cvt_pk_bf16_f32 v108, v56, v57\n\t" \
    "v_cvt_pk_bf16_f32 v109, v58, v59\n\t" \
    "v_cvt_pk_bf16_f32 v110, v60, v61\n\t" \
    "v_cvt_pk_bf16_f32 v111, v62, v63\n\t" \
    "s_setprio 0\n\t" \
    "s_cmp_eq_u32 %[hl], 0\n\t" \
    "s_cbranch_scc1 11f\n\t" \
    "s_waitcnt vmcnt(1)\n\t" \
    "11:\n" \
    "s_waitcnt lgkmcnt(0)\n\t" \
    "s_nop 15\n\t"
#define ATT_L4 \
    "s_nop 1\n\t" \
    "s_setprio 3\n\t" \
    "s_waitcnt lgkmcnt(0)\n\t" \
    "v_mfma_f32_32x32x16_bf16 v[48:63], v[80:83], v[64:67], v[16:31]\n\t" \
    "v_exp_f32_e32 v32, v32\n\t" \
    "v_exp_f32_e32 v33, v33\n\t" \
    "v_exp_f32_e32 v34, v34\n\t" \
    "v_exp_f32_e32 v35, v35\n\t" \
    "v_exp_f32_e32 v36, v36\n\t" \
    "v_mfma_f32_32x32x16_bf16 v[48:63], v[84:87], v[68:71], v[48:63]\n\t" \
    "ds_read_b128 v[80:83], v112 offset:8192\n\t" \
    "ds_read_b128 v[84:87], v112 offset:9216\n\t" \
    "v_cvt_pk_bf16_f32 v104, v32, v33\n\t" \
    "v_cvt_pk_bf16_f32 v105, v34, v35\n\t" \
    "v_exp_f32_e32 v37, v37\n\t" \
    "v_exp_f32_e32 v38, v38\n\t" \
    "v_exp_f32_e32 v39, v39\n\t" \
    "ds_read_b128 v[100:103], v112 offset:3072\n\t" \
    "ds_read_b128 v[96:99], v112 offset:6144\n\t" \
    "v_exp_f32_e32 v40, v40\n\t" \
    "v_cvt_pk_bf16_f32 v106, v36, v37\n\t" \
    "v_cvt_pk_bf16_f32 v107, v38, v39\n\t" \
    "v_exp_f32_e32 v41, v41\n\t" \
    "v_exp_f32_e32 v42, v42\n\t" \
    "v_exp_f32_e32 v43, v43\n\t" \
    "v_mfma_f32_32x32x16_bf16 v[0:15], v[88:91], v[104:107], 0\n\t" \
    "v_exp_f32_e32 v44, v44\n\t" \
    "v_exp_f32_e32 v45, v45\n\t" \
    "v_exp_f32_e32 v46, v46\n\t" \
    "v_exp_f32_e32 v47, v47\n\t" \
    "v_cvt_pk_bf16_f32 v108, v40, v41\n\t" \
    "v_cvt_pk_bf16_f32 v109, v42, v43\n\t" \
    "v_cvt_pk_bf16_f32 v110, v44, v45\n\t" \
    "v_cvt_pk_bf16_f32 v111, v46, v47\n\t" \
    "s_waitcnt lgkmcnt(0)\n\t" \
    "v_mfma_f32_32x32x16_bf16 v[32:47], v[80:83], v[64:67], v[16:31]\n\t" \
    "v_exp_f32_e32 v48, v48\n\t" \
    "v_exp_f32_e32 v49, v49\n\t" \
    "v_exp_f32_e32 v50, v50\n\t" \
    "v_smfmac_f32_16x16x64_bf16 v[76:79], v[72:75], v[104:111], v174\n\t" \
    "v_exp_f32_e32 v51, v51\n\t" \
    "v_exp_f32_e32 v52, v52\n\t" \
    "v_mfma_f32_32x32x16_bf16 v[32:47], v[84:87], v[68:71], v[32:47]\n\t" \
    "ds_read_b128 v[80:83], v112 offset:12288\n\t" \
    "ds_read_b128 v[84:87], v112 offset:13312\n\t" \
    "v_cvt_pk_bf16_f32 v104, v48, v49\n\t" \
    "v_cvt_pk_bf16_f32 v105, v50, v51\n\t" \
    "v_exp_f32_e32 v53, v53\n\t" \
    "v_exp_f32_e32 v54, v54\n\t" \
    "v_exp_f32_e32 v55, v55\n\t" \
    "v_mfma_f32_32x32x16_bf16 v[0:15], v[100:103], v[108:111], v[0:15]\n\t" \
    "ds_read_b128 v[92:95], v112 offset:7168\n\t" \
    "ds_read_b128 v[88:91], v112 offset:10240\n\t" \
    "v_exp_f32_e32 v56, v56\n\t" \
    "v_cvt_pk_bf16_f32 v106, v52, v53\n\t" \
    "v_cvt_pk_bf16_f32 v107, v54, v55\n\t" \
    "v_exp_f32_e32 v57, v57\n\t" \
    "v_exp_f32_e32 v58, v58\n\t" \
    "v_exp_f32_e32 v59, v59\n\t" \
    "v_mfma_f32_32x32x16_bf16 v[0:15], v[96:99], v[104:107], v[0:15]\n\t" \
    "v_exp_f32_e32 v60, v60\n\t" \
    "v_exp_f32_e32 v61, v61\n\t" \
    "v_exp_f32_e32 v62, v62\n\t" \
    "v_exp_f32_e32 v63, v63\n\t" \
    "v_cvt_pk_bf16_f32 v108, v56, v57\n\t" \
    "v_cvt_pk_bf16_f32 v109, v58, v59\n\t" \
    "v_cvt_pk_bf16_f32 v110, v60, v61\n\t" \
    "v_cvt_pk_bf16_f32 v111, v62, v63\n\t" \
    "s_setprio 2\n\t" \
    "s_waitcnt lgkmcnt(0)\n\t" \
    "v_mfma_f32_32x32x16_bf16 v[48:63], v[80:83], v[64:67], v[16:31]\n\t" \
    "v_exp_f32_e32 v32, v32\n\t" \
    "v_exp_f32_e32 v33, v33\n\t" \
    "v_exp_f32_e32 v34, v34\n\t" \
    "v_smfmac_f32_16x16x64_bf16 v[76:79], v[72:75], v[104:111], v174\n\t" \
    "v_exp_f32_e32 v35, v35\n\t" \
    "v_exp_f32_e32 v36, v36\n\t" \
    "v_mfma_f32_32x32x16_bf16 v[48:63], v[84:87], v[68:71], v[48:63]\n\t" \
    "ds_read_b128 v[80:83], v112 offset:16384\n\t" \
    "ds_read_b128 v[84:87], v112 offset:17408\n\t" \
    "v_cvt_pk_bf16_f32 v104, v32, v33\n\t" \
    "v_cvt_pk_bf16_f32 v105, v34, v35\n\t" \
    "v_exp_f32_e32 v37, v37\n\t" \
    "v_exp_f32_e32 v38, v38\n\t" \
    "v_exp_f32_e32 v39, v39\n\t" \
    "v_mfma_f32_32x32x16_bf16 v[0:15], v[92:95], v[108:111], v[0:15]\n\t" \
    "ds_read_b128 v[100:103], v112 offset:11264\n\t" \
    "ds_read_b128 v[96:99], v112 offset:14336\n\t" \
    "v_exp_f32_e32 v40, v40\n\t" \
    "v_cvt_pk_bf16_f32 v106, v36, v37\n\t" \
    "v_cvt_pk_bf16_f32 v107, v38, v39\n\t" \
    "v_exp_f32_e32 v41, v41\n\t" \
    "v_exp_f32_e32 v42, v42\n\t" \
    "v_exp_f32_e32 v43, v43\n\t" \
    "v_mfma_f32_32x32x16_bf16 v[0:15], v[88:91], v[104:107], v[0:15]\n\t" \
    "v_exp_f32_e32 v44, v44\n\t" \
    "v_exp_f32_e32 v45, v45\n\t" \
    "v_exp_f32_e32 v46, v46\n\t" \
    "v_exp_f32_e32 v47, v47\n\t" \
    "v_cvt_pk_bf16_f32 v108, v40, v41\n\t" \
    "v_cvt_pk_bf16_f32 v109, v42, v43\n\t" \
    "v_cvt_pk_bf16_f32 v110, v44, v45\n\t" \
    "v_cvt_pk_bf16_f32 v111, v46, v47\n\t" \
    "s_waitcnt lgkmcnt(0)\n\t" \
    "v_mfma_f32_32x32x16_bf16 v[32:47], v[80:83], v[64:67], v[16:31]\n\t" \
    "v_exp_f32_e32 v48, v48\n\t" \
    "v_exp_f32_e32 v49, v49\n\t" \
    "v_exp_f32_e32 v50, v50\n\t" \
    "v_smfmac_f32_16x16x64_bf16 v[76:79], v[72:75], v[104:111], v174\n\t" \
    "v_exp_f32_e32 v51, v51\n\t" \
    "v_exp_f32_e32 v52, v52\n\t" \
    "v_mfma_f32_32x32x16_bf16 v[32:47], v[84:87], v[68:71], v[32:47]\n\t" \
    "ds_read_b128 v[80:83], v112 offset:20480\n\t" \
    "ds_read_b128 v[84:87], v112 offset:21504\n\t" \
    "v_cvt_pk_bf16_f32 v104, v48, v49\n\t" \
    "v_cvt_pk_bf16_f32 v105, v50, v51\n\t" \
    "v_exp_f32_e32 v53, v53\n\t" \
    "v_exp_f32_e32 v54, v54\n\t" \
    "v_exp_f32_e32 v55, v55\n\t" \
    "v_mfma_f32_32x32x16_bf16 v[0:15], v[100:103], v[108:111], v[0:15]\n\t" \
    "ds_read_b128 v[92:95], v112 offset:15360\n\t" \
    "ds_read_b128 v[88:91], v112 offset:18432\n\t" \
    "v_exp_f32_e32 v56, v56\n\t" \
    "v_cvt_pk_bf16_f32 v106, v52, v53\n\t" \
    "v_cvt_pk_bf16_f32 v107, v54, v55\n\t" \
    "v_exp_f32_e32 v57, v57\n\t" \
    "v_exp_f32_e32 v58, v58\n\t" \
    "v_exp_f32_e32 v59, v59\n\t" \
    "v_mfma_f32_32x32x16_bf16 v[0:15], v[96:99], v[104:107], v[0:15]\n\t" \
    "v_exp_f32_e32 v60, v60\n\t" \
    "v_exp_f32_e32 v61, v61\n\t" \
    "v_exp_f32_e32 v62, v62\n\t" \
    "v_exp_f32_e32 v63, v63\n\t" \
    "v_cvt_pk_bf16_f32 v108, v56, v57\n\t" \
    "v_cvt_pk_bf16_f32 v109, v58, v59\n\t" \
    "v_cvt_pk_bf16_f32 v110, v60, v61\n\t" \
    "v_cvt_pk_bf16_f32 v111, v62, v63\n\t" \
    "s_setprio 0\n\t" \
    "s_waitcnt lgkmcnt(0)\n\t" \
    "s_nop 15\n\t"
#define ATT_L2 \
    "s_nop 1\n\t" \
    "s_setprio 1\n\t" \
    "s_waitcnt lgkmcnt(0)\n\t" \
    "v_mfma_f32_32x32x16_bf16 v[48:63], v[80:83], v[64:67], v[16:31]\n\t" \
    "v_exp_f32_e32 v32, v32\n\t" \
    "v_exp_f32_e32 v33, v33\n\t" \
    "v_exp_f32_e32 v34, v34\n\t" \
    "v_smfmac_f32_16x16x64_bf16 v[76:79], v[72:75], v[104:111], v174\n\t" \
    "v_exp_f32_e32 v35, v35\n\t" \
    "v_exp_f32_e32 v36, v36\n\t" \
    "v_mfma_f32_32x32x16_bf16 v[48:63], v[84:87], v[68:71], v[48:63]\n\t" \
    "ds_read_b128 v[80:83], v112 offset:24576\n\t" \
    "ds_read_b128 v[84:87], v112 offset:25600\n\t" \
    "v_cvt_pk_bf16_f32 v104, v32, v33\n\t" \
    "v_cvt_pk_bf16_f32 v105, v34, v35\n\t" \
    "v_exp_f32_e32 v37, v37\n\t" \
    "v_exp_f32_e32 v38, v38\n\t" \
    "v_exp_f32_e32 v39, v39\n\t" \
    "v_mfma_f32_32x32x16_bf16 v[0:15], v[92:95], v[108:111], v[0:15]\n\t" \
    "ds_read_b128 v[100:103], v112 offset:19456\n\t" \
    "ds_read_b128 v[96:99], v112 offset:22528\n\t" \
    "v_exp_f32_e32 v40, v40\n\t" \
    "v_cvt_pk_bf16_f32 v106, v36, v37\n\t" \
    "v_cvt_pk_bf16_f32 v107, v38, v39\n\t" \
    "v_exp_f32_e32 v41, v41\n\t" \
    "v_exp_f32_e32 v42, v42\n\t" \
    "v_exp_f32_e32 v43, v43\n\t" \
    "v_mfma_f32_32x32x16_bf16 v[0:15], v[88:91], v[104:107], v[0:15]\n\t" \
    "v_exp_f32_e32 v44, v44\n\t" \
    "v_exp_f32_e32 v45, v45\n\t" \
    "v_exp_f32_e32 v46, v46\n\t" \
    "v_exp_f32_e32 v47, v47\n\t" \
    "v_cvt_pk_bf16_f32 v108, v40, v41\n\t" \
    "v_cvt_pk_bf16_f32 v109, v42, v43\n\t" \
    "v_cvt_pk_bf16_f32 v110, v44, v45\n\t" \
    "v_cvt_pk_bf16_f32 v111, v46, v47\n\t" \
    "s_waitcnt lgkmcnt(0)\n\t" \
    "v_mfma_f32_32x32x16_bf16 v[32:47], v[80:83], v[64:67], v[16:31]\n\t" \
    "v_exp_f32_e32 v48, v48\n\t" \
    "v_exp_f32_e32 v49, v49\n\t" \
    "v_exp_f32_e32 v50, v50\n\t" \
    "v_smfmac_f32_16x16x64_bf16 v[76:79], v[72:75], v[104:111], v174\n\t" \
    "v_exp_f32_e32 v51, v51\n\t" \
    "v_exp_f32_e32 v52, v52\n\t" \
    "v_mfma_f32_32x32x16_bf16 v[32:47], v[84:87], v[68:71], v[32:47]\n\t" \
    "ds_read_b128 v[80:83], v112 offset:28672\n\t" \
    "ds_read_b128 v[84:87], v112 offset:29696\n\t" \
    "v_cvt_pk_bf16_f32 v104, v48, v49\n\t" \
    "v_cvt_pk_bf16_f32 v105, v50, v51\n\t" \
    "v_exp_f32_e32 v53, v53\n\t" \
    "v_exp_f32_e32 v54, v54\n\t" \
    "v_exp_f32_e32 v55, v55\n\t" \
    "v_mfma_f32_32x32x16_bf16 v[0:15], v[100:103], v[108:111], v[0:15]\n\t" \
    "ds_read_b128 v[92:95], v112 offset:23552\n\t" \
    "ds_read_b128 v[88:91], v112 offset:26624\n\t" \
    "v_exp_f32_e32 v56, v56\n\t" \
    "v_cvt_pk_bf16_f32 v106, v52, v53\n\t" \
    "v_cvt_pk_bf16_f32 v107, v54, v55\n\t" \
    "v_exp_f32_e32 v57, v57\n\t" \
    "v_exp_f32_e32 v58, v58\n\t" \
    "v_exp_f32_e32 v59, v59\n\t" \
    "v_mfma_f32_32x32x16_bf16 v[0:15], v[96:99], v[104:107], v[0:15]\n\t" \
    "v_exp_f32_e32 v60, v60\n\t" \
    "v_exp_f32_e32 v61, v61\n\t" \
    "v_exp_f32_e32 v62, v62\n\t" \
    "v_exp_f32_e32 v63, v63\n\t" \
    "v_cvt_pk_bf16_f32 v108, v56, v57\n\t" \
    "v_cvt_pk_bf16_f32 v109, v58, v59\n\t" \
    "v_cvt_pk_bf16_f32 v110, v60, v61\n\t" \
    "v_cvt_pk_bf16_f32 v111, v62, v63\n\t" \
    "s_setprio 0\n\t" \
    "s_waitcnt lgkmcnt(0)\n\t" \
    "s_nop 15\n\t"
#define ATT_XT \
    "s_nop 1\n\t" \
    "s_waitcnt lgkmcnt(0)\n\t" \
    "v_mfma_f32_32x32x16_bf16 v[48:63], v[80:83], v[64:67], v[16:31]\n\t" \
    "v_exp_f32_e32 v32, v32\n\t" \
    "v_exp_f32_e32 v33, v33\n\t" \
    "v_exp_f32_e32 v34, v34\n\t" \
    "v_smfmac_f32_16x16x64_bf16 v[76:79], v[72:75], v[104:111], v174\n\t" \
    "v_exp_f32_e32 v35, v35\n\t" \
    "v_exp_f32_e32 v36, v36\n\t" \
    "v_mfma_f32_32x32x16_bf16 v[48:63], v[84:87], v[68:71], v[48:63]\n\t" \
    "v_cvt_pk_bf16_f32 v104, v32, v33\n\t" \
    "v_cvt_pk_bf16_f32 v105, v34, v35\n\t" \
    "v_exp_f32_e32 v37, v37\n\t" \
    "v_exp_f32_e32 v38, v38\n\t" \
    "v_exp_f32_e32 v39, v39\n\t" \
    "v_mfma_f32_32x32x16_bf16 v[0:15], v[92:95], v[108:111], v[0:15]\n\t" \
    "ds_read_b128 v[100:103], v112 offset:27648\n\t" \
    "ds_read_b128 v[96:99], v112 offset:30720\n\t" \
    "v_exp_f32_e32 v40, v40\n\t" \
    "v_cvt_pk_bf16_f32 v106, v36, v37\n\t" \
    "v_cvt_pk_bf16_f32 v107, v38, v39\n\t" \
    "v_exp_f32_e32 v41, v41\n\t" \
    "v_exp_f32_e32 v42, v42\n\t" \
    "v_exp_f32_e32 v43, v43\n\t" \
    "v_mfma_f32_32x32x16_bf16 v[0:15], v[88:91], v[104:107], v[0:15]\n\t" \
    "v_exp_f32_e32 v44, v44\n\t" \
    "v_exp_f32_e32 v45, v45\n\t" \
    "v_exp_f32_e32 v46, v46\n\t" \
    "v_exp_f32_e32 v47, v47\n\t" \
    "v_cvt_pk_bf16_f32 v108, v40, v41\n\t" \
    "v_cvt_pk_bf16_f32 v109, v42, v43\n\t" \
    "v_cvt_pk_bf16_f32 v110, v44, v45\n\t" \
    "v_cvt_pk_bf16_f32 v111, v46, v47\n\t" \
    "s_waitcnt lgkmcnt(0)\n\t" \
    "v_exp_f32_e32 v48, v48\n\t" \
    "v_exp_f32_e32 v49, v49\n\t" \
    "v_exp_f32_e32 v50, v50\n\t" \
    "v_smfmac_f32_16x16x64_bf16 v[76:79], v[72:75], v[104:111], v174\n\t" \
    "v_exp_f32_e32 v51, v51\n\t" \
    "v_exp_f32_e32 v52, v52\n\t" \
    "v_cvt_pk_bf16_f32 v104, v48, v49\n\t" \
    "v_cvt_pk_bf16_f32 v105, v50, v51\n\t" \
    "v_exp_f32_e32 v53, v53\n\t" \
    "v_exp_f32_e32 v54, v54\n\t" \
    "v_exp_f32_e32 v55, v55\n\t" \
    "v_mfma_f32_32x32x16_bf16 v[0:15], v[100:103], v[108:111], v[0:15]\n\t" \
    "ds_read_b128 v[92:95], v112 offset:31744\n\t" \
    "v_exp_f32_e32 v56, v56\n\t" \
    "v_cvt_pk_bf16_f32 v106, v52, v53\n\t" \
    "v_cvt_pk_bf16_f32 v107, v54, v55\n\t" \
    "v_exp_f32_e32 v57, v57\n\t" \
    "v_exp_f32_e32 v58, v58\n\t" \
    "v_exp_f32_e32 v59, v59\n\t" \
    "v_mfma_f32_32x32x16_bf16 v[0:15], v[96:99], v[104:107], v[0:15]\n\t" \
    "v_exp_f32_e32 v60, v60\n\t" \
    "v_exp_f32_e32 v61, v61\n\t" \
    "v_exp_f32_e32 v62, v62\n\t" \
    "v_exp_f32_e32 v63, v63\n\t" \
    "v_cvt_pk_bf16_f32 v108, v56, v57\n\t" \
    "v_cvt_pk_bf16_f32 v109, v58, v59\n\t" \
    "v_cvt_pk_bf16_f32 v110, v60, v61\n\t" \
    "v_cvt_pk_bf16_f32 v111, v62, v63\n\t" \
    "s_waitcnt lgkmcnt(0)\n\t" \
    "s_nop 0\n\t" \
    "v_mfma_f32_32x32x16_bf16 v[0:15], v[92:95], v[108:111], v[0:15]\n\t" \
    "v_smfmac_f32_16x16x64_bf16 v[76:79], v[72:75], v[104:111], v174\n\t" \
    "s_nop 15\n\t"
#define ATT_MAIN \
    "s_mov_b64 s[48:49], %[tab]\n\t" \
    "s_mov_b32 s46, %[k0]\n\t" \
    "s_and_b32 s40, s48, 63\n\t" \
    "s_lshl_b32 s40, s40, 12\n\t" \
    "s_add_u32 s44, %[srclo], s40\n\t" \
    "s_addc_u32 s45, %[srchi], 0\n\t" \
    "s_lshr_b64 s[48:49], s[48:49], 6\n\t" \
    "s_add_u32 s46, s46, 1\n\t" \
    "s_mov_b64 vcc, -1\n\t" \
    "s_waitcnt vmcnt(0)\n\t" \
    "ds_read_b64 v[120:121], v173\n\t" \
    "s_waitcnt lgkmcnt(0)\n\t" \
    "1:\n" \
    "s_cmp_eq_u32 %[cnt], 4\n\t" \
    "s_cbranch_scc1 5f\n\t" \
    "s_waitcnt vmcnt(0)\n\t" \
    "5:\n" \
    "s_waitcnt vmcnt(9)\n\t" \
    "s_barrier\n\t" \
    "s_bitcmp1_b32 %[act], %[cnt]\n\t" \
    "s_cbranch_scc1 30f\n\t" \
    "2:\n" \
    "s_setprio 3\n\t" \
    "s_waitcnt lgkmcnt(0)\n\t" \
    "v_mfma_f32_32x32x16_bf16 v[48:63], v[80:83], v[64:67], v[16:31]\n\t" \
    "v_exp_f32_e32 v32, v32\n\t" \
    "v_exp_f32_e32 v33, v33\n\t" \
    "v_exp_f32_e32 v34, v34\n\t" \
    "v_smfmac_f32_16x16x64_bf16 v[76:79], v[72:75], v[104:111], v174\n\t" \
    "v_exp_f32_e32 v35, v35\n\t" \
    "v_exp_f32_e32 v36, v36\n\t" \
    "v_mfma_f32_32x32x16_bf16 v[48:63], v[84:87], v[68:71], v[48:63]\n\t" \
    "ds_read_b128 v[80:83], v113 offset:0\n\t" \
    "ds_read_b128 v[84:87], v113 offset:1024\n\t" \
    "v_cvt_pk_bf16_f32 v104, v32, v33\n\t" \
    "v_cvt_pk_bf16_f32 v105, v34, v35\n\t" \
    "v_exp_f32_e32 v37, v37\n\t" \
    "v_exp_f32_e32 v38, v38\n\t" \
    "v_exp_f32_e32 v39, v39\n\t" \
    "v_mfma_f32_32x32x16_bf16 v[0:15], v[92:95], v[108:111], v[0:15]\n\t" \
    "ds_read_b128 v[100:103], v112 offset:27648\n\t" \
    "ds_read_b128 v[96:99], v112 offset:30720\n\t" \
    "v_exp_f32_e32 v40, v40\n\t" \
    "v_cvt_pk_bf16_f32 v106, v36, v37\n\t" \
    "v_cvt_pk_bf16_f32 v107, v38, v39\n\t" \
    "v_exp_f32_e32 v41, v41\n\t" \
    "v_exp_f32_e32 v42, v42\n\t" \
    "v_exp_f32_e32 v43, v43\n\t" \
    "v_mfma_f32_32x32x16_bf16 v[0:15], v[88:91], v[104:107], v[0:15]\n\t" \
    "s_cbranch_vccz 20f\n\t" \
    "v_cmp_eq_u64_e64 s[42:43], v[120:121], v[140:141]\n\t" \
    "s_mov_b32 m0, %[dst]\n\t" \
    "s_nop 3\n\t" \
    "s_bitcmp1_b64 s[42:43], s46\n\t" \
    "s_cbranch_scc0 9f\n\t" \
    "12:\n" \
    "global_load_lds_dwordx4 v114, s[44:45] offset:0 sc1\n\t" \
    "20:\n" \
    "v_exp_f32_e32 v44, v44\n\t" \
    "v_exp_f32_e32 v45, v45\n\t" \
    "v_exp_f32_e32 v46, v46\n\t" \
    "v_exp_f32_e32 v47, v47\n\t" \
    "v_cvt_pk_bf16_f32 v108, v40, v41\n\t" \
    "v_cvt_pk_bf16_f32 v109, v42, v43\n\t" \
    "v_cvt_pk_bf16_f32 v110, v44, v45\n\t" \
    "v_cvt_pk_bf16_f32 v111, v46, v47\n\t" \
    "s_waitcnt lgkmcnt(0)\n\t" \
    "v_mfma_f32_32x32x16_bf16 v[32:47], v[80:83], v[64:67], v[16:31]\n\t" \
    "v_exp_f32_e32 v48, v48\n\t" \
    "v_exp_f32_e32 v49, v49\n\t" \
    "v_exp_f32_e32 v50, v50\n\t" \
    "v_smfmac_f32_16x16x64_bf16 v[76:79], v[72:75], v[104:111], v174\n\t" \
    "v_exp_f32_e32 v51, v51\n\t" \
    "v_exp_f32_e32 v52, v52\n\t" \
    "v_mfma_f32_32x32x16_bf16 v[32:47], v[84:87], v[68:71], v[32:47]\n\t" \
    "ds_read_b128 v[80:83], v113 offset:4096\n\t" \
    "ds_read_b128 v[84:87], v113 offset:5120\n\t" \
    "v_cvt_pk_bf16_f32 v104, v48, v49\n\t" \
    "v_cvt_pk_bf16_f32 v105, v50, v51\n\t" \
    "v_exp_f32_e32 v53, v53\n\t" \
    "v_exp_f32_e32 v54, v54\n\t" \
    "v_exp_f32_e32 v55, v55\n\t" \
    "v_mfma_f32_32x32x16_bf16 v[0:15], v[100:103], v[108:111], v[0:15]\n\t" \
    "ds_read_b128 v[92:95], v112 offset:31744\n\t" \
    "ds_read_b128 v[88:91], v113 offset:2048\n\t" \
    "v_exp_f32_e32 v56, v56\n\t" \
    "v_cvt_pk_bf16_f32 v106, v52, v53\n\t" \
    "v_cvt_pk_bf16_f32 v107, v54, v55\n\t" \
    "v_exp_f32_e32 v57, v57\n\t" \
    "v_exp_f32_e32 v58, v58\n\t" \
    "v_exp_f32_e32 v59, v59\n\t" \
    "v_mfma_f32_32x32x16_bf16 v[0:15], v[96:99], v[104:107], v[0:15]\n\t" \
    "s_cbranch_vccz 21f\n\t" \
    "global_load_lds_dwordx4 v114, s[44:45] offset:1024 sc1\n\t" \
    "21:\n" \
    "v_exp_f32_e32 v60, v60\n\t" \
    "v_exp_f32_e32 v61, v61\n\t" \
    "v_exp_f32_e32 v62, v62\n\t" \
    "v_exp_f32_e32 v63, v63\n\t" \
    "v_cvt_pk_bf16_f32 v108, v56, v57\n\t" \
    "v_cvt_pk_bf16_f32 v109, v58, v59\n\t" \
    "v_cvt_pk_bf16_f32 v110, v60, v61\n\t" \
    "v_cvt_pk_bf16_f32 v111, v62, v63\n\t" \
    "s_setprio 2\n\t" \
    "s_waitcnt lgkmcnt(0)\n\t" \
    "v_mfma_f32_32x32x16_bf16 v[48:63], v[80:83], v[64:67], v[16:31]\n\t" \
    "v_exp_f32_e32 v32, v32\n\t" \
    "v_exp_f32_e32 v33, v33\n\t" \
    "v_exp_f32_e32 v34, v34\n\t" \
    "v_smfmac_f32_16x16x64_bf16 v[76:79], v[72:75], v[104:111], v174\n\t" \
    "v_exp_f32_e32 v35, v35\n\t" \
    "v_exp_f32_e32 v36, v36\n\t" \
    "v_mfma_f32_32x32x16_bf16 v[48:63], v[84:87], v[68:71], v[48:63]\n\t" \
    "ds_read_b128 v[80:83], v113 offset:8192\n\t" \
    "ds_read_b128 v[84:87], v113 offset:9216\n\t" \
    "v_cvt_pk_bf16_f32 v104, v32, v33\n\t" \
    "v_cvt_pk_bf16_f32 v105, v34, v35\n\t" \
    "v_exp_f32_e32 v37, v37\n\t" \
    "v_exp_f32_e32 v38, v38\n\t" \
    "v_exp_f32_e32 v39, v39\n\t" \
    "v_mfma_f32_32x32x16_bf16 v[0:15], v[92:95], v[108:111], v[0:15]\n\t" \
    "ds_read_b128 v[100:103], v113 offset:3072\n\t" \
    "ds_read_b128 v[96:99], v113 offset:6144\n\t" \
    "v_exp_f32_e32 v40, v40\n\t" \
    "v_cvt_pk_bf16_f32 v106, v36, v37\n\t" \
    "v_cvt_pk_bf16_f32 v107, v38, v39\n\t" \
    "v_exp_f32_e32 v41, v41\n\t" \
    "v_exp_f32_e32 v42, v42\n\t" \
    "v_exp_f32_e32 v43, v43\n\t" \
    "v_mfma_f32_32x32x16_bf16 v[0:15], v[88:91], v[104:107], v[0:15]\n\t" \
    "s_cbranch_vccz 22f\n\t" \
    "global_load_lds_dwordx4 v114, s[44:45] offset:2048 sc1\n\t" \
    "22:\n" \
    "v_exp_f32_e32 v44, v44\n\t" \
    "v_exp_f32_e32 v45, v45\n\t" \
    "v_exp_f32_e32 v46, v46\n\t" \
    "v_exp_f32_e32 v47, v47\n\t" \
    "v_cvt_pk_bf16_f32 v108, v40, v41\n\t" \
    "v_cvt_pk_bf16_f32 v109, v42, v43\n\t" \
    "v_cvt_pk_bf16_f32 v110, v44, v45\n\t" \
    "v_cvt_pk_bf16_f32 v111, v46, v47\n\t" \
    "s_waitcnt lgkmcnt(0)\n\t" \
    "v_mfma_f32_32x32x16_bf16 v[32:47], v[80:83], v[64:67], v[16:31]\n\t" \
    "v_exp_f32_e32 v48, v48\n\t" \
    "v_exp_f32_e32 v49, v49\n\t" \
    "v_exp_f32_e32 v50, v50\n\t" \
    "v_smfmac_f32_16x16x64_bf16 v[76:79], v[72:75], v[104:111], v174\n\t" \
    "v_exp_f32_e32 v51, v51\n\t" \
    "v_exp_f32_e32 v52, v52\n\t" \
    "v_mfma_f32_32x32x16_bf16 v[32:47], v[84:87], v[68:71], v[32:47]\n\t" \
    "ds_read_b128 v[80:83], v113 offset:12288\n\t" \
    "ds_read_b128 v[84:87], v113 offset:13312\n\t" \
    "v_cvt_pk_bf16_f32 v104, v48, v49\n\t" \
    "v_cvt_pk_bf16_f32 v105, v50, v51\n\t" \
    "v_exp_f32_e32 v53, v53\n\t" \
    "v_exp_f32_e32 v54, v54\n\t" \
    "v_exp_f32_e32 v55, v55\n\t" \
    "v_mfma_f32_32x32x16_bf16 v[0:15], v[100:103], v[108:111], v[0:15]\n\t" \
    "ds_read_b128 v[92:95], v113 offset:7168\n\t" \
    "ds_read_b128 v[88:91], v113 offset:10240\n\t" \
    "v_exp_f32_e32 v56, v56\n\t" \
    "v_cvt_pk_bf16_f32 v106, v52, v53\n\t" \
    "v_cvt_pk_bf16_f32 v107, v54, v55\n\t" \
    "v_exp_f32_e32 v57, v57\n\t" \
    "v_exp_f32_e32 v58, v58\n\t" \
    "v_exp_f32_e32 v59, v59\n\t" \
    "v_mfma_f32_32x32x16_bf16 v[0:15], v[96:99], v[104:107], v[0:15]\n\t" \
    "s_cbranch_vccz 23f\n\t" \
    "global_load_lds_dwordx4 v114, s[44:45] offset:3072 sc1\n\t" \
    "global_load_dwordx2 v[120:121], v170, %[ws] sc1\n\t" \
    "23:\n" \
    "v_exp_f32_e32 v60, v60\n\t" \
    "v_exp_f32_e32 v61, v61\n\t" \
    "v_exp_f32_e32 v62, v62\n\t" \
    "v_exp_f32_e32 v63, v63\n\t" \
    "v_cvt_pk_bf16_f32 v108, v56, v57\n\t" \
    "v_cvt_pk_bf16_f32 v109, v58, v59\n\t" \
    "v_cvt_pk_bf16_f32 v110, v60, v61\n\t" \
    "v_cvt_pk_bf16_f32 v111, v62, v63\n\t" \
    "s_setprio 1\n\t" \
    "s_waitcnt lgkmcnt(0)\n\t" \
    "v_mfma_f32_32x32x16_bf16 v[48:63], v[80:83], v[64:67], v[16:31]\n\t" \
    "v_exp_f32_e32 v32, v32\n\t" \
    "v_exp_f32_e32 v33, v33\n\t" \
    "v_exp_f32_e32 v34, v34\n\t" \
    "v_smfmac_f32_16x16x64_bf16 v[76:79], v[72:75], v[104:111], v174\n\t" \
    "v_exp_f32_e32 v35, v35\n\t" \
    "v_exp_f32_e32 v36, v36\n\t" \
    "v_mfma_f32_32x32x16_bf16 v[48:63], v[84:87], v[68:71], v[48:63]\n\t" \
    "ds_read_b128 v[80:83], v113 offset:16384\n\t" \
    "ds_read_b128 v[84:87], v113 offset:17408\n\t" \
    "v_cvt_pk_bf16_f32 v104, v32, v33\n\t" \
    "v_cvt_pk_bf16_f32 v105, v34, v35\n\t" \
    "v_exp_f32_e32 v37, v37\n\t" \
    "v_exp_f32_e32 v38, v38\n\t" \
    "v_exp_f32_e32 v39, v39\n\t" \
    "v_mfma_f32_32x32x16_bf16 v[0:15], v[92:95], v[108:111], v[0:15]\n\t" \
    "ds_read_b128 v[100:103], v113 offset:11264\n\t" \
    "ds_read_b128 v[96:99], v113 offset:14336\n\t" \
    "v_exp_f32_e32 v40, v40\n\t" \
    "v_cvt_pk_bf16_f32 v106, v36, v37\n\t" \
    "v_cvt_pk_bf16_f32 v107, v38, v39\n\t" \
    "v_exp_f32_e32 v41, v41\n\t" \
    "v_exp_f32_e32 v42, v42\n\t" \
    "v_exp_f32_e32 v43, v43\n\t" \
    "v_mfma_f32_32x32x16_bf16 v[0:15], v[88:91], v[104:107], v[0:15]\n\t" \
    "s_add_u32 %[dst], %[dst], 0x8000\n\t" \
    "s_cmp_eq_u32 %[dst], %[dend]\n\t" \
    "s_cselect_b32 %[dst], %[dbeg], %[dst]\n\t" \
    "s_add_u32 %[nx], %[nx], 0x8000\n\t" \
    "s_cmp_eq_u32 %[nx], 0x18000\n\t" \
    "s_cselect_b32 %[nx], 0, %[nx]\n\t" \
    "s_bitcmp1_b32 %[hk], %[cnt]\n\t" \
    "s_cbranch_scc1 40f\n\t" \
    "24:\n" \
    "v_exp_f32_e32 v44, v44\n\t" \
    "v_exp_f32_e32 v45, v45\n\t" \
    "v_exp_f32_e32 v46, v46\n\t" \
    "v_exp_f32_e32 v47, v47\n\t" \
    "v_cvt_pk_bf16_f32 v108, v40, v41\n\t" \
    "v_cvt_pk_bf16_f32 v109, v42, v43\n\t" \
    "v_cvt_pk_bf16_f32 v110, v44, v45\n\t" \
    "v_cvt_pk_bf16_f32 v111, v46, v47\n\t" \
    "s_waitcnt lgkmcnt(0)\n\t" \
    "v_mfma_f32_32x32x16_bf16 v[32:47], v[80:83], v[64:67], v[16:31]\n\t" \
    "v_exp_f32_e32 v48, v48\n\t" \
    "v_exp_f32_e32 v49, v49\n\t" \
    "v_exp_f32_e32 v50, v50\n\t" \
    "v_smfmac_f32_16x16x64_bf16 v[76:79], v[72:75], v[104:111], v174\n\t" \
    "v_exp_f32_e32 v51, v51\n\t" \
    "v_exp_f32_e32 v52, v52\n\t" \
    "v_mfma_f32_32x32x16_bf16 v[32:47], v[84:87], v[68:71], v[32:47]\n\t" \
    "ds_read_b128 v[80:83], v113 offset:20480\n\t" \
    "ds_read_b128 v[84:87], v113 offset:21504\n\t" \
    "v_cvt_pk_bf16_f32 v104, v48, v49\n\t" \
    "v_cvt_pk_bf16_f32 v105, v50, v51\n\t" \
    "v_exp_f32_e32 v53, v53\n\t" \
    "v_exp_f32_e32 v54, v54\n\t" \
    "v_exp_f32_e32 v55, v55\n\t" \
    "v_mfma_f32_32x32x16_bf16 v[0:15], v[100:103], v[108:111], v[0:15]\n\t" \
    "ds_read_b128 v[92:95], v113 offset:15360\n\t" \
    "ds_read_b128 v[88:91], v113 offset:18432\n\t" \
    "v_exp_f32_e32 v56, v56\n\t" \
    "v_cvt_pk_bf16_f32 v106, v52, v53\n\t" \
    "v_cvt_pk_bf16_f32 v107, v54, v55\n\t" \
    "v_exp_f32_e32 v57, v57\n\t" \
    "v_exp_f32_e32 v58, v58\n\t" \
    "v_exp_f32_e32 v59, v59\n\t" \
    "v_mfma_f32_32x32x16_bf16 v[0:15], v[96:99], v[104:107], v[0:15]\n\t" \
    "s_bitcmp1_b32 %[hk], %[cnt]\n\t" \
    "s_cbranch_scc1 41f\n\t" \
    "25:\n" \
    "v_exp_f32_e32 v60, v60\n\t" \
    "v_exp_f32_e32 v61, v61\n\t" \
    "v_exp_f32_e32 v62, v62\n\t" \
    "v_exp_f32_e32 v63, v63\n\t" \
    "v_cvt_pk_bf16_f32 v108, v56, v57\n\t" \
    "v_cvt_pk_bf16_f32 v109, v58, v59\n\t" \
    "v_cvt_pk_bf16_f32 v110, v60, v61\n\t" \
    "v_cvt_pk_bf16_f32 v111, v62, v63\n\t" \
    "s_setprio 0\n\t" \
    "s_waitcnt lgkmcnt(0)\n\t" \
    "v_mfma_f32_32x32x16_bf16 v[48:63], v[80:83], v[64:67], v[16:31]\n\t" \
    "v_exp_f32_e32 v32, v32\n\t" \
    "v_exp_f32_e32 v33, v33\n\t" \
    "v_exp_f32_e32 v34, v34\n\t" \
    "v_smfmac_f32_16x16x64_bf16 v[76:79], v[72:75], v[104:111], v174\n\t" \
    "v_exp_f32_e32 v35, v35\n\t" \
    "v_exp_f32_e32 v36, v36\n\t" \
    "v_mfma_f32_32x32x16_bf16 v[48:63], v[84:87], v[68:71], v[48:63]\n\t" \
    "ds_read_b128 v[80:83], v113 offset:24576\n\t" \
    "ds_read_b128 v[84:87], v113 offset:25600\n\t" \
    "v_cvt_pk_bf16_f32 v104, v32, v33\n\t" \
    "v_cvt_pk_bf16_f32 v105, v34, v35\n\t" \
    "v_exp_f32_e32 v37, v37\n\t" \
    "v_exp_f32_e32 v38, v38\n\t" \
    "v_exp_f32_e32 v39, v39\n\t" \
    "v_mfma_f32_32x32x16_bf16 v[0:15], v[92:95], v[108:111], v[0:15]\n\t" \
    "ds_read_b128 v[100:103], v113 offset:19456\n\t" \
    "ds_read_b128 v[96:99], v113 offset:22528\n\t" \
    "v_exp_f32_e32 v40, v40\n\t" \
    "v_cvt_pk_bf16_f32 v106, v36, v37\n\t" \
    "v_cvt_pk_bf16_f32 v107, v38, v39\n\t" \
    "v_exp_f32_e32 v41, v41\n\t" \
    "v_exp_f32_e32 v42, v42\n\t" \
    "v_exp_f32_e32 v43, v43\n\t" \
    "v_mfma_f32_32x32x16_bf16 v[0:15], v[88:91], v[104:107], v[0:15]\n\t" \
    "s_and_b32 s40, s48, 63\n\t" \
    "s_lshl_b32 s40, s40, 12\n\t" \
    "s_add_u32 s44, %[srclo], s40\n\t" \
    "s_addc_u32 s45, %[srchi], 0\n\t" \
    "s_lshr_b64 s[48:49], s[48:49], 6\n\t" \
    "s_add_u32 s46, s46, 1\n\t" \
    "s_bitcmp1_b32 %[hk], %[cnt]\n\t" \
    "s_cbranch_scc1 42f\n\t" \
    "26:\n" \
    "v_exp_f32_e32 v44, v44\n\t" \
    "v_exp_f32_e32 v45, v45\n\t" \
    "v_exp_f32_e32 v46, v46\n\t" \
    "v_exp_f32_e32 v47, v47\n\t" \
    "v_cvt_pk_bf16_f32 v108, v40, v41\n\t" \
    "v_cvt_pk_bf16_f32 v109, v42, v43\n\t" \
    "v_cvt_pk_bf16_f32 v110, v44, v45\n\t" \
    "v_cvt_pk_bf16_f32 v111, v46, v47\n\t" \
    "s_waitcnt lgkmcnt(0)\n\t" \
    "v_mfma_f32_32x32x16_bf16 v[32:47], v[80:83], v[64:67], v[16:31]\n\t" \
    "v_exp_f32_e32 v48, v48\n\t" \
    "v_exp_f32_e32 v49, v49\n\t" \
    "v_exp_f32_e32 v50, v50\n\t" \
    "v_smfmac_f32_16x16x64_bf16 v[76:79], v[72:75], v[104:111], v174\n\t" \
    "v_exp_f32_e32 v51, v51\n\t" \
    "v_exp_f32_e32 v52, v52\n\t" \
    "v_mfma_f32_32x32x16_bf16 v[32:47], v[84:87], v[68:71], v[32:47]\n\t" \
    "ds_read_b128 v[80:83], v113 offset:28672\n\t" \
    "ds_read_b128 v[84:87], v113 offset:29696\n\t" \
    "v_cvt_pk_bf16_f32 v104, v48, v49\n\t" \
    "v_cvt_pk_bf16_f32 v105, v50, v51\n\t" \
    "v_exp_f32_e32 v53, v53\n\t" \
    "v_exp_f32_e32 v54, v54\n\t" \
    "v_exp_f32_e32 v55, v55\n\t" \
    "v_mfma_f32_32x32x16_bf16 v[0:15], v[100:103], v[108:111], v[0:15]\n\t" \
    "ds_read_b128 v[92:95], v113 offset:23552\n\t" \
    "ds_read_b128 v[88:91], v113 offset:26624\n\t" \
    "v_exp_f32_e32 v56, v56\n\t" \
    "v_cvt_pk_bf16_f32 v106, v52, v53\n\t" \
    "v_cvt_pk_bf16_f32 v107, v54, v55\n\t" \
    "v_exp_f32_e32 v57, v57\n\t" \
    "v_exp_f32_e32 v58, v58\n\t" \
    "v_exp_f32_e32 v59, v59\n\t" \
    "v_mfma_f32_32x32x16_bf16 v[0:15], v[96:99], v[104:107], v[0:15]\n\t" \
    "s_bitcmp1_b32 %[hk], %[cnt]\n\t" \
    "s_cbranch_scc1 43f\n\t" \
    "27:\n" \
    "v_mov_b32_e32 v112, v113\n\t" \
    "v_add_u32_e32 v113, %[nx], v115\n\t" \
    "s_sub_u32 %[cnt], %[cnt], 1\n\t" \
    "s_cmp_lg_u32 %[cnt], 0\n\t" \
    "v_exp_f32_e32 v60, v60\n\t" \
    "v_exp_f32_e32 v61, v61\n\t" \
    "v_exp_f32_e32 v62, v62\n\t" \
    "v_exp_f32_e32 v63, v63\n\t" \
    "v_cvt_pk_bf16_f32 v108, v56, v57\n\t" \
    "v_cvt_pk_bf16_f32 v109, v58, v59\n\t" \
    "v_cvt_pk_bf16_f32 v110, v60, v61\n\t" \
    "v_cvt_pk_bf16_f32 v111, v62, v63\n\t" \
    "s_cbranch_scc1 1b\n\t" \
    "7:\n" \
    "s_setprio 0\n\t" \
    "s_waitcnt lgkmcnt(0)\n\t" \
    "v_mfma_f32_32x32x16_bf16 v[48:63], v[80:83], v[64:67], v[16:31]\n\t" \
    "v_exp_f32_e32 v32, v32\n\t" \
    "v_exp_f32_e32 v33, v33\n\t" \
    "v_exp_f32_e32 v34, v34\n\t" \
    "v_smfmac_f32_16x16x64_bf16 v[76:79], v[72:75], v[104:111], v174\n\t" \
    "v_exp_f32_e32 v35, v35\n\t" \
    "v_exp_f32_e32 v36, v36\n\t" \
    "v_mfma_f32_32x32x16_bf16 v[48:63], v[84:87], v[68:71], v[48:63]\n\t" \
    "v_cvt_pk_bf16_f32 v104, v32, v33\n\t" \
    "v_cvt_pk_bf16_f32 v105, v34, v35\n\t" \
    "v_exp_f32_e32 v37, v37\n\t" \
    "v_exp_f32_e32 v38, v38\n\t" \
    "v_exp_f32_e32 v39, v39\n\t" \
    "v_mfma_f32_32x32x16_bf16 v[0:15], v[92:95], v[108:111], v[0:15]\n\t" \
    "ds_read_b128 v[100:103], v112 offset:27648\n\t" \
    "ds_read_b128 v[96:99], v112 offset:30720\n\t" \
    "v_exp_f32_e32 v40, v40\n\t" \
    "v_cvt_pk_bf16_f32 v106, v36, v37\n\t" \
    "v_cvt_pk_bf16_f32 v107, v38, v39\n\t" \
    "v_exp_f32_e32 v41, v41\n\t" \
    "v_exp_f32_e32 v42, v42\n\t" \
    "v_exp_f32_e32 v43, v43\n\t" \
    "v_mfma_f32_32x32x16_bf16 v[0:15], v[88:91], v[104:107], v[0:15]\n\t" \
    "v_exp_f32_e32 v44, v44\n\t" \
    "v_exp_f32_e32 v45, v45\n\t" \
    "v_exp_f32_e32 v46, v46\n\t" \
    "v_exp_f32_e32 v47, v47\n\t" \
    "v_cvt_pk_bf16_f32 v108, v40, v41\n\t" \
    "v_cvt_pk_bf16_f32 v109, v42, v43\n\t" \
    "v_cvt_pk_bf16_f32 v110, v44, v45\n\t" \
    "v_cvt_pk_bf16_f32 v111, v46, v47\n\t" \
    "s_waitcnt lgkmcnt(0)\n\t" \
    "v_exp_f32_e32 v48, v48\n\t" \
    "v_exp_f32_e32 v49, v49\n\t" \
    "v_exp_f32_e32 v50, v50\n\t" \
    "v_smfmac_f32_16x16x64_bf16 v[76:79], v[72:75], v[104:111], v174\n\t" \
    "v_exp_f32_e32 v51, v51\n\t" \
    "v_exp_f32_e32 v52, v52\n\t" \
    "v_cvt_pk_bf16_f32 v104, v48, v49\n\t" \
    "v_cvt_pk_bf16_f32 v105, v50, v51\n\t" \
    "v_exp_f32_e32 v53, v53\n\t" \
    "v_exp_f32_e32 v54, v54\n\t" \
    "v_exp_f32_e32 v55, v55\n\t" \
    "v_mfma_f32_32x32x16_bf16 v[0:15], v[100:103], v[108:111], v[0:15]\n\t" \
    "ds_read_b128 v[92:95], v112 offset:31744\n\t" \
    "v_exp_f32_e32 v56, v56\n\t" \
    "v_cvt_pk_bf16_f32 v106, v52, v53\n\t" \
    "v_cvt_pk_bf16_f32 v107, v54, v55\n\t" \
    "v_exp_f32_e32 v57, v57\n\t" \
    "v_exp_f32_e32 v58, v58\n\t" \
    "v_exp_f32_e32 v59, v59\n\t" \
    "v_mfma_f32_32x32x16_bf16 v[0:15], v[96:99], v[104:107], v[0:15]\n\t" \
    "v_exp_f32_e32 v60, v60\n\t" \
    "v_exp_f32_e32 v61, v61\n\t" \
    "v_exp_f32_e32 v62, v62\n\t" \
    "v_exp_f32_e32 v63, v63\n\t" \
    "v_cvt_pk_bf16_f32 v108, v56, v57\n\t" \
    "v_cvt_pk_bf16_f32 v109, v58, v59\n\t" \
    "v_cvt_pk_bf16_f32 v110, v60, v61\n\t" \
    "v_cvt_pk_bf16_f32 v111, v62, v63\n\t" \
    "s_waitcnt lgkmcnt(0)\n\t" \
    "s_nop 0\n\t" \
    "v_mfma_f32_32x32x16_bf16 v[0:15], v[92:95], v[108:111], v[0:15]\n\t" \
    "v_smfmac_f32_16x16x64_bf16 v[76:79], v[72:75], v[104:111], v174\n\t" \
    "s_nop 15\n\t" \
    "s_branch 99f\n\t" \
    "9:\n" \
    "s_mov_b32 s47, 0x40000\n\t" \
    "10:\n" \
    "s_sub_u32 s47, s47, 1\n\t" \
    "s_cbranch_scc1 12b\n\t" \
    "global_load_dwordx2 v[120:121], v170, %[ws] sc1\n\t" \
    "s_waitcnt vmcnt(0)\n\t" \
    "v_cmp_eq_u64_e64 s[42:43], v[120:121], v[140:141]\n\t" \
    "s_nop 4\n\t" \
    "s_bitcmp1_b64 s[42:43], s46\n\t" \
    "s_cbranch_scc1 12b\n\t" \
    "s_sleep 4\n\t" \
    "s_branch 10b\n\t" \
    "50:\n" \
    "global_store_dwordx4 v171, v[176:179], %[ws] offset:-3072 sc1\n\t" \
    "global_store_dwordx4 v171, v[180:183], %[ws] offset:-2944 sc1\n\t" \
    "global_store_dwordx4 v172, v[184:187], %[ws] offset:-1024 sc1\n\t" \
    "global_store_dwordx4 v172, v[188:191], %[ws] offset:-768 sc1\n\t" \
    "s_branch 24b\n\t" \
    "51:\n" \
    "global_store_dwordx4 v171, v[192:195], %[ws] offset:1024 sc1\n\t" \
    "global_store_dwordx4 v171, v[196:199], %[ws] offset:1152 sc1\n\t" \
    "global_store_dwordx4 v172, v[200:203], %[ws] offset:3072 sc1\n\t" \
    "global_store_dwordx4 v172, v[204:207], %[ws] offset:3328 sc1\n\t" \
    "s_branch 25b\n\t" \
    "40:\n" \
    "s_cmp_eq_u32 %[cnt], 7\n\t" \
    "s_cbranch_scc1 50b\n\t" \
    "s_add_u32 m0, %[wdst], 0\n\t" \
    "s_nop 0\n\t" \
    "global_load_lds_dwordx4 v116, %[wo01]\n\t" \
    "global_load_lds_dwordx4 v117, %[wo01] offset:1024\n\t" \
    "v_add_u32_e32 v116, 0x20000, v116\n\t" \
    "v_add_u32_e32 v117, 0x20000, v117\n\t" \
    "s_branch 24b\n\t" \
    "41:\n" \
    "s_cmp_eq_u32 %[cnt], 7\n\t" \
    "s_cbranch_scc1 51b\n\t" \
    "s_add_u32 m0, %[wdst], 16384\n\t" \
    "s_nop 0\n\t" \
    "global_load_lds_dwordx4 v116, %[wo01]\n\t" \
    "global_load_lds_dwordx4 v117, %[wo01] offset:1024\n\t" \
    "v_subrev_u32_e32 v116, 0x20000, v116\n\t" \
    "v_subrev_u32_e32 v117, 0x20000, v117\n\t" \
    "s_branch 25b\n\t" \
    "42:\n" \
    "s_cmp_eq_u32 %[cnt], 7\n\t" \
    "s_cbranch_scc1 26b\n\t" \
    "s_add_u32 m0, %[wdst], 32768\n\t" \
    "s_nop 0\n\t" \
    "global_load_lds_dwordx4 v116, %[wo23]\n\t" \
    "global_load_lds_dwordx4 v117, %[wo23] offset:1024\n\t" \
    "v_add_u32_e32 v116, 0x20000, v116\n\t" \
    "v_add_u32_e32 v117, 0x20000, v117\n\t" \
    "s_branch 26b\n\t" \
    "43:\n" \
    "s_cmp_eq_u32 %[cnt], 7\n\t" \
    "s_cbranch_scc1 27b\n\t" \
    "s_add_u32 m0, %[wdst], 49152\n\t" \
    "s_nop 0\n\t" \
    "global_load_lds_dwordx4 v116, %[wo23]\n\t" \
    "global_load_lds_dwordx4 v117, %[wo23] offset:1024\n\t" \
    "global_load_dword v168, v119, %[wo]\n\t" \
    "s_branch 27b\n\t" \
    "30:\n" \
    "s_cmp_eq_u32 %[cnt], 1\n\t" \
    "s_cbranch_scc0 31f\n\t" \
    "s_mov_b64 vcc, 0\n\t" \
    "s_bitcmp1_b32 %[mode], 1\n\t" \
    "s_cbranch_scc1 7b\n\t" \
    "s_branch 2b\n\t" \
    "31:\n" \
    "s_cmp_eq_u32 %[cnt], 6\n\t" \
    "s_cbranch_scc0 33f\n\t" \
    "global_store_dwordx2 v175, v[140:141], %[ws] sc1\n\t" \
    "s_branch 2b\n\t" \
    "33:\n" \
    "s_cmp_eq_u32 %[cnt], 5\n\t" \
    "s_cbranch_scc0 34f\n\t" \
    "global_store_dwordx2 v118, v[140:141], %[ws] sc1\n\t" \
    "s_branch 2b\n\t" \
    "34:\n" \
    "s_cmp_eq_u32 %[cnt], 3\n\t" \
    "s_cbranch_scc0 35f\n\t" \
    "global_load_dwordx2 v[168:169], v142, %[ws] sc1\n\t" \
    "s_branch 2b\n\t" \
    "35:\n" \
    "v_cmp_eq_u64_e64 s[42:43], v[168:169], v[140:141]\n\t" \
    "s_nop 4\n\t" \
    "s_cmp_eq_u64 s[42:43], -1\n\t" \
    "s_cbranch_scc0 2b\n\t" \
    "global_load_dwordx4 v[122:125], v143, %[ws] offset:-2048 sc1\n\t" \
    "global_load_dwordx4 v[126:129], v143, %[ws] offset:-1024 sc1\n\t" \
    "global_load_dwordx4 v[130:133], v143, %[ws] offset:0 sc1\n\t" \
    "global_load_dwordx4 v[134:137], v143, %[ws] offset:1024 sc1\n\t" \
    "global_load_dwordx2 v[138:139], v143, %[ws] offset:2048 sc1\n\t" \
    "s_bitset1_b32 %[mode], 2\n\t" \
    "s_branch 2b\n\t" \
    "99:\n"
#define ATT_TMP "v48", "v49", "v50", "v51", "v52", "v53", "v54", "v55", "v56", "v57", "v58", "v59", "v60", "v61", "v62", "v63", "v96", "v97", "v98", "v99", "v100", "v101", "v102", "v103"

constexpr int K1_X = 0, K1_W = 81920, K1_WB = 24576;
constexpr int K2_SLOT = 32768, K2_QX = 3 * K2_SLOT, K2_FL = 147456;
constexpr int LDS_BYTES = 163840;
constexpr int p3_wslot(int t) { return (t % 6) < 4 ? 98304 + (t % 6) * 16384 : 65536 + ((t % 6) - 4) * 16384; }
constexpr int p3_allow(int t) { return (((t < 2 || t - 2 >= 1) && t + 3 < 16) ? 4 : 0) + (t + 3 < 16 ? 4 : 0) + ((t >= 1 && t + 5 < 16) ? 4 : 0); }

__global__ __launch_bounds__(512, 2) void mk_fused(const u16* __restrict__ xA, const u16* __restrict__ wA, const u16* __restrict__ woA,
                                                   const u16* __restrict__ xB, const u16* __restrict__ wB, const u16* __restrict__ woB, char* ws0, u16* __restrict__ out) {
    char* ws = ws0;
    __shared__ __attribute__((aligned(1024))) char smem[LDS_BYTES];
    const unsigned lds0 = (unsigned)(uintptr_t)(LAS char*)smem;
    const int wid = __builtin_amdgcn_readfirstlane(threadIdx.x >> 6);
    int lane = threadIdx.x & 63;
#define BRX 1
    const int b = blockIdx.x, xcd = b & 7, j = b >> 3, br = (xcd >> 2) ^ BRX, qd = xcd & 3;
    const int mt = (qd & 1) * 4 + (j & 3), hp = (qd >> 1) * 8 + (j >> 2);
    const int mt3 = qd * 4 + (j & 3), nt3 = j >> 2;
    u64 tag;
    unsigned pst[8];
    u32x4 fq[2][2], fk[2][2], fv[2][2];
    f32x16 band;
#define RBLK(k, w) ((((k) + (w) - (((k) + (w) >= 7) ? 7 : 0)) + (((k) + (w) - (((k) + (w) >= 7) ? 7 : 0)) >= mt)) * 8 + (k) + ((k) + (w) >= 7))
    u64 tabf;
    unsigned c_fl, c_pv, c_wb, c_tw, c_aw, c_fo;
#define PRECOMPUTE(BAND) do { const int g_ = wid >> 2, wi_ = wid & 3, c16_ = lane & 15, g4_ = lane >> 4; \
        if (BAND) band = band_mask(lane); \
        tabf = (u64)(mt * 8 + wid) << 42; \
        _Pragma("unroll") for (int k = 0; k < 7; ++k) tabf |= (u64)RBLK(k, wid) << (6 * k); \
        c_fl = (unsigned)WS_FK + (2 * hp + br) * 32 * FS + (((unsigned)(tabf >> (6 * (lane & 7))) & 63u) >> 1) * FS;        \
        c_wb = g4_ * 2048 + ((c16_ ^ (g4_ << 1)) << 4); \
        { const int i = (mt3 * 8 + wid) * 12 + ((lane * 3) >> 4); c_tw = ((((4 + (i >> 7) + 2 * nt3) & 15) * 64 + ((i & 127) >> 1)) * 2048 + (i & 1) * 128); } \
        { const int q4 = c16_ >> 2, p = lane & 3, k = 16 * (g4_ >> 1) + 4 * (g4_ & 1) + q4; \
          c_aw = lds0 + p3_wslot(0) + g_ * 16384 + k * 256 + (((8 * (wi_ & 1) + (p >> 1)) ^ ((k & 7) << 1)) << 4) + (p & 1) * 8; } \
        c_fo = (unsigned)WS_FO + ((br * 64 + mt3 * 4 + wi_) * 32 + 4 * (c16_ >> 1) + 2 * (1 - g_) + (lane & 1)) * FS; \
        asm volatile("" : "+v"(band), "+s"(tabf), "+v"(c_fl), "+v"(c_wb), "+v"(c_tw), "+v"(c_aw), "+v"(c_fo)); } while (0)
    STAMP_AT(0, 0);
    {
        const int g = wid >> 2, wi = wid & 3, mw = wi >> 1, hd = wi & 1;
        if (wid == 7) {
            const char* pc; unsigned len;
            asm volatile("s_getpc_b64 %0\n.Lmk_code_begin:\n\ts_mov_b32 %1, .Lmk_code_end-.Lmk_code_begin" : "=s"(pc), "=s"(len));
            const unsigned off = (unsigned)(j * 64 + lane) * 64u;
            asm volatile("s_mov_b32 m0, %2\n\ts_nop 0\n\tglobal_load_lds_dword %0, %1" :: "v"(off < len ? off : 0u), "s"(pc), "s"(lds0 + 159744) : "memory");
        }
        unsigned vx[4], vw;
        { const unsigned r8 = lane >> 3, c8 = lane & 7, b1 = (lane >> 4) & 1;
          vw = (wid * 8 + r8) * 6144 + ((c8 ^ ((b1 | ((wid & 1) << 1)) << 1)) << 4);
          const unsigned st = (16384u << br) - 1024u;
          vx[0] = (((r8 << br) + (br ? (wi >> 1) * 64 + (wi & 1) : wi * 32)) << 11) + ((c8 ^ (b1 | ((lane >> 5) << 2))) << 4);
          vx[1] = vx[0] + st; vx[2] = (vx[0] ^ 32) + 2 * st; vx[3] = vx[2] + st; }
        asm volatile("" : "+v"(vx[0]), "+v"(vx[1]), "+v"(vx[2]), "+v"(vx[3]), "+v"(vw));
        SBAR();
        const char* x = (const char*)(br ? xB : xA) + (size_t)mt * 256 * 2048;
        const char* w = (const char*)(br ? wB : wA) + hp * 128;
        const unsigned xdst_o = lds0 + K1_X + wi * 4096;
        const unsigned wdst = lds0 + K1_W + wid * 1024;
        const char* xo = x + (size_t)g * 128 * 2048;
#define ROTM 4
        const int kt0 = ROTM * (j & 3);
#define STAGE_X(kt, dst) do { const char* s_ = xo + (((kt) + kt0) & 15) * 128; glds_run<0>(s_, vx[0], dst); glds_run<1>(s_, vx[1], 0); glds_run<2>(s_, vx[2], 0); glds_run<3>(s_, vx[3], 0); } while (0)
#define STAGE_W(kt, buf) do { const char* s_ = w + (size_t)(((kt) + kt0) & 15) * 64 * 6144; glds_sec<0>(s_, vw, wdst + (buf) * K1_WB); glds_sec<1>(s_, vw, wdst + (buf) * K1_WB); glds_sec<2>(s_, vw, wdst + (buf) * K1_WB); } while (0)
        JIT(14);
        STAGE_W(0, 0); STAGE_X(0, xdst_o + g * 16384); STAGE_W(1, 1); STAGE_X(1, xdst_o + (2 + g) * 16384);
        { u64 e; asm volatile("s_load_dwordx2 %0, %1, 0x0\n\ts_waitcnt lgkmcnt(0)" : "=s"(e) : "s"(ws + WS_EP) : "memory");
          e += 0x9E3779B97F4A7C15ull; e = (e ^ (e >> 30)) * 0xBF58476D1CE4E5B9ull; e = (e ^ (e >> 27)) * 0x94D049BB133111EBull; tag = e ^ (e >> 31); }
        const int c16 = lane & 15, g4 = lane >> 4;
        unsigned ax[2], aw[2];
        ax[0] = lds0 + K1_X + g * 16384 + (mw * 64 + 16 * (c16 >> 3) + (c16 & 7)) * 128 + ((g4 ^ (((c16 >> 1) & 1) | ((c16 >> 3) << 1) | (((c16 >> 2) & 1) << 2))) << 4);
        ax[1] = ax[0] ^ 64;
        { const int q4 = c16 >> 2, p = lane & 3;
          aw[0] = lds0 + K1_W + (8 * g4 + q4) * 128 + (((hd * 4 + (p >> 1)) ^ ((((q4 >> 1) & 1) | ((g4 & 1) << 1)) << 1)) << 4) + (p & 1) * 8; aw[1] = aw[0] ^ 32; }
        if (g == 1) WAITBAR_(11); else WAITBAR_(7);
        SA_(1);
        if (g == 1) { PRECOMPUTE(true); WAITBAR_(7); }
        f32x4 acc[4][6];
#pragma unroll
        for (int m = 0; m < 4; ++m)
#pragma unroll
            for (int n = 0; n < 6; ++n) acc[m][n] = f32x4{};
        SBAR();
        unsigned wr_ = 0, xr_ = g * 16384, dw = wdst + 2 * K1_WB, dx = xdst_o + (g ? 0 : 4 * 16384);
        int tk = (2 + kt0) & 15;
        const char* sx = xo + tk * 128; const char* sw = w + (size_t)tk * 64 * 6144;
#define RDX(m) do { xf[m][0] = ldsrd128(ax[0] + ((m) >> 1) * 4096 + ((m) & 1) * 1024); xf[m][1] = ldsrd128(ax[1] + ((m) >> 1) * 4096 + ((m) & 1) * 1024); } while (0)
#define RDW(n, ks) wf[n][ks] = cat8(ldstr(aw[(n) & 1] + ((n) >> 1) * 8192 + (ks) * 4096), ldstr(aw[(n) & 1] + ((n) >> 1) * 8192 + (ks) * 4096 + 512))
#define MM(m, n, k) do { if ((n) < 4) acc[m][n] = MFMA16(wf[n][k], xf[m][k], acc[m][n]); else acc[m][n] = MFMA16(xf[m][k], wf[n][k], acc[m][n]); } while (0)
#define P1_MMA() _Pragma("unroll") for (int m = 0; m < 4; ++m) _Pragma("unroll") for (int n = 0; n < 6; ++n) { MM(m, n, 0); MM(m, n, 1); CHAIN(); }
#define P1_TILE(DO, WAIT1, WAIT2) do { \
            bf16x8 xf[4][2], wf[6][2]; \
            RDX(0); RDW(0, 0); if (DO) glds_sec<0>(sw, vw, dw); \
            RDW(0, 1); RDW(1, 0); if (DO) glds_sec<1>(sw, vw, dw); \
            RDX(1); RDW(1, 1); if (DO) glds_sec<2>(sw, vw, dw); \
            RDW(2, 0); RDW(2, 1); if (DO) glds_run<0>(sx, vx[0], dx); \
            RDX(2); RDW(3, 0); if (DO) glds_run<1>(sx, vx[1], 0); \
            RDW(3, 1); RDW(4, 0); if (DO) glds_run<2>(sx, vx[2], 0); \
            RDX(3); RDW(4, 1); if (DO) glds_run<3>(sx, vx[3], 0); \
            RDW(5, 0); RDW(5, 1); \
            SBAR(); \
            {   \
              const unsigned d_ = (wr_ == 2 * K1_WB) ? (unsigned)(-2 * K1_WB) : (unsigned)K1_WB, e_ = (xr_ >= 3 * 16384) ? (unsigned)(-3 * 16384) : (unsigned)(2 * 16384); \
              dw = wdst + wr_; wr_ += d_; aw[0] += d_; aw[1] += d_; dx = xdst_o + ((xr_ == 4 * 16384) ? 0u : xr_ + 16384); xr_ += e_; ax[0] += e_; ax[1] += e_; \
              tk = (tk + 1) & 15; sx = xo + tk * 128; sw = w + (size_t)tk * 64 * 6144; \
              asm volatile("" : "+v"(aw[0]), "+v"(aw[1]), "+v"(ax[0]), "+v"(ax[1]), "+s"(sx), "+s"(sw)); } \
            WAIT1; \
            SBAR(); \
            __builtin_amdgcn_s_setprio(1); \
            P1_MMA(); \
            __builtin_amdgcn_s_setprio(0); \
            SBAR(); \
            WAIT2; \
            SBAR(); \
        } while (0)
#pragma unroll 1
        for (int t = 0; t < 14; ++t) P1_TILE(true, WAITBAR_(11), WAITBAR_(7));
        P1_TILE(false, WAITBAR_(4), WAITBAR_(0));
        P1_TILE(false, WAITBAR_(0), if (g == 0) asm volatile("s_barrier" ::: "memory"));
#undef P1_TILE
#undef RDW
#undef RDX
#undef STAGE_X
#undef STAGE_W
        SA_(2); SC_(1);
        JIT(1);
        FRESH();
#pragma unroll
        for (int mb = 0; mb < 2; ++mb)
#pragma unroll
            for (int u = 0; u < 2; ++u) {
                const f32x4 a = acc[2 * mb + u][0] * QSCALE, c = acc[2 * mb + u][1] * QSCALE;
                fq[mb][u] = u32x4{cvtpk(a[0], a[1]), cvtpk(a[2], a[3]), cvtpk(c[0], c[1]), cvtpk(c[2], c[3])};
                fk[mb][u] = pack44(acc[2 * mb + u][2], acc[2 * mb + u][3]);
                fv[mb][u] = pack44(acc[2 * mb][4 + u], acc[2 * mb + 1][4 + u]);
            }
    }
    const char* wo = (const char*)(br ? woB : woA) + nt3 * 256;
    const int k30 = 2 * nt3;
    {
        const int g = wid >> 2, wr = wid >> 1, hd = wid & 1;
        const int ph1 = 2 * hp + hd, pb1 = mt * 8 + wr * 2;
        const int head = 2 * hp + br, blk = mt * 8 + wid;
        const char* kv = ws + WS_KV + (size_t)head * 262144;
        unsigned l16 = lane * 16;
        const unsigned dbeg = lds0 + wid * 4096, lbase = lds0 + l16;
        const unsigned fk0 = (unsigned)WS_FK + head * 32 * FS;
        const unsigned fxo = (unsigned)WS_FX + ((hp * 8 + mt) * 8 + wid) * FS, xrec0 = (unsigned)WS_XP + ((hp * 8 + mt) * 8 + wid) * 5120 + 2048;
        const f32x16 z = {};
        f32x16 sa, negm, o;
        u32x16 qol, kvf, pub0, pub1; u32x8 ph;
        const unsigned sidx = (lane & 1) ? 0xEEEEEEEEu : 0x44444444u;

#define FIRST_TILE(base, q0, q1) do { const unsigned on_ = (lane & 2) ? 0u : 0x3F803F80u, e_ = (lane & 4) ? 0u : on_, f_ = (lane & 4) ? on_ : 0u; const u32x4 ones = {e_, e_, f_, f_}; \
            const bf16x8 k0_ = ldsrd128(base), k1_ = ldsrd128((base) + 1024); kvf = cat16(asu(ldsrd128((base) + 4096)), asu(ldsrd128((base) + 5120)), asu(ldsrd128((base) + 2048)), ones); \
            sa = MFMA32(k0_, q0, z); sa = MFMA32(k1_, q1, sa); float m_ = sa[0]; \
            _Pragma("unroll") for (int r = 1; r < 16; ++r) m_ = __builtin_fmaxf(m_, sa[r]); \
            m_ = -swap_max(m_); _Pragma("unroll") for (int r = 0; r < 16; ++r) { negm[r] = m_; sa[r] += m_; } \
            qol = cat16(asu(q0), asu(q1), ones, u32x4{0u, 0u, 0u, 0u}); } while (0)
#define L6_OUT "={v[0:15]}"(o), "+{v[32:47]}"(sa), "+{v[64:79]}"(qol), "+{v[80:95]}"(kvf), "={v[104:111]}"(ph)
#define L6C(HL, BASE) asm volatile(ATT_L6C : L6_OUT, "={v[144:159]}"(h0), "={v[160:167]}"(h1) \
            : "{v[16:31]}"(negm), "{v174}"(sidx), "{v112}"(BASE), "{v[140:143]}"(u32x4{0u, 0u, c_fl, (unsigned)WS_HALO + (mt * 32 + ph1) * 6144 + 3072 + l16}), \
              [ws] "s"(ws), [hl] "s"(HL), [fld] "s"(fld) : ATT_TMP, "scc", "memory")
        const unsigned own = (ph1 * 32 + mt * 4 + wr) * FS;
        const unsigned fld = lds0 + K2_FL + wid * 1024 + br * (2 * K2_SLOT + 4096 - K2_FL + wid * 11264);
        c_pv = (lane >> 5) * 1024 + ((lane & 15) + 32 * ((lane >> 4) & 1)) * 16;
        const unsigned pv = c_pv, pq = pv + (lane & 8) * 16;
        if (br == 0) {
            const char* kp = ws + WS_KV + (size_t)((ph1 * 64 + pb1) * 4) * 1024;
#define PUB_KV() do { st16_dev<0>(kp, pq, fk[0][0]); st16_dev<128>(kp, pq, fk[0][1]); st16_dev<2048>(kp, pv, fv[0][0]); st16_dev<2304>(kp, pv, fv[0][1]); \
            st16_dev<0>(kp + 4096, pq, fk[1][0]); st16_dev<128>(kp + 4096, pq, fk[1][1]); st16_dev<2048>(kp + 4096, pv, fv[1][0]); st16_dev<2304>(kp + 4096, pv, fv[1][1]); } while (0)
            if (hd == 1) {
                const char* qp = ws + WS_QF + (size_t)((ph1 * 64 + pb1) * 2) * 1024;
                st16_dev<0>(qp, pq, fq[0][0]); st16_dev<128>(qp, pq, fq[0][1]); st16_dev<2048>(qp, pq, fq[1][0]); st16_dev<2176>(qp, pq, fq[1][1]);
            }
            JIT(2);
            if (wr == 0) PUB_KV();
#pragma unroll
            for (int mb = 0; mb < 2; ++mb) {
                const unsigned a = lds0 + hd * 2 * K2_SLOT + (wr * 2 + mb) * 4096, aq = lds0 + K2_QX + hd * 16384 + (wr * 2 + mb) * 2048 + pq;
                ldswr128(a + pq, fk[mb][0]); ldswr128(a + pq + 128, fk[mb][1]); ldswr128(a + 2048 + pv, fv[mb][0]); ldswr128(a + 2304 + pv, fv[mb][1]);
                if (hd == 0 || g == 0) { ldswr128(aq, fq[mb][0]); ldswr128(aq + 128, fq[mb][1]); }
            }
            BARRIER_L();
            if (g == 0) PRECOMPUTE(false);
            if (wr == 0 || hd == 1) {
                drain();
                if (lane == 0) { if (wr == 0) flag_set(ws + WS_FK, own, tag); if (hd == 1) flag_set(ws + WS_FQ, own, tag); }
            }
            JIT(3);
            if (wr == 1) PUB_KV();
            const unsigned qa = lbase + K2_QX + (1 - g) * 16384 + wid * 2048, cx = lbase + (1 - g) * 2 * K2_SLOT;
            SA_(3); SC_(2);
            pub0 = cat16(fk[0][0], fk[0][1], fv[0][0], fv[0][1]); pub1 = cat16(fk[1][0], fk[1][1], fv[1][0], fv[1][1]);
            { const bf16x8 q0 = ldsrd128(qa), q1 = ldsrd128(qa + 1024); FIRST_TILE(cx, q0, q1); }
            JIT(4);
            asm volatile(ATT_L4 : L6_OUT : "{v[16:31]}"(negm), "{v174}"(sidx), "{v112}"(cx) : ATT_TMP, "memory");
            SC_(3);
            if (g == 1) {
                const int b1 = (int)(tabf & 63), b0 = RBLK(0, wid - 4);
                flag_wait(ws, fk0 + (((lane & 1) ? b1 : b0) >> 1) * FS, tag);
                glds4k_dev(kv + b1 * 4096, l16, dbeg + K2_SLOT);
                glds4k_dev(kv + b0 * 4096, l16, dbeg + K2_SLOT - 16384);
                glds16_dev(ws, c_fl, fld);
            } else if (wr == 1) { drain(); if (lane == 0) flag_set(ws + WS_FK, own, tag); }
            SC_(4);
            JIT(5);
            asm volatile(ATT_L2 : "+{v[0:15]}"(o), "+{v[32:47]}"(sa), "+{v[64:79]}"(qol), "+{v[80:95]}"(kvf), "+{v[104:111]}"(ph) : "{v[16:31]}"(negm), "{v174}"(sidx), "{v112}"(cx) : ATT_TMP, "memory");
            if (g == 0) {
                asm volatile(ATT_XT : "+{v[0:15]}"(o), "+{v[32:47]}"(sa), "+{v[64:79]}"(qol), "+{v[80:95]}"(kvf), "+{v[104:111]}"(ph) : "{v[16:31]}"(negm), "{v174}"(sidx), "{v112}"(cx) : ATT_TMP, "memory");
                st16_dev<-2048>(ws, xrec0 + l16, quad(o, 0)); st16_dev<-1024>(ws, xrec0 + l16, quad(o, 1)); st16_dev<0>(ws, xrec0 + l16, quad(o, 2)); st16_dev<1024>(ws, xrec0 + l16, quad(o, 3));
                st16_dev<2048>(ws, xrec0 + l16, u32x4{__float_as_uint(__uint_as_float(qol[12]) + __uint_as_float(qol[13])), __float_as_uint(negm[0]), 0u, 0u});
                SC_(5);
                JIT(16);
                const bf16x8 q0 = ldsrd128(lbase + K2_QX + wid * 2048), q1 = ldsrd128(lbase + K2_QX + wid * 2048 + 1024);
                FIRST_TILE(lbase, q0, q1);
                u32x16 h0; u32x8 h1;
                L6C(0, lbase);
            }
#undef PUB_KV
        } else {
            if (wr == 3 && mt < 7) {
                const char* p = ws + WS_HALO + (size_t)(((mt + 1) * 32 + ph1) * 6) * 1024;
                st16_dev<0>(p, pq, fk[0][0]); st16_dev<128>(p, pq, fk[0][1]); st16_dev<3072>(p, pq, fk[1][0]); st16_dev<3200>(p, pq, fk[1][1]);
                if (lane >= 32) { st16_dev<1024>(p, pv, fv[0][0]); st16_dev<1280>(p, pv, fv[0][1]); st16_dev<1024>(p + 3072, pv, fv[1][0]); st16_dev<1280>(p + 3072, pv, fv[1][1]); }
            }
            JIT(15);
            const unsigned ex = lds0 + 2 * K2_SLOT + wid * 12288;
#pragma unroll
            for (int mb = 0; mb < 2; ++mb) {
                const unsigned a = ex + mb * 6144;
                ldswr128(a + pq, fq[mb][0]); ldswr128(a + pq + 128, fq[mb][1]); ldswr128(a + 2048 + pq, fk[mb][0]); ldswr128(a + 2176 + pq, fk[mb][1]);
                ldswr128(a + 4096 + pv, fv[mb][0]); ldswr128(a + 4352 + pv, fv[mb][1]);
            }
            if (g == 0) PRECOMPUTE(true);
            BARRIER_L();
            if (wr == 3 && mt < 7) { drain(); if (lane == 0) flag_set(ws + WS_FH, (ph1 * 8 + mt + 1) * FS, tag); }
            SC_(2);
            LocalPart lp[2];
            bf16x8 bq[2][2];
#pragma unroll
            for (int mb = 0; mb < 2; ++mb) { bq[mb][0] = ldsrd128(ex + mb * 6144 + l16); bq[mb][1] = ldsrd128(ex + mb * 6144 + 1024 + l16); }
#define LOCAL_BLOCK(mb) do { const unsigned a_ = ex + mb * 6144 + l16; \
                const bf16x8 k0_ = ldsrd128(a_ + 2048), k1_ = ldsrd128(a_ + 3072), v0_ = ldsrd128(a_ + 4096), v1_ = ldsrd128(a_ + 5120); \
                if (wr > 0) local_own<true>(bq[mb][0], bq[mb][1], k0_, k1_, v0_, v1_, ldsrd128(a_ - 24576 + 2048), ldsrd128(a_ - 24576 + 3072), ldsrd128(a_ - 24576 + 5120), band, lane, lp[mb]); \
                else local_own<false>(bq[mb][0], bq[mb][1], k0_, k1_, v0_, v1_, k0_, k0_, k0_, band, lane, lp[mb]); \
                if (wr > 0 || mt == 0) local_fin(lp[mb]); } while (0)
            JIT(6);
            LOCAL_BLOCK(0);
            SC_(3);
            const int hl = (wid < 2) ? mt : 0;
            const unsigned fh = (unsigned)WS_FH + (ph1 * 8 + mt) * FS;
            const int b0 = (int)(tabf & 63), b1 = (int)((tabf >> 6) & 63);
            const int q = lane & 3;
            unsigned fa0 = fk0 - 1024 * FS + (blk >> 1) * FS, fa1 = fk0 + (b0 >> 1) * FS, fa2 = fk0 + (b1 >> 1) * FS, fa3 = fh;
            asm volatile("" : "+s"(fa0), "+s"(fa1), "+s"(fa2), "+s"(fa3));
            const unsigned lo_ = (q & 1) ? fa1 : fa0, hi_ = (q & 1) ? fa3 : fa2;
            const u64 fm = flag_wait_some(ws, (q & 2) ? hi_ : lo_, tag, 3);
            SC_(4);
            glds4k_dev(kv + b0 * 4096, l16, dbeg);
            const char* qp = ws + WS_QF + (size_t)((head * 64 + blk) * 2) * 1024;
            glds16_dev(qp, l16, ex); glds16_dev(qp + 1024, l16, ex + 1024);
            if (fm & 4) glds4k_dev(kv + b1 * 4096, l16, dbeg + K2_SLOT);
            JIT(7);
            LOCAL_BLOCK(1);
#undef LOCAL_BLOCK
            if (fm & 4) WAITBAR_(4);
            else {
                WAITBAR_(0);
                flag_wait(ws, fk0 + (b1 >> 1) * FS, tag);
                glds4k_dev(kv + b1 * 4096, l16, dbeg + K2_SLOT);
            }
            JIT(8);
            const bf16x8 q0 = ldsrd128(ex + l16), q1 = ldsrd128(ex + 1024 + l16);
            SA_(3); SC_(5);
            if (hl && !(fm & 8)) flag_wait(ws, fh, tag);
            FIRST_TILE(lbase, q0, q1);
            u32x16 h0; u32x8 h1;
            JIT(9);
            L6C(hl, lbase);
            if (hl) {
                local_prev(bq[0][0], bq[0][1], asbf(u32x4{h0[0], h0[1], h0[2], h0[3]}), asbf(u32x4{h0[4], h0[5], h0[6], h0[7]}), asbf(u32x4{h0[8], h0[9], h0[10], h0[11]}), lane, lp[0]);
                local_prev(bq[1][0], bq[1][1], asbf(u32x4{h0[12], h0[13], h0[14], h0[15]}), asbf(u32x4{h1[0], h1[1], h1[2], h1[3]}), asbf(u32x4{h1[4], h1[5], h1[6], h1[7]}), lane, lp[1]);
                local_fin(lp[0]); local_fin(lp[1]);
            }
            const char* op = ws + WS_OF + (size_t)(((64 + pb1) * 32 + ph1) * 2) * 1024;
            st16_dev<0>(op, l16, quad(lp[0].o, 0)); st16_dev<1024>(op, l16, quad(lp[0].o, 1)); st16_dev<0>(op + 65536, l16, quad(lp[1].o, 0)); st16_dev<1024>(op + 65536, l16, quad(lp[1].o, 1));
        }
#undef FIRST_TILE
#undef L6_OUT
#undef L6C
        FRESH();
        l16 = lane * 16;
        SA_(4); SC_(6);
        const int isx = br & (1 - g);
        unsigned cnt = 7, dst = dbeg + 2 * K2_SLOT, nx = K2_SLOT;
        unsigned mode = isx << 1;
        const unsigned late = (1 - br) & g;
        const unsigned act = 2u | (isx * 12u) | ((br | (1 - g)) << 5) | (late << 6), hk = 32u | (late << 7);
        const u64 tab = tabf >> (6 + 6 * br);
        u32x8 adr = {lds0 + l16, lds0 + l16 + K2_SLOT, l16, lds0 + l16, c_wb + wid * 16384, (c_wb ^ 128) + wid * 16384 + 7168,
                     br ? (unsigned)WS_FO + ((64 + pb1 + (lane & 1)) * 32 + ph1) * FS : fxo, c_tw};
        const unsigned kvrec = (unsigned)WS_KV + (ph1 * 64 + pb1) * 4096 + 3072, pv2 = c_pv;
        f32x16 xo; f32x2 xl;
#define DBGOUT
        JIT(10);
        asm volatile(ATT_MAIN
                     : DBGOUT"+{v[0:15]}"(o), "+{v[32:47]}"(sa), "+{v[64:79]}"(qol), "+{v[80:95]}"(kvf), "+{v[104:111]}"(ph), "+{v[112:119]}"(adr), "={v[122:137]}"(xo), "={v[138:139]}"(xl),
                       [cnt] "+s"(cnt), [dst] "+s"(dst), [nx] "+s"(nx), [mode] "+s"(mode)
                     : "{v[16:31]}"(negm), "{v[174:175]}"(u32x2{sidx, (unsigned)WS_FK + own}), "{v[140:143]}"(u32x4{(unsigned)tag, (unsigned)(tag >> 32), fxo, xrec0 + l16}),
                       "{v[170:173]}"(u32x4{c_fl, kvrec + pv2 + (lane & 8) * 16, kvrec + pv2, fld + l16}), "{v[176:191]}"(pub0), "{v[192:207]}"(pub1),
                       [srclo] "s"((unsigned)(uintptr_t)kv), [srchi] "s"((unsigned)((uintptr_t)kv >> 32)), [dbeg] "s"(dbeg), [dend] "s"(dbeg + 3 * K2_SLOT),
                       [wo01] "s"(wo + (size_t)k30 * 131072), [wo23] "s"(wo + (size_t)((k30 + 2) & 15) * 131072), [wo] "s"(wo), [wdst] "s"(__builtin_amdgcn_readfirstlane(lds0 + 98304 + wid * 2048)), [act] "s"(act), [hk] "s"(hk), [tab] "s"(tab), [k0] "s"(br), [ws] "s"(ws)
                     : ATT_TMP, "v120", "v121", "v168", "v169", "s40", "s41", "s42", "s43", "s44", "s45", "s46", "s47", "s48", "s49", "s50", "s51", "vcc", "scc", "memory");
        FRESH();
        l16 = lane * 16;
        const unsigned xrec = xrec0 + l16;
        const bf16x8 q0 = asbf(u32x4{qol[0], qol[1], qol[2], qol[3]}), q1 = asbf(u32x4{qol[4], qol[5], qol[6], qol[7]});
        const unsigned one2 = (((lane >> 4) & 1) == ((lane >> 2) & 1)) ? 0x3F803F80u : 0u;
        const u32x4 ones = {one2, one2, one2, one2};
        SA_(5); SB_(1);
        JIT(11);
        float lsum = __uint_as_float(qol[12]) + __uint_as_float(qol[13]);
        bool ok = lsum < 1.0e18f;
        if (isx) {
            if (__builtin_expect(!(mode & 4), 0)) {
                flag_wait(ws, fxo, tag);
                f32x4 a0, a1, a2, a3;
                asm volatile("global_load_dwordx4 %0, %5, %6 offset:-2048 sc1\n\tglobal_load_dwordx4 %1, %5, %6 offset:-1024 sc1\n\tglobal_load_dwordx4 %2, %5, %6 sc1\n\t"
                             "global_load_dwordx4 %3, %5, %6 offset:1024 sc1\n\tglobal_load_dwordx2 %4, %5, %6 offset:2048 sc1\n\ts_waitcnt vmcnt(0)"
                             : "=&v"(a0), "=&v"(a1), "=&v"(a2), "=&v"(a3), "=&v"(xl) : "v"(xrec), "s"(ws) : "memory");
#pragma unroll
                for (int r = 0; r < 4; ++r) { xo[r] = a0[r]; xo[4 + r] = a1[r]; xo[8 + r] = a2[r]; xo[12 + r] = a3[r]; }
            }
            const float nm = __builtin_fminf(negm[0], xl[1]), fa = EXP2(nm - negm[0]), fx = EXP2(nm - xl[1]);
            ok = ok && (xl[0] < 1.0e18f);
            lsum = lsum * fa + xl[0] * fx;
#pragma unroll
            for (int r = 0; r < 16; ++r) o[r] = o[r] * fa + xo[r] * fx;
        }
#pragma unroll
        for (int r = 0; r < 16; ++r) ok = ok && (__builtin_fabsf(o[r]) < 3.0e38f);
        if (__builtin_expect(__any(!ok), 0)) {
            float m = -__builtin_inff();
            f32x4 l;
            for (int pass = 0; pass < 2; ++pass) {
                o = z; l = f32x4{};
                for (int t = 0; t < 64; ++t) {
                    bf16x8 f0, f1, f2, f3;
                    asm volatile("global_load_dwordx4 %0, %4, %5 sc1\n\tglobal_load_dwordx4 %1, %4, %5 offset:1024 sc1\n\tglobal_load_dwordx4 %2, %4, %5 offset:2048 sc1\n\t"
                                 "global_load_dwordx4 %3, %4, %5 offset:3072 sc1\n\ts_waitcnt vmcnt(0)" : "=&v"(f0), "=&v"(f1), "=&v"(f2), "=&v"(f3) : "v"(l16 + t * 4096), "s"(kv) : "memory");
                    f32x16 s = MFMA32(f0, q0, z); s = MFMA32(f1, q1, s);
                    if (pass == 0) {
#pragma unroll
                        for (int r = 0; r < 16; ++r) m = __builtin_fmaxf(m, s[r]);
                    } else {
#pragma unroll
                        for (int r = 0; r < 16; ++r) s[r] = EXP2(s[r] - m);
                        const bf16x8 p0 = asbf(pack8(s, 0)), p1 = asbf(pack8(s, 1));
                        o = MFMA32(f2, p0, o); o = MFMA32(f3, p1, o);
                        l = MFMA16(asbf(ones), p0, l); l = MFMA16(asbf(ones), p1, l);
                    }
                }
                if (pass == 0) m = swap_max(m);
            }
            lsum = l[0];
        }
        const float inv = __builtin_amdgcn_rcpf(lsum);
#pragma unroll
        for (int r = 0; r < 16; ++r) o[r] *= inv;
        const char* op = ws + WS_OF + (size_t)((blk * 32 + head) * 2) * 1024;
        st16_dev<0>(op, l16, pack8(o, 0)); st16_dev<1024>(op, l16, pack8(o, 1));
    }
    {
        FRESH();
        const int g = wid >> 2, wi = wid & 3, wm = wi >> 1, wn = wi & 1, g4 = lane >> 4;
        const char* of = ws + WS_OF + (size_t)(br * 64 + mt3 * 4 + wi) * 65536;
        const unsigned vw4[4] = {c_wb + wi * 32768, (c_wb ^ 128) + wi * 32768 + 7168, c_wb + wi * 32768 + 14336, (c_wb ^ 128) + wi * 32768 + 21504};
        const unsigned l16 = lane * 16, sdst = lds0 + wi * 4096;
#define STAGE_O(t) glds4k_dev(of + (((t) + k30) & 15) * 4096, l16, sdst + ((t) & 3) * 16384)
#define STAGE_WO(t, slot) do { const char* s_ = wo + (size_t)(((t) + k30) & 15) * 131072; glds_run<0>(s_, vw4[0], sdst + (slot)); glds_run<1>(s_, vw4[1], 0); glds_run<2>(s_, vw4[2], 0); glds_run<3>(s_, vw4[3], 0); } while (0)
        const unsigned fo = c_fo;
        drain();
        if (lane == 0) flag_set(ws + WS_FO, ((mt * 8 + wid) * 32 + 2 * hp + br) * FS, tag);
        SB_(2);
        asm volatile("s_barrier" ::: "memory");
        SB_(3);
        JIT(12);
        const u64 fm = flag_wait_some(ws, fo, tag, (3ull << (2 * nt3)) | (g ? 3ull << (2 * ((nt3 + 1) & 7)) : 0ull));
        STAGE_O(1 - g);
        if (g == 1) STAGE_O(2);
        STAGE_WO(5 - g, 65536 + (1 - g) * 16384);
        if (fm != ~0ull) flag_wait(ws, fo, tag);
        SB_(4);
        SA_(6);
        unsigned ao = lds0 + g * 16384 + wm * 8192 + c_pv;
        unsigned aw[4] = {c_aw, c_aw ^ 32, c_aw ^ 64, c_aw ^ 96};
        const char* so = of + ((3 + g + k30) & 15) * 4096; const char* sw = wo + (size_t)((5 + g + k30) & 15) * 131072;
        unsigned dob = sdst + ((3 + g) & 3) * 16384, dwb = sdst + p3_wslot(6);
        f32x4 acc[4][4];
#pragma unroll
        for (int mb = 0; mb < 4; ++mb)
#pragma unroll
            for (int nb = 0; nb < 4; ++nb) acc[mb][nb] = f32x4{};
        if (g == 1) { WAITBAR_(8); asm volatile("s_barrier" ::: "memory"); } else asm volatile("s_barrier" ::: "memory");
        SB_(5);
        SBAR();
#pragma unroll
        for (int u = 0; u < 8; ++u) {
            bf16x8 of_[4][2], wf[4][2];
            const bool doo = u < 6 || (u == 6 && g == 0), dow = (u >= 1 || g == 1) && (u < 5 || (u == 5 && g == 0));
#define P3RD(i) do { of_[(i) & 3][(i) >> 2] = ldsrd128(ao + (((i) & 3) >> 1) * 4096 + ((i) & 1) * 256 + ((i) >> 2) * 2048); \
                wf[(i) & 3][(i) >> 2] = cat8(ldstr(aw[(i) & 3] + ((i) >> 2) * 8192), ldstr(aw[(i) & 3] + ((i) >> 2) * 8192 + 2048)); } while (0)
            P3RD(0); if (u < 7 && doo) glds_run_dev<0>(so, l16, dob);
            P3RD(1); if (u < 7 && doo) glds_run_dev<1>(so, l16, 0);
            P3RD(2); if (u < 7 && doo) glds_run_dev<2>(so, l16, 0);
            P3RD(3); if (u < 7 && doo) glds_run_dev<3>(so, l16, 0);
            P3RD(4); if (u < 6 && dow) glds_run<0>(sw, vw4[0], dwb);
            P3RD(5); if (u < 6 && dow) glds_run<1>(sw, vw4[1], 0);
            P3RD(6); if (u < 6 && dow) glds_run<2>(sw, vw4[2], 0);
            P3RD(7); if (u < 6 && dow) glds_run<3>(sw, vw4[3], 0);
#undef P3RD
            SBAR();
            if (u < 7) {
                { const int to = 2 * u + 5 + g; so = of + ((to + k30) & 15) * 4096; sw = wo + (size_t)((to + 2 + k30) & 15) * 131072;
                  dob = sdst + (to & 3) * 16384; dwb = sdst + (g ? p3_wslot(2 * u + 8) : p3_wslot(2 * u + 7)); }
                ao ^= 32768;
#pragma unroll
                for (int q = 0; q < 4; ++q) aw[q] += p3_wslot(2 * u + 2) - p3_wslot(2 * u);
                asm volatile("" : "+v"(ao), "+v"(aw[0]), "+v"(aw[1]), "+v"(aw[2]), "+v"(aw[3]), "+s"(so), "+s"(sw), "+s"(dob), "+s"(dwb));
            }
            if (p3_allow(2 * u) == p3_allow(2 * u + 1)) WAITBAR(p3_allow(2 * u)); else if (g == 0) WAITBAR(p3_allow(2 * u)); else WAITBAR(p3_allow(2 * u + 1));
            SBAR();
            __builtin_amdgcn_s_setprio(1);
#pragma unroll
            for (int mb = 0; mb < 4; ++mb)
#pragma unroll
                for (int nb = 0; nb < 4; ++nb) { acc[mb][nb] = MFMA16(wf[nb][0], of_[mb][0], acc[mb][nb]); acc[mb][nb] = MFMA16(wf[nb][1], of_[mb][1], acc[mb][nb]); CHAIN();
                }
            __builtin_amdgcn_s_setprio(0);
            SBAR();
            if (u < 7 || g == 0) asm volatile("s_barrier" ::: "memory");
            SBAR();
        }
        SB_(6);
        FRESH();
        JIT(13);
        const unsigned xw = lds0 + wid * 8192 + l16, xr = lds0 + (wid ^ 4) * 8192 + l16;
#pragma unroll
        for (int i = 0; i < 8; ++i) { const f32x4 v = g ? acc[i >> 2][i & 3] : acc[2 + (i >> 2)][i & 3]; ldswr128(xw + i * 1024, __builtin_bit_cast(u32x4, v)); }
        BARRIER_L();
        const unsigned st = lds0 + 65536 + wid * 4096;
#pragma unroll
        for (int i = 0; i < 8; ++i) {
            const f32x4 v = (g ? acc[2 + (i >> 2)][i & 3] : acc[i >> 2][i & 3]) + __builtin_bit_cast(f32x4, ldsrd128(xr + i * 1024));
            const int row = 16 * (i >> 2) + (lane & 15), c = 2 * (i & 3) + (g4 >> 1);
            *(LAS u32x2*)(uintptr_t)(st + row * 128 + ((c ^ (row & 7)) << 4) + (g4 & 1) * 8) = u32x2{cvtpk(v[0], v[1]), cvtpk(v[2], v[3])};
        }
        const int blk = mt3 * 4 + 2 * wm + g;
        char* op = (char*)out + br * 2048 + nt3 * 256 + wn * 128 + (lane & 7) * 16;
#pragma unroll
        for (int i = 0; i < 4; ++i) {
            const int r = 8 * i + (lane >> 3), row = br ? ((blk >> 1) * 64 + 2 * r + (blk & 1)) : (blk * 32 + r);
            st16_dev<0>(out, (unsigned)(row * 4096 + br * 2048 + nt3 * 256 + wn * 128 + (lane & 7) * 16), __builtin_bit_cast(u32x4, ldsrd128(st + r * 128 + (((lane & 7) ^ (r & 7)) << 4))));
        }
#undef STAGE_O
#undef STAGE_WO
    }
    STAMP_AT(0, 7);
    if (wid == 4) {
        const unsigned rec = (100u << 20) + b * 128;
        if (lane == 0) { st16_dev<0>(ws0, rec, u32x4{pst[0], pst[1], pst[2], pst[3]}); st16_dev<16>(ws0, rec, u32x4{pst[4], pst[5], pst[6], pst[7]}); }
        drain();
        if (lane == 0) flag_set(ws0, rec + 64, tag);
    }
    if (b == 0 && wid == 1) {
        unsigned mn = 0xffffffffu, base = 0, mx = 0;
        for (int pass = 0; pass < 2; ++pass)
        for (int i = 0; i < 4; ++i) { const unsigned rec = (100u << 20) + (i * 64 + lane) * 128;
            flag_wait(ws0, rec + 64, tag);
            unsigned t0v, txv;
            asm volatile("global_load_dword %0, %2, %3 sc1\n\tglobal_load_dword %1, %2, %3 offset:%4 sc1\n\ts_waitcnt vmcnt(0)" : "=&v"(t0v), "=&v"(txv) : "v"(rec), "s"(ws0), "i"((PROBE & 7) * 4) : "memory");
            if (pass == 0) { if (i == 0) base = __builtin_amdgcn_readfirstlane(t0v) - 100000u; mn = min(mn, t0v - base); } else mx = max(mx, txv - base - mn);
            if (pass == 0 && i == 3) { for (int o = 32; o; o >>= 1) mn = min(mn, (unsigned)__shfl_xor((int)mn, o)); } }
        for (int o = 32; o; o >>= 1) mx = max(mx, (unsigned)__shfl_xor((int)mx, o));
        const unsigned until = base + mn + 15000u + ((PROBE & 8) ? 0u : mx);
        while ((int)((unsigned)__builtin_amdgcn_s_memrealtime() - until) < 0) __builtin_amdgcn_s_sleep(1);
    }
    if (b == 0 && wid == 0) {
#pragma unroll
        for (int i = 0; i < 2; ++i) { const int u = i * 64 + lane;
            flag_wait(ws0, (unsigned)WS_FK + ((2 * (u >> 3)) * 32 + (u & 7) * 4) * FS, tag);
            flag_wait(ws0, (unsigned)WS_FO + ((64 + (u & 7) * 8) * 32 + 2 * (u >> 3)) * FS, tag); }
        if (lane == 0) flag_set(ws0, (unsigned)WS_EP, tag);
    }
    asm volatile(".Lmk_code_end:");
}

extern "C" void kernel_launch(void* const* d_in, const int* in_sizes, int n_in, void* d_out, int out_size, void* d_ws, size_t ws_size, hipStream_t stream) {
    mk_fused<<<256, 512, 0, stream>>>((const u16*)d_in[0], (const u16*)d_in[1], (const u16*)d_in[2], (const u16*)d_in[3], (const u16*)d_in[4], (const u16*)d_in[5], (char*)d_ws, (u16*)d_out);
}
```

```cpp
#include <hip/hip_runtime.h>
#include <stdint.h>

typedef unsigned short u16;
typedef unsigned long long u64;
typedef __bf16 bf16x8 __attribute__((ext_vector_type(8)));
typedef __bf16 bf16x2 __attribute__((ext_vector_type(2)));
typedef short s16x4 __attribute__((ext_vector_type(4)));
typedef float f32x16 __attribute__((ext_vector_type(16)));
typedef float f32x4 __attribute__((ext_vector_type(4)));
typedef float f32x2 __attribute__((ext_vector_type(2)));
typedef unsigned u32x4 __attribute__((ext_vector_type(4)));
typedef unsigned u32x2 __attribute__((ext_vector_type(2)));
typedef unsigned u32x8 __attribute__((ext_vector_type(8)));
typedef unsigned u32x16 __attribute__((ext_vector_type(16)));
#define LAS __attribute__((address_space(3)))
#define DI __device__ __forceinline__
#define SBAR() __builtin_amdgcn_sched_barrier(0)
#define MFMA32(a, b, c) __builtin_amdgcn_mfma_f32_32x32x16_bf16((a), (b), (c), 0, 0, 0)
#define MFMA16(a, b, c) __builtin_amdgcn_mfma_f32_16x16x32_bf16((a), (b), (c), 0, 0, 0)
#define CHAIN() SBAR()
#define ORD 0
#define EXP2(x) __builtin_amdgcn_exp2f(x)

constexpr float QSCALE = 0.17677669529663687f * 1.4426950408889634f;

constexpr size_t WS_QF = 0;
constexpr size_t WS_KV = WS_QF + (4u << 20);
constexpr size_t WS_OF = WS_KV + (8u << 20);
constexpr size_t WS_HALO = WS_OF + (8u << 20);
constexpr size_t WS_XP = WS_HALO + (2u << 20);
constexpr size_t WS_FX = WS_XP + (5u << 20);
#define FS 64
constexpr size_t WS_FQ = WS_FX + 1024 * FS;
constexpr size_t WS_FK = WS_FQ + 1024 * FS;
constexpr size_t WS_FO = WS_FK + 1024 * FS;
constexpr size_t WS_FH = WS_FO + 4096 * FS;
constexpr size_t WS_EP = WS_FH + 256 * FS;

#define OUTST 1
#define SLP 1
#define STAMP_AT(K, I)
#define SA_(I)
#define SB_(I)

#define SC_(I)
#define JIT(p)
DI unsigned cvtpk(float lo, float hi) { f32x2 v = {lo, hi}; bf16x2 b = __builtin_convertvector(v, bf16x2); return __builtin_bit_cast(unsigned, b); }
DI u32x4 pack8(const f32x16& a, int s) { u32x4 r; r[0] = cvtpk(a[8 * s], a[8 * s + 1]); r[1] = cvtpk(a[8 * s + 2], a[8 * s + 3]); r[2] = cvtpk(a[8 * s + 4], a[8 * s + 5]); r[3] = cvtpk(a[8 * s + 6], a[8 * s + 7]); return r; }
DI u32x4 pack44(f32x4 a, f32x4 c) { return u32x4{cvtpk(a[0], a[1]), cvtpk(a[2], a[3]), cvtpk(c[0], c[1]), cvtpk(c[2], c[3])}; }
DI u32x4 asu(bf16x8 v) { return __builtin_bit_cast(u32x4, v); }
DI u32x16 cat16(u32x4 a, u32x4 b, u32x4 c, u32x4 d) { return u32x16{a[0], a[1], a[2], a[3], b[0], b[1], b[2], b[3], c[0], c[1], c[2], c[3], d[0], d[1], d[2], d[3]}; }
DI bf16x8 asbf(u32x4 v) { return __builtin_bit_cast(bf16x8, v); }
DI void glds16(const void* sbase, unsigned voff, unsigned lds) {
    asm volatile("s_mov_b32 m0, %2\n\ts_nop 0\n\tglobal_load_lds_dwordx4 %0, %1" :: "v"(voff), "s"(sbase), "s"(lds) : "memory");
}
template <int I> DI void glds_run(const void* sbase, unsigned voff, unsigned lds) {
    if (I == 0) asm volatile("s_mov_b32 m0, %2\n\ts_nop 0\n\tglobal_load_lds_dwordx4 %0, %1" :: "v"(voff), "s"(sbase), "s"(lds) : "memory");
    else asm volatile("global_load_lds_dwordx4 %0, %1 offset:%2" :: "v"(voff), "s"(sbase), "i"(I * 1024) : "memory");
}
template <int I> DI void glds_run_dev(const void* sbase, unsigned voff, unsigned lds) {
    if (I == 0) asm volatile("s_mov_b32 m0, %2\n\ts_nop 0\n\tglobal_load_lds_dwordx4 %0, %1 sc1" :: "v"(voff), "s"(sbase), "s"(lds) : "memory");
    else asm volatile("global_load_lds_dwordx4 %0, %1 offset:%2 sc1" :: "v"(voff), "s"(sbase), "i"(I * 1024) : "memory");
}
template <int I> DI void glds_sec(const void* sbase, unsigned voff, unsigned lds) {
    if (I < 2) asm volatile("s_add_u32 m0, %2, %4\n\ts_nop 0\n\tglobal_load_lds_dwordx4 %0, %1 offset:%3" :: "v"(voff), "s"(sbase), "s"(lds), "i"(I * 2048), "i"(I * 6144) : "memory", "scc");
    else asm volatile("s_add_u32 m0, %2, %4\n\ts_nop 0\n\tglobal_load_lds_dwordx4 %0, %1 offset:%3" :: "v"(voff), "s"((const char*)sbase + 2048), "s"(lds), "i"(2048), "i"(14336) : "memory", "scc");
}
DI void glds16_dev(const void* sbase, unsigned voff, unsigned lds) {
    asm volatile("s_mov_b32 m0, %2\n\ts_nop 0\n\tglobal_load_lds_dwordx4 %0, %1 sc1" :: "v"(voff), "s"(sbase), "s"(lds) : "memory");
}
DI void glds4k_dev(const void* sbase, unsigned voff, unsigned lds) {
    asm volatile("s_mov_b32 m0, %2\n\ts_nop 0\n\tglobal_load_lds_dwordx4 %0, %1 sc1\n\tglobal_load_lds_dwordx4 %0, %1 offset:1024 sc1\n\t"
                 "global_load_lds_dwordx4 %0, %1 offset:2048 sc1\n\tglobal_load_lds_dwordx4 %0, %1 offset:3072 sc1" :: "v"(voff), "s"(sbase), "s"(lds) : "memory");
}
template <int OFF> DI void st16_dev(const void* sbase, unsigned voff, u32x4 v) {
    asm volatile("global_store_dwordx4 %0, %1, %2 offset:%3 sc1\n\ts_nop 1" :: "v"(voff), "v"(v), "s"(sbase), "i"(OFF) : "memory");
}
DI void drain() { asm volatile("s_waitcnt vmcnt(0)" ::: "memory"); }
DI void flag_set(const void* sbase, unsigned voff, u64 tag) {
    asm volatile("global_store_dwordx2 %0, %1, %2 sc1" :: "v"(voff), "v"(tag), "s"(sbase) : "memory");
}
DI void flag_wait(const void* sbase, unsigned voff, u64 tag) {
    const u64 t0 = __builtin_amdgcn_s_memrealtime();
    for (;;) {
        u64 v;
        asm volatile("global_load_dwordx2 %0, %1, %2 sc1\n\ts_waitcnt vmcnt(0)" : "=v"(v) : "v"(voff), "s"(sbase) : "memory");
        if (__all(v == tag)) break;
        if (__builtin_amdgcn_s_memrealtime() - t0 > 2000000ull) break;
        __builtin_amdgcn_s_sleep(SLP);
    }
}
DI u64 flag_wait_some(const void* sbase, unsigned voff, u64 tag, u64 need) {
    const u64 t0 = __builtin_amdgcn_s_memrealtime();
    for (;;) {
        u64 v;
        asm volatile("global_load_dwordx2 %0, %1, %2 sc1\n\ts_waitcnt vmcnt(0)" : "=v"(v) : "v"(voff), "s"(sbase) : "memory");
        const u64 m = __ballot(v == tag);
        if ((m & need) == need || __builtin_amdgcn_s_memrealtime() - t0 > 2000000ull) return m;
        __builtin_amdgcn_s_sleep(SLP);
    }
}
DI bf16x8 ldsrd128(unsigned addr) { return *(const LAS bf16x8*)(uintptr_t)addr; }
DI void ldswr128(unsigned addr, u32x4 v) { *(LAS u32x4*)(uintptr_t)addr = v; }
DI s16x4 ldstr(unsigned addr) { return __builtin_amdgcn_ds_read_tr16_b64_v4i16((LAS s16x4*)(uintptr_t)addr); }
DI bf16x8 cat8(s16x4 lo, s16x4 hi) { typedef short s16x8 __attribute__((ext_vector_type(8))); s16x8 v = {lo[0], lo[1], lo[2], lo[3], hi[0], hi[1], hi[2], hi[3]}; return __builtin_bit_cast(bf16x8, v); }
DI float swap_max(float m) { auto r = __builtin_amdgcn_permlane32_swap(__float_as_uint(m), __float_as_uint(m), false, false); return __builtin_fmaxf(__uint_as_float(r[0]), __uint_as_float(r[1])); }
DI float swap_add(float m) { auto r = __builtin_amdgcn_permlane32_swap(__float_as_uint(m), __float_as_uint(m), false, false); return __uint_as_float(r[0]) + __uint_as_float(r[1]); }
#define FRESH() asm volatile("" : "+v"(lane), "+s"(ws))
#define BARRIER_L() asm volatile("s_waitcnt lgkmcnt(0)\n\ts_barrier" ::: "memory")
#define WAITBAR_(n) asm volatile("s_waitcnt vmcnt(%0) lgkmcnt(0)\n\ts_barrier" :: "i"(n) : "memory")
#define WAITBAR(n) do { switch (n) { case 0: WAITBAR_(0); break; case 2: WAITBAR_(2); break; case 3: WAITBAR_(3); break; case 4: WAITBAR_(4); break; case 6: WAITBAR_(6); break; \
    case 7: WAITBAR_(7); break; case 8: WAITBAR_(8); break; case 10: WAITBAR_(10); break; case 12: WAITBAR_(12); break; default: WAITBAR_(0); } } while (0)

struct LocalPart { f32x16 o; float m, l; };
DI f32x16 band_mask(int lane) {
    const int i = lane & 31, h = lane >> 5;
    f32x16 c;
#pragma unroll
    for (int r = 0; r < 16; ++r) { const int j = (r & 3) + 8 * (r >> 2) + 4 * h; c[r] = ((unsigned)(i - j) <= 7u) ? 0.f : -__builtin_inff(); }
    return c;
}
DI float max3(float a, float b, float c) { float r; asm("v_max3_f32 %0, %1, %2, %3" : "=v"(r) : "v"(a), "v"(b), "v"(c)); return r; }
template <bool PREV> DI void local_own(bf16x8 q0, bf16x8 q1, bf16x8 k0, bf16x8 k1, bf16x8 v0, bf16x8 v1, bf16x8 kp0, bf16x8 kp1, bf16x8 vp1, const f32x16& band, int lane, LocalPart& P) {
    const f32x16 z = {};
    f32x16 so = MFMA32(k0, q0, band), sp = z;
    if (PREV) sp = MFMA32(kp0, q0, z);
    so = MFMA32(k1, q1, so);
    if (PREV) sp = MFMA32(kp1, q1, sp);
    float m = __builtin_fmaxf(so[0], so[1]);
#pragma unroll
    for (int r = 2; r < 16; r += 2) m = __builtin_fmaxf(__builtin_fmaxf(m, so[r]), so[r + 1]);
    if (PREV) {
        const int d = 4 * (lane >> 5) - (lane & 31);
#pragma unroll
        for (int r = 12; r < 16; ++r) { sp[r] = (d + (r & 3) >= 1) ? sp[r] : -__builtin_inff(); m = __builtin_fmaxf(m, sp[r]); }
    }
    m = swap_max(m);
    float l = 0.f;
#pragma unroll
    for (int r = 0; r < 16; ++r) { so[r] = EXP2(so[r] - m); l += so[r]; }
    P.o = MFMA32(v0, asbf(pack8(so, 0)), z); P.o = MFMA32(v1, asbf(pack8(so, 1)), P.o);
    if (PREV) {
#pragma unroll
        for (int r = 12; r < 16; ++r) { sp[r] = EXP2(sp[r] - m); l += sp[r]; }
        P.o = MFMA32(vp1, asbf(u32x4{0u, 0u, cvtpk(sp[12], sp[13]), cvtpk(sp[14], sp[15])}), P.o);
    }
    P.m = m; P.l = l;
}
DI void local_prev(bf16x8 q0, bf16x8 q1, bf16x8 kp0, bf16x8 kp1, bf16x8 vp1, int lane, LocalPart& P) {
    const int i = lane & 31, h = lane >> 5;
    const f32x16 z = {};
    f32x16 sp = MFMA32(kp0, q0, z); sp = MFMA32(kp1, q1, sp);
    const float NEG = -__builtin_inff();
    float m = P.m;
#pragma unroll
    for (int r = 12; r < 16; ++r) { const int j = (r & 3) + 24 + 4 * h; sp[r] = (j >= i + 25) ? sp[r] : NEG; m = __builtin_fmaxf(m, sp[r]); }
    m = swap_max(m);
    const float f = EXP2(P.m - m);
    float l = P.l * f;
#pragma unroll
    for (int r = 12; r < 16; ++r) { sp[r] = EXP2(sp[r] - m); l += sp[r]; }
#pragma unroll
    for (int r = 0; r < 16; ++r) P.o[r] *= f;
    P.o = MFMA32(vp1, asbf(u32x4{0u, 0u, cvtpk(sp[12], sp[13]), cvtpk(sp[14], sp[15])}), P.o);
    P.m = m; P.l = l;
}
DI void local_fin(LocalPart& P) {
    const float inv = __builtin_amdgcn_rcpf(swap_add(P.l));
#pragma unroll
    for (int r = 0; r < 16; ++r) P.o[r] *= inv;
    const u32x4 o0 = pack8(P.o, 0), o1 = pack8(P.o, 1);
#pragma unroll
    for (int r = 0; r < 4; ++r) { P.o[r] = __uint_as_float(o0[r]); P.o[4 + r] = __uint_as_float(o1[r]); }
}
DI u32x4 quad(const f32x16& a, int i) { return __builtin_bit_cast(u32x4, f32x4{a[4 * i], a[4 * i + 1], a[4 * i + 2], a[4 * i + 3]}); }

#define ATT_L6C \
    "s_nop 1\n\t" \
    "s_cmp_eq_u32 %[hl], 0\n\t" \
    "s_cbranch_scc1 10f\n\t" \
    "global_load_dwordx4 v[144:147], v143, %[ws] offset:-3072 sc1\n\t" \
    "global_load_dwordx4 v[148:151], v143, %[ws] offset:-2048 sc1\n\t" \
    "global_load_dwordx4 v[152:155], v143, %[ws] offset:-1024 sc1\n\t" \
    "global_load_dwordx4 v[156:159], v143, %[ws] offset:0 sc1\n\t" \
    "global_load_dwordx4 v[160:163], v143, %[ws] offset:1024 sc1\n\t" \
    "global_load_dwordx4 v[164:167], v143, %[ws] offset:2048 sc1\n\t" \
    "10:\n" \
    "s_setprio 3\n\t" \
    "s_waitcnt lgkmcnt(0)\n\t" \
    "v_mfma_f32_32x32x16_bf16 v[48:63], v[80:83], v[64:67], v[16:31]\n\t" \
    "v_exp_f32_e32 v32, v32\n\t" \
    "v_exp_f32_e32 v33, v33\n\t" \
    "v_exp_f32_e32 v34, v34\n\t" \
    "v_exp_f32_e32 v35, v35\n\t" \
    "v_exp_f32_e32 v36, v36\n\t" \
    "v_mfma_f32_32x32x16_bf16 v[48:63], v[84:87], v[68:71], v[48:63]\n\t" \
    "ds_read_b128 v[80:83], v112 offset:8192\n\t" \
    "ds_read_b128 v[84:87], v112 offset:9216\n\t" \
    "v_cvt_pk_bf16_f32 v104, v32, v33\n\t" \
    "v_cvt_pk_bf16_f32 v105, v34, v35\n\t" \
    "v_exp_f32_e32 v37, v37\n\t" \
    "v_exp_f32_e32 v38, v38\n\t" \
    "v_exp_f32_e32 v39, v39\n\t" \
    "ds_read_b128 v[100:103], v112 offset:3072\n\t" \
    "ds_read_b128 v[96:99], v112 offset:6144\n\t" \
    "v_exp_f32_e32 v40, v40\n\t" \
    "v_cvt_pk_bf16_f32 v106, v36, v37\n\t" \
    "v_cvt_pk_bf16_f32 v107, v38, v39\n\t" \
    "v_exp_f32_e32 v41, v41\n\t" \
    "v_exp_f32_e32 v42, v42\n\t" \
    "v_exp_f32_e32 v43, v43\n\t" \
    "v_mfma_f32_32x32x16_bf16 v[0:15], v[88:91], v[104:107], 0\n\t" \
    "v_exp_f32_e32 v44, v44\n\t" \
    "v_exp_f32_e32 v45, v45\n\t" \
    "v_exp_f32_e32 v46, v46\n\t" \
    "v_exp_f32_e32 v47, v47\n\t" \
    "v_cvt_pk_bf16_f32 v108, v40, v41\n\t" \
    "v_cvt_pk_bf16_f32 v109, v42, v43\n\t" \
    "v_cvt_pk_bf16_f32 v110, v44, v45\n\t" \
    "v_cvt_pk_bf16_f32 v111, v46, v47\n\t" \
    "s_mov_b32 m0, %[fld]\n\t" \
    "s_nop 0\n\t" \
    "global_load_lds_dwordx4 v142, %[ws] sc1\n\t" \
    "s_waitcnt lgkmcnt(0)\n\t" \
    "v_mfma_f32_32x32x16_bf16 v[32:47], v[80:83], v[64:67], v[16:31]\n\t" \
    "v_exp_f32_e32 v48, v48\n\t" \
    "v_exp_f32_e32 v49, v49\n\t" \
    "v_exp_f32_e32 v50, v50\n\t" \
    "v_smfmac_f32_16x16x64_bf16 v[76:79], v[72:75], v[104:111], v174\n\t" \
    "v_exp_f32_e32 v51, v51\n\t" \
    "v_exp_f32_e32 v52, v52\n\t" \
    "v_mfma_f32_32x32x16_bf16 v[32:47], v[84:87], v[68:71], v[32:47]\n\t" \
    "ds_read_b128 v[80:83], v112 offset:12288\n\t" \
    "ds_read_b128 v[84:87], v112 offset:13312\n\t" \
    "v_cvt_pk_bf16_f32 v104, v48, v49\n\t" \
    "v_cvt_pk_bf16_f32 v105, v50, v51\n\t" \
    "v_exp_f32_e32 v53, v53\n\t" \
    "v_exp_f32_e32 v54, v54\n\t" \
    "v_exp_f32_e32 v55, v55\n\t" \
    "v_mfma_f32_32x32x16_bf16 v[0:15], v[100:103], v[108:111], v[0:15]\n\t" \
    "ds_read_b128 v[92:95], v112 offset:7168\n\t" \
    "ds_read_b128 v[88:91], v112 offset:10240\n\t" \
    "v_exp_f32_e32 v56, v56\n\t" \
    "v_cvt_pk_bf16_f32 v106, v52, v53\n\t" \
    "v_cvt_pk_bf16_f32 v107, v54, v55\n\t" \
    "v_exp_f32_e32 v57, v57\n\t" \
    "v_exp_f32_e32 v58, v58\n\t" \
    "v_exp_f32_e32 v59, v59\n\t" \
    "v_mfma_f32_32x32x16_bf16 v[0:15], v[96:99], v[104:107], v[0:15]\n\t" \
    "v_exp_f32_e32 v60, v60\n\t" \
    "v_exp_f32_e32 v61, v61\n\t" \
    "v_exp_f32_e32 v62, v62\n\t" \
    "v_exp_f32_e32 v63, v63\n\t" \
    "v_cvt_pk_bf16_f32 v108, v56, v57\n\t" \
    "v_cvt_pk_bf16_f32 v109, v58, v59\n\t" \
    "v_cvt_pk_bf16_f32 v110, v60, v61\n\t" \
    "v_cvt_pk_bf16_f32 v111, v62, v63\n\t" \
    "s_setprio 2\n\t" \
    "s_waitcnt lgkmcnt(0)\n\t" \
    "v_mfma_f32_32x32x16_bf16 v[48:63], v[80:83], v[64:67], v[16:31]\n\t" \
    "v_exp_f32_e32 v32, v32\n\t" \
    "v_exp_f32_e32 v33, v33\n\t" \
    "v_exp_f32_e32 v34, v34\n\t" \
    "v_smfmac_f32_16x16x64_bf16 v[76:79], v[72:75], v[104:111], v174\n\t" \
    "v_exp_f32_e32 v35, v35\n\t" \
    "v_exp_f32_e32 v36, v36\n\t" \
    "v_mfma_f32_32x32x16_bf16 v[48:63], v[84:87], v[68:71], v[48:63]\n\t" \
    "ds_read_b128 v[80:83], v112 offset:16384\n\t" \
    "ds_read_b128 v[84:87], v112 offset:17408\n\t" \
    "v_cvt_pk_bf16_f32 v104, v32, v33\n\t" \
    "v_cvt_pk_bf16_f32 v105, v34, v35\n\t" \
    "v_exp_f32_e32 v37, v37\n\t" \
    "v_exp_f32_e32 v38, v38\n\t" \
    "v_exp_f32_e32 v39, v39\n\t" \
    "v_mfma_f32_32x32x16_bf16 v[0:15], v[92:95], v[108:111], v[0:15]\n\t" \
    "ds_read_b128 v[100:103], v112 offset:11264\n\t" \
    "ds_read_b128 v[96:99], v112 offset:14336\n\t" \
    "v_exp_f32_e32 v40, v40\n\t" \
    "v_cvt_pk_bf16_f32 v106, v36, v37\n\t" \
    "v_cvt_pk_bf16_f32 v107, v38, v39\n\t" \
    "v_exp_f32_e32 v41, v41\n\t" \
    "v_exp_f32_e32 v42, v42\n\t" \
    "v_exp_f32_e32 v43, v43\n\t" \
    "v_mfma_f32_32x32x16_bf16 v[0:15], v[88:91], v[104:107], v[0:15]\n\t" \
    "v_exp_f32_e32 v44, v44\n\t" \
    "v_exp_f32_e32 v45, v45\n\t" \
    "v_exp_f32_e32 v46, v46\n\t" \
    "v_exp_f32_e32 v47, v47\n\t" \
    "v_cvt_pk_bf16_f32 v108, v40, v41\n\t" \
    "v_cvt_pk_bf16_f32 v109, v42, v43\n\t" \
    "v_cvt_pk_bf16_f32 v110, v44, v45\n\t" \
    "v_cvt_pk_bf16_f32 v111, v46, v47\n\t" \
    "s_waitcnt lgkmcnt(0)\n\t" \
    "v_mfma_f32_32x32x16_bf16 v[32:47], v[80:83], v[64:67], v[16:31]\n\t" \
    "v_exp_f32_e32 v48, v48\n\t" \
    "v_exp_f32_e32 v49, v49\n\t" \
    "v_exp_f32_e32 v50, v50\n\t" \
    "v_smfmac_f32_16x16x64_bf16 v[76:79], v[72:75], v[104:111], v174\n\t" \
    "v_exp_f32_e32 v51, v51\n\t" \
    "v_exp_f32_e32 v52, v52\n\t" \
    "v_mfma_f32_32x32x16_bf16 v[32:47], v[84:87], v[68:71], v[32:47]\n\t" \
    "ds_read_b128 v[80:83], v112 offset:20480\n\t" \
    "ds_read_b128 v[84:87], v112 offset:21504\n\t" \
    "v_cvt_pk_bf16_f32 v104, v48, v49\n\t" \
    "v_cvt_pk_bf16_f32 v105, v50, v51\n\t" \
    "v_exp_f32_e32 v53, v53\n\t" \
    "v_exp_f32_e32 v54, v54\n\t" \
    "v_exp_f32_e32 v55, v55\n\t" \
    "v_mfma_f32_32x32x16_bf16 v[0:15], v[100:103], v[108:111], v[0:15]\n\t" \
    "ds_read_b128 v[92:95], v112 offset:15360\n\t" \
    "ds_read_b128 v[88:91], v112 offset:18432\n\t" \
    "v_exp_f32_e32 v56, v56\n\t" \
    "v_cvt_pk_bf16_f32 v106, v52, v53\n\t" \
    "v_cvt_pk_bf16_f32 v107, v54, v55\n\t" \
    "v_exp_f32_e32 v57, v57\n\t" \
    "v_exp_f32_e32 v58, v58\n\t" \
    "v_exp_f32_e32 v59, v59\n\t" \
    "v_mfma_f32_32x32x16_bf16 v[0:15], v[96:99], v[104:107], v[0:15]\n\t" \
    "v_exp_f32_e32 v60, v60\n\t" \
    "v_exp_f32_e32 v61, v61\n\t" \
    "v_exp_f32_e32 v62, v62\n\t" \
    "v_exp_f32_e32 v63, v63\n\t" \
    "v_cvt_pk_bf16_f32 v108, v56, v57\n\t" \
    "v_cvt_pk_bf16_f32 v109, v58, v59\n\t" \
    "v_cvt_pk_bf16_f32 v110, v60, v61\n\t" \
    "v_cvt_pk_bf16_f32 v111, v62, v63\n\t" \
    "s_setprio 1\n\t" \
    "s_waitcnt lgkmcnt(0)\n\t" \
    "v_mfma_f32_32x32x16_bf16 v[48:63], v[80:83], v[64:67], v[16:31]\n\t" \
    "v_exp_f32_e32 v32, v32\n\t" \
    "v_exp_f32_e32 v33, v33\n\t" \
    "v_exp_f32_e32 v34, v34\n\t" \
    "v_smfmac_f32_16x16x64_bf16 v[76:79], v[72:75], v[104:111], v174\n\t" \
    "v_exp_f32_e32 v35, v35\n\t" \
    "v_exp_f32_e32 v36, v36\n\t" \
    "v_mfma_f32_32x32x16_bf16 v[48:63], v[84:87], v[68:71], v[48:63]\n\t" \
    "ds_read_b128 v[80:83], v112 offset:24576\n\t" \
    "ds_read_b128 v[84:87], v112 offset:25600\n\t" \
    "v_cvt_pk_bf16_f32 v104, v32, v33\n\t" \
    "v_cvt_pk_bf16_f32 v105, v34, v35\n\t" \
    "v_exp_f32_e32 v37, v37\n\t" \
    "v_exp_f32_e32 v38, v38\n\t" \
    "v_exp_f32_e32 v39, v39\n\t" \
    "v_mfma_f32_32x32x16_bf16 v[0:15], v[92:95], v[108:111], v[0:15]\n\t" \
    "ds_read_b128 v[100:103], v112 offset:19456\n\t" \
    "ds_read_b128 v[96:99], v112 offset:22528\n\t" \
    "v_exp_f32_e32 v40, v40\n\t" \
    "v_cvt_pk_bf16_f32 v106, v36, v37\n\t" \
    "v_cvt_pk_bf16_f32 v107, v38, v39\n\t" \
    "v_exp_f32_e32 v41, v41\n\t" \
    "v_exp_f32_e32 v42, v42\n\t" \
    "v_exp_f32_e32 v43, v43\n\t" \
    "v_mfma_f32_32x32x16_bf16 v[0:15], v[88:91], v[104:107], v[0:15]\n\t" \
    "v_exp_f32_e32 v44, v44\n\t" \
    "v_exp_f32_e32 v45, v45\n\t" \
    "v_exp_f32_e32 v46, v46\n\t" \
    "v_exp_f32_e32 v47, v47\n\t" \
    "v_cvt_pk_bf16_f32 v108, v40, v41\n\t" \
    "v_cvt_pk_bf16_f32 v109, v42, v43\n\t" \
    "v_cvt_pk_bf16_f32 v110, v44, v45\n\t" \
    "v_cvt_pk_bf16_f32 v111, v46, v47\n\t" \
    "s_waitcnt lgkmcnt(0)\n\t" \
    "v_mfma_f32_32x32x16_bf16 v[32:47], v[80:83], v[64:67], v[16:31]\n\t" \
    "v_exp_f32_e32 v48, v48\n\t" \
    "v_exp_f32_e32 v49, v49\n\t" \
    "v_exp_f32_e32 v50, v50\n\t" \
    "v_smfmac_f32_16x16x64_bf16 v[76:79], v[72:75], v[104:111], v174\n\t" \
    "v_exp_f32_e32 v51, v51\n\t" \
    "v_exp_f32_e32 v52, v52\n\t" \
    "v_mfma_f32_32x32x16_bf16 v[32:47], v[84:87], v[68:71], v[32:47]\n\t" \
    "ds_read_b128 v[80:83], v112 offset:28672\n\t" \
    "ds_read_b128 v[84:87], v112 offset:29696\n\t" \
    "v_cvt_pk_bf16_f32 v104, v48, v49\n\t" \
    "v_cvt_pk_bf16_f32 v105, v50, v51\n\t" \
    "v_exp_f32_e32 v53, v53\n\t" \
    "v_exp_f32_e32 v54, v54\n\t" \
    "v_exp_f32_e32 v55, v55\n\t" \
    "v_mfma_f32_32x32x16_bf16 v[0:15], v[100:103], v[108:111], v[0:15]\n\t" \
    "ds_read_b128 v[92:95], v112 offset:23552\n\t" \
    "ds_read_b128 v[88:91], v112 offset:26624\n\t" \
    "v_exp_f32_e32 v56, v56\n\t" \
    "v_cvt_pk_bf16_f32 v106, v52, v53\n\t" \
    "v_cvt_pk_bf16_f32 v107, v54, v55\n\t" \
    "v_exp_f32_e32 v57, v57\n\t" \
    "v_exp_f32_e32 v58, v58\n\t" \
    "v_exp_f32_e32 v59, v59\n\t" \
    "v_mfma_f32_32x32x16_bf16 v[0:15], v[96:99], v[104:107], v[0:15]\n\t" \
    "v_exp_f32_e32 v60, v60\n\t" \
    "v_exp_f32_e32 v61, v61\n\t" \
    "v_exp_f32_e32 v62, v62\n\t" \
    "v_exp_f32_e32 v63, v63\n\t" \
    "v_cvt_pk_bf16_f32 v108, v56, v57\n\t" \
    "v_cvt_pk_bf16_f32 v109, v58, v59\n\t" \
    "v_cvt_pk_bf16_f32 v110, v60, v61\n\t" \
    "v_cvt_pk_bf16_f32 v111, v62, v63\n\t" \
    "s_setprio 0\n\t" \
    "s_cmp_eq_u32 %[hl], 0\n\t" \
    "s_cbranch_scc1 11f\n\t" \
    "s_waitcnt vmcnt(1)\n\t" \
    "11:\n" \
    "s_waitcnt lgkmcnt(0)\n\t" \
    "s_nop 15\n\t"
#define ATT_L4 \
    "s_nop 1\n\t" \
    "s_setprio 3\n\t" \
    "s_waitcnt lgkmcnt(0)\n\t" \
    "v_mfma_f32_32x32x16_bf16 v[48:63], v[80:83], v[64:67], v[16:31]\n\t" \
    "v_exp_f32_e32 v32, v32\n\t" \
    "v_exp_f32_e32 v33, v33\n\t" \
    "v_exp_f32_e32 v34, v34\n\t" \
    "v_exp_f32_e32 v35, v35\n\t" \
    "v_exp_f32_e32 v36, v36\n\t" \
    "v_mfma_f32_32x32x16_bf16 v[48:63], v[84:87], v[68:71], v[48:63]\n\t" \
    "ds_read_b128 v[80:83], v112 offset:8192\n\t" \
    "ds_read_b128 v[84:87], v112 offset:9216\n\t" \
    "v_cvt_pk_bf16_f32 v104, v32, v33\n\t" \
    "v_cvt_pk_bf16_f32 v105, v34, v35\n\t" \
    "v_exp_f32_e32 v37, v37\n\t" \
    "v_exp_f32_e32 v38, v38\n\t" \
    "v_exp_f32_e32 v39, v39\n\t" \
    "ds_read_b128 v[100:103], v112 offset:3072\n\t" \
    "ds_read_b128 v[96:99], v112 offset:6144\n\t" \
    "v_exp_f32_e32 v40, v40\n\t" \
    "v_cvt_pk_bf16_f32 v106, v36, v37\n\t" \
    "v_cvt_pk_bf16_f32 v107, v38, v39\n\t" \
    "v_exp_f32_e32 v41, v41\n\t" \
    "v_exp_f32_e32 v42, v42\n\t" \
    "v_exp_f32_e32 v43, v43\n\t" \
    "v_mfma_f32_32x32x16_bf16 v[0:15], v[88:91], v[104:107], 0\n\t" \
    "v_exp_f32_e32 v44, v44\n\t" \
    "v_exp_f32_e32 v45, v45\n\t" \
    "v_exp_f32_e32 v46, v46\n\t" \
    "v_exp_f32_e32 v47, v47\n\t" \
    "v_cvt_pk_bf16_f32 v108, v40, v41\n\t" \
    "v_cvt_pk_bf16_f32 v109, v42, v43\n\t" \
    "v_cvt_pk_bf16_f32 v110, v44, v45\n\t" \
    "v_cvt_pk_bf16_f32 v111, v46, v47\n\t" \
    "s_waitcnt lgkmcnt(0)\n\t" \
    "v_mfma_f32_32x32x16_bf16 v[32:47], v[80:83], v[64:67], v[16:31]\n\t" \
    "v_exp_f32_e32 v48, v48\n\t" \
    "v_exp_f32_e32 v49, v49\n\t" \
    "v_exp_f32_e32 v50, v50\n\t" \
    "v_smfmac_f32_16x16x64_bf16 v[76:79], v[72:75], v[104:111], v174\n\t" \
    "v_exp_f32_e32 v51, v51\n\t" \
    "v_exp_f32_e32 v52, v52\n\t" \
    "v_mfma_f32_32x32x16_bf16 v[32:47], v[84:87], v[68:71], v[32:47]\n\t" \
    "ds_read_b128 v[80:83], v112 offset:12288\n\t" \
    "ds_read_b128 v[84:87], v112 offset:13312\n\t" \
    "v_cvt_pk_bf16_f32 v104, v48, v49\n\t" \
    "v_cvt_pk_bf16_f32 v105, v50, v51\n\t" \
    "v_exp_f32_e32 v53, v53\n\t" \
    "v_exp_f32_e32 v54, v54\n\t" \
    "v_exp_f32_e32 v55, v55\n\t" \
    "v_mfma_f32_32x32x16_bf16 v[0:15], v[100:103], v[108:111], v[0:15]\n\t" \
    "ds_read_b128 v[92:95], v112 offset:7168\n\t" \
    "ds_read_b128 v[88:91], v112 offset:10240\n\t" \
    "v_exp_f32_e32 v56, v56\n\t" \
    "v_cvt_pk_bf16_f32 v106, v52, v53\n\t" \
    "v_cvt_pk_bf16_f32 v107, v54, v55\n\t" \
    "v_exp_f32_e32 v57, v57\n\t" \
    "v_exp_f32_e32 v58, v58\n\t" \
    "v_exp_f32_e32 v59, v59\n\t" \
    "v_mfma_f32_32x32x16_bf16 v[0:15], v[96:99], v[104:107], v[0:15]\n\t" \
    "v_exp_f32_e32 v60, v60\n\t" \
    "v_exp_f32_e32 v61, v61\n\t" \
    "v_exp_f32_e32 v62, v62\n\t" \
    "v_exp_f32_e32 v63, v63\n\t" \
    "v_cvt_pk_bf16_f32 v108, v56, v57\n\t" \
    "v_cvt_pk_bf16_f32 v109, v58, v59\n\t" \
    "v_cvt_pk_bf16_f32 v110, v60, v61\n\t" \
    "v_cvt_pk_bf16_f32 v111, v62, v63\n\t" \
    "s_setprio 2\n\t" \
    "s_waitcnt lgkmcnt(0)\n\t" \
    "v_mfma_f32_32x32x16_bf16 v[48:63], v[80:83], v[64:67], v[16:31]\n\t" \
    "v_exp_f32_e32 v32, v32\n\t" \
    "v_exp_f32_e32 v33, v33\n\t" \
    "v_exp_f32_e32 v34, v34\n\t" \
    "v_smfmac_f32_16x16x64_bf16 v[76:79], v[72:75], v[104:111], v174\n\t" \
    "v_exp_f32_e32 v35, v35\n\t" \
    "v_exp_f32_e32 v36, v36\n\t" \
    "v_mfma_f32_32x32x16_bf16 v[48:63], v[84:87], v[68:71], v[48:63]\n\t" \
    "ds_read_b128 v[80:83], v112 offset:16384\n\t" \
    "ds_read_b128 v[84:87], v112 offset:17408\n\t" \
    "v_cvt_pk_bf16_f32 v104, v32, v33\n\t" \
    "v_cvt_pk_bf16_f32 v105, v34, v35\n\t" \
    "v_exp_f32_e32 v37, v37\n\t" \
    "v_exp_f32_e32 v38, v38\n\t" \
    "v_exp_f32_e32 v39, v39\n\t" \
    "v_mfma_f32_32x32x16_bf16 v[0:15], v[92:95], v[108:111], v[0:15]\n\t" \
    "ds_read_b128 v[100:103], v112 offset:11264\n\t" \
    "ds_read_b128 v[96:99], v112 offset:14336\n\t" \
    "v_exp_f32_e32 v40, v40\n\t" \
    "v_cvt_pk_bf16_f32 v106, v36, v37\n\t" \
    "v_cvt_pk_bf16_f32 v107, v38, v39\n\t" \
    "v_exp_f32_e32 v41, v41\n\t" \
    "v_exp_f32_e32 v42, v42\n\t" \
    "v_exp_f32_e32 v43, v43\n\t" \
    "v_mfma_f32_32x32x16_bf16 v[0:15], v[88:91], v[104:107], v[0:15]\n\t" \
    "v_exp_f32_e32 v44, v44\n\t" \
    "v_exp_f32_e32 v45, v45\n\t" \
    "v_exp_f32_e32 v46, v46\n\t" \
    "v_exp_f32_e32 v47, v47\n\t" \
    "v_cvt_pk_bf16_f32 v108, v40, v41\n\t" \
    "v_cvt_pk_bf16_f32 v109, v42, v43\n\t" \
    "v_cvt_pk_bf16_f32 v110, v44, v45\n\t" \
    "v_cvt_pk_bf16_f32 v111, v46, v47\n\t" \
    "s_waitcnt lgkmcnt(0)\n\t" \
    "v_mfma_f32_32x32x16_bf16 v[32:47], v[80:83], v[64:67], v[16:31]\n\t" \
    "v_exp_f32_e32 v48, v48\n\t" \
    "v_exp_f32_e32 v49, v49\n\t" \
    "v_exp_f32_e32 v50, v50\n\t" \
    "v_smfmac_f32_16x16x64_bf16 v[76:79], v[72:75], v[104:111], v174\n\t" \
    "v_exp_f32_e32 v51, v51\n\t" \
    "v_exp_f32_e32 v52, v52\n\t" \
    "v_mfma_f32_32x32x16_bf16 v[32:47], v[84:87], v[68:71], v[32:47]\n\t" \
    "ds_read_b128 v[80:83], v112 offset:20480\n\t" \
    "ds_read_b128 v[84:87], v112 offset:21504\n\t" \
    "v_cvt_pk_bf16_f32 v104, v48, v49\n\t" \
    "v_cvt_pk_bf16_f32 v105, v50, v51\n\t" \
    "v_exp_f32_e32 v53, v53\n\t" \
    "v_exp_f32_e32 v54, v54\n\t" \
    "v_exp_f32_e32 v55, v55\n\t" \
    "v_mfma_f32_32x32x16_bf16 v[0:15], v[100:103], v[108:111], v[0:15]\n\t" \
    "ds_read_b128 v[92:95], v112 offset:15360\n\t" \
    "ds_read_b128 v[88:91], v112 offset:18432\n\t" \
    "v_exp_f32_e32 v56, v56\n\t" \
    "v_cvt_pk_bf16_f32 v106, v52, v53\n\t" \
    "v_cvt_pk_bf16_f32 v107, v54, v55\n\t" \
    "v_exp_f32_e32 v57, v57\n\t" \
    "v_exp_f32_e32 v58, v58\n\t" \
    "v_exp_f32_e32 v59, v59\n\t" \
    "v_mfma_f32_32x32x16_bf16 v[0:15], v[96:99], v[104:107], v[0:15]\n\t" \
    "v_exp_f32_e32 v60, v60\n\t" \
    "v_exp_f32_e32 v61, v61\n\t" \
    "v_exp_f32_e32 v62, v62\n\t" \
    "v_exp_f32_e32 v63, v63\n\t" \
    "v_cvt_pk_bf16_f32 v108, v56, v57\n\t" \
    "v_cvt_pk_bf16_f32 v109, v58, v59\n\t" \
    "v_cvt_pk_bf16_f32 v110, v60, v61\n\t" \
    "v_cvt_pk_bf16_f32 v111, v62, v63\n\t" \
    "s_setprio 0\n\t" \
    "s_waitcnt lgkmcnt(0)\n\t" \
    "s_nop 15\n\t"
#define ATT_L2 \
    "s_nop 1\n\t" \
    "s_setprio 1\n\t" \
    "s_waitcnt lgkmcnt(0)\n\t" \
    "v_mfma_f32_32x32x16_bf16 v[48:63], v[80:83], v[64:67], v[16:31]\n\t" \
    "v_exp_f32_e32 v32, v32\n\t" \
    "v_exp_f32_e32 v33, v33\n\t" \
    "v_exp_f32_e32 v34, v34\n\t" \
    "v_smfmac_f32_16x16x64_bf16 v[76:79], v[72:75], v[104:111], v174\n\t" \
    "v_exp_f32_e32 v35, v35\n\t" \
    "v_exp_f32_e32 v36, v36\n\t" \
    "v_mfma_f32_32x32x16_bf16 v[48:63], v[84:87], v[68:71], v[48:63]\n\t" \
    "ds_read_b128 v[80:83], v112 offset:24576\n\t" \
    "ds_read_b128 v[84:87], v112 offset:25600\n\t" \
    "v_cvt_pk_bf16_f32 v104, v32, v33\n\t" \
    "v_cvt_pk_bf16_f32 v105, v34, v35\n\t" \
    "v_exp_f32_e32 v37, v37\n\t" \
    "v_exp_f32_e32 v38, v38\n\t" \
    "v_exp_f32_e32 v39, v39\n\t" \
    "v_mfma_f32_32x32x16_bf16 v[0:15], v[92:95], v[108:111], v[0:15]\n\t" \
    "ds_read_b128 v[100:103], v112 offset:19456\n\t" \
    "ds_read_b128 v[96:99], v112 offset:22528\n\t" \
    "v_exp_f32_e32 v40, v40\n\t" \
    "v_cvt_pk_bf16_f32 v106, v36, v37\n\t" \
    "v_cvt_pk_bf16_f32 v107, v38, v39\n\t" \
    "v_exp_f32_e32 v41, v41\n\t" \
    "v_exp_f32_e32 v42, v42\n\t" \
    "v_exp_f32_e32 v43, v43\n\t" \
    "v_mfma_f32_32x32x16_bf16 v[0:15], v[88:91], v[104:107], v[0:15]\n\t" \
    "v_exp_f32_e32 v44, v44\n\t" \
    "v_exp_f32_e32 v45, v45\n\t" \
    "v_exp_f32_e32 v46, v46\n\t" \
    "v_exp_f32_e32 v47, v47\n\t" \
    "v_cvt_pk_bf16_f32 v108, v40, v41\n\t" \
    "v_cvt_pk_bf16_f32 v109, v42, v43\n\t" \
    "v_cvt_pk_bf16_f32 v110, v44, v45\n\t" \
    "v_cvt_pk_bf16_f32 v111, v46, v47\n\t" \
    "s_waitcnt lgkmcnt(0)\n\t" \
    "v_mfma_f32_32x32x16_bf16 v[32:47], v[80:83], v[64:67], v[16:31]\n\t" \
    "v_exp_f32_e32 v48, v48\n\t" \
    "v_exp_f32_e32 v49, v49\n\t" \
    "v_exp_f32_e32 v50, v50\n\t" \
    "v_smfmac_f32_16x16x64_bf16 v[76:79], v[72:75], v[104:111], v174\n\t" \
    "v_exp_f32_e32 v51, v51\n\t" \
    "v_exp_f32_e32 v52, v52\n\t" \
    "v_mfma_f32_32x32x16_bf16 v[32:47], v[84:87], v[68:71], v[32:47]\n\t" \
    "ds_read_b128 v[80:83], v112 offset:28672\n\t" \
    "ds_read_b128 v[84:87], v112 offset:29696\n\t" \
    "v_cvt_pk_bf16_f32 v104, v48, v49\n\t" \
    "v_cvt_pk_bf16_f32 v105, v50, v51\n\t" \
    "v_exp_f32_e32 v53, v53\n\t" \
    "v_exp_f32_e32 v54, v54\n\t" \
    "v_exp_f32_e32 v55, v55\n\t" \
    "v_mfma_f32_32x32x16_bf16 v[0:15], v[100:103], v[108:111], v[0:15]\n\t" \
    "ds_read_b128 v[92:95], v112 offset:23552\n\t" \
    "ds_read_b128 v[88:91], v112 offset:26624\n\t" \
    "v_exp_f32_e32 v56, v56\n\t" \
    "v_cvt_pk_bf16_f32 v106, v52, v53\n\t" \
    "v_cvt_pk_bf16_f32 v107, v54, v55\n\t" \
    "v_exp_f32_e32 v57, v57\n\t" \
    "v_exp_f32_e32 v58, v58\n\t" \
    "v_exp_f32_e32 v59, v59\n\t" \
    "v_mfma_f32_32x32x16_bf16 v[0:15], v[96:99], v[104:107], v[0:15]\n\t" \
    "v_exp_f32_e32 v60, v60\n\t" \
    "v_exp_f32_e32 v61, v61\n\t" \
    "v_exp_f32_e32 v62, v62\n\t" \
    "v_exp_f32_e32 v63, v63\n\t" \
    "v_cvt_pk_bf16_f32 v108, v56, v57\n\t" \
    "v_cvt_pk_bf16_f32 v109, v58, v59\n\t" \
    "v_cvt_pk_bf16_f32 v110, v60, v61\n\t" \
    "v_cvt_pk_bf16_f32 v111, v62, v63\n\t" \
    "s_setprio 0\n\t" \
    "s_waitcnt lgkmcnt(0)\n\t" \
    "s_nop 15\n\t"
#define ATT_XT \
    "s_nop 1\n\t" \
    "s_waitcnt lgkmcnt(0)\n\t" \
    "v_mfma_f32_32x32x16_bf16 v[48:63], v[80:83], v[64:67], v[16:31]\n\t" \
    "v_exp_f32_e32 v32, v32\n\t" \
    "v_exp_f32_e32 v33, v33\n\t" \
    "v_exp_f32_e32 v34, v34\n\t" \
    "v_smfmac_f32_16x16x64_bf16 v[76:79], v[72:75], v[104:111], v174\n\t" \
    "v_exp_f32_e32 v35, v35\n\t" \
    "v_exp_f32_e32 v36, v36\n\t" \
    "v_mfma_f32_32x32x16_bf16 v[48:63], v[84:87], v[68:71], v[48:63]\n\t" \
    "v_cvt_pk_bf16_f32 v104, v32, v33\n\t" \
    "v_cvt_pk_bf16_f32 v105, v34, v35\n\t" \
    "v_exp_f32_e32 v37, v37\n\t" \
    "v_exp_f32_e32 v38, v38\n\t" \
    "v_exp_f32_e32 v39, v39\n\t" \
    "v_mfma_f32_32x32x16_bf16 v[0:15], v[92:95], v[108:111], v[0:15]\n\t" \
    "ds_read_b128 v[100:103], v112 offset:27648\n\t" \
    "ds_read_b128 v[96:99], v112 offset:30720\n\t" \
    "v_exp_f32_e32 v40, v40\n\t" \
    "v_cvt_pk_bf16_f32 v106, v36, v37\n\t" \
    "v_cvt_pk_bf16_f32 v107, v38, v39\n\t" \
    "v_exp_f32_e32 v41, v41\n\t" \
    "v_exp_f32_e32 v42, v42\n\t" \
    "v_exp_f32_e32 v43, v43\n\t" \
    "v_mfma_f32_32x32x16_bf16 v[0:15], v[88:91], v[104:107], v[0:15]\n\t" \
    "v_exp_f32_e32 v44, v44\n\t" \
    "v_exp_f32_e32 v45, v45\n\t" \
    "v_exp_f32_e32 v46, v46\n\t" \
    "v_exp_f32_e32 v47, v47\n\t" \
    "v_cvt_pk_bf16_f32 v108, v40, v41\n\t" \
    "v_cvt_pk_bf16_f32 v109, v42, v43\n\t" \
    "v_cvt_pk_bf16_f32 v110, v44, v45\n\t" \
    "v_cvt_pk_bf16_f32 v111, v46, v47\n\t" \
    "s_waitcnt lgkmcnt(0)\n\t" \
    "v_exp_f32_e32 v48, v48\n\t" \
    "v_exp_f32_e32 v49, v49\n\t" \
    "v_exp_f32_e32 v50, v50\n\t" \
    "v_smfmac_f32_16x16x64_bf16 v[76:79], v[72:75], v[104:111], v174\n\t" \
    "v_exp_f32_e32 v51, v51\n\t" \
    "v_exp_f32_e32 v52, v52\n\t" \
    "v_cvt_pk_bf16_f32 v104, v48, v49\n\t" \
    "v_cvt_pk_bf16_f32 v105, v50, v51\n\t" \
    "v_exp_f32_e32 v53, v53\n\t" \
    "v_exp_f32_e32 v54, v54\n\t" \
    "v_exp_f32_e32 v55, v55\n\t" \
    "v_mfma_f32_32x32x16_bf16 v[0:15], v[100:103], v[108:111], v[0:15]\n\t" \
    "ds_read_b128 v[92:95], v112 offset:31744\n\t" \
    "v_exp_f32_e32 v56, v56\n\t" \
    "v_cvt_pk_bf16_f32 v106, v52, v53\n\t" \
    "v_cvt_pk_bf16_f32 v107, v54, v55\n\t" \
    "v_exp_f32_e32 v57, v57\n\t" \
    "v_exp_f32_e32 v58, v58\n\t" \
    "v_exp_f32_e32 v59, v59\n\t" \
    "v_mfma_f32_32x32x16_bf16 v[0:15], v[96:99], v[104:107], v[0:15]\n\t" \
    "v_exp_f32_e32 v60, v60\n\t" \
    "v_exp_f32_e32 v61, v61\n\t" \
    "v_exp_f32_e32 v62, v62\n\t" \
    "v_exp_f32_e32 v63, v63\n\t" \
    "v_cvt_pk_bf16_f32 v108, v56, v57\n\t" \
    "v_cvt_pk_bf16_f32 v109, v58, v59\n\t" \
    "v_cvt_pk_bf16_f32 v110, v60, v61\n\t" \
    "v_cvt_pk_bf16_f32 v111, v62, v63\n\t" \
    "s_waitcnt lgkmcnt(0)\n\t" \
    "s_nop 0\n\t" \
    "v_mfma_f32_32x32x16_bf16 v[0:15], v[92:95], v[108:111], v[0:15]\n\t" \
    "v_smfmac_f32_16x16x64_bf16 v[76:79], v[72:75], v[104:111], v174\n\t" \
    "s_nop 15\n\t"
#define ATT_MAIN \
    "s_mov_b64 s[48:49], %[tab]\n\t" \
    "s_mov_b32 s46, %[k0]\n\t" \
    "s_and_b32 s40, s48, 63\n\t" \
    "s_lshl_b32 s40, s40, 12\n\t" \
    "s_add_u32 s44, %[srclo], s40\n\t" \
    "s_addc_u32 s45, %[srchi], 0\n\t" \
    "s_lshr_b64 s[48:49], s[48:49], 6\n\t" \
    "s_add_u32 s46, s46, 1\n\t" \
    "s_mov_b64 vcc, -1\n\t" \
    "s_waitcnt vmcnt(0)\n\t" \
    "ds_read_b64 v[120:121], v173\n\t" \
    "s_waitcnt lgkmcnt(0)\n\t" \
    "1:\n" \
    "s_cmp_eq_u32 %[cnt], 4\n\t" \
    "s_cbranch_scc1 5f\n\t" \
    "s_waitcnt vmcnt(0)\n\t" \
    "5:\n" \
    "s_waitcnt vmcnt(9)\n\t" \
    "s_barrier\n\t" \
    "s_bitcmp1_b32 %[act], %[cnt]\n\t" \
    "s_cbranch_scc1 30f\n\t" \
    "2:\n" \
    "s_setprio 3\n\t" \
    "s_waitcnt lgkmcnt(0)\n\t" \
    "v_mfma_f32_32x32x16_bf16 v[48:63], v[80:83], v[64:67], v[16:31]\n\t" \
    "v_exp_f32_e32 v32, v32\n\t" \
    "v_exp_f32_e32 v33, v33\n\t" \
    "v_exp_f32_e32 v34, v34\n\t" \
    "v_smfmac_f32_16x16x64_bf16 v[76:79], v[72:75], v[104:111], v174\n\t" \
    "v_exp_f32_e32 v35, v35\n\t" \
    "v_exp_f32_e32 v36, v36\n\t" \
    "v_mfma_f32_32x32x16_bf16 v[48:63], v[84:87], v[68:71], v[48:63]\n\t" \
    "ds_read_b128 v[80:83], v113 offset:0\n\t" \
    "ds_read_b128 v[84:87], v113 offset:1024\n\t" \
    "v_cvt_pk_bf16_f32 v104, v32, v33\n\t" \
    "v_cvt_pk_bf16_f32 v105, v34, v35\n\t" \
    "v_exp_f32_e32 v37, v37\n\t" \
    "v_exp_f32_e32 v38, v38\n\t" \
    "v_exp_f32_e32 v39, v39\n\t" \
    "v_mfma_f32_32x32x16_bf16 v[0:15], v[92:95], v[108:111], v[0:15]\n\t" \
    "ds_read_b128 v[100:103], v112 offset:27648\n\t" \
    "ds_read_b128 v[96:99], v112 offset:30720\n\t" \
    "v_exp_f32_e32 v40, v40\n\t" \
    "v_cvt_pk_bf16_f32 v106, v36, v37\n\t" \
    "v_cvt_pk_bf16_f32 v107, v38, v39\n\t" \
    "v_exp_f32_e32 v41, v41\n\t" \
    "v_exp_f32_e32 v42, v42\n\t" \
    "v_exp_f32_e32 v43, v43\n\t" \
    "v_mfma_f32_32x32x16_bf16 v[0:15], v[88:91], v[104:107], v[0:15]\n\t" \
    "s_cbranch_vccz 20f\n\t" \
    "v_cmp_eq_u64_e64 s[42:43], v[120:121], v[140:141]\n\t" \
    "s_mov_b32 m0, %[dst]\n\t" \
    "s_nop 3\n\t" \
    "s_bitcmp1_b64 s[42:43], s46\n\t" \
    "s_cbranch_scc0 9f\n\t" \
    "12:\n" \
    "global_load_lds_dwordx4 v114, s[44:45] offset:0 sc1\n\t" \
    "20:\n" \
    "v_exp_f32_e32 v44, v44\n\t" \
    "v_exp_f32_e32 v45, v45\n\t" \
    "v_exp_f32_e32 v46, v46\n\t" \
    "v_exp_f32_e32 v47, v47\n\t" \
    "v_cvt_pk_bf16_f32 v108, v40, v41\n\t" \
    "v_cvt_pk_bf16_f32 v109, v42, v43\n\t" \
    "v_cvt_pk_bf16_f32 v110, v44, v45\n\t" \
    "v_cvt_pk_bf16_f32 v111, v46, v47\n\t" \
    "s_waitcnt lgkmcnt(0)\n\t" \
    "v_mfma_f32_32x32x16_bf16 v[32:47], v[80:83], v[64:67], v[16:31]\n\t" \
    "v_exp_f32_e32 v48, v48\n\t" \
    "v_exp_f32_e32 v49, v49\n\t" \
    "v_exp_f32_e32 v50, v50\n\t" \
    "v_smfmac_f32_16x16x64_bf16 v[76:79], v[72:75], v[104:111], v174\n\t" \
    "v_exp_f32_e32 v51, v51\n\t" \
    "v_exp_f32_e32 v52, v52\n\t" \
    "v_mfma_f32_32x32x16_bf16 v[32:47], v[84:87], v[68:71], v[32:47]\n\t" \
    "ds_read_b128 v[80:83], v113 offset:4096\n\t" \
    "ds_read_b128 v[84:87], v113 offset:5120\n\t" \
    "v_cvt_pk_bf16_f32 v104, v48, v49\n\t" \
    "v_cvt_pk_bf16_f32 v105, v50, v51\n\t" \
    "v_exp_f32_e32 v53, v53\n\t" \
    "v_exp_f32_e32 v54, v54\n\t" \
    "v_exp_f32_e32 v55, v55\n\t" \
    "v_mfma_f32_32x32x16_bf16 v[0:15], v[100:103], v[108:111], v[0:15]\n\t" \
    "ds_read_b128 v[92:95], v112 offset:31744\n\t" \
    "ds_read_b128 v[88:91], v113 offset:2048\n\t" \
    "v_exp_f32_e32 v56, v56\n\t" \
    "v_cvt_pk_bf16_f32 v106, v52, v53\n\t" \
    "v_cvt_pk_bf16_f32 v107, v54, v55\n\t" \
    "v_exp_f32_e32 v57, v57\n\t" \
    "v_exp_f32_e32 v58, v58\n\t" \
    "v_exp_f32_e32 v59, v59\n\t" \
    "v_mfma_f32_32x32x16_bf16 v[0:15], v[96:99], v[104:107], v[0:15]\n\t" \
    "s_cbranch_vccz 21f\n\t" \
    "global_load_lds_dwordx4 v114, s[44:45] offset:1024 sc1\n\t" \
    "21:\n" \
    "v_exp_f32_e32 v60, v60\n\t" \
    "v_exp_f32_e32 v61, v61\n\t" \
    "v_exp_f32_e32 v62, v62\n\t" \
    "v_exp_f32_e32 v63, v63\n\t" \
    "v_cvt_pk_bf16_f32 v108, v56, v57\n\t" \
    "v_cvt_pk_bf16_f32 v109, v58, v59\n\t" \
    "v_cvt_pk_bf16_f32 v110, v60, v61\n\t" \
    "v_cvt_pk_bf16_f32 v111, v62, v63\n\t" \
    "s_setprio 2\n\t" \
    "s_waitcnt lgkmcnt(0)\n\t" \
    "v_mfma_f32_32x32x16_bf16 v[48:63], v[80:83], v[64:67], v[16:31]\n\t" \
    "v_exp_f32_e32 v32, v32\n\t" \
    "v_exp_f32_e32 v33, v33\n\t" \
    "v_exp_f32_e32 v34, v34\n\t" \
    "v_smfmac_f32_16x16x64_bf16 v[76:79], v[72:75], v[104:111], v174\n\t" \
    "v_exp_f32_e32 v35, v35\n\t" \
    "v_exp_f32_e32 v36, v36\n\t" \
    "v_mfma_f32_32x32x16_bf16 v[48:63], v[84:87], v[68:71], v[48:63]\n\t" \
    "ds_read_b128 v[80:83], v113 offset:8192\n\t" \
    "ds_read_b128 v[84:87], v113 offset:9216\n\t" \
    "v_cvt_pk_bf16_f32 v104, v32, v33\n\t" \
    "v_cvt_pk_bf16_f32 v105, v34, v35\n\t" \
    "v_exp_f32_e32 v37, v37\n\t" \
    "v_exp_f32_e32 v38, v38\n\t" \
    "v_exp_f32_e32 v39, v39\n\t" \
    "v_mfma_f32_32x32x16_bf16 v[0:15], v[92:95], v[108:111], v[0:15]\n\t" \
    "ds_read_b128 v[100:103], v113 offset:3072\n\t" \
    "ds_read_b128 v[96:99], v113 offset:6144\n\t" \
    "v_exp_f32_e32 v40, v40\n\t" \
    "v_cvt_pk_bf16_f32 v106, v36, v37\n\t" \
    "v_cvt_pk_bf16_f32 v107, v38, v39\n\t" \
    "v_exp_f32_e32 v41, v41\n\t" \
    "v_exp_f32_e32 v42, v42\n\t" \
    "v_exp_f32_e32 v43, v43\n\t" \
    "v_mfma_f32_32x32x16_bf16 v[0:15], v[88:91], v[104:107], v[0:15]\n\t" \
    "s_cbranch_vccz 22f\n\t" \
    "global_load_lds_dwordx4 v114, s[44:45] offset:2048 sc1\n\t" \
    "22:\n" \
    "v_exp_f32_e32 v44, v44\n\t" \
    "v_exp_f32_e32 v45, v45\n\t" \
    "v_exp_f32_e32 v46, v46\n\t" \
    "v_exp_f32_e32 v47, v47\n\t" \
    "v_cvt_pk_bf16_f32 v108, v40, v41\n\t" \
    "v_cvt_pk_bf16_f32 v109, v42, v43\n\t" \
    "v_cvt_pk_bf16_f32 v110, v44, v45\n\t" \
    "v_cvt_pk_bf16_f32 v111, v46, v47\n\t" \
    "s_waitcnt lgkmcnt(0)\n\t" \
    "v_mfma_f32_32x32x16_bf16 v[32:47], v[80:83], v[64:67], v[16:31]\n\t" \
    "v_exp_f32_e32 v48, v48\n\t" \
    "v_exp_f32_e32 v49, v49\n\t" \
    "v_exp_f32_e32 v50, v50\n\t" \
    "v_smfmac_f32_16x16x64_bf16 v[76:79], v[72:75], v[104:111], v174\n\t" \
    "v_exp_f32_e32 v51, v51\n\t" \
    "v_exp_f32_e32 v52, v52\n\t" \
    "v_mfma_f32_32x32x16_bf16 v[32:47], v[84:87], v[68:71], v[32:47]\n\t" \
    "ds_read_b128 v[80:83], v113 offset:12288\n\t" \
    "ds_read_b128 v[84:87], v113 offset:13312\n\t" \
    "v_cvt_pk_bf16_f32 v104, v48, v49\n\t" \
    "v_cvt_pk_bf16_f32 v105, v50, v51\n\t" \
    "v_exp_f32_e32 v53, v53\n\t" \
    "v_exp_f32_e32 v54, v54\n\t" \
    "v_exp_f32_e32 v55, v55\n\t" \
    "v_mfma_f32_32x32x16_bf16 v[0:15], v[100:103], v[108:111], v[0:15]\n\t" \
    "ds_read_b128 v[92:95], v113 offset:7168\n\t" \
    "ds_read_b128 v[88:91], v113 offset:10240\n\t" \
    "v_exp_f32_e32 v56, v56\n\t" \
    "v_cvt_pk_bf16_f32 v106, v52, v53\n\t" \
    "v_cvt_pk_bf16_f32 v107, v54, v55\n\t" \
    "v_exp_f32_e32 v57, v57\n\t" \
    "v_exp_f32_e32 v58, v58\n\t" \
    "v_exp_f32_e32 v59, v59\n\t" \
    "v_mfma_f32_32x32x16_bf16 v[0:15], v[96:99], v[104:107], v[0:15]\n\t" \
    "s_cbranch_vccz 23f\n\t" \
    "global_load_lds_dwordx4 v114, s[44:45] offset:3072 sc1\n\t" \
    "global_load_dwordx2 v[120:121], v170, %[ws] sc1\n\t" \
    "23:\n" \
    "v_exp_f32_e32 v60, v60\n\t" \
    "v_exp_f32_e32 v61, v61\n\t" \
    "v_exp_f32_e32 v62, v62\n\t" \
    "v_exp_f32_e32 v63, v63\n\t" \
    "v_cvt_pk_bf16_f32 v108, v56, v57\n\t" \
    "v_cvt_pk_bf16_f32 v109, v58, v59\n\t" \
    "v_cvt_pk_bf16_f32 v110, v60, v61\n\t" \
    "v_cvt_pk_bf16_f32 v111, v62, v63\n\t" \
    "s_setprio 1\n\t" \
    "s_waitcnt lgkmcnt(0)\n\t" \
    "v_mfma_f32_32x32x16_bf16 v[48:63], v[80:83], v[64:67], v[16:31]\n\t" \
    "v_exp_f32_e32 v32, v32\n\t" \
    "v_exp_f32_e32 v33, v33\n\t" \
    "v_exp_f32_e32 v34, v34\n\t" \
    "v_smfmac_f32_16x16x64_bf16 v[76:79], v[72:75], v[104:111], v174\n\t" \
    "v_exp_f32_e32 v35, v35\n\t" \
    "v_exp_f32_e32 v36, v36\n\t" \
    "v_mfma_f32_32x32x16_bf16 v[48:63], v[84:87], v[68:71], v[48:63]\n\t" \
    "ds_read_b128 v[80:83], v113 offset:16384\n\t" \
    "ds_read_b128 v[84:87], v113 offset:17408\n\t" \
    "v_cvt_pk_bf16_f32 v104, v32, v33\n\t" \
    "v_cvt_pk_bf16_f32 v105, v34, v35\n\t" \
    "v_exp_f32_e32 v37, v37\n\t" \
    "v_exp_f32_e32 v38, v38\n\t" \
    "v_exp_f32_e32 v39, v39\n\t" \
    "v_mfma_f32_32x32x16_bf16 v[0:15], v[92:95], v[108:111], v[0:15]\n\t" \
    "ds_read_b128 v[100:103], v113 offset:11264\n\t" \
    "ds_read_b128 v[96:99], v113 offset:14336\n\t" \
    "v_exp_f32_e32 v40, v40\n\t" \
    "v_cvt_pk_bf16_f32 v106, v36, v37\n\t" \
    "v_cvt_pk_bf16_f32 v107, v38, v39\n\t" \
    "v_exp_f32_e32 v41, v41\n\t" \
    "v_exp_f32_e32 v42, v42\n\t" \
    "v_exp_f32_e32 v43, v43\n\t" \
    "v_mfma_f32_32x32x16_bf16 v[0:15], v[88:91], v[104:107], v[0:15]\n\t" \
    "s_add_u32 %[dst], %[dst], 0x8000\n\t" \
    "s_cmp_eq_u32 %[dst], %[dend]\n\t" \
    "s_cselect_b32 %[dst], %[dbeg], %[dst]\n\t" \
    "s_add_u32 %[nx], %[nx], 0x8000\n\t" \
    "s_cmp_eq_u32 %[nx], 0x18000\n\t" \
    "s_cselect_b32 %[nx], 0, %[nx]\n\t" \
    "s_bitcmp1_b32 %[hk], %[cnt]\n\t" \
    "s_cbranch_scc1 40f\n\t" \
    "24:\n" \
    "v_exp_f32_e32 v44, v44\n\t" \
    "v_exp_f32_e32 v45, v45\n\t" \
    "v_exp_f32_e32 v46, v46\n\t" \
    "v_exp_f32_e32 v47, v47\n\t" \
    "v_cvt_pk_bf16_f32 v108, v40, v41\n\t" \
    "v_cvt_pk_bf16_f32 v109, v42, v43\n\t" \
    "v_cvt_pk_bf16_f32 v110, v44, v45\n\t" \
    "v_cvt_pk_bf16_f32 v111, v46, v47\n\t" \
    "s_waitcnt lgkmcnt(0)\n\t" \
    "v_mfma_f32_32x32x16_bf16 v[32:47], v[80:83], v[64:67], v[16:31]\n\t" \
    "v_exp_f32_e32 v48, v48\n\t" \
    "v_exp_f32_e32 v49, v49\n\t" \
    "v_exp_f32_e32 v50, v50\n\t" \
    "v_smfmac_f32_16x16x64_bf16 v[76:79], v[72:75], v[104:111], v174\n\t" \
    "v_exp_f32_e32 v51, v51\n\t" \
    "v_exp_f32_e32 v52, v52\n\t" \
    "v_mfma_f32_32x32x16_bf16 v[32:47], v[84:87], v[68:71], v[32:47]\n\t" \
    "ds_read_b128 v[80:83], v113 offset:20480\n\t" \
    "ds_read_b128 v[84:87], v113 offset:21504\n\t" \
    "v_cvt_pk_bf16_f32 v104, v48, v49\n\t" \
    "v_cvt_pk_bf16_f32 v105, v50, v51\n\t" \
    "v_exp_f32_e32 v53, v53\n\t" \
    "v_exp_f32_e32 v54, v54\n\t" \
    "v_exp_f32_e32 v55, v55\n\t" \
    "v_mfma_f32_32x32x16_bf16 v[0:15], v[100:103], v[108:111], v[0:15]\n\t" \
    "ds_read_b128 v[92:95], v113 offset:15360\n\t" \
    "ds_read_b128 v[88:91], v113 offset:18432\n\t" \
    "v_exp_f32_e32 v56, v56\n\t" \
    "v_cvt_pk_bf16_f32 v106, v52, v53\n\t" \
    "v_cvt_pk_bf16_f32 v107, v54, v55\n\t" \
    "v_exp_f32_e32 v57, v57\n\t" \
    "v_exp_f32_e32 v58, v58\n\t" \
    "v_exp_f32_e32 v59, v59\n\t" \
    "v_mfma_f32_32x32x16_bf16 v[0:15], v[96:99], v[104:107], v[0:15]\n\t" \
    "s_bitcmp1_b32 %[hk], %[cnt]\n\t" \
    "s_cbranch_scc1 41f\n\t" \
    "25:\n" \
    "v_exp_f32_e32 v60, v60\n\t" \
    "v_exp_f32_e32 v61, v61\n\t" \
    "v_exp_f32_e32 v62, v62\n\t" \
    "v_exp_f32_e32 v63, v63\n\t" \
    "v_cvt_pk_bf16_f32 v108, v56, v57\n\t" \
    "v_cvt_pk_bf16_f32 v109, v58, v59\n\t" \
    "v_cvt_pk_bf16_f32 v110, v60, v61\n\t" \
    "v_cvt_pk_bf16_f32 v111, v62, v63\n\t" \
    "s_setprio 0\n\t" \
    "s_waitcnt lgkmcnt(0)\n\t" \
    "v_mfma_f32_32x32x16_bf16 v[48:63], v[80:83], v[64:67], v[16:31]\n\t" \
    "v_exp_f32_e32 v32, v32\n\t" \
    "v_exp_f32_e32 v33, v33\n\t" \
    "v_exp_f32_e32 v34, v34\n\t" \
    "v_smfmac_f32_16x16x64_bf16 v[76:79], v[72:75], v[104:111], v174\n\t" \
    "v_exp_f32_e32 v35, v35\n\t" \
    "v_exp_f32_e32 v36, v36\n\t" \
    "v_mfma_f32_32x32x16_bf16 v[48:63], v[84:87], v[68:71], v[48:63]\n\t" \
    "ds_read_b128 v[80:83], v113 offset:24576\n\t" \
    "ds_read_b128 v[84:87], v113 offset:25600\n\t" \
    "v_cvt_pk_bf16_f32 v104, v32, v33\n\t" \
    "v_cvt_pk_bf16_f32 v105, v34, v35\n\t" \
    "v_exp_f32_e32 v37, v37\n\t" \
    "v_exp_f32_e32 v38, v38\n\t" \
    "v_exp_f32_e32 v39, v39\n\t" \
    "v_mfma_f32_32x32x16_bf16 v[0:15], v[92:95], v[108:111], v[0:15]\n\t" \
    "ds_read_b128 v[100:103], v113 offset:19456\n\t" \
    "ds_read_b128 v[96:99], v113 offset:22528\n\t" \
    "v_exp_f32_e32 v40, v40\n\t" \
    "v_cvt_pk_bf16_f32 v106, v36, v37\n\t" \
    "v_cvt_pk_bf16_f32 v107, v38, v39\n\t" \
    "v_exp_f32_e32 v41, v41\n\t" \
    "v_exp_f32_e32 v42, v42\n\t" \
    "v_exp_f32_e32 v43, v43\n\t" \
    "v_mfma_f32_32x32x16_bf16 v[0:15], v[88:91], v[104:107], v[0:15]\n\t" \
    "s_and_b32 s40, s48, 63\n\t" \
    "s_lshl_b32 s40, s40, 12\n\t" \
    "s_add_u32 s44, %[srclo], s40\n\t" \
    "s_addc_u32 s45, %[srchi], 0\n\t" \
    "s_lshr_b64 s[48:49], s[48:49], 6\n\t" \
    "s_add_u32 s46, s46, 1\n\t" \
    "s_bitcmp1_b32 %[hk], %[cnt]\n\t" \
    "s_cbranch_scc1 42f\n\t" \
    "26:\n" \
    "v_exp_f32_e32 v44, v44\n\t" \
    "v_exp_f32_e32 v45, v45\n\t" \
    "v_exp_f32_e32 v46, v46\n\t" \
    "v_exp_f32_e32 v47, v47\n\t" \
    "v_cvt_pk_bf16_f32 v108, v40, v41\n\t" \
    "v_cvt_pk_bf16_f32 v109, v42, v43\n\t" \
    "v_cvt_pk_bf16_f32 v110, v44, v45\n\t" \
    "v_cvt_pk_bf16_f32 v111, v46, v47\n\t" \
    "s_waitcnt lgkmcnt(0)\n\t" \
    "v_mfma_f32_32x32x16_bf16 v[32:47], v[80:83], v[64:67], v[16:31]\n\t" \
    "v_exp_f32_e32 v48, v48\n\t" \
    "v_exp_f32_e32 v49, v49\n\t" \
    "v_exp_f32_e32 v50, v50\n\t" \
    "v_smfmac_f32_16x16x64_bf16 v[76:79], v[72:75], v[104:111], v174\n\t" \
    "v_exp_f32_e32 v51, v51\n\t" \
    "v_exp_f32_e32 v52, v52\n\t" \
    "v_mfma_f32_32x32x16_bf16 v[32:47], v[84:87], v[68:71], v[32:47]\n\t" \
    "ds_read_b128 v[80:83], v113 offset:28672\n\t" \
    "ds_read_b128 v[84:87], v113 offset:29696\n\t" \
    "v_cvt_pk_bf16_f32 v104, v48, v49\n\t" \
    "v_cvt_pk_bf16_f32 v105, v50, v51\n\t" \
    "v_exp_f32_e32 v53, v53\n\t" \
    "v_exp_f32_e32 v54, v54\n\t" \
    "v_exp_f32_e32 v55, v55\n\t" \
    "v_mfma_f32_32x32x16_bf16 v[0:15], v[100:103], v[108:111], v[0:15]\n\t" \
    "ds_read_b128 v[92:95], v113 offset:23552\n\t" \
    "ds_read_b128 v[88:91], v113 offset:26624\n\t" \
    "v_exp_f32_e32 v56, v56\n\t" \
    "v_cvt_pk_bf16_f32 v106, v52, v53\n\t" \
    "v_cvt_pk_bf16_f32 v107, v54, v55\n\t" \
    "v_exp_f32_e32 v57, v57\n\t" \
    "v_exp_f32_e32 v58, v58\n\t" \
    "v_exp_f32_e32 v59, v59\n\t" \
    "v_mfma_f32_32x32x16_bf16 v[0:15], v[96:99], v[104:107], v[0:15]\n\t" \
    "s_bitcmp1_b32 %[hk], %[cnt]\n\t" \
    "s_cbranch_scc1 43f\n\t" \
    "27:\n" \
    "v_mov_b32_e32 v112, v113\n\t" \
    "v_add_u32_e32 v113, %[nx], v115\n\t" \
    "s_sub_u32 %[cnt], %[cnt], 1\n\t" \
    "s_cmp_lg_u32 %[cnt], 0\n\t" \
    "v_exp_f32_e32 v60, v60\n\t" \
    "v_exp_f32_e32 v61, v61\n\t" \
    "v_exp_f32_e32 v62, v62\n\t" \
    "v_exp_f32_e32 v63, v63\n\t" \
    "v_cvt_pk_bf16_f32 v108, v56, v57\n\t" \
    "v_cvt_pk_bf16_f32 v109, v58, v59\n\t" \
    "v_cvt_pk_bf16_f32 v110, v60, v61\n\t" \
    "v_cvt_pk_bf16_f32 v111, v62, v63\n\t" \
    "s_cbranch_scc1 1b\n\t" \
    "7:\n" \
    "s_setprio 0\n\t" \
    "s_waitcnt lgkmcnt(0)\n\t" \
    "v_mfma_f32_32x32x16_bf16 v[48:63], v[80:83], v[64:67], v[16:31]\n\t" \
    "v_exp_f32_e32 v32, v32\n\t" \
    "v_exp_f32_e32 v33, v33\n\t" \
    "v_exp_f32_e32 v34, v34\n\t" \
    "v_smfmac_f32_16x16x64_bf16 v[76:79], v[72:75], v[104:111], v174\n\t" \
    "v_exp_f32_e32 v35, v35\n\t" \
    "v_exp_f32_e32 v36, v36\n\t" \
    "v_mfma_f32_32x32x16_bf16 v[48:63], v[84:87], v[68:71], v[48:63]\n\t" \
    "v_cvt_pk_bf16_f32 v104, v32, v33\n\t" \
    "v_cvt_pk_bf16_f32 v105, v34, v35\n\t" \
    "v_exp_f32_e32 v37, v37\n\t" \
    "v_exp_f32_e32 v38, v38\n\t" \
    "v_exp_f32_e32 v39, v39\n\t" \
    "v_mfma_f32_32x32x16_bf16 v[0:15], v[92:95], v[108:111], v[0:15]\n\t" \
    "ds_read_b128 v[100:103], v112 offset:27648\n\t" \
    "ds_read_b128 v[96:99], v112 offset:30720\n\t" \
    "v_exp_f32_e32 v40, v40\n\t" \
    "v_cvt_pk_bf16_f32 v106, v36, v37\n\t" \
    "v_cvt_pk_bf16_f32 v107, v38, v39\n\t" \
    "v_exp_f32_e32 v41, v41\n\t" \
    "v_exp_f32_e32 v42, v42\n\t" \
    "v_exp_f32_e32 v43, v43\n\t" \
    "v_mfma_f32_32x32x16_bf16 v[0:15], v[88:91], v[104:107], v[0:15]\n\t" \
    "v_exp_f32_e32 v44, v44\n\t" \
    "v_exp_f32_e32 v45, v45\n\t" \
    "v_exp_f32_e32 v46, v46\n\t" \
    "v_exp_f32_e32 v47, v47\n\t" \
    "v_cvt_pk_bf16_f32 v108, v40, v41\n\t" \
    "v_cvt_pk_bf16_f32 v109, v42, v43\n\t" \
    "v_cvt_pk_bf16_f32 v110, v44, v45\n\t" \
    "v_cvt_pk_bf16_f32 v111, v46, v47\n\t" \
    "s_waitcnt lgkmcnt(0)\n\t" \
    "v_exp_f32_e32 v48, v48\n\t" \
    "v_exp_f32_e32 v49, v49\n\t" \
    "v_exp_f32_e32 v50, v50\n\t" \
    "v_smfmac_f32_16x16x64_bf16 v[76:79], v[72:75], v[104:111], v174\n\t" \
    "v_exp_f32_e32 v51, v51\n\t" \
    "v_exp_f32_e32 v52, v52\n\t" \
    "v_cvt_pk_bf16_f32 v104, v48, v49\n\t" \
    "v_cvt_pk_bf16_f32 v105, v50, v51\n\t" \
    "v_exp_f32_e32 v53, v53\n\t" \
    "v_exp_f32_e32 v54, v54\n\t" \
    "v_exp_f32_e32 v55, v55\n\t" \
    "v_mfma_f32_32x32x16_bf16 v[0:15], v[100:103], v[108:111], v[0:15]\n\t" \
    "ds_read_b128 v[92:95], v112 offset:31744\n\t" \
    "v_exp_f32_e32 v56, v56\n\t" \
    "v_cvt_pk_bf16_f32 v106, v52, v53\n\t" \
    "v_cvt_pk_bf16_f32 v107, v54, v55\n\t" \
    "v_exp_f32_e32 v57, v57\n\t" \
    "v_exp_f32_e32 v58, v58\n\t" \
    "v_exp_f32_e32 v59, v59\n\t" \
    "v_mfma_f32_32x32x16_bf16 v[0:15], v[96:99], v[104:107], v[0:15]\n\t" \
    "v_exp_f32_e32 v60, v60\n\t" \
    "v_exp_f32_e32 v61, v61\n\t" \
    "v_exp_f32_e32 v62, v62\n\t" \
    "v_exp_f32_e32 v63, v63\n\t" \
    "v_cvt_pk_bf16_f32 v108, v56, v57\n\t" \
    "v_cvt_pk_bf16_f32 v109, v58, v59\n\t" \
    "v_cvt_pk_bf16_f32 v110, v60, v61\n\t" \
    "v_cvt_pk_bf16_f32 v111, v62, v63\n\t" \
    "s_waitcnt lgkmcnt(0)\n\t" \
    "s_nop 0\n\t" \
    "v_mfma_f32_32x32x16_bf16 v[0:15], v[92:95], v[108:111], v[0:15]\n\t" \
    "v_smfmac_f32_16x16x64_bf16 v[76:79], v[72:75], v[104:111], v174\n\t" \
    "s_nop 15\n\t" \
    "s_branch 99f\n\t" \
    "9:\n" \
    "s_mov_b32 s47, 0x40000\n\t" \
    "10:\n" \
    "s_sub_u32 s47, s47, 1\n\t" \
    "s_cbranch_scc1 12b\n\t" \
    "global_load_dwordx2 v[120:121], v170, %[ws] sc1\n\t" \
    "s_waitcnt vmcnt(0)\n\t" \
    "v_cmp_eq_u64_e64 s[42:43], v[120:121], v[140:141]\n\t" \
    "s_nop 4\n\t" \
    "s_bitcmp1_b64 s[42:43], s46\n\t" \
    "s_cbranch_scc1 12b\n\t" \
    "s_sleep 4\n\t" \
    "s_branch 10b\n\t" \
    "50:\n" \
    "global_store_dwordx4 v171, v[176:179], %[ws] offset:-3072 sc1\n\t" \
    "global_store_dwordx4 v171, v[180:183], %[ws] offset:-2944 sc1\n\t" \
    "global_store_dwordx4 v172, v[184:187], %[ws] offset:-1024 sc1\n\t" \
    "global_store_dwordx4 v172, v[188:191], %[ws] offset:-768 sc1\n\t" \
    "s_branch 24b\n\t" \
    "51:\n" \
    "global_store_dwordx4 v171, v[192:195], %[ws] offset:1024 sc1\n\t" \
    "global_store_dwordx4 v171, v[196:199], %[ws] offset:1152 sc1\n\t" \
    "global_store_dwordx4 v172, v[200:203], %[ws] offset:3072 sc1\n\t" \
    "global_store_dwordx4 v172, v[204:207], %[ws] offset:3328 sc1\n\t" \
    "s_branch 25b\n\t" \
    "40:\n" \
    "s_cmp_eq_u32 %[cnt], 7\n\t" \
    "s_cbranch_scc1 50b\n\t" \
    "s_add_u32 m0, %[wdst], 0\n\t" \
    "s_nop 0\n\t" \
    "global_load_lds_dwordx4 v116, %[wo01]\n\t" \
    "global_load_lds_dwordx4 v117, %[wo01] offset:1024\n\t" \
    "v_add_u32_e32 v116, 0x20000, v116\n\t" \
    "v_add_u32_e32 v117, 0x20000, v117\n\t" \
    "s_branch 24b\n\t" \
    "41:\n" \
    "s_cmp_eq_u32 %[cnt], 7\n\t" \
    "s_cbranch_scc1 51b\n\t" \
    "s_add_u32 m0, %[wdst], 16384\n\t" \
    "s_nop 0\n\t" \
    "global_load_lds_dwordx4 v116, %[wo01]\n\t" \
    "global_load_lds_dwordx4 v117, %[wo01] offset:1024\n\t" \
    "v_subrev_u32_e32 v116, 0x20000, v116\n\t" \
    "v_subrev_u32_e32 v117, 0x20000, v117\n\t" \
    "s_branch 25b\n\t" \
    "42:\n" \
    "s_cmp_eq_u32 %[cnt], 7\n\t" \
    "s_cbranch_scc1 26b\n\t" \
    "s_add_u32 m0, %[wdst], 32768\n\t" \
    "s_nop 0\n\t" \
    "global_load_lds_dwordx4 v116, %[wo23]\n\t" \
    "global_load_lds_dwordx4 v117, %[wo23] offset:1024\n\t" \
    "v_add_u32_e32 v116, 0x20000, v116\n\t" \
    "v_add_u32_e32 v117, 0x20000, v117\n\t" \
    "s_branch 26b\n\t" \
    "43:\n" \
    "s_cmp_eq_u32 %[cnt], 7\n\t" \
    "s_cbranch_scc1 27b\n\t" \
    "s_add_u32 m0, %[wdst], 49152\n\t" \
    "s_nop 0\n\t" \
    "global_load_lds_dwordx4 v116, %[wo23]\n\t" \
    "global_load_lds_dwordx4 v117, %[wo23] offset:1024\n\t" \
    "global_load_dword v168, v119, %[wo]\n\t" \
    "s_branch 27b\n\t" \
    "30:\n" \
    "s_cmp_eq_u32 %[cnt], 1\n\t" \
    "s_cbranch_scc0 31f\n\t" \
    "s_mov_b64 vcc, 0\n\t" \
    "s_bitcmp1_b32 %[mode], 1\n\t" \
    "s_cbranch_scc1 7b\n\t" \
    "s_branch 2b\n\t" \
    "31:\n" \
    "s_cmp_eq_u32 %[cnt], 6\n\t" \
    "s_cbranch_scc0 33f\n\t" \
    "global_store_dwordx2 v175, v[140:141], %[ws] sc1\n\t" \
    "s_branch 2b\n\t" \
    "33:\n" \
    "s_cmp_eq_u32 %[cnt], 5\n\t" \
    "s_cbranch_scc0 34f\n\t" \
    "global_store_dwordx2 v118, v[140:141], %[ws] sc1\n\t" \
    "s_branch 2b\n\t" \
    "34:\n" \
    "s_cmp_eq_u32 %[cnt], 3\n\t" \
    "s_cbranch_scc0 35f\n\t" \
    "global_load_dwordx2 v[168:169], v142, %[ws] sc1\n\t" \
    "s_branch 2b\n\t" \
    "35:\n" \
    "v_cmp_eq_u64_e64 s[42:43], v[168:169], v[140:141]\n\t" \
    "s_nop 4\n\t" \
    "s_cmp_eq_u64 s[42:43], -1\n\t" \
    "s_cbranch_scc0 2b\n\t" \
    "global_load_dwordx4 v[122:125], v143, %[ws] offset:-2048 sc1\n\t" \
    "global_load_dwordx4 v[126:129], v143, %[ws] offset:-1024 sc1\n\t" \
    "global_load_dwordx4 v[130:133], v143, %[ws] offset:0 sc1\n\t" \
    "global_load_dwordx4 v[134:137], v143, %[ws] offset:1024 sc1\n\t" \
    "global_load_dwordx2 v[138:139], v143, %[ws] offset:2048 sc1\n\t" \
    "s_bitset1_b32 %[mode], 2\n\t" \
    "s_branch 2b\n\t" \
    "99:\n"
#define ATT_TMP "v48", "v49", "v50", "v51", "v52", "v53", "v54", "v55", "v56", "v57", "v58", "v59", "v60", "v61", "v62", "v63", "v96", "v97", "v98", "v99", "v100", "v101", "v102", "v103"

constexpr int K1_X = 0, K1_W = 81920, K1_WB = 24576;
constexpr int K2_SLOT = 32768, K2_QX = 3 * K2_SLOT, K2_FL = 147456;
constexpr int LDS_BYTES = 163840;
constexpr int p3_wslot(int t) { return (t % 6) < 4 ? 98304 + (t % 6) * 16384 : 65536 + ((t % 6) - 4) * 16384; }
constexpr int p3_allow(int t) { return (((t < 2 || t - 2 >= 1) && t + 3 < 16) ? 4 : 0) + (t + 3 < 16 ? 4 : 0) + ((t >= 1 && t + 5 < 16) ? 4 : 0); }

__global__ __launch_bounds__(512, 2) void mk_fused(const u16* __restrict__ xA, const u16* __restrict__ wA, const u16* __restrict__ woA,
                                                   const u16* __restrict__ xB, const u16* __restrict__ wB, const u16* __restrict__ woB, char* ws0, u16* __restrict__ out) {
    char* ws = ws0;
    __shared__ __attribute__((aligned(1024))) char smem[LDS_BYTES];
    const unsigned lds0 = (unsigned)(uintptr_t)(LAS char*)smem;
    const int wid = __builtin_amdgcn_readfirstlane(threadIdx.x >> 6);
    int lane = threadIdx.x & 63;
#define BRX 1
    const int b = blockIdx.x, xcd = b & 7, j = b >> 3, br = (xcd >> 2) ^ BRX, qd = xcd & 3;
    const int mt = (qd & 1) * 4 + (j & 3), hp = (qd >> 1) * 8 + (j >> 2);
    const int mt3 = qd * 4 + (j & 3), nt3 = j >> 2;
    u64 tag;
    u32x4 fq[2][2], fk[2][2], fv[2][2];
    f32x16 band;
#define RBLK(k, w) ((((k) + (w) - (((k) + (w) >= 7) ? 7 : 0)) + (((k) + (w) - (((k) + (w) >= 7) ? 7 : 0)) >= mt)) * 8 + (k) + ((k) + (w) >= 7))
    u64 tabf;
    unsigned c_fl, c_pv, c_wb, c_tw, c_aw, c_fo;
#define PRECOMPUTE(BAND) do { const int g_ = wid >> 2, wi_ = wid & 3, c16_ = lane & 15, g4_ = lane >> 4; \
        if (BAND) band = band_mask(lane); \
        tabf = (u64)(mt * 8 + wid) << 42; \
        _Pragma("unroll") for (int k = 0; k < 7; ++k) tabf |= (u64)RBLK(k, wid) << (6 * k); \
        c_fl = (unsigned)WS_FK + (2 * hp + br) * 32 * FS + (((unsigned)(tabf >> (6 * (lane & 7))) & 63u) >> 1) * FS;        \
        c_wb = g4_ * 2048 + ((c16_ ^ (g4_ << 1)) << 4); \
        { const int i = (mt3 * 8 + wid) * 12 + ((lane * 3) >> 4); c_tw = ((((4 + (i >> 7) + 2 * nt3) & 15) * 64 + ((i & 127) >> 1)) * 2048 + (i & 1) * 128); } \
        { const int q4 = c16_ >> 2, p = lane & 3, k = 16 * (g4_ >> 1) + 4 * (g4_ & 1) + q4; \
          c_aw = lds0 + p3_wslot(0) + g_ * 16384 + k * 256 + (((8 * (wi_ & 1) + (p >> 1)) ^ ((k & 7) << 1)) << 4) + (p & 1) * 8; } \
        c_fo = (unsigned)WS_FO + ((br * 64 + mt3 * 4 + wi_) * 32 + 4 * (c16_ >> 1) + 2 * (1 - g_) + (lane & 1)) * FS; \
        asm volatile("" : "+v"(band), "+s"(tabf), "+v"(c_fl), "+v"(c_wb), "+v"(c_tw), "+v"(c_aw), "+v"(c_fo)); } while (0)
    STAMP_AT(0, 0);
    {
        const int g = wid >> 2, wi = wid & 3, mw = wi >> 1, hd = wi & 1;
        if (wid == 7) {
            const char* pc; unsigned len;
            asm volatile("s_getpc_b64 %0\n.Lmk_code_begin:\n\ts_mov_b32 %1, .Lmk_code_end-.Lmk_code_begin" : "=s"(pc), "=s"(len));
            const unsigned off = (unsigned)(j * 64 + lane) * 64u;
            asm volatile("s_mov_b32 m0, %2\n\ts_nop 0\n\tglobal_load_lds_dword %0, %1" :: "v"(off < len ? off : 0u), "s"(pc), "s"(lds0 + 159744) : "memory");
        }
        unsigned vx[4], vw;
        { const unsigned r8 = lane >> 3, c8 = lane & 7, b1 = (lane >> 4) & 1;
          vw = (wid * 8 + r8) * 6144 + ((c8 ^ ((b1 | ((wid & 1) << 1)) << 1)) << 4);
          const unsigned st = (16384u << br) - 1024u;
          vx[0] = (((r8 << br) + (br ? (wi >> 1) * 64 + (wi & 1) : wi * 32)) << 11) + ((c8 ^ (b1 | ((lane >> 5) << 2))) << 4);
          vx[1] = vx[0] + st; vx[2] = (vx[0] ^ 32) + 2 * st; vx[3] = vx[2] + st; }
        asm volatile("" : "+v"(vx[0]), "+v"(vx[1]), "+v"(vx[2]), "+v"(vx[3]), "+v"(vw));
        SBAR();
        const char* x = (const char*)(br ? xB : xA) + (size_t)mt * 256 * 2048;
        const char* w = (const char*)(br ? wB : wA) + hp * 128;
        const unsigned xdst_o = lds0 + K1_X + wi * 4096;
        const unsigned wdst = lds0 + K1_W + wid * 1024;
        const char* xo = x + (size_t)g * 128 * 2048;
#define ROTM 4
        const int kt0 = ROTM * (j & 3);
#define STAGE_X(kt, dst) do { const char* s_ = xo + (((kt) + kt0) & 15) * 128; glds_run<0>(s_, vx[0], dst); glds_run<1>(s_, vx[1], 0); glds_run<2>(s_, vx[2], 0); glds_run<3>(s_, vx[3], 0); } while (0)
#define STAGE_W(kt, buf) do { const char* s_ = w + (size_t)(((kt) + kt0) & 15) * 64 * 6144; glds_sec<0>(s_, vw, wdst + (buf) * K1_WB); glds_sec<1>(s_, vw, wdst + (buf) * K1_WB); glds_sec<2>(s_, vw, wdst + (buf) * K1_WB); } while (0)
        JIT(14);
        STAGE_W(0, 0); STAGE_X(0, xdst_o + g * 16384); STAGE_W(1, 1); STAGE_X(1, xdst_o + (2 + g) * 16384);
        { u64 e; asm volatile("s_load_dwordx2 %0, %1, 0x0\n\ts_waitcnt lgkmcnt(0)" : "=s"(e) : "s"(ws + WS_EP) : "memory");
          e += 0x9E3779B97F4A7C15ull; e = (e ^ (e >> 30)) * 0xBF58476D1CE4E5B9ull; e = (e ^ (e >> 27)) * 0x94D049BB133111EBull; tag = e ^ (e >> 31); }
        const int c16 = lane & 15, g4 = lane >> 4;
        unsigned ax[2], aw[2];
        ax[0] = lds0 + K1_X + g * 16384 + (mw * 64 + 16 * (c16 >> 3) + (c16 & 7)) * 128 + ((g4 ^ (((c16 >> 1) & 1) | ((c16 >> 3) << 1) | (((c16 >> 2) & 1) << 2))) << 4);
        ax[1] = ax[0] ^ 64;
        { const int q4 = c16 >> 2, p = lane & 3;
          aw[0] = lds0 + K1_W + (8 * g4 + q4) * 128 + (((hd * 4 + (p >> 1)) ^ ((((q4 >> 1) & 1) | ((g4 & 1) << 1)) << 1)) << 4) + (p & 1) * 8; aw[1] = aw[0] ^ 32; }
        if (g == 1) WAITBAR_(11); else WAITBAR_(7);
        SA_(1);
        if (g == 1) { PRECOMPUTE(true); WAITBAR_(7); }
        f32x4 acc[4][6];
#pragma unroll
        for (int m = 0; m < 4; ++m)
#pragma unroll
            for (int n = 0; n < 6; ++n) acc[m][n] = f32x4{};
        SBAR();
        unsigned wr_ = 0, xr_ = g * 16384, dw = wdst + 2 * K1_WB, dx = xdst_o + (g ? 0 : 4 * 16384);
        int tk = (2 + kt0) & 15;
        const char* sx = xo + tk * 128; const char* sw = w + (size_t)tk * 64 * 6144;
#define RDX(m) do { xf[m][0] = ldsrd128(ax[0] + ((m) >> 1) * 4096 + ((m) & 1) * 1024); xf[m][1] = ldsrd128(ax[1] + ((m) >> 1) * 4096 + ((m) & 1) * 1024); } while (0)
#define RDW(n, ks) wf[n][ks] = cat8(ldstr(aw[(n) & 1] + ((n) >> 1) * 8192 + (ks) * 4096), ldstr(aw[(n) & 1] + ((n) >> 1) * 8192 + (ks) * 4096 + 512))
#define MM(m, n, k) do { if ((n) < 4) acc[m][n] = MFMA16(wf[n][k], xf[m][k], acc[m][n]); else acc[m][n] = MFMA16(xf[m][k], wf[n][k], acc[m][n]); } while (0)
#define P1_MMA() _Pragma("unroll") for (int m = 0; m < 4; ++m) _Pragma("unroll") for (int n = 0; n < 6; ++n) { MM(m, n, 0); MM(m, n, 1); CHAIN(); }
#define P1_TILE(DO, WAIT1, WAIT2) do { \
            bf16x8 xf[4][2], wf[6][2]; \
            RDX(0); RDW(0, 0); if (DO) glds_sec<0>(sw, vw, dw); \
            RDW(0, 1); RDW(1, 0); if (DO) glds_sec<1>(sw, vw, dw); \
            RDX(1); RDW(1, 1); if (DO) glds_sec<2>(sw, vw, dw); \
            RDW(2, 0); RDW(2, 1); if (DO) glds_run<0>(sx, vx[0], dx); \
            RDX(2); RDW(3, 0); if (DO) glds_run<1>(sx, vx[1], 0); \
            RDW(3, 1); RDW(4, 0); if (DO) glds_run<2>(sx, vx[2], 0); \
            RDX(3); RDW(4, 1); if (DO) glds_run<3>(sx, vx[3], 0); \
            RDW(5, 0); RDW(5, 1); \
            SBAR(); \
            {   \
              const unsigned d_ = (wr_ == 2 * K1_WB) ? (unsigned)(-2 * K1_WB) : (unsigned)K1_WB, e_ = (xr_ >= 3 * 16384) ? (unsigned)(-3 * 16384) : (unsigned)(2 * 16384); \
              dw = wdst + wr_; wr_ += d_; aw[0] += d_; aw[1] += d_; dx = xdst_o + ((xr_ == 4 * 16384) ? 0u : xr_ + 16384); xr_ += e_; ax[0] += e_; ax[1] += e_; \
              tk = (tk + 1) & 15; sx = xo + tk * 128; sw = w + (size_t)tk * 64 * 6144; \
              asm volatile("" : "+v"(aw[0]), "+v"(aw[1]), "+v"(ax[0]), "+v"(ax[1]), "+s"(sx), "+s"(sw)); } \
            WAIT1; \
            SBAR(); \
            __builtin_amdgcn_s_setprio(1); \
            P1_MMA(); \
            __builtin_amdgcn_s_setprio(0); \
            SBAR(); \
            WAIT2; \
            SBAR(); \
        } while (0)
#pragma unroll 1
        for (int t = 0; t < 14; ++t) P1_TILE(true, WAITBAR_(11), WAITBAR_(7));
        P1_TILE(false, WAITBAR_(4), WAITBAR_(0));
        P1_TILE(false, WAITBAR_(0), if (g == 0) asm volatile("s_barrier" ::: "memory"));
#undef P1_TILE
#undef RDW
#undef RDX
#undef STAGE_X
#undef STAGE_W
        SA_(2); SC_(1);
        JIT(1);
        FRESH();
#pragma unroll
        for (int mb = 0; mb < 2; ++mb)
#pragma unroll
            for (int u = 0; u < 2; ++u) {
                const f32x4 a = acc[2 * mb + u][0] * QSCALE, c = acc[2 * mb + u][1] * QSCALE;
                fq[mb][u] = u32x4{cvtpk(a[0], a[1]), cvtpk(a[2], a[3]), cvtpk(c[0], c[1]), cvtpk(c[2], c[3])};
                fk[mb][u] = pack44(acc[2 * mb + u][2], acc[2 * mb + u][3]);
                fv[mb][u] = pack44(acc[2 * mb][4 + u], acc[2 * mb + 1][4 + u]);
            }
    }
    const char* wo = (const char*)(br ? woB : woA) + nt3 * 256;
    const int k30 = 2 * nt3;
    {
        const int g = wid >> 2, wr = wid >> 1, hd = wid & 1;
        const int ph1 = 2 * hp + hd, pb1 = mt * 8 + wr * 2;
        const int head = 2 * hp + br, blk = mt * 8 + wid;
        const char* kv = ws + WS_KV + (size_t)head * 262144;
        unsigned l16 = lane * 16;
        const unsigned dbeg = lds0 + wid * 4096, lbase = lds0 + l16;
        const unsigned fk0 = (unsigned)WS_FK + head * 32 * FS;
        const unsigned fxo = (unsigned)WS_FX + ((hp * 8 + mt) * 8 + wid) * FS, xrec0 = (unsigned)WS_XP + ((hp * 8 + mt) * 8 + wid) * 5120 + 2048;
        const f32x16 z = {};
        f32x16 sa, negm, o;
        u32x16 qol, kvf, pub0, pub1; u32x8 ph;
        const unsigned sidx = (lane & 1) ? 0xEEEEEEEEu : 0x44444444u;

#define FIRST_TILE(base, q0, q1) do { const unsigned on_ = (lane & 2) ? 0u : 0x3F803F80u, e_ = (lane & 4) ? 0u : on_, f_ = (lane & 4) ? on_ : 0u; const u32x4 ones = {e_, e_, f_, f_}; \
            const bf16x8 k0_ = ldsrd128(base), k1_ = ldsrd128((base) + 1024); kvf = cat16(asu(ldsrd128((base) + 4096)), asu(ldsrd128((base) + 5120)), asu(ldsrd128((base) + 2048)), ones); \
            sa = MFMA32(k0_, q0, z); sa = MFMA32(k1_, q1, sa); float m_ = sa[0]; \
            _Pragma("unroll") for (int r = 1; r < 16; ++r) m_ = __builtin_fmaxf(m_, sa[r]); \
            m_ = -swap_max(m_); _Pragma("unroll") for (int r = 0; r < 16; ++r) { negm[r] = m_; sa[r] += m_; } \
            qol = cat16(asu(q0), asu(q1), ones, u32x4{0u, 0u, 0u, 0u}); } while (0)
#define L6_OUT "={v[0:15]}"(o), "+{v[32:47]}"(sa), "+{v[64:79]}"(qol), "+{v[80:95]}"(kvf), "={v[104:111]}"(ph)
#define L6C(HL, BASE) asm volatile(ATT_L6C : L6_OUT, "={v[144:159]}"(h0), "={v[160:167]}"(h1) \
            : "{v[16:31]}"(negm), "{v174}"(sidx), "{v112}"(BASE), "{v[140:143]}"(u32x4{0u, 0u, c_fl, (unsigned)WS_HALO + (mt * 32 + ph1) * 6144 + 3072 + l16}), \
              [ws] "s"(ws), [hl] "s"(HL), [fld] "s"(fld) : ATT_TMP, "scc", "memory")
        const unsigned own = (ph1 * 32 + mt * 4 + wr) * FS;
        const unsigned fld = lds0 + K2_FL + wid * 1024 + br * (2 * K2_SLOT + 4096 - K2_FL + wid * 11264);
        c_pv = (lane >> 5) * 1024 + ((lane & 15) + 32 * ((lane >> 4) & 1)) * 16;
        const unsigned pv = c_pv, pq = pv + (lane & 8) * 16;
        if (br == 0) {
            const char* kp = ws + WS_KV + (size_t)((ph1 * 64 + pb1) * 4) * 1024;
#define PUB_KV() do { st16_dev<0>(kp, pq, fk[0][0]); st16_dev<128>(kp, pq, fk[0][1]); st16_dev<2048>(kp, pv, fv[0][0]); st16_dev<2304>(kp, pv, fv[0][1]); \
            st16_dev<0>(kp + 4096, pq, fk[1][0]); st16_dev<128>(kp + 4096, pq, fk[1][1]); st16_dev<2048>(kp + 4096, pv, fv[1][0]); st16_dev<2304>(kp + 4096, pv, fv[1][1]); } while (0)
            if (hd == 1) {
                const char* qp = ws + WS_QF + (size_t)((ph1 * 64 + pb1) * 2) * 1024;
                st16_dev<0>(qp, pq, fq[0][0]); st16_dev<128>(qp, pq, fq[0][1]); st16_dev<2048>(qp, pq, fq[1][0]); st16_dev<2176>(qp, pq, fq[1][1]);
            }
            JIT(2);
            if (wr == 0) PUB_KV();
#pragma unroll
            for (int mb = 0; mb < 2; ++mb) {
                const unsigned a = lds0 + hd * 2 * K2_SLOT + (wr * 2 + mb) * 4096, aq = lds0 + K2_QX + hd * 16384 + (wr * 2 + mb) * 2048 + pq;
                ldswr128(a + pq, fk[mb][0]); ldswr128(a + pq + 128, fk[mb][1]); ldswr128(a + 2048 + pv, fv[mb][0]); ldswr128(a + 2304 + pv, fv[mb][1]);
                if (hd == 0 || g == 0) { ldswr128(aq, fq[mb][0]); ldswr128(aq + 128, fq[mb][1]); }
            }
            BARRIER_L();
            if (g == 0) PRECOMPUTE(false);
            if (wr == 0 || hd == 1) {
                drain();
                if (lane == 0) { if (wr == 0) flag_set(ws + WS_FK, own, tag); if (hd == 1) flag_set(ws + WS_FQ, own, tag); }
            }
            JIT(3);
            if (wr == 1) PUB_KV();
            const unsigned qa = lbase + K2_QX + (1 - g) * 16384 + wid * 2048, cx = lbase + (1 - g) * 2 * K2_SLOT;
            SA_(3); SC_(2);
            pub0 = cat16(fk[0][0], fk[0][1], fv[0][0], fv[0][1]); pub1 = cat16(fk[1][0], fk[1][1], fv[1][0], fv[1][1]);
            { const bf16x8 q0 = ldsrd128(qa), q1 = ldsrd128(qa + 1024); FIRST_TILE(cx, q0, q1); }
            JIT(4);
            asm volatile(ATT_L4 : L6_OUT : "{v[16:31]}"(negm), "{v174}"(sidx), "{v112}"(cx) : ATT_TMP, "memory");
            SC_(3);
            if (g == 1) {
                const int b1 = (int)(tabf & 63), b0 = RBLK(0, wid - 4);
                flag_wait(ws, fk0 + (((lane & 1) ? b1 : b0) >> 1) * FS, tag);
                glds4k_dev(kv + b1 * 4096, l16, dbeg + K2_SLOT);
                glds4k_dev(kv + b0 * 4096, l16, dbeg + K2_SLOT - 16384);
                glds16_dev(ws, c_fl, fld);
            } else if (wr == 1) { drain(); if (lane == 0) flag_set(ws + WS_FK, own, tag); }
            SC_(4);
            JIT(5);
            asm volatile(ATT_L2 : "+{v[0:15]}"(o), "+{v[32:47]}"(sa), "+{v[64:79]}"(qol), "+{v[80:95]}"(kvf), "+{v[104:111]}"(ph) : "{v[16:31]}"(negm), "{v174}"(sidx), "{v112}"(cx) : ATT_TMP, "memory");
            if (g == 0) {
                asm volatile(ATT_XT : "+{v[0:15]}"(o), "+{v[32:47]}"(sa), "+{v[64:79]}"(qol), "+{v[80:95]}"(kvf), "+{v[104:111]}"(ph) : "{v[16:31]}"(negm), "{v174}"(sidx), "{v112}"(cx) : ATT_TMP, "memory");
                st16_dev<-2048>(ws, xrec0 + l16, quad(o, 0)); st16_dev<-1024>(ws, xrec0 + l16, quad(o, 1)); st16_dev<0>(ws, xrec0 + l16, quad(o, 2)); st16_dev<1024>(ws, xrec0 + l16, quad(o, 3));
                st16_dev<2048>(ws, xrec0 + l16, u32x4{__float_as_uint(__uint_as_float(qol[12]) + __uint_as_float(qol[13])), __float_as_uint(negm[0]), 0u, 0u});
                SC_(5);
                JIT(16);
                const bf16x8 q0 = ldsrd128(lbase + K2_QX + wid * 2048), q1 = ldsrd128(lbase + K2_QX + wid * 2048 + 1024);
                FIRST_TILE(lbase, q0, q1);
                u32x16 h0; u32x8 h1;
                L6C(0, lbase);
            }
#undef PUB_KV
        } else {
            if (wr == 3 && mt < 7) {
                const char* p = ws + WS_HALO + (size_t)(((mt + 1) * 32 + ph1) * 6) * 1024;
                st16_dev<0>(p, pq, fk[0][0]); st16_dev<128>(p, pq, fk[0][1]); st16_dev<3072>(p, pq, fk[1][0]); st16_dev<3200>(p, pq, fk[1][1]);
                if (lane >= 32) { st16_dev<1024>(p, pv, fv[0][0]); st16_dev<1280>(p, pv, fv[0][1]); st16_dev<1024>(p + 3072, pv, fv[1][0]); st16_dev<1280>(p + 3072, pv, fv[1][1]); }
            }
            JIT(15);
            const unsigned ex = lds0 + 2 * K2_SLOT + wid * 12288;
#pragma unroll
            for (int mb = 0; mb < 2; ++mb) {
                const unsigned a = ex + mb * 6144;
                ldswr128(a + pq, fq[mb][0]); ldswr128(a + pq + 128, fq[mb][1]); ldswr128(a + 2048 + pq, fk[mb][0]); ldswr128(a + 2176 + pq, fk[mb][1]);
                ldswr128(a + 4096 + pv, fv[mb][0]); ldswr128(a + 4352 + pv, fv[mb][1]);
            }
            if (g == 0) PRECOMPUTE(true);
            BARRIER_L();
            if (wr == 3 && mt < 7) { drain(); if (lane == 0) flag_set(ws + WS_FH, (ph1 * 8 + mt + 1) * FS, tag); }
            SC_(2);
            LocalPart lp[2];
            bf16x8 bq[2][2];
#pragma unroll
            for (int mb = 0; mb < 2; ++mb) { bq[mb][0] = ldsrd128(ex + mb * 6144 + l16); bq[mb][1] = ldsrd128(ex + mb * 6144 + 1024 + l16); }
#define LOCAL_BLOCK(mb) do { const unsigned a_ = ex + mb * 6144 + l16; \
                const bf16x8 k0_ = ldsrd128(a_ + 2048), k1_ = ldsrd128(a_ + 3072), v0_ = ldsrd128(a_ + 4096), v1_ = ldsrd128(a_ + 5120); \
                if (wr > 0) local_own<true>(bq[mb][0], bq[mb][1], k0_, k1_, v0_, v1_, ldsrd128(a_ - 24576 + 2048), ldsrd128(a_ - 24576 + 3072), ldsrd128(a_ - 24576 + 5120), band, lane, lp[mb]); \
                else local_own<false>(bq[mb][0], bq[mb][1], k0_, k1_, v0_, v1_, k0_, k0_, k0_, band, lane, lp[mb]); \
                if (wr > 0 || mt == 0) local_fin(lp[mb]); } while (0)
            JIT(6);
            LOCAL_BLOCK(0);
            SC_(3);
            const int hl = (wid < 2) ? mt : 0;
            const unsigned fh = (unsigned)WS_FH + (ph1 * 8 + mt) * FS;
            const int b0 = (int)(tabf & 63), b1 = (int)((tabf >> 6) & 63);
            const int q = lane & 3;
            unsigned fa0 = fk0 - 1024 * FS + (blk >> 1) * FS, fa1 = fk0 + (b0 >> 1) * FS, fa2 = fk0 + (b1 >> 1) * FS, fa3 = fh;
            asm volatile("" : "+s"(fa0), "+s"(fa1), "+s"(fa2), "+s"(fa3));
            const unsigned lo_ = (q & 1) ? fa1 : fa0, hi_ = (q & 1) ? fa3 : fa2;
            const u64 fm = flag_wait_some(ws, (q & 2) ? hi_ : lo_, tag, 3);
            SC_(4);
            glds4k_dev(kv + b0 * 4096, l16, dbeg);
            const char* qp = ws + WS_QF + (size_t)((head * 64 + blk) * 2) * 1024;
            glds16_dev(qp, l16, ex); glds16_dev(qp + 1024, l16, ex + 1024);
            if (fm & 4) glds4k_dev(kv + b1 * 4096, l16, dbeg + K2_SLOT);
            JIT(7);
            LOCAL_BLOCK(1);
#undef LOCAL_BLOCK
            if (fm & 4) WAITBAR_(4);
            else {
                WAITBAR_(0);
                flag_wait(ws, fk0 + (b1 >> 1) * FS, tag);
                glds4k_dev(kv + b1 * 4096, l16, dbeg + K2_SLOT);
            }
            JIT(8);
            const bf16x8 q0 = ldsrd128(ex + l16), q1 = ldsrd128(ex + 1024 + l16);
            SA_(3); SC_(5);
            if (hl && !(fm & 8)) flag_wait(ws, fh, tag);
            FIRST_TILE(lbase, q0, q1);
            u32x16 h0; u32x8 h1;
            JIT(9);
            L6C(hl, lbase);
            if (hl) {
                local_prev(bq[0][0], bq[0][1], asbf(u32x4{h0[0], h0[1], h0[2], h0[3]}), asbf(u32x4{h0[4], h0[5], h0[6], h0[7]}), asbf(u32x4{h0[8], h0[9], h0[10], h0[11]}), lane, lp[0]);
                local_prev(bq[1][0], bq[1][1], asbf(u32x4{h0[12], h0[13], h0[14], h0[15]}), asbf(u32x4{h1[0], h1[1], h1[2], h1[3]}), asbf(u32x4{h1[4], h1[5], h1[6], h1[7]}), lane, lp[1]);
                local_fin(lp[0]); local_fin(lp[1]);
            }
            const char* op = ws + WS_OF + (size_t)(((64 + pb1) * 32 + ph1) * 2) * 1024;
            st16_dev<0>(op, l16, quad(lp[0].o, 0)); st16_dev<1024>(op, l16, quad(lp[0].o, 1)); st16_dev<0>(op + 65536, l16, quad(lp[1].o, 0)); st16_dev<1024>(op + 65536, l16, quad(lp[1].o, 1));
        }
#undef FIRST_TILE
#undef L6_OUT
#undef L6C
        FRESH();
        l16 = lane * 16;
        SA_(4); SC_(6);
        const int isx = br & (1 - g);
        unsigned cnt = 7, dst = dbeg + 2 * K2_SLOT, nx = K2_SLOT;
        unsigned mode = isx << 1;
        const unsigned late = (1 - br) & g;
        const unsigned act = 2u | (isx * 12u) | ((br | (1 - g)) << 5) | (late << 6), hk = 32u | (late << 7);
        const u64 tab = tabf >> (6 + 6 * br);
        u32x8 adr = {lds0 + l16, lds0 + l16 + K2_SLOT, l16, lds0 + l16, c_wb + wid * 16384, (c_wb ^ 128) + wid * 16384 + 7168,
                     br ? (unsigned)WS_FO + ((64 + pb1 + (lane & 1)) * 32 + ph1) * FS : fxo, c_tw};
        const unsigned kvrec = (unsigned)WS_KV + (ph1 * 64 + pb1) * 4096 + 3072, pv2 = c_pv;
        f32x16 xo; f32x2 xl;
#define DBGOUT
        JIT(10);
        asm volatile(ATT_MAIN
                     : DBGOUT"+{v[0:15]}"(o), "+{v[32:47]}"(sa), "+{v[64:79]}"(qol), "+{v[80:95]}"(kvf), "+{v[104:111]}"(ph), "+{v[112:119]}"(adr), "={v[122:137]}"(xo), "={v[138:139]}"(xl),
                       [cnt] "+s"(cnt), [dst] "+s"(dst), [nx] "+s"(nx), [mode] "+s"(mode)
                     : "{v[16:31]}"(negm), "{v[174:175]}"(u32x2{sidx, (unsigned)WS_FK + own}), "{v[140:143]}"(u32x4{(unsigned)tag, (unsigned)(tag >> 32), fxo, xrec0 + l16}),
                       "{v[170:173]}"(u32x4{c_fl, kvrec + pv2 + (lane & 8) * 16, kvrec + pv2, fld + l16}), "{v[176:191]}"(pub0), "{v[192:207]}"(pub1),
                       [srclo] "s"((unsigned)(uintptr_t)kv), [srchi] "s"((unsigned)((uintptr_t)kv >> 32)), [dbeg] "s"(dbeg), [dend] "s"(dbeg + 3 * K2_SLOT),
                       [wo01] "s"(wo + (size_t)k30 * 131072), [wo23] "s"(wo + (size_t)((k30 + 2) & 15) * 131072), [wo] "s"(wo), [wdst] "s"(__builtin_amdgcn_readfirstlane(lds0 + 98304 + wid * 2048)), [act] "s"(act), [hk] "s"(hk), [tab] "s"(tab), [k0] "s"(br), [ws] "s"(ws)
                     : ATT_TMP, "v120", "v121", "v168", "v169", "s40", "s41", "s42", "s43", "s44", "s45", "s46", "s47", "s48", "s49", "s50", "s51", "vcc", "scc", "memory");
        FRESH();
        l16 = lane * 16;
        const unsigned xrec = xrec0 + l16;
        const bf16x8 q0 = asbf(u32x4{qol[0], qol[1], qol[2], qol[3]}), q1 = asbf(u32x4{qol[4], qol[5], qol[6], qol[7]});
        const unsigned one2 = (((lane >> 4) & 1) == ((lane >> 2) & 1)) ? 0x3F803F80u : 0u;
        const u32x4 ones = {one2, one2, one2, one2};
        SA_(5); SB_(1);
        JIT(11);
        float lsum = __uint_as_float(qol[12]) + __uint_as_float(qol[13]);
        bool ok = lsum < 1.0e18f;
        if (isx) {
            if (__builtin_expect(!(mode & 4), 0)) {
                flag_wait(ws, fxo, tag);
                f32x4 a0, a1, a2, a3;
                asm volatile("global_load_dwordx4 %0, %5, %6 offset:-2048 sc1\n\tglobal_load_dwordx4 %1, %5, %6 offset:-1024 sc1\n\tglobal_load_dwordx4 %2, %5, %6 sc1\n\t"
                             "global_load_dwordx4 %3, %5, %6 offset:1024 sc1\n\tglobal_load_dwordx2 %4, %5, %6 offset:2048 sc1\n\ts_waitcnt vmcnt(0)"
                             : "=&v"(a0), "=&v"(a1), "=&v"(a2), "=&v"(a3), "=&v"(xl) : "v"(xrec), "s"(ws) : "memory");
#pragma unroll
                for (int r = 0; r < 4; ++r) { xo[r] = a0[r]; xo[4 + r] = a1[r]; xo[8 + r] = a2[r]; xo[12 + r] = a3[r]; }
            }
            const float nm = __builtin_fminf(negm[0], xl[1]), fa = EXP2(nm - negm[0]), fx = EXP2(nm - xl[1]);
            ok = ok && (xl[0] < 1.0e18f);
            lsum = lsum * fa + xl[0] * fx;
#pragma unroll
            for (int r = 0; r < 16; ++r) o[r] = o[r] * fa + xo[r] * fx;
        }
#pragma unroll
        for (int r = 0; r < 16; ++r) ok = ok && (__builtin_fabsf(o[r]) < 3.0e38f);
        if (__builtin_expect(__any(!ok), 0)) {
            float m = -__builtin_inff();
            f32x4 l;
            for (int pass = 0; pass < 2; ++pass) {
                o = z; l = f32x4{};
                for (int t = 0; t < 64; ++t) {
                    bf16x8 f0, f1, f2, f3;
                    asm volatile("global_load_dwordx4 %0, %4, %5 sc1\n\tglobal_load_dwordx4 %1, %4, %5 offset:1024 sc1\n\tglobal_load_dwordx4 %2, %4, %5 offset:2048 sc1\n\t"
                                 "global_load_dwordx4 %3, %4, %5 offset:3072 sc1\n\ts_waitcnt vmcnt(0)" : "=&v"(f0), "=&v"(f1), "=&v"(f2), "=&v"(f3) : "v"(l16 + t * 4096), "s"(kv) : "memory");
                    f32x16 s = MFMA32(f0, q0, z); s = MFMA32(f1, q1, s);
                    if (pass == 0) {
#pragma unroll
                        for (int r = 0; r < 16; ++r) m = __builtin_fmaxf(m, s[r]);
                    } else {
#pragma unroll
                        for (int r = 0; r < 16; ++r) s[r] = EXP2(s[r] - m);
                        const bf16x8 p0 = asbf(pack8(s, 0)), p1 = asbf(pack8(s, 1));
                        o = MFMA32(f2, p0, o); o = MFMA32(f3, p1, o);
                        l = MFMA16(asbf(ones), p0, l); l = MFMA16(asbf(ones), p1, l);
                    }
                }
                if (pass == 0) m = swap_max(m);
            }
            lsum = l[0];
        }
        const float inv = __builtin_amdgcn_rcpf(lsum);
#pragma unroll
        for (int r = 0; r < 16; ++r) o[r] *= inv;
        const char* op = ws + WS_OF + (size_t)((blk * 32 + head) * 2) * 1024;
        st16_dev<0>(op, l16, pack8(o, 0)); st16_dev<1024>(op, l16, pack8(o, 1));
    }
    {
        FRESH();
        const int g = wid >> 2, wi = wid & 3, wm = wi >> 1, wn = wi & 1, g4 = lane >> 4;
        const char* of = ws + WS_OF + (size_t)(br * 64 + mt3 * 4 + wi) * 65536;
        const unsigned vw4[4] = {c_wb + wi * 32768, (c_wb ^ 128) + wi * 32768 + 7168, c_wb + wi * 32768 + 14336, (c_wb ^ 128) + wi * 32768 + 21504};
        const unsigned l16 = lane * 16, sdst = lds0 + wi * 4096;
#define STAGE_O(t) glds4k_dev(of + (((t) + k30) & 15) * 4096, l16, sdst + ((t) & 3) * 16384)
#define STAGE_WO(t, slot) do { const char* s_ = wo + (size_t)(((t) + k30) & 15) * 131072; glds_run<0>(s_, vw4[0], sdst + (slot)); glds_run<1>(s_, vw4[1], 0); glds_run<2>(s_, vw4[2], 0); glds_run<3>(s_, vw4[3], 0); } while (0)
        const unsigned fo = c_fo;
        drain();
        if (lane == 0) flag_set(ws + WS_FO, ((mt * 8 + wid) * 32 + 2 * hp + br) * FS, tag);
        SB_(2);
        asm volatile("s_barrier" ::: "memory");
        SB_(3);
        JIT(12);
        const u64 fm = flag_wait_some(ws, fo, tag, (3ull << (2 * nt3)) | (g ? 3ull << (2 * ((nt3 + 1) & 7)) : 0ull));
        STAGE_O(1 - g);
        if (g == 1) STAGE_O(2);
        STAGE_WO(5 - g, 65536 + (1 - g) * 16384);
        if (fm != ~0ull) flag_wait(ws, fo, tag);
        SB_(4);
        SA_(6);
        unsigned ao = lds0 + g * 16384 + wm * 8192 + c_pv;
        unsigned aw[4] = {c_aw, c_aw ^ 32, c_aw ^ 64, c_aw ^ 96};
        const char* so = of + ((3 + g + k30) & 15) * 4096; const char* sw = wo + (size_t)((5 + g + k30) & 15) * 131072;
        unsigned dob = sdst + ((3 + g) & 3) * 16384, dwb = sdst + p3_wslot(6);
        f32x4 acc[4][4];
#pragma unroll
        for (int mb = 0; mb < 4; ++mb)
#pragma unroll
            for (int nb = 0; nb < 4; ++nb) acc[mb][nb] = f32x4{};
        if (g == 1) { WAITBAR_(8); asm volatile("s_barrier" ::: "memory"); } else asm volatile("s_barrier" ::: "memory");
        SB_(5);
        SBAR();
#pragma unroll
        for (int u = 0; u < 8; ++u) {
            bf16x8 of_[4][2], wf[4][2];
            const bool doo = u < 6 || (u == 6 && g == 0), dow = (u >= 1 || g == 1) && (u < 5 || (u == 5 && g == 0));
#define P3RD(i) do { of_[(i) & 3][(i) >> 2] = ldsrd128(ao + (((i) & 3) >> 1) * 4096 + ((i) & 1) * 256 + ((i) >> 2) * 2048); \
                wf[(i) & 3][(i) >> 2] = cat8(ldstr(aw[(i) & 3] + ((i) >> 2) * 8192), ldstr(aw[(i) & 3] + ((i) >> 2) * 8192 + 2048)); } while (0)
            P3RD(0); if (u < 7 && doo) glds_run_dev<0>(so, l16, dob);
            P3RD(1); if (u < 7 && doo) glds_run_dev<1>(so, l16, 0);
            P3RD(2); if (u < 7 && doo) glds_run_dev<2>(so, l16, 0);
            P3RD(3); if (u < 7 && doo) glds_run_dev<3>(so, l16, 0);
            P3RD(4); if (u < 6 && dow) glds_run<0>(sw, vw4[0], dwb);
            P3RD(5); if (u < 6 && dow) glds_run<1>(sw, vw4[1], 0);
            P3RD(6); if (u < 6 && dow) glds_run<2>(sw, vw4[2], 0);
            P3RD(7); if (u < 6 && dow) glds_run<3>(sw, vw4[3], 0);
#undef P3RD
            SBAR();
            if (u < 7) {
                { const int to = 2 * u + 5 + g; so = of + ((to + k30) & 15) * 4096; sw = wo + (size_t)((to + 2 + k30) & 15) * 131072;
                  dob = sdst + (to & 3) * 16384; dwb = sdst + (g ? p3_wslot(2 * u + 8) : p3_wslot(2 * u + 7)); }
                ao ^= 32768;
#pragma unroll
                for (int q = 0; q < 4; ++q) aw[q] += p3_wslot(2 * u + 2) - p3_wslot(2 * u);
                asm volatile("" : "+v"(ao), "+v"(aw[0]), "+v"(aw[1]), "+v"(aw[2]), "+v"(aw[3]), "+s"(so), "+s"(sw), "+s"(dob), "+s"(dwb));
            }
            if (p3_allow(2 * u) == p3_allow(2 * u + 1)) WAITBAR(p3_allow(2 * u)); else if (g == 0) WAITBAR(p3_allow(2 * u)); else WAITBAR(p3_allow(2 * u + 1));
            SBAR();
            __builtin_amdgcn_s_setprio(1);
#pragma unroll
            for (int mb = 0; mb < 4; ++mb)
#pragma unroll
                for (int nb = 0; nb < 4; ++nb) { acc[mb][nb] = MFMA16(wf[nb][0], of_[mb][0], acc[mb][nb]); acc[mb][nb] = MFMA16(wf[nb][1], of_[mb][1], acc[mb][nb]); CHAIN();
                }
            __builtin_amdgcn_s_setprio(0);
            SBAR();
            if (u < 7 || g == 0) asm volatile("s_barrier" ::: "memory");
            SBAR();
        }
        SB_(6);
        FRESH();
        JIT(13);
        const unsigned xw = lds0 + wid * 8192 + l16, xr = lds0 + (wid ^ 4) * 8192 + l16;
#pragma unroll
        for (int i = 0; i < 8; ++i) { const f32x4 v = g ? acc[i >> 2][i & 3] : acc[2 + (i >> 2)][i & 3]; ldswr128(xw + i * 1024, __builtin_bit_cast(u32x4, v)); }
        BARRIER_L();
        const unsigned st = lds0 + 65536 + wid * 4096;
#pragma unroll
        for (int i = 0; i < 8; ++i) {
            const f32x4 v = (g ? acc[2 + (i >> 2)][i & 3] : acc[i >> 2][i & 3]) + __builtin_bit_cast(f32x4, ldsrd128(xr + i * 1024));
            const int row = 16 * (i >> 2) + (lane & 15), c = 2 * (i & 3) + (g4 >> 1);
            *(LAS u32x2*)(uintptr_t)(st + row * 128 + ((c ^ (row & 7)) << 4) + (g4 & 1) * 8) = u32x2{cvtpk(v[0], v[1]), cvtpk(v[2], v[3])};
        }
        const int blk = mt3 * 4 + 2 * wm + g;
        char* op = (char*)out + br * 2048 + nt3 * 256 + wn * 128 + (lane & 7) * 16;
#pragma unroll
        for (int i = 0; i < 4; ++i) {
            const int r = 8 * i + (lane >> 3), row = br ? ((blk >> 1) * 64 + 2 * r + (blk & 1)) : (blk * 32 + r);
            st16_dev<0>(out, (unsigned)(row * 4096 + br * 2048 + nt3 * 256 + wn * 128 + (lane & 7) * 16), __builtin_bit_cast(u32x4, ldsrd128(st + r * 128 + (((lane & 7) ^ (r & 7)) << 4))));
        }
#undef STAGE_O
#undef STAGE_WO
    }
    STAMP_AT(0, 7);
    if (b == 0 && wid == 0) {
        const unsigned fa = (unsigned)WS_FK + ((2 * (lane >> 3)) * 32 + (lane & 7) * 4) * FS, fb = (unsigned)WS_FO + ((64 + (lane & 7) * 8) * 32 + 2 * (lane >> 3)) * FS;
        const u64 t0 = __builtin_amdgcn_s_memrealtime();
        for (;;) {
            u64 v0, v1, v2, v3;
            asm volatile("global_load_dwordx2 %0, %4, %8 sc1\n\tglobal_load_dwordx2 %1, %5, %8 sc1\n\tglobal_load_dwordx2 %2, %6, %8 sc1\n\tglobal_load_dwordx2 %3, %7, %8 sc1\n\ts_waitcnt vmcnt(0)"
                         : "=&v"(v0), "=&v"(v1), "=&v"(v2), "=&v"(v3) : "v"(fa), "v"(fa + 16 * 32 * FS), "v"(fb), "v"(fb + 16 * FS), "s"(ws0) : "memory");
            if (__all(v0 == tag && v1 == tag && v2 == tag && v3 == tag) || __builtin_amdgcn_s_memrealtime() - t0 > 2000000ull) break;
            __builtin_amdgcn_s_sleep(SLP);
        }
        if (lane == 0) flag_set(ws0, (unsigned)WS_EP, tag);
    }
    asm volatile(".Lmk_code_end:");
}

extern "C" void kernel_launch(void* const* d_in, const int* in_sizes, int n_in, void* d_out, int out_size, void* d_ws, size_t ws_size, hipStream_t stream) {
    mk_fused<<<256, 512, 0, stream>>>((const u16*)d_in[0], (const u16*)d_in[1], (const u16*)d_in[2], (const u16*)d_in[3], (const u16*)d_in[4], (const u16*)d_in[5], (char*)d_ws, (u16*)d_out);
}
```

```cpp
#define TLBW 524288
#include <hip/hip_runtime.h>
#include <stdint.h>

typedef unsigned short u16;
typedef unsigned long long u64;
typedef __bf16 bf16x8 __attribute__((ext_vector_type(8)));
typedef __bf16 bf16x2 __attribute__((ext_vector_type(2)));
typedef short s16x4 __attribute__((ext_vector_type(4)));
typedef float f32x16 __attribute__((ext_vector_type(16)));
typedef float f32x4 __attribute__((ext_vector_type(4)));
typedef float f32x2 __attribute__((ext_vector_type(2)));
typedef unsigned u32x4 __attribute__((ext_vector_type(4)));
typedef unsigned u32x2 __attribute__((ext_vector_type(2)));
typedef unsigned u32x8 __attribute__((ext_vector_type(8)));
typedef unsigned u32x16 __attribute__((ext_vector_type(16)));
#define LAS __attribute__((address_space(3)))
#define DI __device__ __forceinline__
#define SBAR() __builtin_amdgcn_sched_barrier(0)
#define MFMA32(a, b, c) __builtin_amdgcn_mfma_f32_32x32x16_bf16((a), (b), (c), 0, 0, 0)
#define MFMA16(a, b, c) __builtin_amdgcn_mfma_f32_16x16x32_bf16((a), (b), (c), 0, 0, 0)
#define CHAIN() SBAR()
#define ORD 0
#define EXP2(x) __builtin_amdgcn_exp2f(x)

constexpr float QSCALE = 0.17677669529663687f * 1.4426950408889634f;

constexpr size_t WS_QF = 0;
constexpr size_t WS_KV = WS_QF + (4u << 20);
constexpr size_t WS_OF = WS_KV + (8u << 20);
constexpr size_t WS_HALO = WS_OF + (8u << 20);
constexpr size_t WS_XP = WS_HALO + (2u << 20);
constexpr size_t WS_FX = WS_XP + (5u << 20);
#define FS 64
constexpr size_t WS_FQ = WS_FX + 1024 * FS;
constexpr size_t WS_FK = WS_FQ + 1024 * FS;
constexpr size_t WS_FO = WS_FK + 1024 * FS;
constexpr size_t WS_FH = WS_FO + 4096 * FS;
constexpr size_t WS_EP = WS_FH + 256 * FS;

#define OUTST 1
#define SLP 1
#define STAMP_AT(K, I)
#define SA_(I)
#define SB_(I)

#define SC_(I)
#define JIT(p)
DI unsigned cvtpk(float lo, float hi) { f32x2 v = {lo, hi}; bf16x2 b = __builtin_convertvector(v, bf16x2); return __builtin_bit_cast(unsigned, b); }
DI u32x4 pack8(const f32x16& a, int s) { u32x4 r; r[0] = cvtpk(a[8 * s], a[8 * s + 1]); r[1] = cvtpk(a[8 * s + 2], a[8 * s + 3]); r[2] = cvtpk(a[8 * s + 4], a[8 * s + 5]); r[3] = cvtpk(a[8 * s + 6], a[8 * s + 7]); return r; }
DI u32x4 pack44(f32x4 a, f32x4 c) { return u32x4{cvtpk(a[0], a[1]), cvtpk(a[2], a[3]), cvtpk(c[0], c[1]), cvtpk(c[2], c[3])}; }
DI u32x4 asu(bf16x8 v) { return __builtin_bit_cast(u32x4, v); }
DI u32x16 cat16(u32x4 a, u32x4 b, u32x4 c, u32x4 d) { return u32x16{a[0], a[1], a[2], a[3], b[0], b[1], b[2], b[3], c[0], c[1], c[2], c[3], d[0], d[1], d[2], d[3]}; }
DI bf16x8 asbf(u32x4 v) { return __builtin_bit_cast(bf16x8, v); }
DI void glds16(const void* sbase, unsigned voff, unsigned lds) {
    asm volatile("s_mov_b32 m0, %2\n\ts_nop 0\n\tglobal_load_lds_dwordx4 %0, %1" :: "v"(voff), "s"(sbase), "s"(lds) : "memory");
}
template <int I> DI void glds_run(const void* sbase, unsigned voff, unsigned lds) {
    if (I == 0) asm volatile("s_mov_b32 m0, %2\n\ts_nop 0\n\tglobal_load_lds_dwordx4 %0, %1" :: "v"(voff), "s"(sbase), "s"(lds) : "memory");
    else asm volatile("global_load_lds_dwordx4 %0, %1 offset:%2" :: "v"(voff), "s"(sbase), "i"(I * 1024) : "memory");
}
template <int I> DI void glds_run_dev(const void* sbase, unsigned voff, unsigned lds) {
    if (I == 0) asm volatile("s_mov_b32 m0, %2\n\ts_nop 0\n\tglobal_load_lds_dwordx4 %0, %1 sc1" :: "v"(voff), "s"(sbase), "s"(lds) : "memory");
    else asm volatile("global_load_lds_dwordx4 %0, %1 offset:%2 sc1" :: "v"(voff), "s"(sbase), "i"(I * 1024) : "memory");
}
template <int I> DI void glds_sec(const void* sbase, unsigned voff, unsigned lds) {
    if (I < 2) asm volatile("s_add_u32 m0, %2, %4\n\ts_nop 0\n\tglobal_load_lds_dwordx4 %0, %1 offset:%3" :: "v"(voff), "s"(sbase), "s"(lds), "i"(I * 2048), "i"(I * 6144) : "memory", "scc");
    else asm volatile("s_add_u32 m0, %2, %4\n\ts_nop 0\n\tglobal_load_lds_dwordx4 %0, %1 offset:%3" :: "v"(voff), "s"((const char*)sbase + 2048), "s"(lds), "i"(2048), "i"(14336) : "memory", "scc");
}
DI void glds16_dev(const void* sbase, unsigned voff, unsigned lds) {
    asm volatile("s_mov_b32 m0, %2\n\ts_nop 0\n\tglobal_load_lds_dwordx4 %0, %1 sc1" :: "v"(voff), "s"(sbase), "s"(lds) : "memory");
}
DI void glds4k_dev(const void* sbase, unsigned voff, unsigned lds) {
    asm volatile("s_mov_b32 m0, %2\n\ts_nop 0\n\tglobal_load_lds_dwordx4 %0, %1 sc1\n\tglobal_load_lds_dwordx4 %0, %1 offset:1024 sc1\n\t"
                 "global_load_lds_dwordx4 %0, %1 offset:2048 sc1\n\tglobal_load_lds_dwordx4 %0, %1 offset:3072 sc1" :: "v"(voff), "s"(sbase), "s"(lds) : "memory");
}
template <int OFF> DI void st16_dev(const void* sbase, unsigned voff, u32x4 v) {
    asm volatile("global_store_dwordx4 %0, %1, %2 offset:%3 sc1\n\ts_nop 1" :: "v"(voff), "v"(v), "s"(sbase), "i"(OFF) : "memory");
}
DI void drain() { asm volatile("s_waitcnt vmcnt(0)" ::: "memory"); }
DI void flag_set(const void* sbase, unsigned voff, u64 tag) {
    asm volatile("global_store_dwordx2 %0, %1, %2 sc1" :: "v"(voff), "v"(tag), "s"(sbase) : "memory");
}
DI void flag_wait(const void* sbase, unsigned voff, u64 tag) {
    const u64 t0 = __builtin_amdgcn_s_memrealtime();
    for (;;) {
        u64 v;
        asm volatile("global_load_dwordx2 %0, %1, %2 sc1\n\ts_waitcnt vmcnt(0)" : "=v"(v) : "v"(voff), "s"(sbase) : "memory");
        if (__all(v == tag)) break;
        if (__builtin_amdgcn_s_memrealtime() - t0 > 2000000ull) break;
        __builtin_amdgcn_s_sleep(SLP);
    }
}
DI u64 flag_wait_some(const void* sbase, unsigned voff, u64 tag, u64 need) {
    const u64 t0 = __builtin_amdgcn_s_memrealtime();
    for (;;) {
        u64 v;
        asm volatile("global_load_dwordx2 %0, %1, %2 sc1\n\ts_waitcnt vmcnt(0)" : "=v"(v) : "v"(voff), "s"(sbase) : "memory");
        const u64 m = __ballot(v == tag);
        if ((m & need) == need || __builtin_amdgcn_s_memrealtime() - t0 > 2000000ull) return m;
        __builtin_amdgcn_s_sleep(SLP);
    }
}
DI bf16x8 ldsrd128(unsigned addr) { return *(const LAS bf16x8*)(uintptr_t)addr; }
DI void ldswr128(unsigned addr, u32x4 v) { *(LAS u32x4*)(uintptr_t)addr = v; }
DI s16x4 ldstr(unsigned addr) { return __builtin_amdgcn_ds_read_tr16_b64_v4i16((LAS s16x4*)(uintptr_t)addr); }
DI bf16x8 cat8(s16x4 lo, s16x4 hi) { typedef short s16x8 __attribute__((ext_vector_type(8))); s16x8 v = {lo[0], lo[1], lo[2], lo[3], hi[0], hi[1], hi[2], hi[3]}; return __builtin_bit_cast(bf16x8, v); }
DI float swap_max(float m) { auto r = __builtin_amdgcn_permlane32_swap(__float_as_uint(m), __float_as_uint(m), false, false); return __builtin_fmaxf(__uint_as_float(r[0]), __uint_as_float(r[1])); }
DI float swap_add(float m) { auto r = __builtin_amdgcn_permlane32_swap(__float_as_uint(m), __float_as_uint(m), false, false); return __uint_as_float(r[0]) + __uint_as_float(r[1]); }
#define FRESH() asm volatile("" : "+v"(lane), "+s"(ws))
#define BARRIER_L() asm volatile("s_waitcnt lgkmcnt(0)\n\ts_barrier" ::: "memory")
#define WAITBAR_(n) asm volatile("s_waitcnt vmcnt(%0) lgkmcnt(0)\n\ts_barrier" :: "i"(n) : "memory")
#define WAITBAR(n) do { switch (n) { case 0: WAITBAR_(0); break; case 2: WAITBAR_(2); break; case 3: WAITBAR_(3); break; case 4: WAITBAR_(4); break; case 6: WAITBAR_(6); break; \
    case 7: WAITBAR_(7); break; case 8: WAITBAR_(8); break; case 10: WAITBAR_(10); break; case 12: WAITBAR_(12); break; default: WAITBAR_(0); } } while (0)

struct LocalPart { f32x16 o; float m, l; };
DI f32x16 band_mask(int lane) {
    const int i = lane & 31, h = lane >> 5;
    f32x16 c;
#pragma unroll
    for (int r = 0; r < 16; ++r) { const int j = (r & 3) + 8 * (r >> 2) + 4 * h; c[r] = ((unsigned)(i - j) <= 7u) ? 0.f : -__builtin_inff(); }
    return c;
}
DI float max3(float a, float b, float c) { float r; asm("v_max3_f32 %0, %1, %2, %3" : "=v"(r) : "v"(a), "v"(b), "v"(c)); return r; }
template <bool PREV> DI void local_own(bf16x8 q0, bf16x8 q1, bf16x8 k0, bf16x8 k1, bf16x8 v0, bf16x8 v1, bf16x8 kp0, bf16x8 kp1, bf16x8 vp1, const f32x16& band, int lane, LocalPart& P) {
    const f32x16 z = {};
    f32x16 so = MFMA32(k0, q0, band), sp = z;
    if (PREV) sp = MFMA32(kp0, q0, z);
    so = MFMA32(k1, q1, so);
    if (PREV) sp = MFMA32(kp1, q1, sp);
    float m = __builtin_fmaxf(so[0], so[1]);
#pragma unroll
    for (int r = 2; r < 16; r += 2) m = __builtin_fmaxf(__builtin_fmaxf(m, so[r]), so[r + 1]);
    if (PREV) {
        const int d = 4 * (lane >> 5) - (lane & 31);
#pragma unroll
        for (int r = 12; r < 16; ++r) { sp[r] = (d + (r & 3) >= 1) ? sp[r] : -__builtin_inff(); m = __builtin_fmaxf(m, sp[r]); }
    }
    m = swap_max(m);
    float l = 0.f;
#pragma unroll
    for (int r = 0; r < 16; ++r) { so[r] = EXP2(so[r] - m); l += so[r]; }
    P.o = MFMA32(v0, asbf(pack8(so, 0)), z); P.o = MFMA32(v1, asbf(pack8(so, 1)), P.o);
    if (PREV) {
#pragma unroll
        for (int r = 12; r < 16; ++r) { sp[r] = EXP2(sp[r] - m); l += sp[r]; }
        P.o = MFMA32(vp1, asbf(u32x4{0u, 0u, cvtpk(sp[12], sp[13]), cvtpk(sp[14], sp[15])}), P.o);
    }
    P.m = m; P.l = l;
}
DI void local_prev(bf16x8 q0, bf16x8 q1, bf16x8 kp0, bf16x8 kp1, bf16x8 vp1, int lane, LocalPart& P) {
    const int i = lane & 31, h = lane >> 5;
    const f32x16 z = {};
    f32x16 sp = MFMA32(kp0, q0, z); sp = MFMA32(kp1, q1, sp);
    const float NEG = -__builtin_inff();
    float m = P.m;
#pragma unroll
    for (int r = 12; r < 16; ++r) { const int j = (r & 3) + 24 + 4 * h; sp[r] = (j >= i + 25) ? sp[r] : NEG; m = __builtin_fmaxf(m, sp[r]); }
    m = swap_max(m);
    const float f = EXP2(P.m - m);
    float l = P.l * f;
#pragma unroll
    for (int r = 12; r < 16; ++r) { sp[r] = EXP2(sp[r] - m); l += sp[r]; }
#pragma unroll
    for (int r = 0; r < 16; ++r) P.o[r] *= f;
    P.o = MFMA32(vp1, asbf(u32x4{0u, 0u, cvtpk(sp[12], sp[13]), cvtpk(sp[14], sp[15])}), P.o);
    P.m = m; P.l = l;
}
DI void local_fin(LocalPart& P) {
    const float inv = __builtin_amdgcn_rcpf(swap_add(P.l));
#pragma unroll
    for (int r = 0; r < 16; ++r) P.o[r] *= inv;
    const u32x4 o0 = pack8(P.o, 0), o1 = pack8(P.o, 1);
#pragma unroll
    for (int r = 0; r < 4; ++r) { P.o[r] = __uint_as_float(o0[r]); P.o[4 + r] = __uint_as_float(o1[r]); }
}
DI u32x4 quad(const f32x16& a, int i) { return __builtin_bit_cast(u32x4, f32x4{a[4 * i], a[4 * i + 1], a[4 * i + 2], a[4 * i + 3]}); }

#define ATT_L6C \
    "s_nop 1\n\t" \
    "s_cmp_eq_u32 %[hl], 0\n\t" \
    "s_cbranch_scc1 10f\n\t" \
    "global_load_dwordx4 v[144:147], v143, %[ws] offset:-3072 sc1\n\t" \
    "global_load_dwordx4 v[148:151], v143, %[ws] offset:-2048 sc1\n\t" \
    "global_load_dwordx4 v[152:155], v143, %[ws] offset:-1024 sc1\n\t" \
    "global_load_dwordx4 v[156:159], v143, %[ws] offset:0 sc1\n\t" \
    "global_load_dwordx4 v[160:163], v143, %[ws] offset:1024 sc1\n\t" \
    "global_load_dwordx4 v[164:167], v143, %[ws] offset:2048 sc1\n\t" \
    "10:\n" \
    "s_setprio 3\n\t" \
    "s_waitcnt lgkmcnt(0)\n\t" \
    "v_mfma_f32_32x32x16_bf16 v[48:63], v[80:83], v[64:67], v[16:31]\n\t" \
    "v_exp_f32_e32 v32, v32\n\t" \
    "v_exp_f32_e32 v33, v33\n\t" \
    "v_exp_f32_e32 v34, v34\n\t" \
    "v_exp_f32_e32 v35, v35\n\t" \
    "v_exp_f32_e32 v36, v36\n\t" \
    "v_mfma_f32_32x32x16_bf16 v[48:63], v[84:87], v[68:71], v[48:63]\n\t" \
    "ds_read_b128 v[80:83], v112 offset:8192\n\t" \
    "ds_read_b128 v[84:87], v112 offset:9216\n\t" \
    "v_cvt_pk_bf16_f32 v104, v32, v33\n\t" \
    "v_cvt_pk_bf16_f32 v105, v34, v35\n\t" \
    "v_exp_f32_e32 v37, v37\n\t" \
    "v_exp_f32_e32 v38, v38\n\t" \
    "v_exp_f32_e32 v39, v39\n\t" \
    "ds_read_b128 v[100:103], v112 offset:3072\n\t" \
    "ds_read_b128 v[96:99], v112 offset:6144\n\t" \
    "v_exp_f32_e32 v40, v40\n\t" \
    "v_cvt_pk_bf16_f32 v106, v36, v37\n\t" \
    "v_cvt_pk_bf16_f32 v107, v38, v39\n\t" \
    "v_exp_f32_e32 v41, v41\n\t" \
    "v_exp_f32_e32 v42, v42\n\t" \
    "v_exp_f32_e32 v43, v43\n\t" \
    "v_mfma_f32_32x32x16_bf16 v[0:15], v[88:91], v[104:107], 0\n\t" \
    "v_exp_f32_e32 v44, v44\n\t" \
    "v_exp_f32_e32 v45, v45\n\t" \
    "v_exp_f32_e32 v46, v46\n\t" \
    "v_exp_f32_e32 v47, v47\n\t" \
    "v_cvt_pk_bf16_f32 v108, v40, v41\n\t" \
    "v_cvt_pk_bf16_f32 v109, v42, v43\n\t" \
    "v_cvt_pk_bf16_f32 v110, v44, v45\n\t" \
    "v_cvt_pk_bf16_f32 v111, v46, v47\n\t" \
    "s_mov_b32 m0, %[fld]\n\t" \
    "s_nop 0\n\t" \
    "global_load_lds_dwordx4 v142, %[ws] sc1\n\t" \
    "s_waitcnt lgkmcnt(0)\n\t" \
    "v_mfma_f32_32x32x16_bf16 v[32:47], v[80:83], v[64:67], v[16:31]\n\t" \
    "v_exp_f32_e32 v48, v48\n\t" \
    "v_exp_f32_e32 v49, v49\n\t" \
    "v_exp_f32_e32 v50, v50\n\t" \
    "v_smfmac_f32_16x16x64_bf16 v[76:79], v[72:75], v[104:111], v174\n\t" \
    "v_exp_f32_e32 v51, v51\n\t" \
    "v_exp_f32_e32 v52, v52\n\t" \
    "v_mfma_f32_32x32x16_bf16 v[32:47], v[84:87], v[68:71], v[32:47]\n\t" \
    "ds_read_b128 v[80:83], v112 offset:12288\n\t" \
    "ds_read_b128 v[84:87], v112 offset:13312\n\t" \
    "v_cvt_pk_bf16_f32 v104, v48, v49\n\t" \
    "v_cvt_pk_bf16_f32 v105, v50, v51\n\t" \
    "v_exp_f32_e32 v53, v53\n\t" \
    "v_exp_f32_e32 v54, v54\n\t" \
    "v_exp_f32_e32 v55, v55\n\t" \
    "v_mfma_f32_32x32x16_bf16 v[0:15], v[100:103], v[108:111], v[0:15]\n\t" \
    "ds_read_b128 v[92:95], v112 offset:7168\n\t" \
    "ds_read_b128 v[88:91], v112 offset:10240\n\t" \
    "v_exp_f32_e32 v56, v56\n\t" \
    "v_cvt_pk_bf16_f32 v106, v52, v53\n\t" \
    "v_cvt_pk_bf16_f32 v107, v54, v55\n\t" \
    "v_exp_f32_e32 v57, v57\n\t" \
    "v_exp_f32_e32 v58, v58\n\t" \
    "v_exp_f32_e32 v59, v59\n\t" \
    "v_mfma_f32_32x32x16_bf16 v[0:15], v[96:99], v[104:107], v[0:15]\n\t" \
    "v_exp_f32_e32 v60, v60\n\t" \
    "v_exp_f32_e32 v61, v61\n\t" \
    "v_exp_f32_e32 v62, v62\n\t" \
    "v_exp_f32_e32 v63, v63\n\t" \
    "v_cvt_pk_bf16_f32 v108, v56, v57\n\t" \
    "v_cvt_pk_bf16_f32 v109, v58, v59\n\t" \
    "v_cvt_pk_bf16_f32 v110, v60, v61\n\t" \
    "v_cvt_pk_bf16_f32 v111, v62, v63\n\t" \
    "s_setprio 2\n\t" \
    "s_waitcnt lgkmcnt(0)\n\t" \
    "v_mfma_f32_32x32x16_bf16 v[48:63], v[80:83], v[64:67], v[16:31]\n\t" \
    "v_exp_f32_e32 v32, v32\n\t" \
    "v_exp_f32_e32 v33, v33\n\t" \
    "v_exp_f32_e32 v34, v34\n\t" \
    "v_smfmac_f32_16x16x64_bf16 v[76:79], v[72:75], v[104:111], v174\n\t" \
    "v_exp_f32_e32 v35, v35\n\t" \
    "v_exp_f32_e32 v36, v36\n\t" \
    "v_mfma_f32_32x32x16_bf16 v[48:63], v[84:87], v[68:71], v[48:63]\n\t" \
    "ds_read_b128 v[80:83], v112 offset:16384\n\t" \
    "ds_read_b128 v[84:87], v112 offset:17408\n\t" \
    "v_cvt_pk_bf16_f32 v104, v32, v33\n\t" \
    "v_cvt_pk_bf16_f32 v105, v34, v35\n\t" \
    "v_exp_f32_e32 v37, v37\n\t" \
    "v_exp_f32_e32 v38, v38\n\t" \
    "v_exp_f32_e32 v39, v39\n\t" \
    "v_mfma_f32_32x32x16_bf16 v[0:15], v[92:95], v[108:111], v[0:15]\n\t" \
    "ds_read_b128 v[100:103], v112 offset:11264\n\t" \
    "ds_read_b128 v[96:99], v112 offset:14336\n\t" \
    "v_exp_f32_e32 v40, v40\n\t" \
    "v_cvt_pk_bf16_f32 v106, v36, v37\n\t" \
    "v_cvt_pk_bf16_f32 v107, v38, v39\n\t" \
    "v_exp_f32_e32 v41, v41\n\t" \
    "v_exp_f32_e32 v42, v42\n\t" \
    "v_exp_f32_e32 v43, v43\n\t" \
    "v_mfma_f32_32x32x16_bf16 v[0:15], v[88:91], v[104:107], v[0:15]\n\t" \
    "v_exp_f32_e32 v44, v44\n\t" \
    "v_exp_f32_e32 v45, v45\n\t" \
    "v_exp_f32_e32 v46, v46\n\t" \
    "v_exp_f32_e32 v47, v47\n\t" \
    "v_cvt_pk_bf16_f32 v108, v40, v41\n\t" \
    "v_cvt_pk_bf16_f32 v109, v42, v43\n\t" \
    "v_cvt_pk_bf16_f32 v110, v44, v45\n\t" \
    "v_cvt_pk_bf16_f32 v111, v46, v47\n\t" \
    "s_waitcnt lgkmcnt(0)\n\t" \
    "v_mfma_f32_32x32x16_bf16 v[32:47], v[80:83], v[64:67], v[16:31]\n\t" \
    "v_exp_f32_e32 v48, v48\n\t" \
    "v_exp_f32_e32 v49, v49\n\t" \
    "v_exp_f32_e32 v50, v50\n\t" \
    "v_smfmac_f32_16x16x64_bf16 v[76:79], v[72:75], v[104:111], v174\n\t" \
    "v_exp_f32_e32 v51, v51\n\t" \
    "v_exp_f32_e32 v52, v52\n\t" \
    "v_mfma_f32_32x32x16_bf16 v[32:47], v[84:87], v[68:71], v[32:47]\n\t" \
    "ds_read_b128 v[80:83], v112 offset:20480\n\t" \
    "ds_read_b128 v[84:87], v112 offset:21504\n\t" \
    "v_cvt_pk_bf16_f32 v104, v48, v49\n\t" \
    "v_cvt_pk_bf16_f32 v105, v50, v51\n\t" \
    "v_exp_f32_e32 v53, v53\n\t" \
    "v_exp_f32_e32 v54, v54\n\t" \
    "v_exp_f32_e32 v55, v55\n\t" \
    "v_mfma_f32_32x32x16_bf16 v[0:15], v[100:103], v[108:111], v[0:15]\n\t" \
    "ds_read_b128 v[92:95], v112 offset:15360\n\t" \
    "ds_read_b128 v[88:91], v112 offset:18432\n\t" \
    "v_exp_f32_e32 v56, v56\n\t" \
    "v_cvt_pk_bf16_f32 v106, v52, v53\n\t" \
    "v_cvt_pk_bf16_f32 v107, v54, v55\n\t" \
    "v_exp_f32_e32 v57, v57\n\t" \
    "v_exp_f32_e32 v58, v58\n\t" \
    "v_exp_f32_e32 v59, v59\n\t" \
    "v_mfma_f32_32x32x16_bf16 v[0:15], v[96:99], v[104:107], v[0:15]\n\t" \
    "v_exp_f32_e32 v60, v60\n\t" \
    "v_exp_f32_e32 v61, v61\n\t" \
    "v_exp_f32_e32 v62, v62\n\t" \
    "v_exp_f32_e32 v63, v63\n\t" \
    "v_cvt_pk_bf16_f32 v108, v56, v57\n\t" \
    "v_cvt_pk_bf16_f32 v109, v58, v59\n\t" \
    "v_cvt_pk_bf16_f32 v110, v60, v61\n\t" \
    "v_cvt_pk_bf16_f32 v111, v62, v63\n\t" \
    "s_setprio 1\n\t" \
    "s_waitcnt lgkmcnt(0)\n\t" \
    "v_mfma_f32_32x32x16_bf16 v[48:63], v[80:83], v[64:67], v[16:31]\n\t" \
    "v_exp_f32_e32 v32, v32\n\t" \
    "v_exp_f32_e32 v33, v33\n\t" \
    "v_exp_f32_e32 v34, v34\n\t" \
    "v_smfmac_f32_16x16x64_bf16 v[76:79], v[72:75], v[104:111], v174\n\t" \
    "v_exp_f32_e32 v35, v35\n\t" \
    "v_exp_f32_e32 v36, v36\n\t" \
    "v_mfma_f32_32x32x16_bf16 v[48:63], v[84:87], v[68:71], v[48:63]\n\t" \
    "ds_read_b128 v[80:83], v112 offset:24576\n\t" \
    "ds_read_b128 v[84:87], v112 offset:25600\n\t" \
    "v_cvt_pk_bf16_f32 v104, v32, v33\n\t" \
    "v_cvt_pk_bf16_f32 v105, v34, v35\n\t" \
    "v_exp_f32_e32 v37, v37\n\t" \
    "v_exp_f32_e32 v38, v38\n\t" \
    "v_exp_f32_e32 v39, v39\n\t" \
    "v_mfma_f32_32x32x16_bf16 v[0:15], v[92:95], v[108:111], v[0:15]\n\t" \
    "ds_read_b128 v[100:103], v112 offset:19456\n\t" \
    "ds_read_b128 v[96:99], v112 offset:22528\n\t" \
    "v_exp_f32_e32 v40, v40\n\t" \
    "v_cvt_pk_bf16_f32 v106, v36, v37\n\t" \
    "v_cvt_pk_bf16_f32 v107, v38, v39\n\t" \
    "v_exp_f32_e32 v41, v41\n\t" \
    "v_exp_f32_e32 v42, v42\n\t" \
    "v_exp_f32_e32 v43, v43\n\t" \
    "v_mfma_f32_32x32x16_bf16 v[0:15], v[88:91], v[104:107], v[0:15]\n\t" \
    "v_exp_f32_e32 v44, v44\n\t" \
    "v_exp_f32_e32 v45, v45\n\t" \
    "v_exp_f32_e32 v46, v46\n\t" \
    "v_exp_f32_e32 v47, v47\n\t" \
    "v_cvt_pk_bf16_f32 v108, v40, v41\n\t" \
    "v_cvt_pk_bf16_f32 v109, v42, v43\n\t" \
    "v_cvt_pk_bf16_f32 v110, v44, v45\n\t" \
    "v_cvt_pk_bf16_f32 v111, v46, v47\n\t" \
    "s_waitcnt lgkmcnt(0)\n\t" \
    "v_mfma_f32_32x32x16_bf16 v[32:47], v[80:83], v[64:67], v[16:31]\n\t" \
    "v_exp_f32_e32 v48, v48\n\t" \
    "v_exp_f32_e32 v49, v49\n\t" \
    "v_exp_f32_e32 v50, v50\n\t" \
    "v_smfmac_f32_16x16x64_bf16 v[76:79], v[72:75], v[104:111], v174\n\t" \
    "v_exp_f32_e32 v51, v51\n\t" \
    "v_exp_f32_e32 v52, v52\n\t" \
    "v_mfma_f32_32x32x16_bf16 v[32:47], v[84:87], v[68:71], v[32:47]\n\t" \
    "ds_read_b128 v[80:83], v112 offset:28672\n\t" \
    "ds_read_b128 v[84:87], v112 offset:29696\n\t" \
    "v_cvt_pk_bf16_f32 v104, v48, v49\n\t" \
    "v_cvt_pk_bf16_f32 v105, v50, v51\n\t" \
    "v_exp_f32_e32 v53, v53\n\t" \
    "v_exp_f32_e32 v54, v54\n\t" \
    "v_exp_f32_e32 v55, v55\n\t" \
    "v_mfma_f32_32x32x16_bf16 v[0:15], v[100:103], v[108:111], v[0:15]\n\t" \
    "ds_read_b128 v[92:95], v112 offset:23552\n\t" \
    "ds_read_b128 v[88:91], v112 offset:26624\n\t" \
    "v_exp_f32_e32 v56, v56\n\t" \
    "v_cvt_pk_bf16_f32 v106, v52, v53\n\t" \
    "v_cvt_pk_bf16_f32 v107, v54, v55\n\t" \
    "v_exp_f32_e32 v57, v57\n\t" \
    "v_exp_f32_e32 v58, v58\n\t" \
    "v_exp_f32_e32 v59, v59\n\t" \
    "v_mfma_f32_32x32x16_bf16 v[0:15], v[96:99], v[104:107], v[0:15]\n\t" \
    "v_exp_f32_e32 v60, v60\n\t" \
    "v_exp_f32_e32 v61, v61\n\t" \
    "v_exp_f32_e32 v62, v62\n\t" \
    "v_exp_f32_e32 v63, v63\n\t" \
    "v_cvt_pk_bf16_f32 v108, v56, v57\n\t" \
    "v_cvt_pk_bf16_f32 v109, v58, v59\n\t" \
    "v_cvt_pk_bf16_f32 v110, v60, v61\n\t" \
    "v_cvt_pk_bf16_f32 v111, v62, v63\n\t" \
    "s_setprio 0\n\t" \
    "s_cmp_eq_u32 %[hl], 0\n\t" \
    "s_cbranch_scc1 11f\n\t" \
    "s_waitcnt vmcnt(1)\n\t" \
    "11:\n" \
    "s_waitcnt lgkmcnt(0)\n\t" \
    "s_nop 15\n\t"
#define ATT_L4 \
    "s_nop 1\n\t" \
    "s_setprio 3\n\t" \
    "s_waitcnt lgkmcnt(0)\n\t" \
    "v_mfma_f32_32x32x16_bf16 v[48:63], v[80:83], v[64:67], v[16:31]\n\t" \
    "v_exp_f32_e32 v32, v32\n\t" \
    "v_exp_f32_e32 v33, v33\n\t" \
    "v_exp_f32_e32 v34, v34\n\t" \
    "v_exp_f32_e32 v35, v35\n\t" \
    "v_exp_f32_e32 v36, v36\n\t" \
    "v_mfma_f32_32x32x16_bf16 v[48:63], v[84:87], v[68:71], v[48:63]\n\t" \
    "ds_read_b128 v[80:83], v112 offset:8192\n\t" \
    "ds_read_b128 v[84:87], v112 offset:9216\n\t" \
    "v_cvt_pk_bf16_f32 v104, v32, v33\n\t" \
    "v_cvt_pk_bf16_f32 v105, v34, v35\n\t" \
    "v_exp_f32_e32 v37, v37\n\t" \
    "v_exp_f32_e32 v38, v38\n\t" \
    "v_exp_f32_e32 v39, v39\n\t" \
    "ds_read_b128 v[100:103], v112 offset:3072\n\t" \
    "ds_read_b128 v[96:99], v112 offset:6144\n\t" \
    "v_exp_f32_e32 v40, v40\n\t" \
    "v_cvt_pk_bf16_f32 v106, v36, v37\n\t" \
    "v_cvt_pk_bf16_f32 v107, v38, v39\n\t" \
    "v_exp_f32_e32 v41, v41\n\t" \
    "v_exp_f32_e32 v42, v42\n\t" \
    "v_exp_f32_e32 v43, v43\n\t" \
    "v_mfma_f32_32x32x16_bf16 v[0:15], v[88:91], v[104:107], 0\n\t" \
    "v_exp_f32_e32 v44, v44\n\t" \
    "v_exp_f32_e32 v45, v45\n\t" \
    "v_exp_f32_e32 v46, v46\n\t" \
    "v_exp_f32_e32 v47, v47\n\t" \
    "v_cvt_pk_bf16_f32 v108, v40, v41\n\t" \
    "v_cvt_pk_bf16_f32 v109, v42, v43\n\t" \
    "v_cvt_pk_bf16_f32 v110, v44, v45\n\t" \
    "v_cvt_pk_bf16_f32 v111, v46, v47\n\t" \
    "s_waitcnt lgkmcnt(0)\n\t" \
    "v_mfma_f32_32x32x16_bf16 v[32:47], v[80:83], v[64:67], v[16:31]\n\t" \
    "v_exp_f32_e32 v48, v48\n\t" \
    "v_exp_f32_e32 v49, v49\n\t" \
    "v_exp_f32_e32 v50, v50\n\t" \
    "v_smfmac_f32_16x16x64_bf16 v[76:79], v[72:75], v[104:111], v174\n\t" \
    "v_exp_f32_e32 v51, v51\n\t" \
    "v_exp_f32_e32 v52, v52\n\t" \
    "v_mfma_f32_32x32x16_bf16 v[32:47], v[84:87], v[68:71], v[32:47]\n\t" \
    "ds_read_b128 v[80:83], v112 offset:12288\n\t" \
    "ds_read_b128 v[84:87], v112 offset:13312\n\t" \
    "v_cvt_pk_bf16_f32 v104, v48, v49\n\t" \
    "v_cvt_pk_bf16_f32 v105, v50, v51\n\t" \
    "v_exp_f32_e32 v53, v53\n\t" \
    "v_exp_f32_e32 v54, v54\n\t" \
    "v_exp_f32_e32 v55, v55\n\t" \
    "v_mfma_f32_32x32x16_bf16 v[0:15], v[100:103], v[108:111], v[0:15]\n\t" \
    "ds_read_b128 v[92:95], v112 offset:7168\n\t" \
    "ds_read_b128 v[88:91], v112 offset:10240\n\t" \
    "v_exp_f32_e32 v56, v56\n\t" \
    "v_cvt_pk_bf16_f32 v106, v52, v53\n\t" \
    "v_cvt_pk_bf16_f32 v107, v54, v55\n\t" \
    "v_exp_f32_e32 v57, v57\n\t" \
    "v_exp_f32_e32 v58, v58\n\t" \
    "v_exp_f32_e32 v59, v59\n\t" \
    "v_mfma_f32_32x32x16_bf16 v[0:15], v[96:99], v[104:107], v[0:15]\n\t" \
    "v_exp_f32_e32 v60, v60\n\t" \
    "v_exp_f32_e32 v61, v61\n\t" \
    "v_exp_f32_e32 v62, v62\n\t" \
    "v_exp_f32_e32 v63, v63\n\t" \
    "v_cvt_pk_bf16_f32 v108, v56, v57\n\t" \
    "v_cvt_pk_bf16_f32 v109, v58, v59\n\t" \
    "v_cvt_pk_bf16_f32 v110, v60, v61\n\t" \
    "v_cvt_pk_bf16_f32 v111, v62, v63\n\t" \
    "s_setprio 2\n\t" \
    "s_waitcnt lgkmcnt(0)\n\t" \
    "v_mfma_f32_32x32x16_bf16 v[48:63], v[80:83], v[64:67], v[16:31]\n\t" \
    "v_exp_f32_e32 v32, v32\n\t" \
    "v_exp_f32_e32 v33, v33\n\t" \
    "v_exp_f32_e32 v34, v34\n\t" \
    "v_smfmac_f32_16x16x64_bf16 v[76:79], v[72:75], v[104:111], v174\n\t" \
    "v_exp_f32_e32 v35, v35\n\t" \
    "v_exp_f32_e32 v36, v36\n\t" \
    "v_mfma_f32_32x32x16_bf16 v[48:63], v[84:87], v[68:71], v[48:63]\n\t" \
    "ds_read_b128 v[80:83], v112 offset:16384\n\t" \
    "ds_read_b128 v[84:87], v112 offset:17408\n\t" \
    "v_cvt_pk_bf16_f32 v104, v32, v33\n\t" \
    "v_cvt_pk_bf16_f32 v105, v34, v35\n\t" \
    "v_exp_f32_e32 v37, v37\n\t" \
    "v_exp_f32_e32 v38, v38\n\t" \
    "v_exp_f32_e32 v39, v39\n\t" \
    "v_mfma_f32_32x32x16_bf16 v[0:15], v[92:95], v[108:111], v[0:15]\n\t" \
    "ds_read_b128 v[100:103], v112 offset:11264\n\t" \
    "ds_read_b128 v[96:99], v112 offset:14336\n\t" \
    "v_exp_f32_e32 v40, v40\n\t" \
    "v_cvt_pk_bf16_f32 v106, v36, v37\n\t" \
    "v_cvt_pk_bf16_f32 v107, v38, v39\n\t" \
    "v_exp_f32_e32 v41, v41\n\t" \
    "v_exp_f32_e32 v42, v42\n\t" \
    "v_exp_f32_e32 v43, v43\n\t" \
    "v_mfma_f32_32x32x16_bf16 v[0:15], v[88:91], v[104:107], v[0:15]\n\t" \
    "v_exp_f32_e32 v44, v44\n\t" \
    "v_exp_f32_e32 v45, v45\n\t" \
    "v_exp_f32_e32 v46, v46\n\t" \
    "v_exp_f32_e32 v47, v47\n\t" \
    "v_cvt_pk_bf16_f32 v108, v40, v41\n\t" \
    "v_cvt_pk_bf16_f32 v109, v42, v43\n\t" \
    "v_cvt_pk_bf16_f32 v110, v44, v45\n\t" \
    "v_cvt_pk_bf16_f32 v111, v46, v47\n\t" \
    "s_waitcnt lgkmcnt(0)\n\t" \
    "v_mfma_f32_32x32x16_bf16 v[32:47], v[80:83], v[64:67], v[16:31]\n\t" \
    "v_exp_f32_e32 v48, v48\n\t" \
    "v_exp_f32_e32 v49, v49\n\t" \
    "v_exp_f32_e32 v50, v50\n\t" \
    "v_smfmac_f32_16x16x64_bf16 v[76:79], v[72:75], v[104:111], v174\n\t" \
    "v_exp_f32_e32 v51, v51\n\t" \
    "v_exp_f32_e32 v52, v52\n\t" \
    "v_mfma_f32_32x32x16_bf16 v[32:47], v[84:87], v[68:71], v[32:47]\n\t" \
    "ds_read_b128 v[80:83], v112 offset:20480\n\t" \
    "ds_read_b128 v[84:87], v112 offset:21504\n\t" \
    "v_cvt_pk_bf16_f32 v104, v48, v49\n\t" \
    "v_cvt_pk_bf16_f32 v105, v50, v51\n\t" \
    "v_exp_f32_e32 v53, v53\n\t" \
    "v_exp_f32_e32 v54, v54\n\t" \
    "v_exp_f32_e32 v55, v55\n\t" \
    "v_mfma_f32_32x32x16_bf16 v[0:15], v[100:103], v[108:111], v[0:15]\n\t" \
    "ds_read_b128 v[92:95], v112 offset:15360\n\t" \
    "ds_read_b128 v[88:91], v112 offset:18432\n\t" \
    "v_exp_f32_e32 v56, v56\n\t" \
    "v_cvt_pk_bf16_f32 v106, v52, v53\n\t" \
    "v_cvt_pk_bf16_f32 v107, v54, v55\n\t" \
    "v_exp_f32_e32 v57, v57\n\t" \
    "v_exp_f32_e32 v58, v58\n\t" \
    "v_exp_f32_e32 v59, v59\n\t" \
    "v_mfma_f32_32x32x16_bf16 v[0:15], v[96:99], v[104:107], v[0:15]\n\t" \
    "v_exp_f32_e32 v60, v60\n\t" \
    "v_exp_f32_e32 v61, v61\n\t" \
    "v_exp_f32_e32 v62, v62\n\t" \
    "v_exp_f32_e32 v63, v63\n\t" \
    "v_cvt_pk_bf16_f32 v108, v56, v57\n\t" \
    "v_cvt_pk_bf16_f32 v109, v58, v59\n\t" \
    "v_cvt_pk_bf16_f32 v110, v60, v61\n\t" \
    "v_cvt_pk_bf16_f32 v111, v62, v63\n\t" \
    "s_setprio 0\n\t" \
    "s_waitcnt lgkmcnt(0)\n\t" \
    "s_nop 15\n\t"
#define ATT_L2 \
    "s_nop 1\n\t" \
    "s_setprio 1\n\t" \
    "s_waitcnt lgkmcnt(0)\n\t" \
    "v_mfma_f32_32x32x16_bf16 v[48:63], v[80:83], v[64:67], v[16:31]\n\t" \
    "v_exp_f32_e32 v32, v32\n\t" \
    "v_exp_f32_e32 v33, v33\n\t" \
    "v_exp_f32_e32 v34, v34\n\t" \
    "v_smfmac_f32_16x16x64_bf16 v[76:79], v[72:75], v[104:111], v174\n\t" \
    "v_exp_f32_e32 v35, v35\n\t" \
    "v_exp_f32_e32 v36, v36\n\t" \
    "v_mfma_f32_32x32x16_bf16 v[48:63], v[84:87], v[68:71], v[48:63]\n\t" \
    "ds_read_b128 v[80:83], v112 offset:24576\n\t" \
    "ds_read_b128 v[84:87], v112 offset:25600\n\t" \
    "v_cvt_pk_bf16_f32 v104, v32, v33\n\t" \
    "v_cvt_pk_bf16_f32 v105, v34, v35\n\t" \
    "v_exp_f32_e32 v37, v37\n\t" \
    "v_exp_f32_e32 v38, v38\n\t" \
    "v_exp_f32_e32 v39, v39\n\t" \
    "v_mfma_f32_32x32x16_bf16 v[0:15], v[92:95], v[108:111], v[0:15]\n\t" \
    "ds_read_b128 v[100:103], v112 offset:19456\n\t" \
    "ds_read_b128 v[96:99], v112 offset:22528\n\t" \
    "v_exp_f32_e32 v40, v40\n\t" \
    "v_cvt_pk_bf16_f32 v106, v36, v37\n\t" \
    "v_cvt_pk_bf16_f32 v107, v38, v39\n\t" \
    "v_exp_f32_e32 v41, v41\n\t" \
    "v_exp_f32_e32 v42, v42\n\t" \
    "v_exp_f32_e32 v43, v43\n\t" \
    "v_mfma_f32_32x32x16_bf16 v[0:15], v[88:91], v[104:107], v[0:15]\n\t" \
    "v_exp_f32_e32 v44, v44\n\t" \
    "v_exp_f32_e32 v45, v45\n\t" \
    "v_exp_f32_e32 v46, v46\n\t" \
    "v_exp_f32_e32 v47, v47\n\t" \
    "v_cvt_pk_bf16_f32 v108, v40, v41\n\t" \
    "v_cvt_pk_bf16_f32 v109, v42, v43\n\t" \
    "v_cvt_pk_bf16_f32 v110, v44, v45\n\t" \
    "v_cvt_pk_bf16_f32 v111, v46, v47\n\t" \
    "s_waitcnt lgkmcnt(0)\n\t" \
    "v_mfma_f32_32x32x16_bf16 v[32:47], v[80:83], v[64:67], v[16:31]\n\t" \
    "v_exp_f32_e32 v48, v48\n\t" \
    "v_exp_f32_e32 v49, v49\n\t" \
    "v_exp_f32_e32 v50, v50\n\t" \
    "v_smfmac_f32_16x16x64_bf16 v[76:79], v[72:75], v[104:111], v174\n\t" \
    "v_exp_f32_e32 v51, v51\n\t" \
    "v_exp_f32_e32 v52, v52\n\t" \
    "v_mfma_f32_32x32x16_bf16 v[32:47], v[84:87], v[68:71], v[32:47]\n\t" \
    "ds_read_b128 v[80:83], v112 offset:28672\n\t" \
    "ds_read_b128 v[84:87], v112 offset:29696\n\t" \
    "v_cvt_pk_bf16_f32 v104, v48, v49\n\t" \
    "v_cvt_pk_bf16_f32 v105, v50, v51\n\t" \
    "v_exp_f32_e32 v53, v53\n\t" \
    "v_exp_f32_e32 v54, v54\n\t" \
    "v_exp_f32_e32 v55, v55\n\t" \
    "v_mfma_f32_32x32x16_bf16 v[0:15], v[100:103], v[108:111], v[0:15]\n\t" \
    "ds_read_b128 v[92:95], v112 offset:23552\n\t" \
    "ds_read_b128 v[88:91], v112 offset:26624\n\t" \
    "v_exp_f32_e32 v56, v56\n\t" \
    "v_cvt_pk_bf16_f32 v106, v52, v53\n\t" \
    "v_cvt_pk_bf16_f32 v107, v54, v55\n\t" \
    "v_exp_f32_e32 v57, v57\n\t" \
    "v_exp_f32_e32 v58, v58\n\t" \
    "v_exp_f32_e32 v59, v59\n\t" \
    "v_mfma_f32_32x32x16_bf16 v[0:15], v[96:99], v[104:107], v[0:15]\n\t" \
    "v_exp_f32_e32 v60, v60\n\t" \
    "v_exp_f32_e32 v61, v61\n\t" \
    "v_exp_f32_e32 v62, v62\n\t" \
    "v_exp_f32_e32 v63, v63\n\t" \
    "v_cvt_pk_bf16_f32 v108, v56, v57\n\t" \
    "v_cvt_pk_bf16_f32 v109, v58, v59\n\t" \
    "v_cvt_pk_bf16_f32 v110, v60, v61\n\t" \
    "v_cvt_pk_bf16_f32 v111, v62, v63\n\t" \
    "s_setprio 0\n\t" \
    "s_waitcnt lgkmcnt(0)\n\t" \
    "s_nop 15\n\t"
#define ATT_XT \
    "s_nop 1\n\t" \
    "s_waitcnt lgkmcnt(0)\n\t" \
    "v_mfma_f32_32x32x16_bf16 v[48:63], v[80:83], v[64:67], v[16:31]\n\t" \
    "v_exp_f32_e32 v32, v32\n\t" \
    "v_exp_f32_e32 v33, v33\n\t" \
    "v_exp_f32_e32 v34, v34\n\t" \
    "v_smfmac_f32_16x16x64_bf16 v[76:79], v[72:75], v[104:111], v174\n\t" \
    "v_exp_f32_e32 v35, v35\n\t" \
    "v_exp_f32_e32 v36, v36\n\t" \
    "v_mfma_f32_32x32x16_bf16 v[48:63], v[84:87], v[68:71], v[48:63]\n\t" \
    "v_cvt_pk_bf16_f32 v104, v32, v33\n\t" \
    "v_cvt_pk_bf16_f32 v105, v34, v35\n\t" \
    "v_exp_f32_e32 v37, v37\n\t" \
    "v_exp_f32_e32 v38, v38\n\t" \
    "v_exp_f32_e32 v39, v39\n\t" \
    "v_mfma_f32_32x32x16_bf16 v[0:15], v[92:95], v[108:111], v[0:15]\n\t" \
    "ds_read_b128 v[100:103], v112 offset:27648\n\t" \
    "ds_read_b128 v[96:99], v112 offset:30720\n\t" \
    "v_exp_f32_e32 v40, v40\n\t" \
    "v_cvt_pk_bf16_f32 v106, v36, v37\n\t" \
    "v_cvt_pk_bf16_f32 v107, v38, v39\n\t" \
    "v_exp_f32_e32 v41, v41\n\t" \
    "v_exp_f32_e32 v42, v42\n\t" \
    "v_exp_f32_e32 v43, v43\n\t" \
    "v_mfma_f32_32x32x16_bf16 v[0:15], v[88:91], v[104:107], v[0:15]\n\t" \
    "v_exp_f32_e32 v44, v44\n\t" \
    "v_exp_f32_e32 v45, v45\n\t" \
    "v_exp_f32_e32 v46, v46\n\t" \
    "v_exp_f32_e32 v47, v47\n\t" \
    "v_cvt_pk_bf16_f32 v108, v40, v41\n\t" \
    "v_cvt_pk_bf16_f32 v109, v42, v43\n\t" \
    "v_cvt_pk_bf16_f32 v110, v44, v45\n\t" \
    "v_cvt_pk_bf16_f32 v111, v46, v47\n\t" \
    "s_waitcnt lgkmcnt(0)\n\t" \
    "v_exp_f32_e32 v48, v48\n\t" \
    "v_exp_f32_e32 v49, v49\n\t" \
    "v_exp_f32_e32 v50, v50\n\t" \
    "v_smfmac_f32_16x16x64_bf16 v[76:79], v[72:75], v[104:111], v174\n\t" \
    "v_exp_f32_e32 v51, v51\n\t" \
    "v_exp_f32_e32 v52, v52\n\t" \
    "v_cvt_pk_bf16_f32 v104, v48, v49\n\t" \
    "v_cvt_pk_bf16_f32 v105, v50, v51\n\t" \
    "v_exp_f32_e32 v53, v53\n\t" \
    "v_exp_f32_e32 v54, v54\n\t" \
    "v_exp_f32_e32 v55, v55\n\t" \
    "v_mfma_f32_32x32x16_bf16 v[0:15], v[100:103], v[108:111], v[0:15]\n\t" \
    "ds_read_b128 v[92:95], v112 offset:31744\n\t" \
    "v_exp_f32_e32 v56, v56\n\t" \
    "v_cvt_pk_bf16_f32 v106, v52, v53\n\t" \
    "v_cvt_pk_bf16_f32 v107, v54, v55\n\t" \
    "v_exp_f32_e32 v57, v57\n\t" \
    "v_exp_f32_e32 v58, v58\n\t" \
    "v_exp_f32_e32 v59, v59\n\t" \
    "v_mfma_f32_32x32x16_bf16 v[0:15], v[96:99], v[104:107], v[0:15]\n\t" \
    "v_exp_f32_e32 v60, v60\n\t" \
    "v_exp_f32_e32 v61, v61\n\t" \
    "v_exp_f32_e32 v62, v62\n\t" \
    "v_exp_f32_e32 v63, v63\n\t" \
    "v_cvt_pk_bf16_f32 v108, v56, v57\n\t" \
    "v_cvt_pk_bf16_f32 v109, v58, v59\n\t" \
    "v_cvt_pk_bf16_f32 v110, v60, v61\n\t" \
    "v_cvt_pk_bf16_f32 v111, v62, v63\n\t" \
    "s_waitcnt lgkmcnt(0)\n\t" \
    "s_nop 0\n\t" \
    "v_mfma_f32_32x32x16_bf16 v[0:15], v[92:95], v[108:111], v[0:15]\n\t" \
    "v_smfmac_f32_16x16x64_bf16 v[76:79], v[72:75], v[104:111], v174\n\t" \
    "s_nop 15\n\t"
#define ATT_MAIN \
    "s_mov_b64 s[48:49], %[tab]\n\t" \
    "s_mov_b32 s46, %[k0]\n\t" \
    "s_and_b32 s40, s48, 63\n\t" \
    "s_lshl_b32 s40, s40, 12\n\t" \
    "s_add_u32 s44, %[srclo], s40\n\t" \
    "s_addc_u32 s45, %[srchi], 0\n\t" \
    "s_lshr_b64 s[48:49], s[48:49], 6\n\t" \
    "s_add_u32 s46, s46, 1\n\t" \
    "s_mov_b64 vcc, -1\n\t" \
    "s_waitcnt vmcnt(0)\n\t" \
    "ds_read_b64 v[120:121], v173\n\t" \
    "s_waitcnt lgkmcnt(0)\n\t" \
    "1:\n" \
    "s_cmp_eq_u32 %[cnt], 4\n\t" \
    "s_cbranch_scc1 5f\n\t" \
    "s_waitcnt vmcnt(0)\n\t" \
    "5:\n" \
    "s_waitcnt vmcnt(9)\n\t" \
    "s_barrier\n\t" \
    "s_bitcmp1_b32 %[act], %[cnt]\n\t" \
    "s_cbranch_scc1 30f\n\t" \
    "2:\n" \
    "s_setprio 3\n\t" \
    "s_waitcnt lgkmcnt(0)\n\t" \
    "v_mfma_f32_32x32x16_bf16 v[48:63], v[80:83], v[64:67], v[16:31]\n\t" \
    "v_exp_f32_e32 v32, v32\n\t" \
    "v_exp_f32_e32 v33, v33\n\t" \
    "v_exp_f32_e32 v34, v34\n\t" \
    "v_smfmac_f32_16x16x64_bf16 v[76:79], v[72:75], v[104:111], v174\n\t" \
    "v_exp_f32_e32 v35, v35\n\t" \
    "v_exp_f32_e32 v36, v36\n\t" \
    "v_mfma_f32_32x32x16_bf16 v[48:63], v[84:87], v[68:71], v[48:63]\n\t" \
    "ds_read_b128 v[80:83], v113 offset:0\n\t" \
    "ds_read_b128 v[84:87], v113 offset:1024\n\t" \
    "v_cvt_pk_bf16_f32 v104, v32, v33\n\t" \
    "v_cvt_pk_bf16_f32 v105, v34, v35\n\t" \
    "v_exp_f32_e32 v37, v37\n\t" \
    "v_exp_f32_e32 v38, v38\n\t" \
    "v_exp_f32_e32 v39, v39\n\t" \
    "v_mfma_f32_32x32x16_bf16 v[0:15], v[92:95], v[108:111], v[0:15]\n\t" \
    "ds_read_b128 v[100:103], v112 offset:27648\n\t" \
    "ds_read_b128 v[96:99], v112 offset:30720\n\t" \
    "v_exp_f32_e32 v40, v40\n\t" \
    "v_cvt_pk_bf16_f32 v106, v36, v37\n\t" \
    "v_cvt_pk_bf16_f32 v107, v38, v39\n\t" \
    "v_exp_f32_e32 v41, v41\n\t" \
    "v_exp_f32_e32 v42, v42\n\t" \
    "v_exp_f32_e32 v43, v43\n\t" \
    "v_mfma_f32_32x32x16_bf16 v[0:15], v[88:91], v[104:107], v[0:15]\n\t" \
    "s_cbranch_vccz 20f\n\t" \
    "v_cmp_eq_u64_e64 s[42:43], v[120:121], v[140:141]\n\t" \
    "s_mov_b32 m0, %[dst]\n\t" \
    "s_nop 3\n\t" \
    "s_bitcmp1_b64 s[42:43], s46\n\t" \
    "s_cbranch_scc0 9f\n\t" \
    "12:\n" \
    "global_load_lds_dwordx4 v114, s[44:45] offset:0 sc1\n\t" \
    "20:\n" \
    "v_exp_f32_e32 v44, v44\n\t" \
    "v_exp_f32_e32 v45, v45\n\t" \
    "v_exp_f32_e32 v46, v46\n\t" \
    "v_exp_f32_e32 v47, v47\n\t" \
    "v_cvt_pk_bf16_f32 v108, v40, v41\n\t" \
    "v_cvt_pk_bf16_f32 v109, v42, v43\n\t" \
    "v_cvt_pk_bf16_f32 v110, v44, v45\n\t" \
    "v_cvt_pk_bf16_f32 v111, v46, v47\n\t" \
    "s_waitcnt lgkmcnt(0)\n\t" \
    "v_mfma_f32_32x32x16_bf16 v[32:47], v[80:83], v[64:67], v[16:31]\n\t" \
    "v_exp_f32_e32 v48, v48\n\t" \
    "v_exp_f32_e32 v49, v49\n\t" \
    "v_exp_f32_e32 v50, v50\n\t" \
    "v_smfmac_f32_16x16x64_bf16 v[76:79], v[72:75], v[104:111], v174\n\t" \
    "v_exp_f32_e32 v51, v51\n\t" \
    "v_exp_f32_e32 v52, v52\n\t" \
    "v_mfma_f32_32x32x16_bf16 v[32:47], v[84:87], v[68:71], v[32:47]\n\t" \
    "ds_read_b128 v[80:83], v113 offset:4096\n\t" \
    "ds_read_b128 v[84:87], v113 offset:5120\n\t" \
    "v_cvt_pk_bf16_f32 v104, v48, v49\n\t" \
    "v_cvt_pk_bf16_f32 v105, v50, v51\n\t" \
    "v_exp_f32_e32 v53, v53\n\t" \
    "v_exp_f32_e32 v54, v54\n\t" \
    "v_exp_f32_e32 v55, v55\n\t" \
    "v_mfma_f32_32x32x16_bf16 v[0:15], v[100:103], v[108:111], v[0:15]\n\t" \
    "ds_read_b128 v[92:95], v112 offset:31744\n\t" \
    "ds_read_b128 v[88:91], v113 offset:2048\n\t" \
    "v_exp_f32_e32 v56, v56\n\t" \
    "v_cvt_pk_bf16_f32 v106, v52, v53\n\t" \
    "v_cvt_pk_bf16_f32 v107, v54, v55\n\t" \
    "v_exp_f32_e32 v57, v57\n\t" \
    "v_exp_f32_e32 v58, v58\n\t" \
    "v_exp_f32_e32 v59, v59\n\t" \
    "v_mfma_f32_32x32x16_bf16 v[0:15], v[96:99], v[104:107], v[0:15]\n\t" \
    "s_cbranch_vccz 21f\n\t" \
    "global_load_lds_dwordx4 v114, s[44:45] offset:1024 sc1\n\t" \
    "21:\n" \
    "v_exp_f32_e32 v60, v60\n\t" \
    "v_exp_f32_e32 v61, v61\n\t" \
    "v_exp_f32_e32 v62, v62\n\t" \
    "v_exp_f32_e32 v63, v63\n\t" \
    "v_cvt_pk_bf16_f32 v108, v56, v57\n\t" \
    "v_cvt_pk_bf16_f32 v109, v58, v59\n\t" \
    "v_cvt_pk_bf16_f32 v110, v60, v61\n\t" \
    "v_cvt_pk_bf16_f32 v111, v62, v63\n\t" \
    "s_setprio 2\n\t" \
    "s_waitcnt lgkmcnt(0)\n\t" \
    "v_mfma_f32_32x32x16_bf16 v[48:63], v[80:83], v[64:67], v[16:31]\n\t" \
    "v_exp_f32_e32 v32, v32\n\t" \
    "v_exp_f32_e32 v33, v33\n\t" \
    "v_exp_f32_e32 v34, v34\n\t" \
    "v_smfmac_f32_16x16x64_bf16 v[76:79], v[72:75], v[104:111], v174\n\t" \
    "v_exp_f32_e32 v35, v35\n\t" \
    "v_exp_f32_e32 v36, v36\n\t" \
    "v_mfma_f32_32x32x16_bf16 v[48:63], v[84:87], v[68:71], v[48:63]\n\t" \
    "ds_read_b128 v[80:83], v113 offset:8192\n\t" \
    "ds_read_b128 v[84:87], v113 offset:9216\n\t" \
    "v_cvt_pk_bf16_f32 v104, v32, v33\n\t" \
    "v_cvt_pk_bf16_f32 v105, v34, v35\n\t" \
    "v_exp_f32_e32 v37, v37\n\t" \
    "v_exp_f32_e32 v38, v38\n\t" \
    "v_exp_f32_e32 v39, v39\n\t" \
    "v_mfma_f32_32x32x16_bf16 v[0:15], v[92:95], v[108:111], v[0:15]\n\t" \
    "ds_read_b128 v[100:103], v113 offset:3072\n\t" \
    "ds_read_b128 v[96:99], v113 offset:6144\n\t" \
    "v_exp_f32_e32 v40, v40\n\t" \
    "v_cvt_pk_bf16_f32 v106, v36, v37\n\t" \
    "v_cvt_pk_bf16_f32 v107, v38, v39\n\t" \
    "v_exp_f32_e32 v41, v41\n\t" \
    "v_exp_f32_e32 v42, v42\n\t" \
    "v_exp_f32_e32 v43, v43\n\t" \
    "v_mfma_f32_32x32x16_bf16 v[0:15], v[88:91], v[104:107], v[0:15]\n\t" \
    "s_cbranch_vccz 22f\n\t" \
    "global_load_lds_dwordx4 v114, s[44:45] offset:2048 sc1\n\t" \
    "22:\n" \
    "v_exp_f32_e32 v44, v44\n\t" \
    "v_exp_f32_e32 v45, v45\n\t" \
    "v_exp_f32_e32 v46, v46\n\t" \
    "v_exp_f32_e32 v47, v47\n\t" \
    "v_cvt_pk_bf16_f32 v108, v40, v41\n\t" \
    "v_cvt_pk_bf16_f32 v109, v42, v43\n\t" \
    "v_cvt_pk_bf16_f32 v110, v44, v45\n\t" \
    "v_cvt_pk_bf16_f32 v111, v46, v47\n\t" \
    "s_waitcnt lgkmcnt(0)\n\t" \
    "v_mfma_f32_32x32x16_bf16 v[32:47], v[80:83], v[64:67], v[16:31]\n\t" \
    "v_exp_f32_e32 v48, v48\n\t" \
    "v_exp_f32_e32 v49, v49\n\t" \
    "v_exp_f32_e32 v50, v50\n\t" \
    "v_smfmac_f32_16x16x64_bf16 v[76:79], v[72:75], v[104:111], v174\n\t" \
    "v_exp_f32_e32 v51, v51\n\t" \
    "v_exp_f32_e32 v52, v52\n\t" \
    "v_mfma_f32_32x32x16_bf16 v[32:47], v[84:87], v[68:71], v[32:47]\n\t" \
    "ds_read_b128 v[80:83], v113 offset:12288\n\t" \
    "ds_read_b128 v[84:87], v113 offset:13312\n\t" \
    "v_cvt_pk_bf16_f32 v104, v48, v49\n\t" \
    "v_cvt_pk_bf16_f32 v105, v50, v51\n\t" \
    "v_exp_f32_e32 v53, v53\n\t" \
    "v_exp_f32_e32 v54, v54\n\t" \
    "v_exp_f32_e32 v55, v55\n\t" \
    "v_mfma_f32_32x32x16_bf16 v[0:15], v[100:103], v[108:111], v[0:15]\n\t" \
    "ds_read_b128 v[92:95], v113 offset:7168\n\t" \
    "ds_read_b128 v[88:91], v113 offset:10240\n\t" \
    "v_exp_f32_e32 v56, v56\n\t" \
    "v_cvt_pk_bf16_f32 v106, v52, v53\n\t" \
    "v_cvt_pk_bf16_f32 v107, v54, v55\n\t" \
    "v_exp_f32_e32 v57, v57\n\t" \
    "v_exp_f32_e32 v58, v58\n\t" \
    "v_exp_f32_e32 v59, v59\n\t" \
    "v_mfma_f32_32x32x16_bf16 v[0:15], v[96:99], v[104:107], v[0:15]\n\t" \
    "s_cbranch_vccz 23f\n\t" \
    "global_load_lds_dwordx4 v114, s[44:45] offset:3072 sc1\n\t" \
    "global_load_dwordx2 v[120:121], v170, %[ws] sc1\n\t" \
    "23:\n" \
    "v_exp_f32_e32 v60, v60\n\t" \
    "v_exp_f32_e32 v61, v61\n\t" \
    "v_exp_f32_e32 v62, v62\n\t" \
    "v_exp_f32_e32 v63, v63\n\t" \
    "v_cvt_pk_bf16_f32 v108, v56, v57\n\t" \
    "v_cvt_pk_bf16_f32 v109, v58, v59\n\t" \
    "v_cvt_pk_bf16_f32 v110, v60, v61\n\t" \
    "v_cvt_pk_bf16_f32 v111, v62, v63\n\t" \
    "s_setprio 1\n\t" \
    "s_waitcnt lgkmcnt(0)\n\t" \
    "v_mfma_f32_32x32x16_bf16 v[48:63], v[80:83], v[64:67], v[16:31]\n\t" \
    "v_exp_f32_e32 v32, v32\n\t" \
    "v_exp_f32_e32 v33, v33\n\t" \
    "v_exp_f32_e32 v34, v34\n\t" \
    "v_smfmac_f32_16x16x64_bf16 v[76:79], v[72:75], v[104:111], v174\n\t" \
    "v_exp_f32_e32 v35, v35\n\t" \
    "v_exp_f32_e32 v36, v36\n\t" \
    "v_mfma_f32_32x32x16_bf16 v[48:63], v[84:87], v[68:71], v[48:63]\n\t" \
    "ds_read_b128 v[80:83], v113 offset:16384\n\t" \
    "ds_read_b128 v[84:87], v113 offset:17408\n\t" \
    "v_cvt_pk_bf16_f32 v104, v32, v33\n\t" \
    "v_cvt_pk_bf16_f32 v105, v34, v35\n\t" \
    "v_exp_f32_e32 v37, v37\n\t" \
    "v_exp_f32_e32 v38, v38\n\t" \
    "v_exp_f32_e32 v39, v39\n\t" \
    "v_mfma_f32_32x32x16_bf16 v[0:15], v[92:95], v[108:111], v[0:15]\n\t" \
    "ds_read_b128 v[100:103], v113 offset:11264\n\t" \
    "ds_read_b128 v[96:99], v113 offset:14336\n\t" \
    "v_exp_f32_e32 v40, v40\n\t" \
    "v_cvt_pk_bf16_f32 v106, v36, v37\n\t" \
    "v_cvt_pk_bf16_f32 v107, v38, v39\n\t" \
    "v_exp_f32_e32 v41, v41\n\t" \
    "v_exp_f32_e32 v42, v42\n\t" \
    "v_exp_f32_e32 v43, v43\n\t" \
    "v_mfma_f32_32x32x16_bf16 v[0:15], v[88:91], v[104:107], v[0:15]\n\t" \
    "s_add_u32 %[dst], %[dst], 0x8000\n\t" \
    "s_cmp_eq_u32 %[dst], %[dend]\n\t" \
    "s_cselect_b32 %[dst], %[dbeg], %[dst]\n\t" \
    "s_add_u32 %[nx], %[nx], 0x8000\n\t" \
    "s_cmp_eq_u32 %[nx], 0x18000\n\t" \
    "s_cselect_b32 %[nx], 0, %[nx]\n\t" \
    "s_bitcmp1_b32 %[hk], %[cnt]\n\t" \
    "s_cbranch_scc1 40f\n\t" \
    "24:\n" \
    "v_exp_f32_e32 v44, v44\n\t" \
    "v_exp_f32_e32 v45, v45\n\t" \
    "v_exp_f32_e32 v46, v46\n\t" \
    "v_exp_f32_e32 v47, v47\n\t" \
    "v_cvt_pk_bf16_f32 v108, v40, v41\n\t" \
    "v_cvt_pk_bf16_f32 v109, v42, v43\n\t" \
    "v_cvt_pk_bf16_f32 v110, v44, v45\n\t" \
    "v_cvt_pk_bf16_f32 v111, v46, v47\n\t" \
    "s_waitcnt lgkmcnt(0)\n\t" \
    "v_mfma_f32_32x32x16_bf16 v[32:47], v[80:83], v[64:67], v[16:31]\n\t" \
    "v_exp_f32_e32 v48, v48\n\t" \
    "v_exp_f32_e32 v49, v49\n\t" \
    "v_exp_f32_e32 v50, v50\n\t" \
    "v_smfmac_f32_16x16x64_bf16 v[76:79], v[72:75], v[104:111], v174\n\t" \
    "v_exp_f32_e32 v51, v51\n\t" \
    "v_exp_f32_e32 v52, v52\n\t" \
    "v_mfma_f32_32x32x16_bf16 v[32:47], v[84:87], v[68:71], v[32:47]\n\t" \
    "ds_read_b128 v[80:83], v113 offset:20480\n\t" \
    "ds_read_b128 v[84:87], v113 offset:21504\n\t" \
    "v_cvt_pk_bf16_f32 v104, v48, v49\n\t" \
    "v_cvt_pk_bf16_f32 v105, v50, v51\n\t" \
    "v_exp_f32_e32 v53, v53\n\t" \
    "v_exp_f32_e32 v54, v54\n\t" \
    "v_exp_f32_e32 v55, v55\n\t" \
    "v_mfma_f32_32x32x16_bf16 v[0:15], v[100:103], v[108:111], v[0:15]\n\t" \
    "ds_read_b128 v[92:95], v113 offset:15360\n\t" \
    "ds_read_b128 v[88:91], v113 offset:18432\n\t" \
    "v_exp_f32_e32 v56, v56\n\t" \
    "v_cvt_pk_bf16_f32 v106, v52, v53\n\t" \
    "v_cvt_pk_bf16_f32 v107, v54, v55\n\t" \
    "v_exp_f32_e32 v57, v57\n\t" \
    "v_exp_f32_e32 v58, v58\n\t" \
    "v_exp_f32_e32 v59, v59\n\t" \
    "v_mfma_f32_32x32x16_bf16 v[0:15], v[96:99], v[104:107], v[0:15]\n\t" \
    "s_bitcmp1_b32 %[hk], %[cnt]\n\t" \
    "s_cbranch_scc1 41f\n\t" \
    "25:\n" \
    "v_exp_f32_e32 v60, v60\n\t" \
    "v_exp_f32_e32 v61, v61\n\t" \
    "v_exp_f32_e32 v62, v62\n\t" \
    "v_exp_f32_e32 v63, v63\n\t" \
    "v_cvt_pk_bf16_f32 v108, v56, v57\n\t" \
    "v_cvt_pk_bf16_f32 v109, v58, v59\n\t" \
    "v_cvt_pk_bf16_f32 v110, v60, v61\n\t" \
    "v_cvt_pk_bf16_f32 v111, v62, v63\n\t" \
    "s_setprio 0\n\t" \
    "s_waitcnt lgkmcnt(0)\n\t" \
    "v_mfma_f32_32x32x16_bf16 v[48:63], v[80:83], v[64:67], v[16:31]\n\t" \
    "v_exp_f32_e32 v32, v32\n\t" \
    "v_exp_f32_e32 v33, v33\n\t" \
    "v_exp_f32_e32 v34, v34\n\t" \
    "v_smfmac_f32_16x16x64_bf16 v[76:79], v[72:75], v[104:111], v174\n\t" \
    "v_exp_f32_e32 v35, v35\n\t" \
    "v_exp_f32_e32 v36, v36\n\t" \
    "v_mfma_f32_32x32x16_bf16 v[48:63], v[84:87], v[68:71], v[48:63]\n\t" \
    "ds_read_b128 v[80:83], v113 offset:24576\n\t" \
    "ds_read_b128 v[84:87], v113 offset:25600\n\t" \
    "v_cvt_pk_bf16_f32 v104, v32, v33\n\t" \
    "v_cvt_pk_bf16_f32 v105, v34, v35\n\t" \
    "v_exp_f32_e32 v37, v37\n\t" \
    "v_exp_f32_e32 v38, v38\n\t" \
    "v_exp_f32_e32 v39, v39\n\t" \
    "v_mfma_f32_32x32x16_bf16 v[0:15], v[92:95], v[108:111], v[0:15]\n\t" \
    "ds_read_b128 v[100:103], v113 offset:19456\n\t" \
    "ds_read_b128 v[96:99], v113 offset:22528\n\t" \
    "v_exp_f32_e32 v40, v40\n\t" \
    "v_cvt_pk_bf16_f32 v106, v36, v37\n\t" \
    "v_cvt_pk_bf16_f32 v107, v38, v39\n\t" \
    "v_exp_f32_e32 v41, v41\n\t" \
    "v_exp_f32_e32 v42, v42\n\t" \
    "v_exp_f32_e32 v43, v43\n\t" \
    "v_mfma_f32_32x32x16_bf16 v[0:15], v[88:91], v[104:107], v[0:15]\n\t" \
    "s_and_b32 s40, s48, 63\n\t" \
    "s_lshl_b32 s40, s40, 12\n\t" \
    "s_add_u32 s44, %[srclo], s40\n\t" \
    "s_addc_u32 s45, %[srchi], 0\n\t" \
    "s_lshr_b64 s[48:49], s[48:49], 6\n\t" \
    "s_add_u32 s46, s46, 1\n\t" \
    "s_bitcmp1_b32 %[hk], %[cnt]\n\t" \
    "s_cbranch_scc1 42f\n\t" \
    "26:\n" \
    "v_exp_f32_e32 v44, v44\n\t" \
    "v_exp_f32_e32 v45, v45\n\t" \
    "v_exp_f32_e32 v46, v46\n\t" \
    "v_exp_f32_e32 v47, v47\n\t" \
    "v_cvt_pk_bf16_f32 v108, v40, v41\n\t" \
    "v_cvt_pk_bf16_f32 v109, v42, v43\n\t" \
    "v_cvt_pk_bf16_f32 v110, v44, v45\n\t" \
    "v_cvt_pk_bf16_f32 v111, v46, v47\n\t" \
    "s_waitcnt lgkmcnt(0)\n\t" \
    "v_mfma_f32_32x32x16_bf16 v[32:47], v[80:83], v[64:67], v[16:31]\n\t" \
    "v_exp_f32_e32 v48, v48\n\t" \
    "v_exp_f32_e32 v49, v49\n\t" \
    "v_exp_f32_e32 v50, v50\n\t" \
    "v_smfmac_f32_16x16x64_bf16 v[76:79], v[72:75], v[104:111], v174\n\t" \
    "v_exp_f32_e32 v51, v51\n\t" \
    "v_exp_f32_e32 v52, v52\n\t" \
    "v_mfma_f32_32x32x16_bf16 v[32:47], v[84:87], v[68:71], v[32:47]\n\t" \
    "ds_read_b128 v[80:83], v113 offset:28672\n\t" \
    "ds_read_b128 v[84:87], v113 offset:29696\n\t" \
    "v_cvt_pk_bf16_f32 v104, v48, v49\n\t" \
    "v_cvt_pk_bf16_f32 v105, v50, v51\n\t" \
    "v_exp_f32_e32 v53, v53\n\t" \
    "v_exp_f32_e32 v54, v54\n\t" \
    "v_exp_f32_e32 v55, v55\n\t" \
    "v_mfma_f32_32x32x16_bf16 v[0:15], v[100:103], v[108:111], v[0:15]\n\t" \
    "ds_read_b128 v[92:95], v113 offset:23552\n\t" \
    "ds_read_b128 v[88:91], v113 offset:26624\n\t" \
    "v_exp_f32_e32 v56, v56\n\t" \
    "v_cvt_pk_bf16_f32 v106, v52, v53\n\t" \
    "v_cvt_pk_bf16_f32 v107, v54, v55\n\t" \
    "v_exp_f32_e32 v57, v57\n\t" \
    "v_exp_f32_e32 v58, v58\n\t" \
    "v_exp_f32_e32 v59, v59\n\t" \
    "v_mfma_f32_32x32x16_bf16 v[0:15], v[96:99], v[104:107], v[0:15]\n\t" \
    "s_bitcmp1_b32 %[hk], %[cnt]\n\t" \
    "s_cbranch_scc1 43f\n\t" \
    "27:\n" \
    "v_mov_b32_e32 v112, v113\n\t" \
    "v_add_u32_e32 v113, %[nx], v115\n\t" \
    "s_sub_u32 %[cnt], %[cnt], 1\n\t" \
    "s_cmp_lg_u32 %[cnt], 0\n\t" \
    "v_exp_f32_e32 v60, v60\n\t" \
    "v_exp_f32_e32 v61, v61\n\t" \
    "v_exp_f32_e32 v62, v62\n\t" \
    "v_exp_f32_e32 v63, v63\n\t" \
    "v_cvt_pk_bf16_f32 v108, v56, v57\n\t" \
    "v_cvt_pk_bf16_f32 v109, v58, v59\n\t" \
    "v_cvt_pk_bf16_f32 v110, v60, v61\n\t" \
    "v_cvt_pk_bf16_f32 v111, v62, v63\n\t" \
    "s_cbranch_scc1 1b\n\t" \
    "7:\n" \
    "s_setprio 0\n\t" \
    "s_waitcnt lgkmcnt(0)\n\t" \
    "v_mfma_f32_32x32x16_bf16 v[48:63], v[80:83], v[64:67], v[16:31]\n\t" \
    "v_exp_f32_e32 v32, v32\n\t" \
    "v_exp_f32_e32 v33, v33\n\t" \
    "v_exp_f32_e32 v34, v34\n\t" \
    "v_smfmac_f32_16x16x64_bf16 v[76:79], v[72:75], v[104:111], v174\n\t" \
    "v_exp_f32_e32 v35, v35\n\t" \
    "v_exp_f32_e32 v36, v36\n\t" \
    "v_mfma_f32_32x32x16_bf16 v[48:63], v[84:87], v[68:71], v[48:63]\n\t" \
    "v_cvt_pk_bf16_f32 v104, v32, v33\n\t" \
    "v_cvt_pk_bf16_f32 v105, v34, v35\n\t" \
    "v_exp_f32_e32 v37, v37\n\t" \
    "v_exp_f32_e32 v38, v38\n\t" \
    "v_exp_f32_e32 v39, v39\n\t" \
    "v_mfma_f32_32x32x16_bf16 v[0:15], v[92:95], v[108:111], v[0:15]\n\t" \
    "ds_read_b128 v[100:103], v112 offset:27648\n\t" \
    "ds_read_b128 v[96:99], v112 offset:30720\n\t" \
    "v_exp_f32_e32 v40, v40\n\t" \
    "v_cvt_pk_bf16_f32 v106, v36, v37\n\t" \
    "v_cvt_pk_bf16_f32 v107, v38, v39\n\t" \
    "v_exp_f32_e32 v41, v41\n\t" \
    "v_exp_f32_e32 v42, v42\n\t" \
    "v_exp_f32_e32 v43, v43\n\t" \
    "v_mfma_f32_32x32x16_bf16 v[0:15], v[88:91], v[104:107], v[0:15]\n\t" \
    "v_exp_f32_e32 v44, v44\n\t" \
    "v_exp_f32_e32 v45, v45\n\t" \
    "v_exp_f32_e32 v46, v46\n\t" \
    "v_exp_f32_e32 v47, v47\n\t" \
    "v_cvt_pk_bf16_f32 v108, v40, v41\n\t" \
    "v_cvt_pk_bf16_f32 v109, v42, v43\n\t" \
    "v_cvt_pk_bf16_f32 v110, v44, v45\n\t" \
    "v_cvt_pk_bf16_f32 v111, v46, v47\n\t" \
    "s_waitcnt lgkmcnt(0)\n\t" \
    "v_exp_f32_e32 v48, v48\n\t" \
    "v_exp_f32_e32 v49, v49\n\t" \
    "v_exp_f32_e32 v50, v50\n\t" \
    "v_smfmac_f32_16x16x64_bf16 v[76:79], v[72:75], v[104:111], v174\n\t" \
    "v_exp_f32_e32 v51, v51\n\t" \
    "v_exp_f32_e32 v52, v52\n\t" \
    "v_cvt_pk_bf16_f32 v104, v48, v49\n\t" \
    "v_cvt_pk_bf16_f32 v105, v50, v51\n\t" \
    "v_exp_f32_e32 v53, v53\n\t" \
    "v_exp_f32_e32 v54, v54\n\t" \
    "v_exp_f32_e32 v55, v55\n\t" \
    "v_mfma_f32_32x32x16_bf16 v[0:15], v[100:103], v[108:111], v[0:15]\n\t" \
    "ds_read_b128 v[92:95], v112 offset:31744\n\t" \
    "v_exp_f32_e32 v56, v56\n\t" \
    "v_cvt_pk_bf16_f32 v106, v52, v53\n\t" \
    "v_cvt_pk_bf16_f32 v107, v54, v55\n\t" \
    "v_exp_f32_e32 v57, v57\n\t" \
    "v_exp_f32_e32 v58, v58\n\t" \
    "v_exp_f32_e32 v59, v59\n\t" \
    "v_mfma_f32_32x32x16_bf16 v[0:15], v[96:99], v[104:107], v[0:15]\n\t" \
    "v_exp_f32_e32 v60, v60\n\t" \
    "v_exp_f32_e32 v61, v61\n\t" \
    "v_exp_f32_e32 v62, v62\n\t" \
    "v_exp_f32_e32 v63, v63\n\t" \
    "v_cvt_pk_bf16_f32 v108, v56, v57\n\t" \
    "v_cvt_pk_bf16_f32 v109, v58, v59\n\t" \
    "v_cvt_pk_bf16_f32 v110, v60, v61\n\t" \
    "v_cvt_pk_bf16_f32 v111, v62, v63\n\t" \
    "s_waitcnt lgkmcnt(0)\n\t" \
    "s_nop 0\n\t" \
    "v_mfma_f32_32x32x16_bf16 v[0:15], v[92:95], v[108:111], v[0:15]\n\t" \
    "v_smfmac_f32_16x16x64_bf16 v[76:79], v[72:75], v[104:111], v174\n\t" \
    "s_nop 15\n\t" \
    "s_branch 99f\n\t" \
    "9:\n" \
    "s_mov_b32 s47, 0x40000\n\t" \
    "10:\n" \
    "s_sub_u32 s47, s47, 1\n\t" \
    "s_cbranch_scc1 12b\n\t" \
    "global_load_dwordx2 v[120:121], v170, %[ws] sc1\n\t" \
    "s_waitcnt vmcnt(0)\n\t" \
    "v_cmp_eq_u64_e64 s[42:43], v[120:121], v[140:141]\n\t" \
    "s_nop 4\n\t" \
    "s_bitcmp1_b64 s[42:43], s46\n\t" \
    "s_cbranch_scc1 12b\n\t" \
    "s_sleep 4\n\t" \
    "s_branch 10b\n\t" \
    "50:\n" \
    "global_store_dwordx4 v171, v[176:179], %[ws] offset:-3072 sc1\n\t" \
    "global_store_dwordx4 v171, v[180:183], %[ws] offset:-2944 sc1\n\t" \
    "global_store_dwordx4 v172, v[184:187], %[ws] offset:-1024 sc1\n\t" \
    "global_store_dwordx4 v172, v[188:191], %[ws] offset:-768 sc1\n\t" \
    "s_branch 24b\n\t" \
    "51:\n" \
    "global_store_dwordx4 v171, v[192:195], %[ws] offset:1024 sc1\n\t" \
    "global_store_dwordx4 v171, v[196:199], %[ws] offset:1152 sc1\n\t" \
    "global_store_dwordx4 v172, v[200:203], %[ws] offset:3072 sc1\n\t" \
    "global_store_dwordx4 v172, v[204:207], %[ws] offset:3328 sc1\n\t" \
    "s_branch 25b\n\t" \
    "40:\n" \
    "s_cmp_eq_u32 %[cnt], 7\n\t" \
    "s_cbranch_scc1 50b\n\t" \
    "s_add_u32 m0, %[wdst], 0\n\t" \
    "s_nop 0\n\t" \
    "global_load_lds_dwordx4 v116, %[wo01]\n\t" \
    "global_load_lds_dwordx4 v117, %[wo01] offset:1024\n\t" \
    "v_add_u32_e32 v116, 0x20000, v116\n\t" \
    "v_add_u32_e32 v117, 0x20000, v117\n\t" \
    "s_branch 24b\n\t" \
    "41:\n" \
    "s_cmp_eq_u32 %[cnt], 7\n\t" \
    "s_cbranch_scc1 51b\n\t" \
    "s_add_u32 m0, %[wdst], 16384\n\t" \
    "s_nop 0\n\t" \
    "global_load_lds_dwordx4 v116, %[wo01]\n\t" \
    "global_load_lds_dwordx4 v117, %[wo01] offset:1024\n\t" \
    "v_subrev_u32_e32 v116, 0x20000, v116\n\t" \
    "v_subrev_u32_e32 v117, 0x20000, v117\n\t" \
    "s_branch 25b\n\t" \
    "42:\n" \
    "s_cmp_eq_u32 %[cnt], 7\n\t" \
    "s_cbranch_scc1 26b\n\t" \
    "s_add_u32 m0, %[wdst], 32768\n\t" \
    "s_nop 0\n\t" \
    "global_load_lds_dwordx4 v116, %[wo23]\n\t" \
    "global_load_lds_dwordx4 v117, %[wo23] offset:1024\n\t" \
    "v_add_u32_e32 v116, 0x20000, v116\n\t" \
    "v_add_u32_e32 v117, 0x20000, v117\n\t" \
    "s_branch 26b\n\t" \
    "43:\n" \
    "s_cmp_eq_u32 %[cnt], 7\n\t" \
    "s_cbranch_scc1 27b\n\t" \
    "s_add_u32 m0, %[wdst], 49152\n\t" \
    "s_nop 0\n\t" \
    "global_load_lds_dwordx4 v116, %[wo23]\n\t" \
    "global_load_lds_dwordx4 v117, %[wo23] offset:1024\n\t" \
    "global_load_dword v168, v119, %[wo]\n\t" \
    "s_branch 27b\n\t" \
    "30:\n" \
    "s_cmp_eq_u32 %[cnt], 1\n\t" \
    "s_cbranch_scc0 31f\n\t" \
    "s_mov_b64 vcc, 0\n\t" \
    "s_bitcmp1_b32 %[mode], 1\n\t" \
    "s_cbranch_scc1 7b\n\t" \
    "s_branch 2b\n\t" \
    "31:\n" \
    "s_cmp_eq_u32 %[cnt], 6\n\t" \
    "s_cbranch_scc0 33f\n\t" \
    "global_store_dwordx2 v175, v[140:141], %[ws] sc1\n\t" \
    "s_branch 2b\n\t" \
    "33:\n" \
    "s_cmp_eq_u32 %[cnt], 5\n\t" \
    "s_cbranch_scc0 34f\n\t" \
    "global_store_dwordx2 v118, v[140:141], %[ws] sc1\n\t" \
    "s_branch 2b\n\t" \
    "34:\n" \
    "s_cmp_eq_u32 %[cnt], 3\n\t" \
    "s_cbranch_scc0 35f\n\t" \
    "global_load_dwordx2 v[168:169], v142, %[ws] sc1\n\t" \
    "s_branch 2b\n\t" \
    "35:\n" \
    "v_cmp_eq_u64_e64 s[42:43], v[168:169], v[140:141]\n\t" \
    "s_nop 4\n\t" \
    "s_cmp_eq_u64 s[42:43], -1\n\t" \
    "s_cbranch_scc0 2b\n\t" \
    "global_load_dwordx4 v[122:125], v143, %[ws] offset:-2048 sc1\n\t" \
    "global_load_dwordx4 v[126:129], v143, %[ws] offset:-1024 sc1\n\t" \
    "global_load_dwordx4 v[130:133], v143, %[ws] offset:0 sc1\n\t" \
    "global_load_dwordx4 v[134:137], v143, %[ws] offset:1024 sc1\n\t" \
    "global_load_dwordx2 v[138:139], v143, %[ws] offset:2048 sc1\n\t" \
    "s_bitset1_b32 %[mode], 2\n\t" \
    "s_branch 2b\n\t" \
    "99:\n"
#define ATT_TMP "v48", "v49", "v50", "v51", "v52", "v53", "v54", "v55", "v56", "v57", "v58", "v59", "v60", "v61", "v62", "v63", "v96", "v97", "v98", "v99", "v100", "v101", "v102", "v103"

constexpr int K1_X = 0, K1_W = 81920, K1_WB = 24576, K1_EP = 155648;
constexpr int K2_SLOT = 32768, K2_QX = 3 * K2_SLOT, K2_FL = 147456;
constexpr int LDS_BYTES = 163840;
constexpr int p3_wslot(int t) { return (t % 6) < 4 ? 98304 + (t % 6) * 16384 : 65536 + ((t % 6) - 4) * 16384; }
constexpr int p3_allow(int t) { return (((t < 2 || t - 2 >= 1) && t + 3 < 16) ? 4 : 0) + (t + 3 < 16 ? 4 : 0) + ((t >= 1 && t + 5 < 16) ? 4 : 0); }

__global__ __launch_bounds__(512, 2) void mk_fused(const u16* __restrict__ xA, const u16* __restrict__ wA, const u16* __restrict__ woA,
                                                   const u16* __restrict__ xB, const u16* __restrict__ wB, const u16* __restrict__ woB, char* ws0, u16* __restrict__ out) {
    char* ws = ws0;
    __shared__ __attribute__((aligned(1024))) char smem[LDS_BYTES];
    const unsigned lds0 = (unsigned)(uintptr_t)(LAS char*)smem;
    const int wid = __builtin_amdgcn_readfirstlane(threadIdx.x >> 6);
    int lane = threadIdx.x & 63;
#define BRX 1
    const int b = blockIdx.x, xcd = b & 7, j = b >> 3, br = (xcd >> 2) ^ BRX, qd = xcd & 3;
    const int mt = (qd & 1) * 4 + (j & 3), hp = (qd >> 1) * 8 + (j >> 2);
    const int mt3 = qd * 4 + (j & 3), nt3 = j >> 2;
    u64 tag;
    u32x4 fq[2][2], fk[2][2], fv[2][2];
    f32x16 band;
#define RBLK(k, w) ((((k) + (w) - (((k) + (w) >= 7) ? 7 : 0)) + (((k) + (w) - (((k) + (w) >= 7) ? 7 : 0)) >= mt)) * 8 + (k) + ((k) + (w) >= 7))
    u64 tabf;
    unsigned c_fl, c_pv, c_wb, c_tw, c_aw, c_fo;
#define PRECOMPUTE(BAND) do { const int g_ = wid >> 2, wi_ = wid & 3, c16_ = lane & 15, g4_ = lane >> 4; \
        if (BAND) band = band_mask(lane); \
        tabf = (u64)(mt * 8 + wid) << 42; \
        _Pragma("unroll") for (int k = 0; k < 7; ++k) tabf |= (u64)RBLK(k, wid) << (6 * k); \
        c_fl = (unsigned)WS_FK + (2 * hp + br) * 32 * FS + (((unsigned)(tabf >> (6 * (lane & 7))) & 63u) >> 1) * FS;        \
        c_wb = g4_ * 2048 + ((c16_ ^ (g4_ << 1)) << 4); \
        { const int i = (mt3 * 8 + wid) * 12 + ((lane * 3) >> 4); c_tw = ((((4 + (i >> 7) + 2 * nt3) & 15) * 64 + ((i & 127) >> 1)) * 2048 + (i & 1) * 128); } \
        { const int q4 = c16_ >> 2, p = lane & 3, k = 16 * (g4_ >> 1) + 4 * (g4_ & 1) + q4; \
          c_aw = lds0 + p3_wslot(0) + g_ * 16384 + k * 256 + (((8 * (wi_ & 1) + (p >> 1)) ^ ((k & 7) << 1)) << 4) + (p & 1) * 8; } \
        c_fo = (unsigned)WS_FO + ((br * 64 + mt3 * 4 + wi_) * 32 + 4 * (c16_ >> 1) + 2 * (1 - g_) + (lane & 1)) * FS; \
        asm volatile("" : "+v"(band), "+s"(tabf), "+v"(c_fl), "+v"(c_wb), "+v"(c_tw), "+v"(c_aw), "+v"(c_fo)); } while (0)
    STAMP_AT(0, 0);
    {
        const int g = wid >> 2, wi = wid & 3, mw = wi >> 1, hd = wi & 1;
        if (wid == 7) {
            const char* pc; unsigned len;
            asm volatile("s_getpc_b64 %0\n.Lmk_code_begin:\n\ts_mov_b32 %1, .Lmk_code_end-.Lmk_code_begin" : "=s"(pc), "=s"(len));
            const unsigned off = (unsigned)(j * 64 + lane) * 64u;
            asm volatile("s_mov_b32 m0, %2\n\ts_nop 0\n\tglobal_load_lds_dword %0, %1" :: "v"(off < len ? off : 0u), "s"(pc), "s"(lds0 + 159744) : "memory");
        }
        unsigned vx[4], vw;
        { const unsigned r8 = lane >> 3, c8 = lane & 7, b1 = (lane >> 4) & 1;
          vw = (wid * 8 + r8) * 6144 + ((c8 ^ ((b1 | ((wid & 1) << 1)) << 1)) << 4);
          const unsigned st = (16384u << br) - 1024u;
          vx[0] = (((r8 << br) + (br ? (wi >> 1) * 64 + (wi & 1) : wi * 32)) << 11) + ((c8 ^ (b1 | ((lane >> 5) << 2))) << 4);
          vx[1] = vx[0] + st; vx[2] = (vx[0] ^ 32) + 2 * st; vx[3] = vx[2] + st; }
        asm volatile("" : "+v"(vx[0]), "+v"(vx[1]), "+v"(vx[2]), "+v"(vx[3]), "+v"(vw));
        SBAR();
        const char* x = (const char*)(br ? xB : xA) + (size_t)mt * 256 * 2048;
        const char* w = (const char*)(br ? wB : wA) + hp * 128;
        const unsigned xdst_o = lds0 + K1_X + wi * 4096;
        const unsigned wdst = lds0 + K1_W + wid * 1024;
        const char* xo = x + (size_t)g * 128 * 2048;
#define ROTM 4
        const int kt0 = ROTM * (j & 3);
#define STAGE_X(kt, dst) do { const char* s_ = xo + (((kt) + kt0) & 15) * 128; glds_run<0>(s_, vx[0], dst); glds_run<1>(s_, vx[1], 0); glds_run<2>(s_, vx[2], 0); glds_run<3>(s_, vx[3], 0); } while (0)
#define STAGE_W(kt, buf) do { const char* s_ = w + (size_t)(((kt) + kt0) & 15) * 64 * 6144; glds_sec<0>(s_, vw, wdst + (buf) * K1_WB); glds_sec<1>(s_, vw, wdst + (buf) * K1_WB); glds_sec<2>(s_, vw, wdst + (buf) * K1_WB); } while (0)
        JIT(14);
        STAGE_W(0, 0); STAGE_X(0, xdst_o + g * 16384); STAGE_W(1, 1); STAGE_X(1, xdst_o + (2 + g) * 16384);
        if (wid >= 5) {
            const char* base = wid == 5 ? (const char*)ws : wid == 6 ? (const char*)out : (const char*)(br ? woB : woA);
            const unsigned span = wid == 5 ? (unsigned)WS_EP : wid == 6 ? (8u << 20) : (2u << 20);
            const unsigned off = (unsigned)lane * TLBW;
            asm volatile("s_mov_b32 m0, %2\n\ts_nop 0\n\tglobal_load_lds_dword %0, %1" :: "v"(off < span ? off : 0u), "s"(base), "s"(lds0 + 159744) : "memory");
        }
        if (wid == 4) glds16_dev(ws, (unsigned)WS_EP, lds0 + K1_EP);
        const int c16 = lane & 15, g4 = lane >> 4;
        unsigned ax[2], aw[2];
        ax[0] = lds0 + K1_X + g * 16384 + (mw * 64 + 16 * (c16 >> 3) + (c16 & 7)) * 128 + ((g4 ^ (((c16 >> 1) & 1) | ((c16 >> 3) << 1) | (((c16 >> 2) & 1) << 2))) << 4);
        ax[1] = ax[0] ^ 64;
        { const int q4 = c16 >> 2, p = lane & 3;
          aw[0] = lds0 + K1_W + (8 * g4 + q4) * 128 + (((hd * 4 + (p >> 1)) ^ ((((q4 >> 1) & 1) | ((g4 & 1) << 1)) << 1)) << 4) + (p & 1) * 8; aw[1] = aw[0] ^ 32; }
        if (g == 1) WAITBAR_(11); else WAITBAR_(7);
        SA_(1);
        if (g == 1) { PRECOMPUTE(true); WAITBAR_(7); }
        f32x4 acc[4][6];
#pragma unroll
        for (int m = 0; m < 4; ++m)
#pragma unroll
            for (int n = 0; n < 6; ++n) acc[m][n] = f32x4{};
        SBAR();
        unsigned wr_ = 0, xr_ = g * 16384, dw = wdst + 2 * K1_WB, dx = xdst_o + (g ? 0 : 4 * 16384);
        int tk = (2 + kt0) & 15;
        const char* sx = xo + tk * 128; const char* sw = w + (size_t)tk * 64 * 6144;
#define RDX(m) do { xf[m][0] = ldsrd128(ax[0] + ((m) >> 1) * 4096 + ((m) & 1) * 1024); xf[m][1] = ldsrd128(ax[1] + ((m) >> 1) * 4096 + ((m) & 1) * 1024); } while (0)
#define RDW(n, ks) wf[n][ks] = cat8(ldstr(aw[(n) & 1] + ((n) >> 1) * 8192 + (ks) * 4096), ldstr(aw[(n) & 1] + ((n) >> 1) * 8192 + (ks) * 4096 + 512))
#define MM(m, n, k) do { if ((n) < 4) acc[m][n] = MFMA16(wf[n][k], xf[m][k], acc[m][n]); else acc[m][n] = MFMA16(xf[m][k], wf[n][k], acc[m][n]); } while (0)
#define P1_MMA() _Pragma("unroll") for (int m = 0; m < 4; ++m) _Pragma("unroll") for (int n = 0; n < 6; ++n) { MM(m, n, 0); MM(m, n, 1); CHAIN(); }
#define P1_TILE(DO, WAIT1, WAIT2) do { \
            bf16x8 xf[4][2], wf[6][2]; \
            RDX(0); RDW(0, 0); if (DO) glds_sec<0>(sw, vw, dw); \
            RDW(0, 1); RDW(1, 0); if (DO) glds_sec<1>(sw, vw, dw); \
            RDX(1); RDW(1, 1); if (DO) glds_sec<2>(sw, vw, dw); \
            RDW(2, 0); RDW(2, 1); if (DO) glds_run<0>(sx, vx[0], dx); \
            RDX(2); RDW(3, 0); if (DO) glds_run<1>(sx, vx[1], 0); \
            RDW(3, 1); RDW(4, 0); if (DO) glds_run<2>(sx, vx[2], 0); \
            RDX(3); RDW(4, 1); if (DO) glds_run<3>(sx, vx[3], 0); \
            RDW(5, 0); RDW(5, 1); \
            SBAR(); \
            {   \
              const unsigned d_ = (wr_ == 2 * K1_WB) ? (unsigned)(-2 * K1_WB) : (unsigned)K1_WB, e_ = (xr_ >= 3 * 16384) ? (unsigned)(-3 * 16384) : (unsigned)(2 * 16384); \
              dw = wdst + wr_; wr_ += d_; aw[0] += d_; aw[1] += d_; dx = xdst_o + ((xr_ == 4 * 16384) ? 0u : xr_ + 16384); xr_ += e_; ax[0] += e_; ax[1] += e_; \
              tk = (tk + 1) & 15; sx = xo + tk * 128; sw = w + (size_t)tk * 64 * 6144; \
              asm volatile("" : "+v"(aw[0]), "+v"(aw[1]), "+v"(ax[0]), "+v"(ax[1]), "+s"(sx), "+s"(sw)); } \
            WAIT1; \
            SBAR(); \
            __builtin_amdgcn_s_setprio(1); \
            P1_MMA(); \
            __builtin_amdgcn_s_setprio(0); \
            SBAR(); \
            WAIT2; \
            SBAR(); \
        } while (0)
#pragma unroll 1
        for (int t = 0; t < 14; ++t) P1_TILE(true, WAITBAR_(11), WAITBAR_(7));
        u32x2 ev;
        P1_TILE(false, ev = *(const LAS u32x2*)(uintptr_t)(lds0 + K1_EP); WAITBAR_(4), WAITBAR_(0));
#define MAKE_TAG() do { u64 e = ((u64)(unsigned)__builtin_amdgcn_readfirstlane(ev[1]) << 32) | (unsigned)__builtin_amdgcn_readfirstlane(ev[0]); \
          e += 0x9E3779B97F4A7C15ull; e = (e ^ (e >> 30)) * 0xBF58476D1CE4E5B9ull; e = (e ^ (e >> 27)) * 0x94D049BB133111EBull; tag = e ^ (e >> 31); asm volatile("" : "+s"(tag)); } while (0)
        P1_TILE(false, MAKE_TAG(); WAITBAR_(0), if (g == 0) asm volatile("s_barrier" ::: "memory"));
#undef MAKE_TAG
#undef P1_TILE
#undef RDW
#undef RDX
#undef STAGE_X
#undef STAGE_W
        SA_(2); SC_(1);
        JIT(1);
        FRESH();
#pragma unroll
        for (int mb = 0; mb < 2; ++mb)
#pragma unroll
            for (int u = 0; u < 2; ++u) {
                const f32x4 a = acc[2 * mb + u][0] * QSCALE, c = acc[2 * mb + u][1] * QSCALE;
                fq[mb][u] = u32x4{cvtpk(a[0], a[1]), cvtpk(a[2], a[3]), cvtpk(c[0], c[1]), cvtpk(c[2], c[3])};
                fk[mb][u] = pack44(acc[2 * mb + u][2], acc[2 * mb + u][3]);
                fv[mb][u] = pack44(acc[2 * mb][4 + u], acc[2 * mb + 1][4 + u]);
            }
    }
    const char* wo = (const char*)(br ? woB : woA) + nt3 * 256;
    const int k30 = 2 * nt3;
    {
        const int g = wid >> 2, wr = wid >> 1, hd = wid & 1;
        const int ph1 = 2 * hp + hd, pb1 = mt * 8 + wr * 2;
        const int head = 2 * hp + br, blk = mt * 8 + wid;
        const char* kv = ws + WS_KV + (size_t)head * 262144;
        unsigned l16 = lane * 16;
        const unsigned dbeg = lds0 + wid * 4096, lbase = lds0 + l16;
        const unsigned fk0 = (unsigned)WS_FK + head * 32 * FS;
        const unsigned fxo = (unsigned)WS_FX + ((hp * 8 + mt) * 8 + wid) * FS, xrec0 = (unsigned)WS_XP + ((hp * 8 + mt) * 8 + wid) * 5120 + 2048;
        const f32x16 z = {};
        f32x16 sa, negm, o;
        u32x16 qol, kvf, pub0, pub1; u32x8 ph;
        const unsigned sidx = (lane & 1) ? 0xEEEEEEEEu : 0x44444444u;

#define FIRST_TILE(base, q0, q1) do { const unsigned on_ = (lane & 2) ? 0u : 0x3F803F80u, e_ = (lane & 4) ? 0u : on_, f_ = (lane & 4) ? on_ : 0u; const u32x4 ones = {e_, e_, f_, f_}; \
            const bf16x8 k0_ = ldsrd128(base), k1_ = ldsrd128((base) + 1024); kvf = cat16(asu(ldsrd128((base) + 4096)), asu(ldsrd128((base) + 5120)), asu(ldsrd128((base) + 2048)), ones); \
            sa = MFMA32(k0_, q0, z); sa = MFMA32(k1_, q1, sa); float m_ = sa[0]; \
            _Pragma("unroll") for (int r = 1; r < 16; ++r) m_ = __builtin_fmaxf(m_, sa[r]); \
            m_ = -swap_max(m_); _Pragma("unroll") for (int r = 0; r < 16; ++r) { negm[r] = m_; sa[r] += m_; } \
            qol = cat16(asu(q0), asu(q1), ones, u32x4{0u, 0u, 0u, 0u}); } while (0)
#define L6_OUT "={v[0:15]}"(o), "+{v[32:47]}"(sa), "+{v[64:79]}"(qol), "+{v[80:95]}"(kvf), "={v[104:111]}"(ph)
#define L6C(HL, BASE) asm volatile(ATT_L6C : L6_OUT, "={v[144:159]}"(h0), "={v[160:167]}"(h1) \
            : "{v[16:31]}"(negm), "{v174}"(sidx), "{v112}"(BASE), "{v[140:143]}"(u32x4{0u, 0u, c_fl, (unsigned)WS_HALO + (mt * 32 + ph1) * 6144 + 3072 + l16}), \
              [ws] "s"(ws), [hl] "s"(HL), [fld] "s"(fld) : ATT_TMP, "scc", "memory")
        const unsigned own = (ph1 * 32 + mt * 4 + wr) * FS;
        const unsigned fld = lds0 + K2_FL + wid * 1024 + br * (2 * K2_SLOT + 4096 - K2_FL + wid * 11264);
        c_pv = (lane >> 5) * 1024 + ((lane & 15) + 32 * ((lane >> 4) & 1)) * 16;
        const unsigned pv = c_pv, pq = pv + (lane & 8) * 16;
        if (br == 0) {
            const char* kp = ws + WS_KV + (size_t)((ph1 * 64 + pb1) * 4) * 1024;
#define PUB_KV() do { st16_dev<0>(kp, pq, fk[0][0]); st16_dev<128>(kp, pq, fk[0][1]); st16_dev<2048>(kp, pv, fv[0][0]); st16_dev<2304>(kp, pv, fv[0][1]); \
            st16_dev<0>(kp + 4096, pq, fk[1][0]); st16_dev<128>(kp + 4096, pq, fk[1][1]); st16_dev<2048>(kp + 4096, pv, fv[1][0]); st16_dev<2304>(kp + 4096, pv, fv[1][1]); } while (0)
            if (hd == 1) {
                const char* qp = ws + WS_QF + (size_t)((ph1 * 64 + pb1) * 2) * 1024;
                st16_dev<0>(qp, pq, fq[0][0]); st16_dev<128>(qp, pq, fq[0][1]); st16_dev<2048>(qp, pq, fq[1][0]); st16_dev<2176>(qp, pq, fq[1][1]);
            }
            JIT(2);
            if (wr == 0) PUB_KV();
#pragma unroll
            for (int mb = 0; mb < 2; ++mb) {
                const unsigned a = lds0 + hd * 2 * K2_SLOT + (wr * 2 + mb) * 4096, aq = lds0 + K2_QX + hd * 16384 + (wr * 2 + mb) * 2048 + pq;
                ldswr128(a + pq, fk[mb][0]); ldswr128(a + pq + 128, fk[mb][1]); ldswr128(a + 2048 + pv, fv[mb][0]); ldswr128(a + 2304 + pv, fv[mb][1]);
                if (hd == 0 || g == 0) { ldswr128(aq, fq[mb][0]); ldswr128(aq + 128, fq[mb][1]); }
            }
            BARRIER_L();
            if (g == 0) PRECOMPUTE(false);
            if (wr == 0 || hd == 1) {
                drain();
                if (lane == 0) { if (wr == 0) flag_set(ws + WS_FK, own, tag); if (hd == 1) flag_set(ws + WS_FQ, own, tag); }
            }
            JIT(3);
            if (wr == 1) PUB_KV();
            const unsigned qa = lbase + K2_QX + (1 - g) * 16384 + wid * 2048, cx = lbase + (1 - g) * 2 * K2_SLOT;
            SA_(3); SC_(2);
            pub0 = cat16(fk[0][0], fk[0][1], fv[0][0], fv[0][1]); pub1 = cat16(fk[1][0], fk[1][1], fv[1][0], fv[1][1]);
            { const bf16x8 q0 = ldsrd128(qa), q1 = ldsrd128(qa + 1024); FIRST_TILE(cx, q0, q1); }
            JIT(4);
            asm volatile(ATT_L4 : L6_OUT : "{v[16:31]}"(negm), "{v174}"(sidx), "{v112}"(cx) : ATT_TMP, "memory");
            SC_(3);
            if (g == 1) {
                const int b1 = (int)(tabf & 63), b0 = RBLK(0, wid - 4);
                flag_wait(ws, fk0 + (((lane & 1) ? b1 : b0) >> 1) * FS, tag);
                glds4k_dev(kv + b1 * 4096, l16, dbeg + K2_SLOT);
                glds4k_dev(kv + b0 * 4096, l16, dbeg + K2_SLOT - 16384);
                glds16_dev(ws, c_fl, fld);
            } else if (wr == 1) { drain(); if (lane == 0) flag_set(ws + WS_FK, own, tag); }
            SC_(4);
            JIT(5);
            asm volatile(ATT_L2 : "+{v[0:15]}"(o), "+{v[32:47]}"(sa), "+{v[64:79]}"(qol), "+{v[80:95]}"(kvf), "+{v[104:111]}"(ph) : "{v[16:31]}"(negm), "{v174}"(sidx), "{v112}"(cx) : ATT_TMP, "memory");
            if (g == 0) {
                asm volatile(ATT_XT : "+{v[0:15]}"(o), "+{v[32:47]}"(sa), "+{v[64:79]}"(qol), "+{v[80:95]}"(kvf), "+{v[104:111]}"(ph) : "{v[16:31]}"(negm), "{v174}"(sidx), "{v112}"(cx) : ATT_TMP, "memory");
                st16_dev<-2048>(ws, xrec0 + l16, quad(o, 0)); st16_dev<-1024>(ws, xrec0 + l16, quad(o, 1)); st16_dev<0>(ws, xrec0 + l16, quad(o, 2)); st16_dev<1024>(ws, xrec0 + l16, quad(o, 3));
                st16_dev<2048>(ws, xrec0 + l16, u32x4{__float_as_uint(__uint_as_float(qol[12]) + __uint_as_float(qol[13])), __float_as_uint(negm[0]), 0u, 0u});
                SC_(5);
                JIT(16);
                const bf16x8 q0 = ldsrd128(lbase + K2_QX + wid * 2048), q1 = ldsrd128(lbase + K2_QX + wid * 2048 + 1024);
                FIRST_TILE(lbase, q0, q1);
                u32x16 h0; u32x8 h1;
                L6C(0, lbase);
            }
#undef PUB_KV
        } else {
            if (wr == 3 && mt < 7) {
                const char* p = ws + WS_HALO + (size_t)(((mt + 1) * 32 + ph1) * 6) * 1024;
                st16_dev<0>(p, pq, fk[0][0]); st16_dev<128>(p, pq, fk[0][1]); st16_dev<3072>(p, pq, fk[1][0]); st16_dev<3200>(p, pq, fk[1][1]);
                if (lane >= 32) { st16_dev<1024>(p, pv, fv[0][0]); st16_dev<1280>(p, pv, fv[0][1]); st16_dev<1024>(p + 3072, pv, fv[1][0]); st16_dev<1280>(p + 3072, pv, fv[1][1]); }
            }
            JIT(15);
            const unsigned ex = lds0 + 2 * K2_SLOT + wid * 12288;
#pragma unroll
            for (int mb = 0; mb < 2; ++mb) {
                const unsigned a = ex + mb * 6144;
                ldswr128(a + pq, fq[mb][0]); ldswr128(a + pq + 128, fq[mb][1]); ldswr128(a + 2048 + pq, fk[mb][0]); ldswr128(a + 2176 + pq, fk[mb][1]);
                ldswr128(a + 4096 + pv, fv[mb][0]); ldswr128(a + 4352 + pv, fv[mb][1]);
            }
            if (g == 0) PRECOMPUTE(true);
            BARRIER_L();
            if (wr == 3 && mt < 7) { drain(); if (lane == 0) flag_set(ws + WS_FH, (ph1 * 8 + mt + 1) * FS, tag); }
            SC_(2);
            LocalPart lp[2];
            bf16x8 bq[2][2];
#pragma unroll
            for (int mb = 0; mb < 2; ++mb) { bq[mb][0] = ldsrd128(ex + mb * 6144 + l16); bq[mb][1] = ldsrd128(ex + mb * 6144 + 1024 + l16); }
#define LOCAL_BLOCK(mb) do { const unsigned a_ = ex + mb * 6144 + l16; \
                const bf16x8 k0_ = ldsrd128(a_ + 2048), k1_ = ldsrd128(a_ + 3072), v0_ = ldsrd128(a_ + 4096), v1_ = ldsrd128(a_ + 5120); \
                if (wr > 0) local_own<true>(bq[mb][0], bq[mb][1], k0_, k1_, v0_, v1_, ldsrd128(a_ - 24576 + 2048), ldsrd128(a_ - 24576 + 3072), ldsrd128(a_ - 24576 + 5120), band, lane, lp[mb]); \
                else local_own<false>(bq[mb][0], bq[mb][1], k0_, k1_, v0_, v1_, k0_, k0_, k0_, band, lane, lp[mb]); \
                if (wr > 0 || mt == 0) local_fin(lp[mb]); } while (0)
            JIT(6);
            LOCAL_BLOCK(0);
            SC_(3);
            const int hl = (wid < 2) ? mt : 0;
            const unsigned fh = (unsigned)WS_FH + (ph1 * 8 + mt) * FS;
            const int b0 = (int)(tabf & 63), b1 = (int)((tabf >> 6) & 63);
            const int q = lane & 3;
            unsigned fa0 = fk0 - 1024 * FS + (blk >> 1) * FS, fa1 = fk0 + (b0 >> 1) * FS, fa2 = fk0 + (b1 >> 1) * FS, fa3 = fh;
            asm volatile("" : "+s"(fa0), "+s"(fa1), "+s"(fa2), "+s"(fa3));
            const unsigned lo_ = (q & 1) ? fa1 : fa0, hi_ = (q & 1) ? fa3 : fa2;
            const u64 fm = flag_wait_some(ws, (q & 2) ? hi_ : lo_, tag, 3);
            SC_(4);
            glds4k_dev(kv + b0 * 4096, l16, dbeg);
            const char* qp = ws + WS_QF + (size_t)((head * 64 + blk) * 2) * 1024;
            glds16_dev(qp, l16, ex); glds16_dev(qp + 1024, l16, ex + 1024);
            if (fm & 4) glds4k_dev(kv + b1 * 4096, l16, dbeg + K2_SLOT);
            JIT(7);
            LOCAL_BLOCK(1);
#undef LOCAL_BLOCK
            if (fm & 4) WAITBAR_(4);
            else {
                WAITBAR_(0);
                flag_wait(ws, fk0 + (b1 >> 1) * FS, tag);
                glds4k_dev(kv + b1 * 4096, l16, dbeg + K2_SLOT);
            }
            JIT(8);
            const bf16x8 q0 = ldsrd128(ex + l16), q1 = ldsrd128(ex + 1024 + l16);
            SA_(3); SC_(5);
            if (hl && !(fm & 8)) flag_wait(ws, fh, tag);
            FIRST_TILE(lbase, q0, q1);
            u32x16 h0; u32x8 h1;
            JIT(9);
            L6C(hl, lbase);
            if (hl) {
                local_prev(bq[0][0], bq[0][1], asbf(u32x4{h0[0], h0[1], h0[2], h0[3]}), asbf(u32x4{h0[4], h0[5], h0[6], h0[7]}), asbf(u32x4{h0[8], h0[9], h0[10], h0[11]}), lane, lp[0]);
                local_prev(bq[1][0], bq[1][1], asbf(u32x4{h0[12], h0[13], h0[14], h0[15]}), asbf(u32x4{h1[0], h1[1], h1[2], h1[3]}), asbf(u32x4{h1[4], h1[5], h1[6], h1[7]}), lane, lp[1]);
                local_fin(lp[0]); local_fin(lp[1]);
            }
            const char* op = ws + WS_OF + (size_t)(((64 + pb1) * 32 + ph1) * 2) * 1024;
            st16_dev<0>(op, l16, quad(lp[0].o, 0)); st16_dev<1024>(op, l16, quad(lp[0].o, 1)); st16_dev<0>(op + 65536, l16, quad(lp[1].o, 0)); st16_dev<1024>(op + 65536, l16, quad(lp[1].o, 1));
        }
#undef FIRST_TILE
#undef L6_OUT
#undef L6C
        FRESH();
        l16 = lane * 16;
        SA_(4); SC_(6);
        const int isx = br & (1 - g);
        unsigned cnt = 7, dst = dbeg + 2 * K2_SLOT, nx = K2_SLOT;
        unsigned mode = isx << 1;
        const unsigned late = (1 - br) & g;
        const unsigned act = 2u | (isx * 12u) | ((br | (1 - g)) << 5) | (late << 6), hk = 32u | (late << 7);
        const u64 tab = tabf >> (6 + 6 * br);
        u32x8 adr = {lds0 + l16, lds0 + l16 + K2_SLOT, l16, lds0 + l16, c_wb + wid * 16384, (c_wb ^ 128) + wid * 16384 + 7168,
                     br ? (unsigned)WS_FO + ((64 + pb1 + (lane & 1)) * 32 + ph1) * FS : fxo, c_tw};
        const unsigned kvrec = (unsigned)WS_KV + (ph1 * 64 + pb1) * 4096 + 3072, pv2 = c_pv;
        f32x16 xo; f32x2 xl;
#define DBGOUT
        JIT(10);
        asm volatile(ATT_MAIN
                     : DBGOUT"+{v[0:15]}"(o), "+{v[32:47]}"(sa), "+{v[64:79]}"(qol), "+{v[80:95]}"(kvf), "+{v[104:111]}"(ph), "+{v[112:119]}"(adr), "={v[122:137]}"(xo), "={v[138:139]}"(xl),
                       [cnt] "+s"(cnt), [dst] "+s"(dst), [nx] "+s"(nx), [mode] "+s"(mode)
                     : "{v[16:31]}"(negm), "{v[174:175]}"(u32x2{sidx, (unsigned)WS_FK + own}), "{v[140:143]}"(u32x4{(unsigned)tag, (unsigned)(tag >> 32), fxo, xrec0 + l16}),
                       "{v[170:173]}"(u32x4{c_fl, kvrec + pv2 + (lane & 8) * 16, kvrec + pv2, fld + l16}), "{v[176:191]}"(pub0), "{v[192:207]}"(pub1),
                       [srclo] "s"((unsigned)(uintptr_t)kv), [srchi] "s"((unsigned)((uintptr_t)kv >> 32)), [dbeg] "s"(dbeg), [dend] "s"(dbeg + 3 * K2_SLOT),
                       [wo01] "s"(wo + (size_t)k30 * 131072), [wo23] "s"(wo + (size_t)((k30 + 2) & 15) * 131072), [wo] "s"(wo), [wdst] "s"(__builtin_amdgcn_readfirstlane(lds0 + 98304 + wid * 2048)), [act] "s"(act), [hk] "s"(hk), [tab] "s"(tab), [k0] "s"(br), [ws] "s"(ws)
                     : ATT_TMP, "v120", "v121", "v168", "v169", "s40", "s41", "s42", "s43", "s44", "s45", "s46", "s47", "s48", "s49", "s50", "s51", "vcc", "scc", "memory");
        FRESH();
        l16 = lane * 16;
        const unsigned xrec = xrec0 + l16;
        const bf16x8 q0 = asbf(u32x4{qol[0], qol[1], qol[2], qol[3]}), q1 = asbf(u32x4{qol[4], qol[5], qol[6], qol[7]});
        const unsigned one2 = (((lane >> 4) & 1) == ((lane >> 2) & 1)) ? 0x3F803F80u : 0u;
        const u32x4 ones = {one2, one2, one2, one2};
        SA_(5); SB_(1);
        JIT(11);
        float lsum = __uint_as_float(qol[12]) + __uint_as_float(qol[13]);
        bool ok = lsum < 1.0e18f;
        if (isx) {
            if (__builtin_expect(!(mode & 4), 0)) {
                flag_wait(ws, fxo, tag);
                f32x4 a0, a1, a2, a3;
                asm volatile("global_load_dwordx4 %0, %5, %6 offset:-2048 sc1\n\tglobal_load_dwordx4 %1, %5, %6 offset:-1024 sc1\n\tglobal_load_dwordx4 %2, %5, %6 sc1\n\t"
                             "global_load_dwordx4 %3, %5, %6 offset:1024 sc1\n\tglobal_load_dwordx2 %4, %5, %6 offset:2048 sc1\n\ts_waitcnt vmcnt(0)"
                             : "=&v"(a0), "=&v"(a1), "=&v"(a2), "=&v"(a3), "=&v"(xl) : "v"(xrec), "s"(ws) : "memory");
#pragma unroll
                for (int r = 0; r < 4; ++r) { xo[r] = a0[r]; xo[4 + r] = a1[r]; xo[8 + r] = a2[r]; xo[12 + r] = a3[r]; }
            }
            const float nm = __builtin_fminf(negm[0], xl[1]), fa = EXP2(nm - negm[0]), fx = EXP2(nm - xl[1]);
            ok = ok && (xl[0] < 1.0e18f);
            lsum = lsum * fa + xl[0] * fx;
#pragma unroll
            for (int r = 0; r < 16; ++r) o[r] = o[r] * fa + xo[r] * fx;
        }
#pragma unroll
        for (int r = 0; r < 16; ++r) ok = ok && (__builtin_fabsf(o[r]) < 3.0e38f);
        if (__builtin_expect(__any(!ok), 0)) {
            float m = -__builtin_inff();
            f32x4 l;
            for (int pass = 0; pass < 2; ++pass) {
                o = z; l = f32x4{};
                for (int t = 0; t < 64; ++t) {
                    bf16x8 f0, f1, f2, f3;
                    asm volatile("global_load_dwordx4 %0, %4, %5 sc1\n\tglobal_load_dwordx4 %1, %4, %5 offset:1024 sc1\n\tglobal_load_dwordx4 %2, %4, %5 offset:2048 sc1\n\t"
                                 "global_load_dwordx4 %3, %4, %5 offset:3072 sc1\n\ts_waitcnt vmcnt(0)" : "=&v"(f0), "=&v"(f1), "=&v"(f2), "=&v"(f3) : "v"(l16 + t * 4096), "s"(kv) : "memory");
                    f32x16 s = MFMA32(f0, q0, z); s = MFMA32(f1, q1, s);
                    if (pass == 0) {
#pragma unroll
                        for (int r = 0; r < 16; ++r) m = __builtin_fmaxf(m, s[r]);
                    } else {
#pragma unroll
                        for (int r = 0; r < 16; ++r) s[r] = EXP2(s[r] - m);
                        const bf16x8 p0 = asbf(pack8(s, 0)), p1 = asbf(pack8(s, 1));
                        o = MFMA32(f2, p0, o); o = MFMA32(f3, p1, o);
                        l = MFMA16(asbf(ones), p0, l); l = MFMA16(asbf(ones), p1, l);
                    }
                }
                if (pass == 0) m = swap_max(m);
            }
            lsum = l[0];
        }
        const float inv = __builtin_amdgcn_rcpf(lsum);
#pragma unroll
        for (int r = 0; r < 16; ++r) o[r] *= inv;
        const char* op = ws + WS_OF + (size_t)((blk * 32 + head) * 2) * 1024;
        st16_dev<0>(op, l16, pack8(o, 0)); st16_dev<1024>(op, l16, pack8(o, 1));
    }
    {
        FRESH();
        const int g = wid >> 2, wi = wid & 3, wm = wi >> 1, wn = wi & 1, g4 = lane >> 4;
        const char* of = ws + WS_OF + (size_t)(br * 64 + mt3 * 4 + wi) * 65536;
        const unsigned vw4[4] = {c_wb + wi * 32768, (c_wb ^ 128) + wi * 32768 + 7168, c_wb + wi * 32768 + 14336, (c_wb ^ 128) + wi * 32768 + 21504};
        const unsigned l16 = lane * 16, sdst = lds0 + wi * 4096;
#define STAGE_O(t) glds4k_dev(of + (((t) + k30) & 15) * 4096, l16, sdst + ((t) & 3) * 16384)
#define STAGE_WO(t, slot) do { const char* s_ = wo + (size_t)(((t) + k30) & 15) * 131072; glds_run<0>(s_, vw4[0], sdst + (slot)); glds_run<1>(s_, vw4[1], 0); glds_run<2>(s_, vw4[2], 0); glds_run<3>(s_, vw4[3], 0); } while (0)
        const unsigned fo = c_fo;
        drain();
        if (lane == 0) flag_set(ws + WS_FO, ((mt * 8 + wid) * 32 + 2 * hp + br) * FS, tag);
        if (b == 0 && wid == 0) {
            const unsigned fa = (unsigned)WS_FK + ((2 * (lane >> 3)) * 32 + (lane & 7) * 4) * FS, fb = (unsigned)WS_FO + ((64 + (lane & 7) * 8) * 32 + 2 * (lane >> 3)) * FS;
            const u64 t0 = __builtin_amdgcn_s_memrealtime();
            for (;;) {
                u64 v0, v1, v2, v3;
                asm volatile("global_load_dwordx2 %0, %4, %8 sc1\n\tglobal_load_dwordx2 %1, %5, %8 sc1\n\tglobal_load_dwordx2 %2, %6, %8 sc1\n\tglobal_load_dwordx2 %3, %7, %8 sc1\n\ts_waitcnt vmcnt(0)"
                             : "=&v"(v0), "=&v"(v1), "=&v"(v2), "=&v"(v3) : "v"(fa), "v"(fa + 16 * 32 * FS), "v"(fb), "v"(fb + 16 * FS), "s"(ws0) : "memory");
                if (__all(v0 == tag && v1 == tag && v2 == tag && v3 == tag) || __builtin_amdgcn_s_memrealtime() - t0 > 2000000ull) break;
                __builtin_amdgcn_s_sleep(SLP);
            }
            if (lane == 0) flag_set(ws0, (unsigned)WS_EP, tag);
        }
        SB_(2);
        asm volatile("s_barrier" ::: "memory");
        SB_(3);
        JIT(12);
        const u64 fm = flag_wait_some(ws, fo, tag, (3ull << (2 * nt3)) | (g ? 3ull << (2 * ((nt3 + 1) & 7)) : 0ull));
        STAGE_O(1 - g);
        if (g == 1) STAGE_O(2);
        STAGE_WO(5 - g, 65536 + (1 - g) * 16384);
        if (fm != ~0ull) flag_wait(ws, fo, tag);
        SB_(4);
        SA_(6);
        unsigned ao = lds0 + g * 16384 + wm * 8192 + c_pv;
        unsigned aw[4] = {c_aw, c_aw ^ 32, c_aw ^ 64, c_aw ^ 96};
        const char* so = of + ((3 + g + k30) & 15) * 4096; const char* sw = wo + (size_t)((5 + g + k30) & 15) * 131072;
        unsigned dob = sdst + ((3 + g) & 3) * 16384, dwb = sdst + p3_wslot(6);
        f32x4 acc[4][4];
#pragma unroll
        for (int mb = 0; mb < 4; ++mb)
#pragma unroll
            for (int nb = 0; nb < 4; ++nb) acc[mb][nb] = f32x4{};
        if (g == 1) { WAITBAR_(8); asm volatile("s_barrier" ::: "memory"); } else asm volatile("s_barrier" ::: "memory");
        SB_(5);
        SBAR();
#pragma unroll
        for (int u = 0; u < 8; ++u) {
            bf16x8 of_[4][2], wf[4][2];
            const bool doo = u < 6 || (u == 6 && g == 0), dow = (u >= 1 || g == 1) && (u < 5 || (u == 5 && g == 0));
#define P3RD(i) do { of_[(i) & 3][(i) >> 2] = ldsrd128(ao + (((i) & 3) >> 1) * 4096 + ((i) & 1) * 256 + ((i) >> 2) * 2048); \
                wf[(i) & 3][(i) >> 2] = cat8(ldstr(aw[(i) & 3] + ((i) >> 2) * 8192), ldstr(aw[(i) & 3] + ((i) >> 2) * 8192 + 2048)); } while (0)
            P3RD(0); if (u < 7 && doo) glds_run_dev<0>(so, l16, dob);
            P3RD(1); if (u < 7 && doo) glds_run_dev<1>(so, l16, 0);
            P3RD(2); if (u < 7 && doo) glds_run_dev<2>(so, l16, 0);
            P3RD(3); if (u < 7 && doo) glds_run_dev<3>(so, l16, 0);
            P3RD(4); if (u < 6 && dow) glds_run<0>(sw, vw4[0], dwb);
            P3RD(5); if (u < 6 && dow) glds_run<1>(sw, vw4[1], 0);
            P3RD(6); if (u < 6 && dow) glds_run<2>(sw, vw4[2], 0);
            P3RD(7); if (u < 6 && dow) glds_run<3>(sw, vw4[3], 0);
#undef P3RD
            SBAR();
            if (u < 7) {
                { const int to = 2 * u + 5 + g; so = of + ((to + k30) & 15) * 4096; sw = wo + (size_t)((to + 2 + k30) & 15) * 131072;
                  dob = sdst + (to & 3) * 16384; dwb = sdst + (g ? p3_wslot(2 * u + 8) : p3_wslot(2 * u + 7)); }
                ao ^= 32768;
#pragma unroll
                for (int q = 0; q < 4; ++q) aw[q] += p3_wslot(2 * u + 2) - p3_wslot(2 * u);
                asm volatile("" : "+v"(ao), "+v"(aw[0]), "+v"(aw[1]), "+v"(aw[2]), "+v"(aw[3]), "+s"(so), "+s"(sw), "+s"(dob), "+s"(dwb));
            }
            if (p3_allow(2 * u) == p3_allow(2 * u + 1)) WAITBAR(p3_allow(2 * u)); else if (g == 0) WAITBAR(p3_allow(2 * u)); else WAITBAR(p3_allow(2 * u + 1));
            SBAR();
            __builtin_amdgcn_s_setprio(1);
#pragma unroll
            for (int mb = 0; mb < 4; ++mb)
#pragma unroll
                for (int nb = 0; nb < 4; ++nb) { acc[mb][nb] = MFMA16(wf[nb][0], of_[mb][0], acc[mb][nb]); acc[mb][nb] = MFMA16(wf[nb][1], of_[mb][1], acc[mb][nb]); CHAIN();
                }
            __builtin_amdgcn_s_setprio(0);
            SBAR();
            if (u < 7 || g == 0) asm volatile("s_barrier" ::: "memory");
            SBAR();
        }
        SB_(6);
        FRESH();
        JIT(13);
        const unsigned xw = lds0 + wid * 8192 + l16, xr = lds0 + (wid ^ 4) * 8192 + l16;
#pragma unroll
        for (int i = 0; i < 8; ++i) { const f32x4 v = g ? acc[i >> 2][i & 3] : acc[2 + (i >> 2)][i & 3]; ldswr128(xw + i * 1024, __builtin_bit_cast(u32x4, v)); }
        BARRIER_L();
        const unsigned st = lds0 + 65536 + wid * 4096;
#pragma unroll
        for (int i = 0; i < 8; ++i) {
            const f32x4 v = (g ? acc[2 + (i >> 2)][i & 3] : acc[i >> 2][i & 3]) + __builtin_bit_cast(f32x4, ldsrd128(xr + i * 1024));
            const int row = 16 * (i >> 2) + (lane & 15), c = 2 * (i & 3) + (g4 >> 1);
            *(LAS u32x2*)(uintptr_t)(st + row * 128 + ((c ^ (row & 7)) << 4) + (g4 & 1) * 8) = u32x2{cvtpk(v[0], v[1]), cvtpk(v[2], v[3])};
        }
        const int blk = mt3 * 4 + 2 * wm + g;
        char* op = (char*)out + br * 2048 + nt3 * 256 + wn * 128 + (lane & 7) * 16;
#pragma unroll
        for (int i = 0; i < 4; ++i) {
            const int r = 8 * i + (lane >> 3), row = br ? ((blk >> 1) * 64 + 2 * r + (blk & 1)) : (blk * 32 + r);
            st16_dev<0>(out, (unsigned)(row * 4096 + br * 2048 + nt3 * 256 + wn * 128 + (lane & 7) * 16), __builtin_bit_cast(u32x4, ldsrd128(st + r * 128 + (((lane & 7) ^ (r & 7)) << 4))));
        }
#undef STAGE_O
#undef STAGE_WO
    }
    STAMP_AT(0, 7);
    asm volatile(".Lmk_code_end:");
}

extern "C" void kernel_launch(void* const* d_in, const int* in_sizes, int n_in, void* d_out, int out_size, void* d_ws, size_t ws_size, hipStream_t stream) {
    mk_fused<<<256, 512, 0, stream>>>((const u16*)d_in[0], (const u16*)d_in[1], (const u16*)d_in[2], (const u16*)d_in[3], (const u16*)d_in[4], (const u16*)d_in[5], (char*)d_ws, (u16*)d_out);
}
```

```cpp
#include <hip/hip_runtime.h>
#include <stdint.h>

typedef unsigned short u16;
typedef unsigned long long u64;
typedef __bf16 bf16x8 __attribute__((ext_vector_type(8)));
typedef __bf16 bf16x2 __attribute__((ext_vector_type(2)));
typedef short s16x4 __attribute__((ext_vector_type(4)));
typedef float f32x16 __attribute__((ext_vector_type(16)));
typedef float f32x4 __attribute__((ext_vector_type(4)));
typedef float f32x2 __attribute__((ext_vector_type(2)));
typedef unsigned u32x4 __attribute__((ext_vector_type(4)));
typedef unsigned u32x2 __attribute__((ext_vector_type(2)));
typedef unsigned u32x8 __attribute__((ext_vector_type(8)));
typedef unsigned u32x16 __attribute__((ext_vector_type(16)));
#define LAS __attribute__((address_space(3)))
#define DI __device__ __forceinline__
#define SBAR() __builtin_amdgcn_sched_barrier(0)
#define MFMA32(a, b, c) __builtin_amdgcn_mfma_f32_32x32x16_bf16((a), (b), (c), 0, 0, 0)
#define MFMA16(a, b, c) __builtin_amdgcn_mfma_f32_16x16x32_bf16((a), (b), (c), 0, 0, 0)
#define CHAIN() SBAR()
#define EXP2(x) __builtin_amdgcn_exp2f(x)

constexpr float QSCALE = 0.17677669529663687f * 1.4426950408889634f;

constexpr size_t WS_QF = 0;
constexpr size_t WS_KV = WS_QF + (4u << 20);
constexpr size_t WS_OF = WS_KV + (8u << 20);
constexpr size_t WS_HALO = WS_OF + (8u << 20);
constexpr size_t WS_XP = WS_HALO + (2u << 20);
constexpr size_t WS_FX = WS_XP + (5u << 20);
#define FS 64
constexpr size_t WS_FQ = WS_FX + 1024 * FS;
constexpr size_t WS_FK = WS_FQ + 1024 * FS;
constexpr size_t WS_FO = WS_FK + 1024 * FS;
constexpr size_t WS_FH = WS_FO + 4096 * FS;
constexpr size_t WS_EP = WS_FH + 256 * FS;

#define SLP 1

DI unsigned cvtpk(float lo, float hi) { f32x2 v = {lo, hi}; bf16x2 b = __builtin_convertvector(v, bf16x2); return __builtin_bit_cast(unsigned, b); }
DI u32x4 pack8(const f32x16& a, int s) { u32x4 r; r[0] = cvtpk(a[8 * s], a[8 * s + 1]); r[1] = cvtpk(a[8 * s + 2], a[8 * s + 3]); r[2] = cvtpk(a[8 * s + 4], a[8 * s + 5]); r[3] = cvtpk(a[8 * s + 6], a[8 * s + 7]); return r; }
DI u32x4 pack44(f32x4 a, f32x4 c) { return u32x4{cvtpk(a[0], a[1]), cvtpk(a[2], a[3]), cvtpk(c[0], c[1]), cvtpk(c[2], c[3])}; }
DI u32x4 asu(bf16x8 v) { return __builtin_bit_cast(u32x4, v); }
DI u32x16 cat16(u32x4 a, u32x4 b, u32x4 c, u32x4 d) { return u32x16{a[0], a[1], a[2], a[3], b[0], b[1], b[2], b[3], c[0], c[1], c[2], c[3], d[0], d[1], d[2], d[3]}; }
DI bf16x8 asbf(u32x4 v) { return __builtin_bit_cast(bf16x8, v); }
DI void glds16(const void* sbase, unsigned voff, unsigned lds) {
    asm volatile("s_mov_b32 m0, %2\n\ts_nop 0\n\tglobal_load_lds_dwordx4 %0, %1" :: "v"(voff), "s"(sbase), "s"(lds) : "memory");
}
template <int I> DI void glds_run(const void* sbase, unsigned voff, unsigned lds) {
    if (I == 0) asm volatile("s_mov_b32 m0, %2\n\ts_nop 0\n\tglobal_load_lds_dwordx4 %0, %1" :: "v"(voff), "s"(sbase), "s"(lds) : "memory");
    else asm volatile("global_load_lds_dwordx4 %0, %1 offset:%2" :: "v"(voff), "s"(sbase), "i"(I * 1024) : "memory");
}
template <int I> DI void glds_run_dev(const void* sbase, unsigned voff, unsigned lds) {
    if (I == 0) asm volatile("s_mov_b32 m0, %2\n\ts_nop 0\n\tglobal_load_lds_dwordx4 %0, %1 sc1" :: "v"(voff), "s"(sbase), "s"(lds) : "memory");
    else asm volatile("global_load_lds_dwordx4 %0, %1 offset:%2 sc1" :: "v"(voff), "s"(sbase), "i"(I * 1024) : "memory");
}
template <int I> DI void glds_sec(const void* sbase, unsigned voff, unsigned lds) {
    if (I < 2) asm volatile("s_add_u32 m0, %2, %4\n\ts_nop 0\n\tglobal_load_lds_dwordx4 %0, %1 offset:%3" :: "v"(voff), "s"(sbase), "s"(lds), "i"(I * 2048), "i"(I * 6144) : "memory", "scc");
    else asm volatile("s_add_u32 m0, %2, %4\n\ts_nop 0\n\tglobal_load_lds_dwordx4 %0, %1 offset:%3" :: "v"(voff), "s"((const char*)sbase + 2048), "s"(lds), "i"(2048), "i"(14336) : "memory", "scc");
}
DI void glds16_dev(const void* sbase, unsigned voff, unsigned lds) {
    asm volatile("s_mov_b32 m0, %2\n\ts_nop 0\n\tglobal_load_lds_dwordx4 %0, %1 sc1" :: "v"(voff), "s"(sbase), "s"(lds) : "memory");
}
DI void glds4k_dev(const void* sbase, unsigned voff, unsigned lds) {
    asm volatile("s_mov_b32 m0, %2\n\ts_nop 0\n\tglobal_load_lds_dwordx4 %0, %1 sc1\n\tglobal_load_lds_dwordx4 %0, %1 offset:1024 sc1\n\t"
                 "global_load_lds_dwordx4 %0, %1 offset:2048 sc1\n\tglobal_load_lds_dwordx4 %0, %1 offset:3072 sc1" :: "v"(voff), "s"(sbase), "s"(lds) : "memory");
}
template <int OFF> DI void st16_dev(const void* sbase, unsigned voff, u32x4 v) {
    asm volatile("global_store_dwordx4 %0, %1, %2 offset:%3 sc1\n\ts_nop 1" :: "v"(voff), "v"(v), "s"(sbase), "i"(OFF) : "memory");
}
DI void drain() { asm volatile("s_waitcnt vmcnt(0)" ::: "memory"); }
DI void flag_set(const void* sbase, unsigned voff, u64 tag) {
    asm volatile("global_store_dwordx2 %0, %1, %2 sc1" :: "v"(voff), "v"(tag), "s"(sbase) : "memory");
}
DI void flag_wait(const void* sbase, unsigned voff, u64 tag) {
    const u64 t0 = __builtin_amdgcn_s_memrealtime();
    for (;;) {
        u64 v;
        asm volatile("global_load_dwordx2 %0, %1, %2 sc1\n\ts_waitcnt vmcnt(0)" : "=v"(v) : "v"(voff), "s"(sbase) : "memory");
        if (__all(v == tag)) break;
        if (__builtin_amdgcn_s_memrealtime() - t0 > 2000000ull) break;
        __builtin_amdgcn_s_sleep(SLP);
    }
}
DI u64 flag_wait_some(const void* sbase, unsigned voff, u64 tag, u64 need) {
    const u64 t0 = __builtin_amdgcn_s_memrealtime();
    for (;;) {
        u64 v;
        asm volatile("global_load_dwordx2 %0, %1, %2 sc1\n\ts_waitcnt vmcnt(0)" : "=v"(v) : "v"(voff), "s"(sbase) : "memory");
        const u64 m = __ballot(v == tag);
        if ((m & need) == need || __builtin_amdgcn_s_memrealtime() - t0 > 2000000ull) return m;
        __builtin_amdgcn_s_sleep(SLP);
    }
}
DI bf16x8 ldsrd128(unsigned addr) { return *(const LAS bf16x8*)(uintptr_t)addr; }
DI void ldswr128(unsigned addr, u32x4 v) { *(LAS u32x4*)(uintptr_t)addr = v; }
DI s16x4 ldstr(unsigned addr) { return __builtin_amdgcn_ds_read_tr16_b64_v4i16((LAS s16x4*)(uintptr_t)addr); }
DI bf16x8 cat8(s16x4 lo, s16x4 hi) { typedef short s16x8 __attribute__((ext_vector_type(8))); s16x8 v = {lo[0], lo[1], lo[2], lo[3], hi[0], hi[1], hi[2], hi[3]}; return __builtin_bit_cast(bf16x8, v); }
DI float swap_max(float m) { auto r = __builtin_amdgcn_permlane32_swap(__float_as_uint(m), __float_as_uint(m), false, false); return __builtin_fmaxf(__uint_as_float(r[0]), __uint_as_float(r[1])); }
DI float swap_add(float m) { auto r = __builtin_amdgcn_permlane32_swap(__float_as_uint(m), __float_as_uint(m), false, false); return __uint_as_float(r[0]) + __uint_as_float(r[1]); }
#define FRESH() asm volatile("" : "+v"(lane), "+s"(ws))
#define BARRIER_L() asm volatile("s_waitcnt lgkmcnt(0)\n\ts_barrier" ::: "memory")
#define WAITBAR_(n) asm volatile("s_waitcnt vmcnt(%0) lgkmcnt(0)\n\ts_barrier" :: "i"(n) : "memory")
#define WAITBAR(n) do { switch (n) { case 0: WAITBAR_(0); break; case 2: WAITBAR_(2); break; case 3: WAITBAR_(3); break; case 4: WAITBAR_(4); break; case 6: WAITBAR_(6); break; \
    case 7: WAITBAR_(7); break; case 8: WAITBAR_(8); break; case 10: WAITBAR_(10); break; case 12: WAITBAR_(12); break; default: WAITBAR_(0); } } while (0)

struct LocalPart { f32x16 o; float m, l; };
DI f32x16 band_mask(int lane) {
    const int i = lane & 31, h = lane >> 5;
    f32x16 c;
#pragma unroll
    for (int r = 0; r < 16; ++r) { const int j = (r & 3) + 8 * (r >> 2) + 4 * h; c[r] = ((unsigned)(i - j) <= 7u) ? 0.f : -__builtin_inff(); }
    return c;
}
template <bool PREV> DI void local_own(bf16x8 q0, bf16x8 q1, bf16x8 k0, bf16x8 k1, bf16x8 v0, bf16x8 v1, bf16x8 kp0, bf16x8 kp1, bf16x8 vp1, const f32x16& band, int lane, LocalPart& P) {
    const f32x16 z = {};
    f32x16 so = MFMA32(k0, q0, band), sp = z;
    if (PREV) sp = MFMA32(kp0, q0, z);
    so = MFMA32(k1, q1, so);
    if (PREV) sp = MFMA32(kp1, q1, sp);
    float m = __builtin_fmaxf(so[0], so[1]);
#pragma unroll
    for (int r = 2; r < 16; r += 2) m = __builtin_fmaxf(__builtin_fmaxf(m, so[r]), so[r + 1]);
    if (PREV) {
        const int d = 4 * (lane >> 5) - (lane & 31);
#pragma unroll
        for (int r = 12; r < 16; ++r) { sp[r] = (d + (r & 3) >= 1) ? sp[r] : -__builtin_inff(); m = __builtin_fmaxf(m, sp[r]); }
    }
    m = swap_max(m);
    float l = 0.f;
#pragma unroll
    for (int r = 0; r < 16; ++r) { so[r] = EXP2(so[r] - m); l += so[r]; }
    P.o = MFMA32(v0, asbf(pack8(so, 0)), z); P.o = MFMA32(v1, asbf(pack8(so, 1)), P.o);
    if (PREV) {
#pragma unroll
        for (int r = 12; r < 16; ++r) { sp[r] = EXP2(sp[r] - m); l += sp[r]; }
        P.o = MFMA32(vp1, asbf(u32x4{0u, 0u, cvtpk(sp[12], sp[13]), cvtpk(sp[14], sp[15])}), P.o);
    }
    P.m = m; P.l = l;
}
DI void local_prev(bf16x8 q0, bf16x8 q1, bf16x8 kp0, bf16x8 kp1, bf16x8 vp1, int lane, LocalPart& P) {
    const int i = lane & 31, h = lane >> 5;
    const f32x16 z = {};
    f32x16 sp = MFMA32(kp0, q0, z); sp = MFMA32(kp1, q1, sp);
    const float NEG = -__builtin_inff();
    float m = P.m;
#pragma unroll
    for (int r = 12; r < 16; ++r) { const int j = (r & 3) + 24 + 4 * h; sp[r] = (j >= i + 25) ? sp[r] : NEG; m = __builtin_fmaxf(m, sp[r]); }
    m = swap_max(m);
    const float f = EXP2(P.m - m);
    float l = P.l * f;
#pragma unroll
    for (int r = 12; r < 16; ++r) { sp[r] = EXP2(sp[r] - m); l += sp[r]; }
#pragma unroll
    for (int r = 0; r < 16; ++r) P.o[r] *= f;
    P.o = MFMA32(vp1, asbf(u32x4{0u, 0u, cvtpk(sp[12], sp[13]), cvtpk(sp[14], sp[15])}), P.o);
    P.m = m; P.l = l;
}
DI void local_fin(LocalPart& P) {
    const float inv = __builtin_amdgcn_rcpf(swap_add(P.l));
#pragma unroll
    for (int r = 0; r < 16; ++r) P.o[r] *= inv;
    const u32x4 o0 = pack8(P.o, 0), o1 = pack8(P.o, 1);
#pragma unroll
    for (int r = 0; r < 4; ++r) { P.o[r] = __uint_as_float(o0[r]); P.o[4 + r] = __uint_as_float(o1[r]); }
}
DI u32x4 quad(const f32x16& a, int i) { return __builtin_bit_cast(u32x4, f32x4{a[4 * i], a[4 * i + 1], a[4 * i + 2], a[4 * i + 3]}); }

#define ATT_L6C \
    "s_nop 1\n\t" \
    "s_cmp_eq_u32 %[hl], 0\n\t" \
    "s_cbranch_scc1 10f\n\t" \
    "global_load_dwordx4 v[144:147], v143, %[ws] offset:-3072 sc1\n\t" \
    "global_load_dwordx4 v[148:151], v143, %[ws] offset:-2048 sc1\n\t" \
    "global_load_dwordx4 v[152:155], v143, %[ws] offset:-1024 sc1\n\t" \
    "global_load_dwordx4 v[156:159], v143, %[ws] offset:0 sc1\n\t" \
    "global_load_dwordx4 v[160:163], v143, %[ws] offset:1024 sc1\n\t" \
    "global_load_dwordx4 v[164:167], v143, %[ws] offset:2048 sc1\n\t" \
    "10:\n" \
    "s_setprio 3\n\t" \
    "s_waitcnt lgkmcnt(0)\n\t" \
    "v_mfma_f32_32x32x16_bf16 v[48:63], v[80:83], v[64:67], v[16:31]\n\t" \
    "v_exp_f32_e32 v32, v32\n\t" \
    "v_exp_f32_e32 v33, v33\n\t" \
    "v_exp_f32_e32 v34, v34\n\t" \
    "v_exp_f32_e32 v35, v35\n\t" \
    "v_exp_f32_e32 v36, v36\n\t" \
    "v_mfma_f32_32x32x16_bf16 v[48:63], v[84:87], v[68:71], v[48:63]\n\t" \
    "ds_read_b128 v[80:83], v112 offset:8192\n\t" \
    "ds_read_b128 v[84:87], v112 offset:9216\n\t" \
    "v_cvt_pk_bf16_f32 v104, v32, v33\n\t" \
    "v_cvt_pk_bf16_f32 v105, v34, v35\n\t" \
    "v_exp_f32_e32 v37, v37\n\t" \
    "v_exp_f32_e32 v38, v38\n\t" \
    "v_exp_f32_e32 v39, v39\n\t" \
    "ds_read_b128 v[100:103], v112 offset:3072\n\t" \
    "ds_read_b128 v[96:99], v112 offset:6144\n\t" \
    "v_exp_f32_e32 v40, v40\n\t" \
    "v_cvt_pk_bf16_f32 v106, v36, v37\n\t" \
    "v_cvt_pk_bf16_f32 v107, v38, v39\n\t" \
    "v_exp_f32_e32 v41, v41\n\t" \
    "v_exp_f32_e32 v42, v42\n\t" \
    "v_exp_f32_e32 v43, v43\n\t" \
    "v_mfma_f32_32x32x16_bf16 v[0:15], v[88:91], v[104:107], 0\n\t" \
    "v_exp_f32_e32 v44, v44\n\t" \
    "v_exp_f32_e32 v45, v45\n\t" \
    "v_exp_f32_e32 v46, v46\n\t" \
    "v_exp_f32_e32 v47, v47\n\t" \
    "v_cvt_pk_bf16_f32 v108, v40, v41\n\t" \
    "v_cvt_pk_bf16_f32 v109, v42, v43\n\t" \
    "v_cvt_pk_bf16_f32 v110, v44, v45\n\t" \
    "v_cvt_pk_bf16_f32 v111, v46, v47\n\t" \
    "s_mov_b32 m0, %[fld]\n\t" \
    "s_nop 0\n\t" \
    "global_load_lds_dwordx4 v142, %[ws] sc1\n\t" \
    "s_waitcnt lgkmcnt(0)\n\t" \
    "v_mfma_f32_32x32x16_bf16 v[32:47], v[80:83], v[64:67], v[16:31]\n\t" \
    "v_exp_f32_e32 v48, v48\n\t" \
    "v_exp_f32_e32 v49, v49\n\t" \
    "v_exp_f32_e32 v50, v50\n\t" \
    "v_smfmac_f32_16x16x64_bf16 v[76:79], v[72:75], v[104:111], v174\n\t" \
    "v_exp_f32_e32 v51, v51\n\t" \
    "v_exp_f32_e32 v52, v52\n\t" \
    "v_mfma_f32_32x32x16_bf16 v[32:47], v[84:87], v[68:71], v[32:47]\n\t" \
    "ds_read_b128 v[80:83], v112 offset:12288\n\t" \
    "ds_read_b128 v[84:87], v112 offset:13312\n\t" \
    "v_cvt_pk_bf16_f32 v104, v48, v49\n\t" \
    "v_cvt_pk_bf16_f32 v105, v50, v51\n\t" \
    "v_exp_f32_e32 v53, v53\n\t" \
    "v_exp_f32_e32 v54, v54\n\t" \
    "v_exp_f32_e32 v55, v55\n\t" \
    "v_mfma_f32_32x32x16_bf16 v[0:15], v[100:103], v[108:111], v[0:15]\n\t" \
    "ds_read_b128 v[92:95], v112 offset:7168\n\t" \
    "ds_read_b128 v[88:91], v112 offset:10240\n\t" \
    "v_exp_f32_e32 v56, v56\n\t" \
    "v_cvt_pk_bf16_f32 v106, v52, v53\n\t" \
    "v_cvt_pk_bf16_f32 v107, v54, v55\n\t" \
    "v_exp_f32_e32 v57, v57\n\t" \
    "v_exp_f32_e32 v58, v58\n\t" \
    "v_exp_f32_e32 v59, v59\n\t" \
    "v_mfma_f32_32x32x16_bf16 v[0:15], v[96:99], v[104:107], v[0:15]\n\t" \
    "v_exp_f32_e32 v60, v60\n\t" \
    "v_exp_f32_e32 v61, v61\n\t" \
    "v_exp_f32_e32 v62, v62\n\t" \
    "v_exp_f32_e32 v63, v63\n\t" \
    "v_cvt_pk_bf16_f32 v108, v56, v57\n\t" \
    "v_cvt_pk_bf16_f32 v109, v58, v59\n\t" \
    "v_cvt_pk_bf16_f32 v110, v60, v61\n\t" \
    "v_cvt_pk_bf16_f32 v111, v62, v63\n\t" \
    "s_setprio 2\n\t" \
    "s_waitcnt lgkmcnt(0)\n\t" \
    "v_mfma_f32_32x32x16_bf16 v[48:63], v[80:83], v[64:67], v[16:31]\n\t" \
    "v_exp_f32_e32 v32, v32\n\t" \
    "v_exp_f32_e32 v33, v33\n\t" \
    "v_exp_f32_e32 v34, v34\n\t" \
    "v_smfmac_f32_16x16x64_bf16 v[76:79], v[72:75], v[104:111], v174\n\t" \
    "v_exp_f32_e32 v35, v35\n\t" \
    "v_exp_f32_e32 v36, v36\n\t" \
    "v_mfma_f32_32x32x16_bf16 v[48:63], v[84:87], v[68:71], v[48:63]\n\t" \
    "ds_read_b128 v[80:83], v112 offset:16384\n\t" \
    "ds_read_b128 v[84:87], v112 offset:17408\n\t" \
    "v_cvt_pk_bf16_f32 v104, v32, v33\n\t" \
    "v_cvt_pk_bf16_f32 v105, v34, v35\n\t" \
    "v_exp_f32_e32 v37, v37\n\t" \
    "v_exp_f32_e32 v38, v38\n\t" \
    "v_exp_f32_e32 v39, v39\n\t" \
    "v_mfma_f32_32x32x16_bf16 v[0:15], v[92:95], v[108:111], v[0:15]\n\t" \
    "ds_read_b128 v[100:103], v112 offset:11264\n\t" \
    "ds_read_b128 v[96:99], v112 offset:14336\n\t" \
    "v_exp_f32_e32 v40, v40\n\t" \
    "v_cvt_pk_bf16_f32 v106, v36, v37\n\t" \
    "v_cvt_pk_bf16_f32 v107, v38, v39\n\t" \
    "v_exp_f32_e32 v41, v41\n\t" \
    "v_exp_f32_e32 v42, v42\n\t" \
    "v_exp_f32_e32 v43, v43\n\t" \
    "v_mfma_f32_32x32x16_bf16 v[0:15], v[88:91], v[104:107], v[0:15]\n\t" \
    "v_exp_f32_e32 v44, v44\n\t" \
    "v_exp_f32_e32 v45, v45\n\t" \
    "v_exp_f32_e32 v46, v46\n\t" \
    "v_exp_f32_e32 v47, v47\n\t" \
    "v_cvt_pk_bf16_f32 v108, v40, v41\n\t" \
    "v_cvt_pk_bf16_f32 v109, v42, v43\n\t" \
    "v_cvt_pk_bf16_f32 v110, v44, v45\n\t" \
    "v_cvt_pk_bf16_f32 v111, v46, v47\n\t" \
    "s_waitcnt lgkmcnt(0)\n\t" \
    "v_mfma_f32_32x32x16_bf16 v[32:47], v[80:83], v[64:67], v[16:31]\n\t" \
    "v_exp_f32_e32 v48, v48\n\t" \
    "v_exp_f32_e32 v49, v49\n\t" \
    "v_exp_f32_e32 v50, v50\n\t" \
    "v_smfmac_f32_16x16x64_bf16 v[76:79], v[72:75], v[104:111], v174\n\t" \
    "v_exp_f32_e32 v51, v51\n\t" \
    "v_exp_f32_e32 v52, v52\n\t" \
    "v_mfma_f32_32x32x16_bf16 v[32:47], v[84:87], v[68:71], v[32:47]\n\t" \
    "ds_read_b128 v[80:83], v112 offset:20480\n\t" \
    "ds_read_b128 v[84:87], v112 offset:21504\n\t" \
    "v_cvt_pk_bf16_f32 v104, v48, v49\n\t" \
    "v_cvt_pk_bf16_f32 v105, v50, v51\n\t" \
    "v_exp_f32_e32 v53, v53\n\t" \
    "v_exp_f32_e32 v54, v54\n\t" \
    "v_exp_f32_e32 v55, v55\n\t" \
    "v_mfma_f32_32x32x16_bf16 v[0:15], v[100:103], v[108:111], v[0:15]\n\t" \
    "ds_read_b128 v[92:95], v112 offset:15360\n\t" \
    "ds_read_b128 v[88:91], v112 offset:18432\n\t" \
    "v_exp_f32_e32 v56, v56\n\t" \
    "v_cvt_pk_bf16_f32 v106, v52, v53\n\t" \
    "v_cvt_pk_bf16_f32 v107, v54, v55\n\t" \
    "v_exp_f32_e32 v57, v57\n\t" \
    "v_exp_f32_e32 v58, v58\n\t" \
    "v_exp_f32_e32 v59, v59\n\t" \
    "v_mfma_f32_32x32x16_bf16 v[0:15], v[96:99], v[104:107], v[0:15]\n\t" \
    "v_exp_f32_e32 v60, v60\n\t" \
    "v_exp_f32_e32 v61, v61\n\t" \
    "v_exp_f32_e32 v62, v62\n\t" \
    "v_exp_f32_e32 v63, v63\n\t" \
    "v_cvt_pk_bf16_f32 v108, v56, v57\n\t" \
    "v_cvt_pk_bf16_f32 v109, v58, v59\n\t" \
    "v_cvt_pk_bf16_f32 v110, v60, v61\n\t" \
    "v_cvt_pk_bf16_f32 v111, v62, v63\n\t" \
    "s_setprio 1\n\t" \
    "s_waitcnt lgkmcnt(0)\n\t" \
    "v_mfma_f32_32x32x16_bf16 v[48:63], v[80:83], v[64:67], v[16:31]\n\t" \
    "v_exp_f32_e32 v32, v32\n\t" \
    "v_exp_f32_e32 v33, v33\n\t" \
    "v_exp_f32_e32 v34, v34\n\t" \
    "v_smfmac_f32_16x16x64_bf16 v[76:79], v[72:75], v[104:111], v174\n\t" \
    "v_exp_f32_e32 v35, v35\n\t" \
    "v_exp_f32_e32 v36, v36\n\t" \
    "v_mfma_f32_32x32x16_bf16 v[48:63], v[84:87], v[68:71], v[48:63]\n\t" \
    "ds_read_b128 v[80:83], v112 offset:24576\n\t" \
    "ds_read_b128 v[84:87], v112 offset:25600\n\t" \
    "v_cvt_pk_bf16_f32 v104, v32, v33\n\t" \
    "v_cvt_pk_bf16_f32 v105, v34, v35\n\t" \
    "v_exp_f32_e32 v37, v37\n\t" \
    "v_exp_f32_e32 v38, v38\n\t" \
    "v_exp_f32_e32 v39, v39\n\t" \
    "v_mfma_f32_32x32x16_bf16 v[0:15], v[92:95], v[108:111], v[0:15]\n\t" \
    "ds_read_b128 v[100:103], v112 offset:19456\n\t" \
    "ds_read_b128 v[96:99], v112 offset:22528\n\t" \
    "v_exp_f32_e32 v40, v40\n\t" \
    "v_cvt_pk_bf16_f32 v106, v36, v37\n\t" \
    "v_cvt_pk_bf16_f32 v107, v38, v39\n\t" \
    "v_exp_f32_e32 v41, v41\n\t" \
    "v_exp_f32_e32 v42, v42\n\t" \
    "v_exp_f32_e32 v43, v43\n\t" \
    "v_mfma_f32_32x32x16_bf16 v[0:15], v[88:91], v[104:107], v[0:15]\n\t" \
    "v_exp_f32_e32 v44, v44\n\t" \
    "v_exp_f32_e32 v45, v45\n\t" \
    "v_exp_f32_e32 v46, v46\n\t" \
    "v_exp_f32_e32 v47, v47\n\t" \
    "v_cvt_pk_bf16_f32 v108, v40, v41\n\t" \
    "v_cvt_pk_bf16_f32 v109, v42, v43\n\t" \
    "v_cvt_pk_bf16_f32 v110, v44, v45\n\t" \
    "v_cvt_pk_bf16_f32 v111, v46, v47\n\t" \
    "s_waitcnt lgkmcnt(0)\n\t" \
    "v_mfma_f32_32x32x16_bf16 v[32:47], v[80:83], v[64:67], v[16:31]\n\t" \
    "v_exp_f32_e32 v48, v48\n\t" \
    "v_exp_f32_e32 v49, v49\n\t" \
    "v_exp_f32_e32 v50, v50\n\t" \
    "v_smfmac_f32_16x16x64_bf16 v[76:79], v[72:75], v[104:111], v174\n\t" \
    "v_exp_f32_e32 v51, v51\n\t" \
    "v_exp_f32_e32 v52, v52\n\t" \
    "v_mfma_f32_32x32x16_bf16 v[32:47], v[84:87], v[68:71], v[32:47]\n\t" \
    "ds_read_b128 v[80:83], v112 offset:28672\n\t" \
    "ds_read_b128 v[84:87], v112 offset:29696\n\t" \
    "v_cvt_pk_bf16_f32 v104, v48, v49\n\t" \
    "v_cvt_pk_bf16_f32 v105, v50, v51\n\t" \
    "v_exp_f32_e32 v53, v53\n\t" \
    "v_exp_f32_e32 v54, v54\n\t" \
    "v_exp_f32_e32 v55, v55\n\t" \
    "v_mfma_f32_32x32x16_bf16 v[0:15], v[100:103], v[108:111], v[0:15]\n\t" \
    "ds_read_b128 v[92:95], v112 offset:23552\n\t" \
    "ds_read_b128 v[88:91], v112 offset:26624\n\t" \
    "v_exp_f32_e32 v56, v56\n\t" \
    "v_cvt_pk_bf16_f32 v106, v52, v53\n\t" \
    "v_cvt_pk_bf16_f32 v107, v54, v55\n\t" \
    "v_exp_f32_e32 v57, v57\n\t" \
    "v_exp_f32_e32 v58, v58\n\t" \
    "v_exp_f32_e32 v59, v59\n\t" \
    "v_mfma_f32_32x32x16_bf16 v[0:15], v[96:99], v[104:107], v[0:15]\n\t" \
    "v_exp_f32_e32 v60, v60\n\t" \
    "v_exp_f32_e32 v61, v61\n\t" \
    "v_exp_f32_e32 v62, v62\n\t" \
    "v_exp_f32_e32 v63, v63\n\t" \
    "v_cvt_pk_bf16_f32 v108, v56, v57\n\t" \
    "v_cvt_pk_bf16_f32 v109, v58, v59\n\t" \
    "v_cvt_pk_bf16_f32 v110, v60, v61\n\t" \
    "v_cvt_pk_bf16_f32 v111, v62, v63\n\t" \
    "s_setprio 0\n\t" \
    "s_cmp_eq_u32 %[hl], 0\n\t" \
    "s_cbranch_scc1 11f\n\t" \
    "s_waitcnt vmcnt(1)\n\t" \
    "11:\n" \
    "s_waitcnt lgkmcnt(0)\n\t" \
    "s_nop 15\n\t"
#define ATT_L4 \
    "s_nop 1\n\t" \
    "s_setprio 3\n\t" \
    "s_waitcnt lgkmcnt(0)\n\t" \
    "v_mfma_f32_32x32x16_bf16 v[48:63], v[80:83], v[64:67], v[16:31]\n\t" \
    "v_exp_f32_e32 v32, v32\n\t" \
    "v_exp_f32_e32 v33, v33\n\t" \
    "v_exp_f32_e32 v34, v34\n\t" \
    "v_exp_f32_e32 v35, v35\n\t" \
    "v_exp_f32_e32 v36, v36\n\t" \
    "v_mfma_f32_32x32x16_bf16 v[48:63], v[84:87], v[68:71], v[48:63]\n\t" \
    "ds_read_b128 v[80:83], v112 offset:8192\n\t" \
    "ds_read_b128 v[84:87], v112 offset:9216\n\t" \
    "v_cvt_pk_bf16_f32 v104, v32, v33\n\t" \
    "v_cvt_pk_bf16_f32 v105, v34, v35\n\t" \
    "v_exp_f32_e32 v37, v37\n\t" \
    "v_exp_f32_e32 v38, v38\n\t" \
    "v_exp_f32_e32 v39, v39\n\t" \
    "ds_read_b128 v[100:103], v112 offset:3072\n\t" \
    "ds_read_b128 v[96:99], v112 offset:6144\n\t" \
    "v_exp_f32_e32 v40, v40\n\t" \
    "v_cvt_pk_bf16_f32 v106, v36, v37\n\t" \
    "v_cvt_pk_bf16_f32 v107, v38, v39\n\t" \
    "v_exp_f32_e32 v41, v41\n\t" \
    "v_exp_f32_e32 v42, v42\n\t" \
    "v_exp_f32_e32 v43, v43\n\t" \
    "v_mfma_f32_32x32x16_bf16 v[0:15], v[88:91], v[104:107], 0\n\t" \
    "v_exp_f32_e32 v44, v44\n\t" \
    "v_exp_f32_e32 v45, v45\n\t" \
    "v_exp_f32_e32 v46, v46\n\t" \
    "v_exp_f32_e32 v47, v47\n\t" \
    "v_cvt_pk_bf16_f32 v108, v40, v41\n\t" \
    "v_cvt_pk_bf16_f32 v109, v42, v43\n\t" \
    "v_cvt_pk_bf16_f32 v110, v44, v45\n\t" \
    "v_cvt_pk_bf16_f32 v111, v46, v47\n\t" \
    "s_waitcnt lgkmcnt(0)\n\t" \
    "v_mfma_f32_32x32x16_bf16 v[32:47], v[80:83], v[64:67], v[16:31]\n\t" \
    "v_exp_f32_e32 v48, v48\n\t" \
    "v_exp_f32_e32 v49, v49\n\t" \
    "v_exp_f32_e32 v50, v50\n\t" \
    "v_smfmac_f32_16x16x64_bf16 v[76:79], v[72:75], v[104:111], v174\n\t" \
    "v_exp_f32_e32 v51, v51\n\t" \
    "v_exp_f32_e32 v52, v52\n\t" \
    "v_mfma_f32_32x32x16_bf16 v[32:47], v[84:87], v[68:71], v[32:47]\n\t" \
    "ds_read_b128 v[80:83], v112 offset:12288\n\t" \
    "ds_read_b128 v[84:87], v112 offset:13312\n\t" \
    "v_cvt_pk_bf16_f32 v104, v48, v49\n\t" \
    "v_cvt_pk_bf16_f32 v105, v50, v51\n\t" \
    "v_exp_f32_e32 v53, v53\n\t" \
    "v_exp_f32_e32 v54, v54\n\t" \
    "v_exp_f32_e32 v55, v55\n\t" \
    "v_mfma_f32_32x32x16_bf16 v[0:15], v[100:103], v[108:111], v[0:15]\n\t" \
    "ds_read_b128 v[92:95], v112 offset:7168\n\t" \
    "ds_read_b128 v[88:91], v112 offset:10240\n\t" \
    "v_exp_f32_e32 v56, v56\n\t" \
    "v_cvt_pk_bf16_f32 v106, v52, v53\n\t" \
    "v_cvt_pk_bf16_f32 v107, v54, v55\n\t" \
    "v_exp_f32_e32 v57, v57\n\t" \
    "v_exp_f32_e32 v58, v58\n\t" \
    "v_exp_f32_e32 v59, v59\n\t" \
    "v_mfma_f32_32x32x16_bf16 v[0:15], v[96:99], v[104:107], v[0:15]\n\t" \
    "v_exp_f32_e32 v60, v60\n\t" \
    "v_exp_f32_e32 v61, v61\n\t" \
    "v_exp_f32_e32 v62, v62\n\t" \
    "v_exp_f32_e32 v63, v63\n\t" \
    "v_cvt_pk_bf16_f32 v108, v56, v57\n\t" \
    "v_cvt_pk_bf16_f32 v109, v58, v59\n\t" \
    "v_cvt_pk_bf16_f32 v110, v60, v61\n\t" \
    "v_cvt_pk_bf16_f32 v111, v62, v63\n\t" \
    "s_setprio 2\n\t" \
    "s_waitcnt lgkmcnt(0)\n\t" \
    "v_mfma_f32_32x32x16_bf16 v[48:63], v[80:83], v[64:67], v[16:31]\n\t" \
    "v_exp_f32_e32 v32, v32\n\t" \
    "v_exp_f32_e32 v33, v33\n\t" \
    "v_exp_f32_e32 v34, v34\n\t" \
    "v_smfmac_f32_16x16x64_bf16 v[76:79], v[72:75], v[104:111], v174\n\t" \
    "v_exp_f32_e32 v35, v35\n\t" \
    "v_exp_f32_e32 v36, v36\n\t" \
    "v_mfma_f32_32x32x16_bf16 v[48:63], v[84:87], v[68:71], v[48:63]\n\t" \
    "ds_read_b128 v[80:83], v112 offset:16384\n\t" \
    "ds_read_b128 v[84:87], v112 offset:17408\n\t" \
    "v_cvt_pk_bf16_f32 v104, v32, v33\n\t" \
    "v_cvt_pk_bf16_f32 v105, v34, v35\n\t" \
    "v_exp_f32_e32 v37, v37\n\t" \
    "v_exp_f32_e32 v38, v38\n\t" \
    "v_exp_f32_e32 v39, v39\n\t" \
    "v_mfma_f32_32x32x16_bf16 v[0:15], v[92:95], v[108:111], v[0:15]\n\t" \
    "ds_read_b128 v[100:103], v112 offset:11264\n\t" \
    "ds_read_b128 v[96:99], v112 offset:14336\n\t" \
    "v_exp_f32_e32 v40, v40\n\t" \
    "v_cvt_pk_bf16_f32 v106, v36, v37\n\t" \
    "v_cvt_pk_bf16_f32 v107, v38, v39\n\t" \
    "v_exp_f32_e32 v41, v41\n\t" \
    "v_exp_f32_e32 v42, v42\n\t" \
    "v_exp_f32_e32 v43, v43\n\t" \
    "v_mfma_f32_32x32x16_bf16 v[0:15], v[88:91], v[104:107], v[0:15]\n\t" \
    "v_exp_f32_e32 v44, v44\n\t" \
    "v_exp_f32_e32 v45, v45\n\t" \
    "v_exp_f32_e32 v46, v46\n\t" \
    "v_exp_f32_e32 v47, v47\n\t" \
    "v_cvt_pk_bf16_f32 v108, v40, v41\n\t" \
    "v_cvt_pk_bf16_f32 v109, v42, v43\n\t" \
    "v_cvt_pk_bf16_f32 v110, v44, v45\n\t" \
    "v_cvt_pk_bf16_f32 v111, v46, v47\n\t" \
    "s_waitcnt lgkmcnt(0)\n\t" \
    "v_mfma_f32_32x32x16_bf16 v[32:47], v[80:83], v[64:67], v[16:31]\n\t" \
    "v_exp_f32_e32 v48, v48\n\t" \
    "v_exp_f32_e32 v49, v49\n\t" \
    "v_exp_f32_e32 v50, v50\n\t" \
    "v_smfmac_f32_16x16x64_bf16 v[76:79], v[72:75], v[104:111], v174\n\t" \
    "v_exp_f32_e32 v51, v51\n\t" \
    "v_exp_f32_e32 v52, v52\n\t" \
    "v_mfma_f32_32x32x16_bf16 v[32:47], v[84:87], v[68:71], v[32:47]\n\t" \
    "ds_read_b128 v[80:83], v112 offset:20480\n\t" \
    "ds_read_b128 v[84:87], v112 offset:21504\n\t" \
    "v_cvt_pk_bf16_f32 v104, v48, v49\n\t" \
    "v_cvt_pk_bf16_f32 v105, v50, v51\n\t" \
    "v_exp_f32_e32 v53, v53\n\t" \
    "v_exp_f32_e32 v54, v54\n\t" \
    "v_exp_f32_e32 v55, v55\n\t" \
    "v_mfma_f32_32x32x16_bf16 v[0:15], v[100:103], v[108:111], v[0:15]\n\t" \
    "ds_read_b128 v[92:95], v112 offset:15360\n\t" \
    "ds_read_b128 v[88:91], v112 offset:18432\n\t" \
    "v_exp_f32_e32 v56, v56\n\t" \
    "v_cvt_pk_bf16_f32 v106, v52, v53\n\t" \
    "v_cvt_pk_bf16_f32 v107, v54, v55\n\t" \
    "v_exp_f32_e32 v57, v57\n\t" \
    "v_exp_f32_e32 v58, v58\n\t" \
    "v_exp_f32_e32 v59, v59\n\t" \
    "v_mfma_f32_32x32x16_bf16 v[0:15], v[96:99], v[104:107], v[0:15]\n\t" \
    "v_exp_f32_e32 v60, v60\n\t" \
    "v_exp_f32_e32 v61, v61\n\t" \
    "v_exp_f32_e32 v62, v62\n\t" \
    "v_exp_f32_e32 v63, v63\n\t" \
    "v_cvt_pk_bf16_f32 v108, v56, v57\n\t" \
    "v_cvt_pk_bf16_f32 v109, v58, v59\n\t" \
    "v_cvt_pk_bf16_f32 v110, v60, v61\n\t" \
    "v_cvt_pk_bf16_f32 v111, v62, v63\n\t" \
    "s_setprio 0\n\t" \
    "s_waitcnt lgkmcnt(0)\n\t" \
    "s_nop 15\n\t"
#define ATT_L2 \
    "s_nop 1\n\t" \
    "s_setprio 1\n\t" \
    "s_waitcnt lgkmcnt(0)\n\t" \
    "v_mfma_f32_32x32x16_bf16 v[48:63], v[80:83], v[64:67], v[16:31]\n\t" \
    "v_exp_f32_e32 v32, v32\n\t" \
    "v_exp_f32_e32 v33, v33\n\t" \
    "v_exp_f32_e32 v34, v34\n\t" \
    "v_smfmac_f32_16x16x64_bf16 v[76:79], v[72:75], v[104:111], v174\n\t" \
    "v_exp_f32_e32 v35, v35\n\t" \
    "v_exp_f32_e32 v36, v36\n\t" \
    "v_mfma_f32_32x32x16_bf16 v[48:63], v[84:87], v[68:71], v[48:63]\n\t" \
    "ds_read_b128 v[80:83], v112 offset:24576\n\t" \
    "ds_read_b128 v[84:87], v112 offset:25600\n\t" \
    "v_cvt_pk_bf16_f32 v104, v32, v33\n\t" \
    "v_cvt_pk_bf16_f32 v105, v34, v35\n\t" \
    "v_exp_f32_e32 v37, v37\n\t" \
    "v_exp_f32_e32 v38, v38\n\t" \
    "v_exp_f32_e32 v39, v39\n\t" \
    "v_mfma_f32_32x32x16_bf16 v[0:15], v[92:95], v[108:111], v[0:15]\n\t" \
    "ds_read_b128 v[100:103], v112 offset:19456\n\t" \
    "ds_read_b128 v[96:99], v112 offset:22528\n\t" \
    "v_exp_f32_e32 v40, v40\n\t" \
    "v_cvt_pk_bf16_f32 v106, v36, v37\n\t" \
    "v_cvt_pk_bf16_f32 v107, v38, v39\n\t" \
    "v_exp_f32_e32 v41, v41\n\t" \
    "v_exp_f32_e32 v42, v42\n\t" \
    "v_exp_f32_e32 v43, v43\n\t" \
    "v_mfma_f32_32x32x16_bf16 v[0:15], v[88:91], v[104:107], v[0:15]\n\t" \
    "v_exp_f32_e32 v44, v44\n\t" \
    "v_exp_f32_e32 v45, v45\n\t" \
    "v_exp_f32_e32 v46, v46\n\t" \
    "v_exp_f32_e32 v47, v47\n\t" \
    "v_cvt_pk_bf16_f32 v108, v40, v41\n\t" \
    "v_cvt_pk_bf16_f32 v109, v42, v43\n\t" \
    "v_cvt_pk_bf16_f32 v110, v44, v45\n\t" \
    "v_cvt_pk_bf16_f32 v111, v46, v47\n\t" \
    "s_waitcnt lgkmcnt(0)\n\t" \
    "v_mfma_f32_32x32x16_bf16 v[32:47], v[80:83], v[64:67], v[16:31]\n\t" \
    "v_exp_f32_e32 v48, v48\n\t" \
    "v_exp_f32_e32 v49, v49\n\t" \
    "v_exp_f32_e32 v50, v50\n\t" \
    "v_smfmac_f32_16x16x64_bf16 v[76:79], v[72:75], v[104:111], v174\n\t" \
    "v_exp_f32_e32 v51, v51\n\t" \
    "v_exp_f32_e32 v52, v52\n\t" \
    "v_mfma_f32_32x32x16_bf16 v[32:47], v[84:87], v[68:71], v[32:47]\n\t" \
    "ds_read_b128 v[80:83], v112 offset:28672\n\t" \
    "ds_read_b128 v[84:87], v112 offset:29696\n\t" \
    "v_cvt_pk_bf16_f32 v104, v48, v49\n\t" \
    "v_cvt_pk_bf16_f32 v105, v50, v51\n\t" \
    "v_exp_f32_e32 v53, v53\n\t" \
    "v_exp_f32_e32 v54, v54\n\t" \
    "v_exp_f32_e32 v55, v55\n\t" \
    "v_mfma_f32_32x32x16_bf16 v[0:15], v[100:103], v[108:111], v[0:15]\n\t" \
    "ds_read_b128 v[92:95], v112 offset:23552\n\t" \
    "ds_read_b128 v[88:91], v112 offset:26624\n\t" \
    "v_exp_f32_e32 v56, v56\n\t" \
    "v_cvt_pk_bf16_f32 v106, v52, v53\n\t" \
    "v_cvt_pk_bf16_f32 v107, v54, v55\n\t" \
    "v_exp_f32_e32 v57, v57\n\t" \
    "v_exp_f32_e32 v58, v58\n\t" \
    "v_exp_f32_e32 v59, v59\n\t" \
    "v_mfma_f32_32x32x16_bf16 v[0:15], v[96:99], v[104:107], v[0:15]\n\t" \
    "v_exp_f32_e32 v60, v60\n\t" \
    "v_exp_f32_e32 v61, v61\n\t" \
    "v_exp_f32_e32 v62, v62\n\t" \
    "v_exp_f32_e32 v63, v63\n\t" \
    "v_cvt_pk_bf16_f32 v108, v56, v57\n\t" \
    "v_cvt_pk_bf16_f32 v109, v58, v59\n\t" \
    "v_cvt_pk_bf16_f32 v110, v60, v61\n\t" \
    "v_cvt_pk_bf16_f32 v111, v62, v63\n\t" \
    "s_setprio 0\n\t" \
    "s_waitcnt lgkmcnt(0)\n\t" \
    "s_nop 15\n\t"
#define ATT_XT \
    "s_nop 1\n\t" \
    "s_waitcnt lgkmcnt(0)\n\t" \
    "v_mfma_f32_32x32x16_bf16 v[48:63], v[80:83], v[64:67], v[16:31]\n\t" \
    "v_exp_f32_e32 v32, v32\n\t" \
    "v_exp_f32_e32 v33, v33\n\t" \
    "v_exp_f32_e32 v34, v34\n\t" \
    "v_smfmac_f32_16x16x64_bf16 v[76:79], v[72:75], v[104:111], v174\n\t" \
    "v_exp_f32_e32 v35, v35\n\t" \
    "v_exp_f32_e32 v36, v36\n\t" \
    "v_mfma_f32_32x32x16_bf16 v[48:63], v[84:87], v[68:71], v[48:63]\n\t" \
    "v_cvt_pk_bf16_f32 v104, v32, v33\n\t" \
    "v_cvt_pk_bf16_f32 v105, v34, v35\n\t" \
    "v_exp_f32_e32 v37, v37\n\t" \
    "v_exp_f32_e32 v38, v38\n\t" \
    "v_exp_f32_e32 v39, v39\n\t" \
    "v_mfma_f32_32x32x16_bf16 v[0:15], v[92:95], v[108:111], v[0:15]\n\t" \
    "ds_read_b128 v[100:103], v112 offset:27648\n\t" \
    "ds_read_b128 v[96:99], v112 offset:30720\n\t" \
    "v_exp_f32_e32 v40, v40\n\t" \
    "v_cvt_pk_bf16_f32 v106, v36, v37\n\t" \
    "v_cvt_pk_bf16_f32 v107, v38, v39\n\t" \
    "v_exp_f32_e32 v41, v41\n\t" \
    "v_exp_f32_e32 v42, v42\n\t" \
    "v_exp_f32_e32 v43, v43\n\t" \
    "v_mfma_f32_32x32x16_bf16 v[0:15], v[88:91], v[104:107], v[0:15]\n\t" \
    "v_exp_f32_e32 v44, v44\n\t" \
    "v_exp_f32_e32 v45, v45\n\t" \
    "v_exp_f32_e32 v46, v46\n\t" \
    "v_exp_f32_e32 v47, v47\n\t" \
    "v_cvt_pk_bf16_f32 v108, v40, v41\n\t" \
    "v_cvt_pk_bf16_f32 v109, v42, v43\n\t" \
    "v_cvt_pk_bf16_f32 v110, v44, v45\n\t" \
    "v_cvt_pk_bf16_f32 v111, v46, v47\n\t" \
    "s_waitcnt lgkmcnt(0)\n\t" \
    "v_exp_f32_e32 v48, v48\n\t" \
    "v_exp_f32_e32 v49, v49\n\t" \
    "v_exp_f32_e32 v50, v50\n\t" \
    "v_smfmac_f32_16x16x64_bf16 v[76:79], v[72:75], v[104:111], v174\n\t" \
    "v_exp_f32_e32 v51, v51\n\t" \
    "v_exp_f32_e32 v52, v52\n\t" \
    "v_cvt_pk_bf16_f32 v104, v48, v49\n\t" \
    "v_cvt_pk_bf16_f32 v105, v50, v51\n\t" \
    "v_exp_f32_e32 v53, v53\n\t" \
    "v_exp_f32_e32 v54, v54\n\t" \
    "v_exp_f32_e32 v55, v55\n\t" \
    "v_mfma_f32_32x32x16_bf16 v[0:15], v[100:103], v[108:111], v[0:15]\n\t" \
    "ds_read_b128 v[92:95], v112 offset:31744\n\t" \
    "v_exp_f32_e32 v56, v56\n\t" \
    "v_cvt_pk_bf16_f32 v106, v52, v53\n\t" \
    "v_cvt_pk_bf16_f32 v107, v54, v55\n\t" \
    "v_exp_f32_e32 v57, v57\n\t" \
    "v_exp_f32_e32 v58, v58\n\t" \
    "v_exp_f32_e32 v59, v59\n\t" \
    "v_mfma_f32_32x32x16_bf16 v[0:15], v[96:99], v[104:107], v[0:15]\n\t" \
    "v_exp_f32_e32 v60, v60\n\t" \
    "v_exp_f32_e32 v61, v61\n\t" \
    "v_exp_f32_e32 v62, v62\n\t" \
    "v_exp_f32_e32 v63, v63\n\t" \
    "v_cvt_pk_bf16_f32 v108, v56, v57\n\t" \
    "v_cvt_pk_bf16_f32 v109, v58, v59\n\t" \
    "v_cvt_pk_bf16_f32 v110, v60, v61\n\t" \
    "v_cvt_pk_bf16_f32 v111, v62, v63\n\t" \
    "s_waitcnt lgkmcnt(0)\n\t" \
    "s_nop 0\n\t" \
    "v_mfma_f32_32x32x16_bf16 v[0:15], v[92:95], v[108:111], v[0:15]\n\t" \
    "v_smfmac_f32_16x16x64_bf16 v[76:79], v[72:75], v[104:111], v174\n\t" \
    "s_nop 15\n\t"
#define ATT_MAIN \
    "s_mov_b64 s[48:49], %[tab]\n\t" \
    "s_mov_b32 s46, %[k0]\n\t" \
    "s_and_b32 s40, s48, 63\n\t" \
    "s_lshl_b32 s40, s40, 12\n\t" \
    "s_add_u32 s44, %[srclo], s40\n\t" \
    "s_addc_u32 s45, %[srchi], 0\n\t" \
    "s_lshr_b64 s[48:49], s[48:49], 6\n\t" \
    "s_add_u32 s46, s46, 1\n\t" \
    "s_mov_b64 vcc, -1\n\t" \
    "s_waitcnt vmcnt(0)\n\t" \
    "ds_read_b64 v[120:121], v173\n\t" \
    "s_waitcnt lgkmcnt(0)\n\t" \
    "1:\n" \
    "s_cmp_eq_u32 %[cnt], 4\n\t" \
    "s_cbranch_scc1 5f\n\t" \
    "s_waitcnt vmcnt(0)\n\t" \
    "5:\n" \
    "s_waitcnt vmcnt(9)\n\t" \
    "s_barrier\n\t" \
    "s_bitcmp1_b32 %[act], %[cnt]\n\t" \
    "s_cbranch_scc1 30f\n\t" \
    "2:\n" \
    "s_setprio 3\n\t" \
    "s_waitcnt lgkmcnt(0)\n\t" \
    "v_mfma_f32_32x32x16_bf16 v[48:63], v[80:83], v[64:67], v[16:31]\n\t" \
    "v_exp_f32_e32 v32, v32\n\t" \
    "v_exp_f32_e32 v33, v33\n\t" \
    "v_exp_f32_e32 v34, v34\n\t" \
    "v_smfmac_f32_16x16x64_bf16 v[76:79], v[72:75], v[104:111], v174\n\t" \
    "v_exp_f32_e32 v35, v35\n\t" \
    "v_exp_f32_e32 v36, v36\n\t" \
    "v_mfma_f32_32x32x16_bf16 v[48:63], v[84:87], v[68:71], v[48:63]\n\t" \
    "ds_read_b128 v[80:83], v113 offset:0\n\t" \
    "ds_read_b128 v[84:87], v113 offset:1024\n\t" \
    "v_cvt_pk_bf16_f32 v104, v32, v33\n\t" \
    "v_cvt_pk_bf16_f32 v105, v34, v35\n\t" \
    "v_exp_f32_e32 v37, v37\n\t" \
    "v_exp_f32_e32 v38, v38\n\t" \
    "v_exp_f32_e32 v39, v39\n\t" \
    "v_mfma_f32_32x32x16_bf16 v[0:15], v[92:95], v[108:111], v[0:15]\n\t" \
    "ds_read_b128 v[100:103], v112 offset:27648\n\t" \
    "ds_read_b128 v[96:99], v112 offset:30720\n\t" \
    "v_exp_f32_e32 v40, v40\n\t" \
    "v_cvt_pk_bf16_f32 v106, v36, v37\n\t" \
    "v_cvt_pk_bf16_f32 v107, v38, v39\n\t" \
    "v_exp_f32_e32 v41, v41\n\t" \
    "v_exp_f32_e32 v42, v42\n\t" \
    "v_exp_f32_e32 v43, v43\n\t" \
    "v_mfma_f32_32x32x16_bf16 v[0:15], v[88:91], v[104:107], v[0:15]\n\t" \
    "s_cbranch_vccz 20f\n\t" \
    "v_cmp_eq_u64_e64 s[42:43], v[120:121], v[140:141]\n\t" \
    "s_mov_b32 m0, %[dst]\n\t" \
    "s_nop 3\n\t" \
    "s_bitcmp1_b64 s[42:43], s46\n\t" \
    "s_cbranch_scc0 9f\n\t" \
    "12:\n" \
    "global_load_lds_dwordx4 v114, s[44:45] offset:0 sc1\n\t" \
    "20:\n" \
    "v_exp_f32_e32 v44, v44\n\t" \
    "v_exp_f32_e32 v45, v45\n\t" \
    "v_exp_f32_e32 v46, v46\n\t" \
    "v_exp_f32_e32 v47, v47\n\t" \
    "v_cvt_pk_bf16_f32 v108, v40, v41\n\t" \
    "v_cvt_pk_bf16_f32 v109, v42, v43\n\t" \
    "v_cvt_pk_bf16_f32 v110, v44, v45\n\t" \
    "v_cvt_pk_bf16_f32 v111, v46, v47\n\t" \
    "s_waitcnt lgkmcnt(0)\n\t" \
    "v_mfma_f32_32x32x16_bf16 v[32:47], v[80:83], v[64:67], v[16:31]\n\t" \
    "v_exp_f32_e32 v48, v48\n\t" \
    "v_exp_f32_e32 v49, v49\n\t" \
    "v_exp_f32_e32 v50, v50\n\t" \
    "v_smfmac_f32_16x16x64_bf16 v[76:79], v[72:75], v[104:111], v174\n\t" \
    "v_exp_f32_e32 v51, v51\n\t" \
    "v_exp_f32_e32 v52, v52\n\t" \
    "v_mfma_f32_32x32x16_bf16 v[32:47], v[84:87], v[68:71], v[32:47]\n\t" \
    "ds_read_b128 v[80:83], v113 offset:4096\n\t" \
    "ds_read_b128 v[84:87], v113 offset:5120\n\t" \
    "v_cvt_pk_bf16_f32 v104, v48, v49\n\t" \
    "v_cvt_pk_bf16_f32 v105, v50, v51\n\t" \
    "v_exp_f32_e32 v53, v53\n\t" \
    "v_exp_f32_e32 v54, v54\n\t" \
    "v_exp_f32_e32 v55, v55\n\t" \
    "v_mfma_f32_32x32x16_bf16 v[0:15], v[100:103], v[108:111], v[0:15]\n\t" \
    "ds_read_b128 v[92:95], v112 offset:31744\n\t" \
    "ds_read_b128 v[88:91], v113 offset:2048\n\t" \
    "v_exp_f32_e32 v56, v56\n\t" \
    "v_cvt_pk_bf16_f32 v106, v52, v53\n\t" \
    "v_cvt_pk_bf16_f32 v107, v54, v55\n\t" \
    "v_exp_f32_e32 v57, v57\n\t" \
    "v_exp_f32_e32 v58, v58\n\t" \
    "v_exp_f32_e32 v59, v59\n\t" \
    "v_mfma_f32_32x32x16_bf16 v[0:15], v[96:99], v[104:107], v[0:15]\n\t" \
    "s_cbranch_vccz 21f\n\t" \
    "global_load_lds_dwordx4 v114, s[44:45] offset:1024 sc1\n\t" \
    "21:\n" \
    "v_exp_f32_e32 v60, v60\n\t" \
    "v_exp_f32_e32 v61, v61\n\t" \
    "v_exp_f32_e32 v62, v62\n\t" \
    "v_exp_f32_e32 v63, v63\n\t" \
    "v_cvt_pk_bf16_f32 v108, v56, v57\n\t" \
    "v_cvt_pk_bf16_f32 v109, v58, v59\n\t" \
    "v_cvt_pk_bf16_f32 v110, v60, v61\n\t" \
    "v_cvt_pk_bf16_f32 v111, v62, v63\n\t" \
    "s_setprio 2\n\t" \
    "s_waitcnt lgkmcnt(0)\n\t" \
    "v_mfma_f32_32x32x16_bf16 v[48:63], v[80:83], v[64:67], v[16:31]\n\t" \
    "v_exp_f32_e32 v32, v32\n\t" \
    "v_exp_f32_e32 v33, v33\n\t" \
    "v_exp_f32_e32 v34, v34\n\t" \
    "v_smfmac_f32_16x16x64_bf16 v[76:79], v[72:75], v[104:111], v174\n\t" \
    "v_exp_f32_e32 v35, v35\n\t" \
    "v_exp_f32_e32 v36, v36\n\t" \
    "v_mfma_f32_32x32x16_bf16 v[48:63], v[84:87], v[68:71], v[48:63]\n\t" \
    "ds_read_b128 v[80:83], v113 offset:8192\n\t" \
    "ds_read_b128 v[84:87], v113 offset:9216\n\t" \
    "v_cvt_pk_bf16_f32 v104, v32, v33\n\t" \
    "v_cvt_pk_bf16_f32 v105, v34, v35\n\t" \
    "v_exp_f32_e32 v37, v37\n\t" \
    "v_exp_f32_e32 v38, v38\n\t" \
    "v_exp_f32_e32 v39, v39\n\t" \
    "v_mfma_f32_32x32x16_bf16 v[0:15], v[92:95], v[108:111], v[0:15]\n\t" \
    "ds_read_b128 v[100:103], v113 offset:3072\n\t" \
    "ds_read_b128 v[96:99], v113 offset:6144\n\t" \
    "v_exp_f32_e32 v40, v40\n\t" \
    "v_cvt_pk_bf16_f32 v106, v36, v37\n\t" \
    "v_cvt_pk_bf16_f32 v107, v38, v39\n\t" \
    "v_exp_f32_e32 v41, v41\n\t" \
    "v_exp_f32_e32 v42, v42\n\t" \
    "v_exp_f32_e32 v43, v43\n\t" \
    "v_mfma_f32_32x32x16_bf16 v[0:15], v[88:91], v[104:107], v[0:15]\n\t" \
    "s_cbranch_vccz 22f\n\t" \
    "global_load_lds_dwordx4 v114, s[44:45] offset:2048 sc1\n\t" \
    "22:\n" \
    "v_exp_f32_e32 v44, v44\n\t" \
    "v_exp_f32_e32 v45, v45\n\t" \
    "v_exp_f32_e32 v46, v46\n\t" \
    "v_exp_f32_e32 v47, v47\n\t" \
    "v_cvt_pk_bf16_f32 v108, v40, v41\n\t" \
    "v_cvt_pk_bf16_f32 v109, v42, v43\n\t" \
    "v_cvt_pk_bf16_f32 v110, v44, v45\n\t" \
    "v_cvt_pk_bf16_f32 v111, v46, v47\n\t" \
    "s_waitcnt lgkmcnt(0)\n\t" \
    "v_mfma_f32_32x32x16_bf16 v[32:47], v[80:83], v[64:67], v[16:31]\n\t" \
    "v_exp_f32_e32 v48, v48\n\t" \
    "v_exp_f32_e32 v49, v49\n\t" \
    "v_exp_f32_e32 v50, v50\n\t" \
    "v_smfmac_f32_16x16x64_bf16 v[76:79], v[72:75], v[104:111], v174\n\t" \
    "v_exp_f32_e32 v51, v51\n\t" \
    "v_exp_f32_e32 v52, v52\n\t" \
    "v_mfma_f32_32x32x16_bf16 v[32:47], v[84:87], v[68:71], v[32:47]\n\t" \
    "ds_read_b128 v[80:83], v113 offset:12288\n\t" \
    "ds_read_b128 v[84:87], v113 offset:13312\n\t" \
    "v_cvt_pk_bf16_f32 v104, v48, v49\n\t" \
    "v_cvt_pk_bf16_f32 v105, v50, v51\n\t" \
    "v_exp_f32_e32 v53, v53\n\t" \
    "v_exp_f32_e32 v54, v54\n\t" \
    "v_exp_f32_e32 v55, v55\n\t" \
    "v_mfma_f32_32x32x16_bf16 v[0:15], v[100:103], v[108:111], v[0:15]\n\t" \
    "ds_read_b128 v[92:95], v113 offset:7168\n\t" \
    "ds_read_b128 v[88:91], v113 offset:10240\n\t" \
    "v_exp_f32_e32 v56, v56\n\t" \
    "v_cvt_pk_bf16_f32 v106, v52, v53\n\t" \
    "v_cvt_pk_bf16_f32 v107, v54, v55\n\t" \
    "v_exp_f32_e32 v57, v57\n\t" \
    "v_exp_f32_e32 v58, v58\n\t" \
    "v_exp_f32_e32 v59, v59\n\t" \
    "v_mfma_f32_32x32x16_bf16 v[0:15], v[96:99], v[104:107], v[0:15]\n\t" \
    "s_cbranch_vccz 23f\n\t" \
    "global_load_lds_dwordx4 v114, s[44:45] offset:3072 sc1\n\t" \
    "global_load_dwordx2 v[120:121], v170, %[ws] sc1\n\t" \
    "23:\n" \
    "v_exp_f32_e32 v60, v60\n\t" \
    "v_exp_f32_e32 v61, v61\n\t" \
    "v_exp_f32_e32 v62, v62\n\t" \
    "v_exp_f32_e32 v63, v63\n\t" \
    "v_cvt_pk_bf16_f32 v108, v56, v57\n\t" \
    "v_cvt_pk_bf16_f32 v109, v58, v59\n\t" \
    "v_cvt_pk_bf16_f32 v110, v60, v61\n\t" \
    "v_cvt_pk_bf16_f32 v111, v62, v63\n\t" \
    "s_setprio 1\n\t" \
    "s_waitcnt lgkmcnt(0)\n\t" \
    "v_mfma_f32_32x32x16_bf16 v[48:63], v[80:83], v[64:67], v[16:31]\n\t" \
    "v_exp_f32_e32 v32, v32\n\t" \
    "v_exp_f32_e32 v33, v33\n\t" \
    "v_exp_f32_e32 v34, v34\n\t" \
    "v_smfmac_f32_16x16x64_bf16 v[76:79], v[72:75], v[104:111], v174\n\t" \
    "v_exp_f32_e32 v35, v35\n\t" \
    "v_exp_f32_e32 v36, v36\n\t" \
    "v_mfma_f32_32x32x16_bf16 v[48:63], v[84:87], v[68:71], v[48:63]\n\t" \
    "ds_read_b128 v[80:83], v113 offset:16384\n\t" \
    "ds_read_b128 v[84:87], v113 offset:17408\n\t" \
    "v_cvt_pk_bf16_f32 v104, v32, v33\n\t" \
    "v_cvt_pk_bf16_f32 v105, v34, v35\n\t" \
    "v_exp_f32_e32 v37, v37\n\t" \
    "v_exp_f32_e32 v38, v38\n\t" \
    "v_exp_f32_e32 v39, v39\n\t" \
    "v_mfma_f32_32x32x16_bf16 v[0:15], v[92:95], v[108:111], v[0:15]\n\t" \
    "ds_read_b128 v[100:103], v113 offset:11264\n\t" \
    "ds_read_b128 v[96:99], v113 offset:14336\n\t" \
    "v_exp_f32_e32 v40, v40\n\t" \
    "v_cvt_pk_bf16_f32 v106, v36, v37\n\t" \
    "v_cvt_pk_bf16_f32 v107, v38, v39\n\t" \
    "v_exp_f32_e32 v41, v41\n\t" \
    "v_exp_f32_e32 v42, v42\n\t" \
    "v_exp_f32_e32 v43, v43\n\t" \
    "v_mfma_f32_32x32x16_bf16 v[0:15], v[88:91], v[104:107], v[0:15]\n\t" \
    "s_add_u32 %[dst], %[dst], 0x8000\n\t" \
    "s_cmp_eq_u32 %[dst], %[dend]\n\t" \
    "s_cselect_b32 %[dst], %[dbeg], %[dst]\n\t" \
    "s_add_u32 %[nx], %[nx], 0x8000\n\t" \
    "s_cmp_eq_u32 %[nx], 0x18000\n\t" \
    "s_cselect_b32 %[nx], 0, %[nx]\n\t" \
    "s_bitcmp1_b32 %[hk], %[cnt]\n\t" \
    "s_cbranch_scc1 40f\n\t" \
    "24:\n" \
    "v_exp_f32_e32 v44, v44\n\t" \
    "v_exp_f32_e32 v45, v45\n\t" \
    "v_exp_f32_e32 v46, v46\n\t" \
    "v_exp_f32_e32 v47, v47\n\t" \
    "v_cvt_pk_bf16_f32 v108, v40, v41\n\t" \
    "v_cvt_pk_bf16_f32 v109, v42, v43\n\t" \
    "v_cvt_pk_bf16_f32 v110, v44, v45\n\t" \
    "v_cvt_pk_bf16_f32 v111, v46, v47\n\t" \
    "s_waitcnt lgkmcnt(0)\n\t" \
    "v_mfma_f32_32x32x16_bf16 v[32:47], v[80:83], v[64:67], v[16:31]\n\t" \
    "v_exp_f32_e32 v48, v48\n\t" \
    "v_exp_f32_e32 v49, v49\n\t" \
    "v_exp_f32_e32 v50, v50\n\t" \
    "v_smfmac_f32_16x16x64_bf16 v[76:79], v[72:75], v[104:111], v174\n\t" \
    "v_exp_f32_e32 v51, v51\n\t" \
    "v_exp_f32_e32 v52, v52\n\t" \
    "v_mfma_f32_32x32x16_bf16 v[32:47], v[84:87], v[68:71], v[32:47]\n\t" \
    "ds_read_b128 v[80:83], v113 offset:20480\n\t" \
    "ds_read_b128 v[84:87], v113 offset:21504\n\t" \
    "v_cvt_pk_bf16_f32 v104, v48, v49\n\t" \
    "v_cvt_pk_bf16_f32 v105, v50, v51\n\t" \
    "v_exp_f32_e32 v53, v53\n\t" \
    "v_exp_f32_e32 v54, v54\n\t" \
    "v_exp_f32_e32 v55, v55\n\t" \
    "v_mfma_f32_32x32x16_bf16 v[0:15], v[100:103], v[108:111], v[0:15]\n\t" \
    "ds_read_b128 v[92:95], v113 offset:15360\n\t" \
    "ds_read_b128 v[88:91], v113 offset:18432\n\t" \
    "v_exp_f32_e32 v56, v56\n\t" \
    "v_cvt_pk_bf16_f32 v106, v52, v53\n\t" \
    "v_cvt_pk_bf16_f32 v107, v54, v55\n\t" \
    "v_exp_f32_e32 v57, v57\n\t" \
    "v_exp_f32_e32 v58, v58\n\t" \
    "v_exp_f32_e32 v59, v59\n\t" \
    "v_mfma_f32_32x32x16_bf16 v[0:15], v[96:99], v[104:107], v[0:15]\n\t" \
    "s_bitcmp1_b32 %[hk], %[cnt]\n\t" \
    "s_cbranch_scc1 41f\n\t" \
    "25:\n" \
    "v_exp_f32_e32 v60, v60\n\t" \
    "v_exp_f32_e32 v61, v61\n\t" \
    "v_exp_f32_e32 v62, v62\n\t" \
    "v_exp_f32_e32 v63, v63\n\t" \
    "v_cvt_pk_bf16_f32 v108, v56, v57\n\t" \
    "v_cvt_pk_bf16_f32 v109, v58, v59\n\t" \
    "v_cvt_pk_bf16_f32 v110, v60, v61\n\t" \
    "v_cvt_pk_bf16_f32 v111, v62, v63\n\t" \
    "s_setprio 0\n\t" \
    "s_waitcnt lgkmcnt(0)\n\t" \
    "v_mfma_f32_32x32x16_bf16 v[48:63], v[80:83], v[64:67], v[16:31]\n\t" \
    "v_exp_f32_e32 v32, v32\n\t" \
    "v_exp_f32_e32 v33, v33\n\t" \
    "v_exp_f32_e32 v34, v34\n\t" \
    "v_smfmac_f32_16x16x64_bf16 v[76:79], v[72:75], v[104:111], v174\n\t" \
    "v_exp_f32_e32 v35, v35\n\t" \
    "v_exp_f32_e32 v36, v36\n\t" \
    "v_mfma_f32_32x32x16_bf16 v[48:63], v[84:87], v[68:71], v[48:63]\n\t" \
    "ds_read_b128 v[80:83], v113 offset:24576\n\t" \
    "ds_read_b128 v[84:87], v113 offset:25600\n\t" \
    "v_cvt_pk_bf16_f32 v104, v32, v33\n\t" \
    "v_cvt_pk_bf16_f32 v105, v34, v35\n\t" \
    "v_exp_f32_e32 v37, v37\n\t" \
    "v_exp_f32_e32 v38, v38\n\t" \
    "v_exp_f32_e32 v39, v39\n\t" \
    "v_mfma_f32_32x32x16_bf16 v[0:15], v[92:95], v[108:111], v[0:15]\n\t" \
    "ds_read_b128 v[100:103], v113 offset:19456\n\t" \
    "ds_read_b128 v[96:99], v113 offset:22528\n\t" \
    "v_exp_f32_e32 v40, v40\n\t" \
    "v_cvt_pk_bf16_f32 v106, v36, v37\n\t" \
    "v_cvt_pk_bf16_f32 v107, v38, v39\n\t" \
    "v_exp_f32_e32 v41, v41\n\t" \
    "v_exp_f32_e32 v42, v42\n\t" \
    "v_exp_f32_e32 v43, v43\n\t" \
    "v_mfma_f32_32x32x16_bf16 v[0:15], v[88:91], v[104:107], v[0:15]\n\t" \
    "s_and_b32 s40, s48, 63\n\t" \
    "s_lshl_b32 s40, s40, 12\n\t" \
    "s_add_u32 s44, %[srclo], s40\n\t" \
    "s_addc_u32 s45, %[srchi], 0\n\t" \
    "s_lshr_b64 s[48:49], s[48:49], 6\n\t" \
    "s_add_u32 s46, s46, 1\n\t" \
    "s_bitcmp1_b32 %[hk], %[cnt]\n\t" \
    "s_cbranch_scc1 42f\n\t" \
    "26:\n" \
    "v_exp_f32_e32 v44, v44\n\t" \
    "v_exp_f32_e32 v45, v45\n\t" \
    "v_exp_f32_e32 v46, v46\n\t" \
    "v_exp_f32_e32 v47, v47\n\t" \
    "v_cvt_pk_bf16_f32 v108, v40, v41\n\t" \
    "v_cvt_pk_bf16_f32 v109, v42, v43\n\t" \
    "v_cvt_pk_bf16_f32 v110, v44, v45\n\t" \
    "v_cvt_pk_bf16_f32 v111, v46, v47\n\t" \
    "s_waitcnt lgkmcnt(0)\n\t" \
    "v_mfma_f32_32x32x16_bf16 v[32:47], v[80:83], v[64:67], v[16:31]\n\t" \
    "v_exp_f32_e32 v48, v48\n\t" \
    "v_exp_f32_e32 v49, v49\n\t" \
    "v_exp_f32_e32 v50, v50\n\t" \
    "v_smfmac_f32_16x16x64_bf16 v[76:79], v[72:75], v[104:111], v174\n\t" \
    "v_exp_f32_e32 v51, v51\n\t" \
    "v_exp_f32_e32 v52, v52\n\t" \
    "v_mfma_f32_32x32x16_bf16 v[32:47], v[84:87], v[68:71], v[32:47]\n\t" \
    "ds_read_b128 v[80:83], v113 offset:28672\n\t" \
    "ds_read_b128 v[84:87], v113 offset:29696\n\t" \
    "v_cvt_pk_bf16_f32 v104, v48, v49\n\t" \
    "v_cvt_pk_bf16_f32 v105, v50, v51\n\t" \
    "v_exp_f32_e32 v53, v53\n\t" \
    "v_exp_f32_e32 v54, v54\n\t" \
    "v_exp_f32_e32 v55, v55\n\t" \
    "v_mfma_f32_32x32x16_bf16 v[0:15], v[100:103], v[108:111], v[0:15]\n\t" \
    "ds_read_b128 v[92:95], v113 offset:23552\n\t" \
    "ds_read_b128 v[88:91], v113 offset:26624\n\t" \
    "v_exp_f32_e32 v56, v56\n\t" \
    "v_cvt_pk_bf16_f32 v106, v52, v53\n\t" \
    "v_cvt_pk_bf16_f32 v107, v54, v55\n\t" \
    "v_exp_f32_e32 v57, v57\n\t" \
    "v_exp_f32_e32 v58, v58\n\t" \
    "v_exp_f32_e32 v59, v59\n\t" \
    "v_mfma_f32_32x32x16_bf16 v[0:15], v[96:99], v[104:107], v[0:15]\n\t" \
    "s_bitcmp1_b32 %[hk], %[cnt]\n\t" \
    "s_cbranch_scc1 43f\n\t" \
    "27:\n" \
    "v_mov_b32_e32 v112, v113\n\t" \
    "v_add_u32_e32 v113, %[nx], v115\n\t" \
    "s_sub_u32 %[cnt], %[cnt], 1\n\t" \
    "s_cmp_lg_u32 %[cnt], 0\n\t" \
    "v_exp_f32_e32 v60, v60\n\t" \
    "v_exp_f32_e32 v61, v61\n\t" \
    "v_exp_f32_e32 v62, v62\n\t" \
    "v_exp_f32_e32 v63, v63\n\t" \
    "v_cvt_pk_bf16_f32 v108, v56, v57\n\t" \
    "v_cvt_pk_bf16_f32 v109, v58, v59\n\t" \
    "v_cvt_pk_bf16_f32 v110, v60, v61\n\t" \
    "v_cvt_pk_bf16_f32 v111, v62, v63\n\t" \
    "s_cbranch_scc1 1b\n\t" \
    "7:\n" \
    "s_setprio 0\n\t" \
    "s_waitcnt lgkmcnt(0)\n\t" \
    "v_mfma_f32_32x32x16_bf16 v[48:63], v[80:83], v[64:67], v[16:31]\n\t" \
    "v_exp_f32_e32 v32, v32\n\t" \
    "v_exp_f32_e32 v33, v33\n\t" \
    "v_exp_f32_e32 v34, v34\n\t" \
    "v_smfmac_f32_16x16x64_bf16 v[76:79], v[72:75], v[104:111], v174\n\t" \
    "v_exp_f32_e32 v35, v35\n\t" \
    "v_exp_f32_e32 v36, v36\n\t" \
    "v_mfma_f32_32x32x16_bf16 v[48:63], v[84:87], v[68:71], v[48:63]\n\t" \
    "v_cvt_pk_bf16_f32 v104, v32, v33\n\t" \
    "v_cvt_pk_bf16_f32 v105, v34, v35\n\t" \
    "v_exp_f32_e32 v37, v37\n\t" \
    "v_exp_f32_e32 v38, v38\n\t" \
    "v_exp_f32_e32 v39, v39\n\t" \
    "v_mfma_f32_32x32x16_bf16 v[0:15], v[92:95], v[108:111], v[0:15]\n\t" \
    "ds_read_b128 v[100:103], v112 offset:27648\n\t" \
    "ds_read_b128 v[96:99], v112 offset:30720\n\t" \
    "v_exp_f32_e32 v40, v40\n\t" \
    "v_cvt_pk_bf16_f32 v106, v36, v37\n\t" \
    "v_cvt_pk_bf16_f32 v107, v38, v39\n\t" \
    "v_exp_f32_e32 v41, v41\n\t" \
    "v_exp_f32_e32 v42, v42\n\t" \
    "v_exp_f32_e32 v43, v43\n\t" \
    "v_mfma_f32_32x32x16_bf16 v[0:15], v[88:91], v[104:107], v[0:15]\n\t" \
    "v_exp_f32_e32 v44, v44\n\t" \
    "v_exp_f32_e32 v45, v45\n\t" \
    "v_exp_f32_e32 v46, v46\n\t" \
    "v_exp_f32_e32 v47, v47\n\t" \
    "v_cvt_pk_bf16_f32 v108, v40, v41\n\t" \
    "v_cvt_pk_bf16_f32 v109, v42, v43\n\t" \
    "v_cvt_pk_bf16_f32 v110, v44, v45\n\t" \
    "v_cvt_pk_bf16_f32 v111, v46, v47\n\t" \
    "s_waitcnt lgkmcnt(0)\n\t" \
    "v_exp_f32_e32 v48, v48\n\t" \
    "v_exp_f32_e32 v49, v49\n\t" \
    "v_exp_f32_e32 v50, v50\n\t" \
    "v_smfmac_f32_16x16x64_bf16 v[76:79], v[72:75], v[104:111], v174\n\t" \
    "v_exp_f32_e32 v51, v51\n\t" \
    "v_exp_f32_e32 v52, v52\n\t" \
    "v_cvt_pk_bf16_f32 v104, v48, v49\n\t" \
    "v_cvt_pk_bf16_f32 v105, v50, v51\n\t" \
    "v_exp_f32_e32 v53, v53\n\t" \
    "v_exp_f32_e32 v54, v54\n\t" \
    "v_exp_f32_e32 v55, v55\n\t" \
    "v_mfma_f32_32x32x16_bf16 v[0:15], v[100:103], v[108:111], v[0:15]\n\t" \
    "ds_read_b128 v[92:95], v112 offset:31744\n\t" \
    "v_exp_f32_e32 v56, v56\n\t" \
    "v_cvt_pk_bf16_f32 v106, v52, v53\n\t" \
    "v_cvt_pk_bf16_f32 v107, v54, v55\n\t" \
    "v_exp_f32_e32 v57, v57\n\t" \
    "v_exp_f32_e32 v58, v58\n\t" \
    "v_exp_f32_e32 v59, v59\n\t" \
    "v_mfma_f32_32x32x16_bf16 v[0:15], v[96:99], v[104:107], v[0:15]\n\t" \
    "v_exp_f32_e32 v60, v60\n\t" \
    "v_exp_f32_e32 v61, v61\n\t" \
    "v_exp_f32_e32 v62, v62\n\t" \
    "v_exp_f32_e32 v63, v63\n\t" \
    "v_cvt_pk_bf16_f32 v108, v56, v57\n\t" \
    "v_cvt_pk_bf16_f32 v109, v58, v59\n\t" \
    "v_cvt_pk_bf16_f32 v110, v60, v61\n\t" \
    "v_cvt_pk_bf16_f32 v111, v62, v63\n\t" \
    "s_waitcnt lgkmcnt(0)\n\t" \
    "s_nop 0\n\t" \
    "v_mfma_f32_32x32x16_bf16 v[0:15], v[92:95], v[108:111], v[0:15]\n\t" \
    "v_smfmac_f32_16x16x64_bf16 v[76:79], v[72:75], v[104:111], v174\n\t" \
    "s_nop 15\n\t" \
    "s_branch 99f\n\t" \
    "9:\n" \
    "s_mov_b32 s47, 0x40000\n\t" \
    "10:\n" \
    "s_sub_u32 s47, s47, 1\n\t" \
    "s_cbranch_scc1 12b\n\t" \
    "global_load_dwordx2 v[120:121], v170, %[ws] sc1\n\t" \
    "s_waitcnt vmcnt(0)\n\t" \
    "v_cmp_eq_u64_e64 s[42:43], v[120:121], v[140:141]\n\t" \
    "s_nop 4\n\t" \
    "s_bitcmp1_b64 s[42:43], s46\n\t" \
    "s_cbranch_scc1 12b\n\t" \
    "s_sleep 4\n\t" \
    "s_branch 10b\n\t" \
    "50:\n" \
    "global_store_dwordx4 v171, v[176:179], %[ws] offset:-3072 sc1\n\t" \
    "global_store_dwordx4 v171, v[180:183], %[ws] offset:-2944 sc1\n\t" \
    "global_store_dwordx4 v172, v[184:187], %[ws] offset:-1024 sc1\n\t" \
    "global_store_dwordx4 v172, v[188:191], %[ws] offset:-768 sc1\n\t" \
    "s_branch 24b\n\t" \
    "51:\n" \
    "global_store_dwordx4 v171, v[192:195], %[ws] offset:1024 sc1\n\t" \
    "global_store_dwordx4 v171, v[196:199], %[ws] offset:1152 sc1\n\t" \
    "global_store_dwordx4 v172, v[200:203], %[ws] offset:3072 sc1\n\t" \
    "global_store_dwordx4 v172, v[204:207], %[ws] offset:3328 sc1\n\t" \
    "s_branch 25b\n\t" \
    "40:\n" \
    "s_cmp_eq_u32 %[cnt], 7\n\t" \
    "s_cbranch_scc1 50b\n\t" \
    "s_add_u32 m0, %[wdst], 0\n\t" \
    "s_nop 0\n\t" \
    "global_load_lds_dwordx4 v116, %[wo01]\n\t" \
    "global_load_lds_dwordx4 v117, %[wo01] offset:1024\n\t" \
    "v_add_u32_e32 v116, 0x20000, v116\n\t" \
    "v_add_u32_e32 v117, 0x20000, v117\n\t" \
    "s_branch 24b\n\t" \
    "41:\n" \
    "s_cmp_eq_u32 %[cnt], 7\n\t" \
    "s_cbranch_scc1 51b\n\t" \
    "s_add_u32 m0, %[wdst], 16384\n\t" \
    "s_nop 0\n\t" \
    "global_load_lds_dwordx4 v116, %[wo01]\n\t" \
    "global_load_lds_dwordx4 v117, %[wo01] offset:1024\n\t" \
    "v_subrev_u32_e32 v116, 0x20000, v116\n\t" \
    "v_subrev_u32_e32 v117, 0x20000, v117\n\t" \
    "s_branch 25b\n\t" \
    "42:\n" \
    "s_cmp_eq_u32 %[cnt], 7\n\t" \
    "s_cbranch_scc1 26b\n\t" \
    "s_add_u32 m0, %[wdst], 32768\n\t" \
    "s_nop 0\n\t" \
    "global_load_lds_dwordx4 v116, %[wo23]\n\t" \
    "global_load_lds_dwordx4 v117, %[wo23] offset:1024\n\t" \
    "v_add_u32_e32 v116, 0x20000, v116\n\t" \
    "v_add_u32_e32 v117, 0x20000, v117\n\t" \
    "s_branch 26b\n\t" \
    "43:\n" \
    "s_cmp_eq_u32 %[cnt], 7\n\t" \
    "s_cbranch_scc1 27b\n\t" \
    "s_add_u32 m0, %[wdst], 49152\n\t" \
    "s_nop 0\n\t" \
    "global_load_lds_dwordx4 v116, %[wo23]\n\t" \
    "global_load_lds_dwordx4 v117, %[wo23] offset:1024\n\t" \
    "global_load_dword v168, v119, %[wo]\n\t" \
    "s_branch 27b\n\t" \
    "30:\n" \
    "s_cmp_eq_u32 %[cnt], 1\n\t" \
    "s_cbranch_scc0 31f\n\t" \
    "s_mov_b64 vcc, 0\n\t" \
    "s_bitcmp1_b32 %[mode], 1\n\t" \
    "s_cbranch_scc1 7b\n\t" \
    "s_branch 2b\n\t" \
    "31:\n" \
    "s_cmp_eq_u32 %[cnt], 6\n\t" \
    "s_cbranch_scc0 33f\n\t" \
    "global_store_dwordx2 v175, v[140:141], %[ws] sc1\n\t" \
    "s_branch 2b\n\t" \
    "33:\n" \
    "s_cmp_eq_u32 %[cnt], 5\n\t" \
    "s_cbranch_scc0 34f\n\t" \
    "global_store_dwordx2 v118, v[140:141], %[ws] sc1\n\t" \
    "s_branch 2b\n\t" \
    "34:\n" \
    "s_cmp_eq_u32 %[cnt], 3\n\t" \
    "s_cbranch_scc0 35f\n\t" \
    "global_load_dwordx2 v[168:169], v142, %[ws] sc1\n\t" \
    "s_branch 2b\n\t" \
    "35:\n" \
    "v_cmp_eq_u64_e64 s[42:43], v[168:169], v[140:141]\n\t" \
    "s_nop 4\n\t" \
    "s_cmp_eq_u64 s[42:43], -1\n\t" \
    "s_cbranch_scc0 2b\n\t" \
    "global_load_dwordx4 v[122:125], v143, %[ws] offset:-2048 sc1\n\t" \
    "global_load_dwordx4 v[126:129], v143, %[ws] offset:-1024 sc1\n\t" \
    "global_load_dwordx4 v[130:133], v143, %[ws] offset:0 sc1\n\t" \
    "global_load_dwordx4 v[134:137], v143, %[ws] offset:1024 sc1\n\t" \
    "global_load_dwordx2 v[138:139], v143, %[ws] offset:2048 sc1\n\t" \
    "s_bitset1_b32 %[mode], 2\n\t" \
    "s_branch 2b\n\t" \
    "99:\n"
#define ATT_TMP "v48", "v49", "v50", "v51", "v52", "v53", "v54", "v55", "v56", "v57", "v58", "v59", "v60", "v61", "v62", "v63", "v96", "v97", "v98", "v99", "v100", "v101", "v102", "v103"

constexpr int K1_X = 0, K1_W = 81920, K1_WB = 24576, K1_EP = 155648;
constexpr int K2_SLOT = 32768, K2_QX = 3 * K2_SLOT, K2_FL = 147456;
constexpr int LDS_BYTES = 163840;
constexpr int p3_wslot(int t) { return (t % 6) < 4 ? 98304 + (t % 6) * 16384 : 65536 + ((t % 6) - 4) * 16384; }
constexpr int p3_allow(int t) { return (((t < 2 || t - 2 >= 1) && t + 3 < 16) ? 4 : 0) + (t + 3 < 16 ? 4 : 0) + ((t >= 1 && t + 5 < 16) ? 4 : 0); }

__global__ __launch_bounds__(512, 2) void mk_fused(const u16* __restrict__ xA, const u16* __restrict__ wA, const u16* __restrict__ woA,
                                                   const u16* __restrict__ xB, const u16* __restrict__ wB, const u16* __restrict__ woB, char* ws0, u16* __restrict__ out) {
    char* ws = ws0;
    __shared__ __attribute__((aligned(1024))) char smem[LDS_BYTES];
    const unsigned lds0 = (unsigned)(uintptr_t)(LAS char*)smem;
    const int wid = __builtin_amdgcn_readfirstlane(threadIdx.x >> 6);
    int lane = threadIdx.x & 63;
#define BRX 1
    const int b = blockIdx.x, xcd = b & 7, j = b >> 3, br = (xcd >> 2) ^ BRX, qd = xcd & 3;
    const int mt = (qd & 1) * 4 + (j & 3), hp = (qd >> 1) * 8 + (j >> 2);
    const int mt3 = qd * 4 + (j & 3), nt3 = j >> 2;
    u64 tag;
    u32x4 fq[2][2], fk[2][2], fv[2][2];
    f32x16 band;
#define RBLK(k, w) ((((k) + (w) - (((k) + (w) >= 7) ? 7 : 0)) + (((k) + (w) - (((k) + (w) >= 7) ? 7 : 0)) >= mt)) * 8 + (k) + ((k) + (w) >= 7))
    u64 tabf;
    unsigned c_fl, c_pv, c_wb, c_tw, c_aw, c_fo;
#define PRECOMPUTE(BAND) do { const int g_ = wid >> 2, wi_ = wid & 3, c16_ = lane & 15, g4_ = lane >> 4; \
        if (BAND) band = band_mask(lane); \
        tabf = (u64)(mt * 8 + wid) << 42; \
        _Pragma("unroll") for (int k = 0; k < 7; ++k) tabf |= (u64)RBLK(k, wid) << (6 * k); \
        c_fl = (unsigned)WS_FK + (2 * hp + br) * 32 * FS + (((unsigned)(tabf >> (6 * (lane & 7))) & 63u) >> 1) * FS;        \
        c_wb = g4_ * 2048 + ((c16_ ^ (g4_ << 1)) << 4); \
        { const int i = (mt3 * 8 + wid) * 12 + ((lane * 3) >> 4); c_tw = ((((4 + (i >> 7) + 2 * nt3) & 15) * 64 + ((i & 127) >> 1)) * 2048 + (i & 1) * 128); } \
        { const int q4 = c16_ >> 2, p = lane & 3, k = 16 * (g4_ >> 1) + 4 * (g4_ & 1) + q4; \
          c_aw = lds0 + p3_wslot(0) + g_ * 16384 + k * 256 + (((8 * (wi_ & 1) + (p >> 1)) ^ ((k & 7) << 1)) << 4) + (p & 1) * 8; } \
        c_fo = (unsigned)WS_FO + ((br * 64 + mt3 * 4 + wi_) * 32 + 4 * (c16_ >> 1) + 2 * (1 - g_) + (lane & 1)) * FS; \
        asm volatile("" : "+v"(band), "+s"(tabf), "+v"(c_fl), "+v"(c_wb), "+v"(c_tw), "+v"(c_aw), "+v"(c_fo)); } while (0)
    {
        const int g = wid >> 2, wi = wid & 3, mw = wi >> 1, hd = wi & 1;
        if (wid == 7) {
            const char* pc; unsigned len;
            asm volatile("s_getpc_b64 %0\n.Lmk_code_begin:\n\ts_mov_b32 %1, .Lmk_code_end-.Lmk_code_begin" : "=s"(pc), "=s"(len));
            const unsigned off = (unsigned)(j * 64 + lane) * 64u;
            asm volatile("s_mov_b32 m0, %2\n\ts_nop 0\n\tglobal_load_lds_dword %0, %1" :: "v"(off < len ? off : 0u), "s"(pc), "s"(lds0 + 159744) : "memory");
        }
        unsigned vx[4], vw;
        { const unsigned r8 = lane >> 3, c8 = lane & 7, b1 = (lane >> 4) & 1;
          vw = (wid * 8 + r8) * 6144 + ((c8 ^ ((b1 | ((wid & 1) << 1)) << 1)) << 4);
          const unsigned st = (16384u << br) - 1024u;
          vx[0] = (((r8 << br) + (br ? (wi >> 1) * 64 + (wi & 1) : wi * 32)) << 11) + ((c8 ^ (b1 | ((lane >> 5) << 2))) << 4);
          vx[1] = vx[0] + st; vx[2] = (vx[0] ^ 32) + 2 * st; vx[3] = vx[2] + st; }
        asm volatile("" : "+v"(vx[0]), "+v"(vx[1]), "+v"(vx[2]), "+v"(vx[3]), "+v"(vw));
        SBAR();
        const char* x = (const char*)(br ? xB : xA) + (size_t)mt * 256 * 2048;
        const char* w = (const char*)(br ? wB : wA) + hp * 128;
        const unsigned xdst_o = lds0 + K1_X + wi * 4096;
        const unsigned wdst = lds0 + K1_W + wid * 1024;
        const char* xo = x + (size_t)g * 128 * 2048;
#define ROTM 4
        const int kt0 = ROTM * (j & 3);
#define STAGE_X(kt, dst) do { const char* s_ = xo + (((kt) + kt0) & 15) * 128; glds_run<0>(s_, vx[0], dst); glds_run<1>(s_, vx[1], 0); glds_run<2>(s_, vx[2], 0); glds_run<3>(s_, vx[3], 0); } while (0)
#define STAGE_W(kt, buf) do { const char* s_ = w + (size_t)(((kt) + kt0) & 15) * 64 * 6144; glds_sec<0>(s_, vw, wdst + (buf) * K1_WB); glds_sec<1>(s_, vw, wdst + (buf) * K1_WB); glds_sec<2>(s_, vw, wdst + (buf) * K1_WB); } while (0)
        STAGE_W(0, 0); STAGE_X(0, xdst_o + g * 16384); STAGE_W(1, 1); STAGE_X(1, xdst_o + (2 + g) * 16384);
        if (wid == 4) glds16_dev(ws, (unsigned)WS_EP, lds0 + K1_EP);
        const int c16 = lane & 15, g4 = lane >> 4;
        unsigned ax[2], aw[2];
        ax[0] = lds0 + K1_X + g * 16384 + (mw * 64 + 16 * (c16 >> 3) + (c16 & 7)) * 128 + ((g4 ^ (((c16 >> 1) & 1) | ((c16 >> 3) << 1) | (((c16 >> 2) & 1) << 2))) << 4);
        ax[1] = ax[0] ^ 64;
        { const int q4 = c16 >> 2, p = lane & 3;
          aw[0] = lds0 + K1_W + (8 * g4 + q4) * 128 + (((hd * 4 + (p >> 1)) ^ ((((q4 >> 1) & 1) | ((g4 & 1) << 1)) << 1)) << 4) + (p & 1) * 8; aw[1] = aw[0] ^ 32; }
        if (g == 1) WAITBAR_(11); else WAITBAR_(7);
        if (g == 1) { PRECOMPUTE(true); WAITBAR_(7); }
        f32x4 acc[4][6];
#pragma unroll
        for (int m = 0; m < 4; ++m)
#pragma unroll
            for (int n = 0; n < 6; ++n) acc[m][n] = f32x4{};
        SBAR();
        unsigned wr_ = 0, xr_ = g * 16384, dw = wdst + 2 * K1_WB, dx = xdst_o + (g ? 0 : 4 * 16384);
        int tk = (2 + kt0) & 15;
        const char* sx = xo + tk * 128; const char* sw = w + (size_t)tk * 64 * 6144;
#define RDX(m) do { xf[m][0] = ldsrd128(ax[0] + ((m) >> 1) * 4096 + ((m) & 1) * 1024); xf[m][1] = ldsrd128(ax[1] + ((m) >> 1) * 4096 + ((m) & 1) * 1024); } while (0)
#define RDW(n, ks) wf[n][ks] = cat8(ldstr(aw[(n) & 1] + ((n) >> 1) * 8192 + (ks) * 4096), ldstr(aw[(n) & 1] + ((n) >> 1) * 8192 + (ks) * 4096 + 512))
#define MM(m, n, k) do { if ((n) < 4) acc[m][n] = MFMA16(wf[n][k], xf[m][k], acc[m][n]); else acc[m][n] = MFMA16(xf[m][k], wf[n][k], acc[m][n]); } while (0)
#define P1_MMA() _Pragma("unroll") for (int m = 0; m < 4; ++m) _Pragma("unroll") for (int n = 0; n < 6; ++n) { MM(m, n, 0); MM(m, n, 1); CHAIN(); }
#define P1_TILE(DO, WAIT1, WAIT2) do { \
            bf16x8 xf[4][2], wf[6][2]; \
            RDX(0); RDW(0, 0); if (DO) glds_sec<0>(sw, vw, dw); \
            RDW(0, 1); RDW(1, 0); if (DO) glds_sec<1>(sw, vw, dw); \
            RDX(1); RDW(1, 1); if (DO) glds_sec<2>(sw, vw, dw); \
            RDW(2, 0); RDW(2, 1); if (DO) glds_run<0>(sx, vx[0], dx); \
            RDX(2); RDW(3, 0); if (DO) glds_run<1>(sx, vx[1], 0); \
            RDW(3, 1); RDW(4, 0); if (DO) glds_run<2>(sx, vx[2], 0); \
            RDX(3); RDW(4, 1); if (DO) glds_run<3>(sx, vx[3], 0); \
            RDW(5, 0); RDW(5, 1); \
            SBAR(); \
            {   \
              const unsigned d_ = (wr_ == 2 * K1_WB) ? (unsigned)(-2 * K1_WB) : (unsigned)K1_WB, e_ = (xr_ >= 3 * 16384) ? (unsigned)(-3 * 16384) : (unsigned)(2 * 16384); \
              dw = wdst + wr_; wr_ += d_; aw[0] += d_; aw[1] += d_; dx = xdst_o + ((xr_ == 4 * 16384) ? 0u : xr_ + 16384); xr_ += e_; ax[0] += e_; ax[1] += e_; \
              tk = (tk + 1) & 15; sx = xo + tk * 128; sw = w + (size_t)tk * 64 * 6144; \
              asm volatile("" : "+v"(aw[0]), "+v"(aw[1]), "+v"(ax[0]), "+v"(ax[1]), "+s"(sx), "+s"(sw)); } \
            WAIT1; \
            SBAR(); \
            __builtin_amdgcn_s_setprio(1); \
            P1_MMA(); \
            __builtin_amdgcn_s_setprio(0); \
            SBAR(); \
            WAIT2; \
            SBAR(); \
        } while (0)
#pragma unroll 1
        for (int t = 0; t < 14; ++t) P1_TILE(true, WAITBAR_(11), WAITBAR_(7));
        u32x2 ev;
        P1_TILE(false, ev = *(const LAS u32x2*)(uintptr_t)(lds0 + K1_EP); WAITBAR_(4), WAITBAR_(0));
#define MAKE_TAG() do { u64 e = ((u64)(unsigned)__builtin_amdgcn_readfirstlane(ev[1]) << 32) | (unsigned)__builtin_amdgcn_readfirstlane(ev[0]); \
          e += 0x9E3779B97F4A7C15ull; e = (e ^ (e >> 30)) * 0xBF58476D1CE4E5B9ull; e = (e ^ (e >> 27)) * 0x94D049BB133111EBull; tag = e ^ (e >> 31); asm volatile("" : "+s"(tag)); } while (0)
        P1_TILE(false, MAKE_TAG(); WAITBAR_(0), if (g == 0) asm volatile("s_barrier" ::: "memory"));
#undef MAKE_TAG
#undef P1_TILE
#undef RDW
#undef RDX
#undef STAGE_X
#undef STAGE_W
        FRESH();
#pragma unroll
        for (int mb = 0; mb < 2; ++mb)
#pragma unroll
            for (int u = 0; u < 2; ++u) {
                const f32x4 a = acc[2 * mb + u][0] * QSCALE, c = acc[2 * mb + u][1] * QSCALE;
                fq[mb][u] = u32x4{cvtpk(a[0], a[1]), cvtpk(a[2], a[3]), cvtpk(c[0], c[1]), cvtpk(c[2], c[3])};
                fk[mb][u] = pack44(acc[2 * mb + u][2], acc[2 * mb + u][3]);
                fv[mb][u] = pack44(acc[2 * mb][4 + u], acc[2 * mb + 1][4 + u]);
            }
    }
    const char* wo = (const char*)(br ? woB : woA) + nt3 * 256;
    const int k30 = 2 * nt3;
    {
        const int g = wid >> 2, wr = wid >> 1, hd = wid & 1;
        const int ph1 = 2 * hp + hd, pb1 = mt * 8 + wr * 2;
        const int head = 2 * hp + br, blk = mt * 8 + wid;
        const char* kv = ws + WS_KV + (size_t)head * 262144;
        unsigned l16 = lane * 16;
        const unsigned dbeg = lds0 + wid * 4096, lbase = lds0 + l16;
        const unsigned fk0 = (unsigned)WS_FK + head * 32 * FS;
        const unsigned fxo = (unsigned)WS_FX + ((hp * 8 + mt) * 8 + wid) * FS, xrec0 = (unsigned)WS_XP + ((hp * 8 + mt) * 8 + wid) * 5120 + 2048;
        const f32x16 z = {};
        f32x16 sa, negm, o;
        u32x16 qol, kvf, pub0, pub1; u32x8 ph;
        const unsigned sidx = (lane & 1) ? 0xEEEEEEEEu : 0x44444444u;
#define FIRST_TILE(base, q0, q1) do { const unsigned on_ = (lane & 2) ? 0u : 0x3F803F80u, e_ = (lane & 4) ? 0u : on_, f_ = (lane & 4) ? on_ : 0u; const u32x4 ones = {e_, e_, f_, f_}; \
            const bf16x8 k0_ = ldsrd128(base), k1_ = ldsrd128((base) + 1024); kvf = cat16(asu(ldsrd128((base) + 4096)), asu(ldsrd128((base) + 5120)), asu(ldsrd128((base) + 2048)), ones); \
            sa = MFMA32(k0_, q0, z); sa = MFMA32(k1_, q1, sa); float m_ = sa[0]; \
            _Pragma("unroll") for (int r = 1; r < 16; ++r) m_ = __builtin_fmaxf(m_, sa[r]); \
            m_ = -swap_max(m_); _Pragma("unroll") for (int r = 0; r < 16; ++r) { negm[r] = m_; sa[r] += m_; } \
            qol = cat16(asu(q0), asu(q1), ones, u32x4{0u, 0u, 0u, 0u}); } while (0)
#define L6_OUT "={v[0:15]}"(o), "+{v[32:47]}"(sa), "+{v[64:79]}"(qol), "+{v[80:95]}"(kvf), "={v[104:111]}"(ph)
#define L6C(HL, BASE) asm volatile(ATT_L6C : L6_OUT, "={v[144:159]}"(h0), "={v[160:167]}"(h1) \
            : "{v[16:31]}"(negm), "{v174}"(sidx), "{v112}"(BASE), "{v[140:143]}"(u32x4{0u, 0u, c_fl, (unsigned)WS_HALO + (mt * 32 + ph1) * 6144 + 3072 + l16}), \
              [ws] "s"(ws), [hl] "s"(HL), [fld] "s"(fld) : ATT_TMP, "scc", "memory")
        const unsigned own = (ph1 * 32 + mt * 4 + wr) * FS;
        const unsigned fld = lds0 + K2_FL + wid * 1024 + br * (2 * K2_SLOT + 4096 - K2_FL + wid * 11264);
        c_pv = (lane >> 5) * 1024 + ((lane & 15) + 32 * ((lane >> 4) & 1)) * 16;
        const unsigned pv = c_pv, pq = pv + (lane & 8) * 16;
        if (br == 0) {
            const char* kp = ws + WS_KV + (size_t)((ph1 * 64 + pb1) * 4) * 1024;
#define PUB_KV() do { st16_dev<0>(kp, pq, fk[0][0]); st16_dev<128>(kp, pq, fk[0][1]); st16_dev<2048>(kp, pv, fv[0][0]); st16_dev<2304>(kp, pv, fv[0][1]); \
            st16_dev<0>(kp + 4096, pq, fk[1][0]); st16_dev<128>(kp + 4096, pq, fk[1][1]); st16_dev<2048>(kp + 4096, pv, fv[1][0]); st16_dev<2304>(kp + 4096, pv, fv[1][1]); } while (0)
            if (hd == 1) {
                const char* qp = ws + WS_QF + (size_t)((ph1 * 64 + pb1) * 2) * 1024;
                st16_dev<0>(qp, pq, fq[0][0]); st16_dev<128>(qp, pq, fq[0][1]); st16_dev<2048>(qp, pq, fq[1][0]); st16_dev<2176>(qp, pq, fq[1][1]);
            }
            if (wr == 0) PUB_KV();
#pragma unroll
            for (int mb = 0; mb < 2; ++mb) {
                const unsigned a = lds0 + hd * 2 * K2_SLOT + (wr * 2 + mb) * 4096, aq = lds0 + K2_QX + hd * 16384 + (wr * 2 + mb) * 2048 + pq;
                ldswr128(a + pq, fk[mb][0]); ldswr128(a + pq + 128, fk[mb][1]); ldswr128(a + 2048 + pv, fv[mb][0]); ldswr128(a + 2304 + pv, fv[mb][1]);
                if (hd == 0 || g == 0) { ldswr128(aq, fq[mb][0]); ldswr128(aq + 128, fq[mb][1]); }
            }
            BARRIER_L();
            if (g == 0) PRECOMPUTE(false);
            if (wr == 0 || hd == 1) {
                drain();
                if (lane == 0) { if (wr == 0) flag_set(ws + WS_FK, own, tag); if (hd == 1) flag_set(ws + WS_FQ, own, tag); }
            }
            if (wr == 1) PUB_KV();
            const unsigned qa = lbase + K2_QX + (1 - g) * 16384 + wid * 2048, cx = lbase + (1 - g) * 2 * K2_SLOT;
            pub0 = cat16(fk[0][0], fk[0][1], fv[0][0], fv[0][1]); pub1 = cat16(fk[1][0], fk[1][1], fv[1][0], fv[1][1]);
            { const bf16x8 q0 = ldsrd128(qa), q1 = ldsrd128(qa + 1024); FIRST_TILE(cx, q0, q1); }
            asm volatile(ATT_L4 : L6_OUT : "{v[16:31]}"(negm), "{v174}"(sidx), "{v112}"(cx) : ATT_TMP, "memory");
            if (g == 1) {
                const int b1 = (int)(tabf & 63), b0 = RBLK(0, wid - 4);
                flag_wait(ws, fk0 + (((lane & 1) ? b1 : b0) >> 1) * FS, tag);
                glds4k_dev(kv + b1 * 4096, l16, dbeg + K2_SLOT);
                glds4k_dev(kv + b0 * 4096, l16, dbeg + K2_SLOT - 16384);
                glds16_dev(ws, c_fl, fld);
            } else if (wr == 1) { drain(); if (lane == 0) flag_set(ws + WS_FK, own, tag); }
            asm volatile(ATT_L2 : "+{v[0:15]}"(o), "+{v[32:47]}"(sa), "+{v[64:79]}"(qol), "+{v[80:95]}"(kvf), "+{v[104:111]}"(ph) : "{v[16:31]}"(negm), "{v174}"(sidx), "{v112}"(cx) : ATT_TMP, "memory");
            if (g == 0) {
                asm volatile(ATT_XT : "+{v[0:15]}"(o), "+{v[32:47]}"(sa), "+{v[64:79]}"(qol), "+{v[80:95]}"(kvf), "+{v[104:111]}"(ph) : "{v[16:31]}"(negm), "{v174}"(sidx), "{v112}"(cx) : ATT_TMP, "memory");
                st16_dev<-2048>(ws, xrec0 + l16, quad(o, 0)); st16_dev<-1024>(ws, xrec0 + l16, quad(o, 1)); st16_dev<0>(ws, xrec0 + l16, quad(o, 2)); st16_dev<1024>(ws, xrec0 + l16, quad(o, 3));
                st16_dev<2048>(ws, xrec0 + l16, u32x4{__float_as_uint(__uint_as_float(qol[12]) + __uint_as_float(qol[13])), __float_as_uint(negm[0]), 0u, 0u});
                const bf16x8 q0 = ldsrd128(lbase + K2_QX + wid * 2048), q1 = ldsrd128(lbase + K2_QX + wid * 2048 + 1024);
                FIRST_TILE(lbase, q0, q1);
                u32x16 h0; u32x8 h1;
                L6C(0, lbase);
            }
#undef PUB_KV
        } else {
            if (wr == 3 && mt < 7) {
                const char* p = ws + WS_HALO + (size_t)(((mt + 1) * 32 + ph1) * 6) * 1024;
                st16_dev<0>(p, pq, fk[0][0]); st16_dev<128>(p, pq, fk[0][1]); st16_dev<3072>(p, pq, fk[1][0]); st16_dev<3200>(p, pq, fk[1][1]);
                if (lane >= 32) { st16_dev<1024>(p, pv, fv[0][0]); st16_dev<1280>(p, pv, fv[0][1]); st16_dev<1024>(p + 3072, pv, fv[1][0]); st16_dev<1280>(p + 3072, pv, fv[1][1]); }
            }
            const unsigned ex = lds0 + 2 * K2_SLOT + wid * 12288;
#pragma unroll
            for (int mb = 0; mb < 2; ++mb) {
                const unsigned a = ex + mb * 6144;
                ldswr128(a + pq, fq[mb][0]); ldswr128(a + pq + 128, fq[mb][1]); ldswr128(a + 2048 + pq, fk[mb][0]); ldswr128(a + 2176 + pq, fk[mb][1]);
                ldswr128(a + 4096 + pv, fv[mb][0]); ldswr128(a + 4352 + pv, fv[mb][1]);
            }
            if (g == 0) PRECOMPUTE(true);
            BARRIER_L();
            if (wr == 3 && mt < 7) { drain(); if (lane == 0) flag_set(ws + WS_FH, (ph1 * 8 + mt + 1) * FS, tag); }
            LocalPart lp[2];
            bf16x8 bq[2][2];
#pragma unroll
            for (int mb = 0; mb < 2; ++mb) { bq[mb][0] = ldsrd128(ex + mb * 6144 + l16); bq[mb][1] = ldsrd128(ex + mb * 6144 + 1024 + l16); }
#define LOCAL_BLOCK(mb) do { const unsigned a_ = ex + mb * 6144 + l16; \
                const bf16x8 k0_ = ldsrd128(a_ + 2048), k1_ = ldsrd128(a_ + 3072), v0_ = ldsrd128(a_ + 4096), v1_ = ldsrd128(a_ + 5120); \
                if (wr > 0) local_own<true>(bq[mb][0], bq[mb][1], k0_, k1_, v0_, v1_, ldsrd128(a_ - 24576 + 2048), ldsrd128(a_ - 24576 + 3072), ldsrd128(a_ - 24576 + 5120), band, lane, lp[mb]); \
                else local_own<false>(bq[mb][0], bq[mb][1], k0_, k1_, v0_, v1_, k0_, k0_, k0_, band, lane, lp[mb]); \
                if (wr > 0 || mt == 0) local_fin(lp[mb]); } while (0)
            LOCAL_BLOCK(0);
            const int hl = (wid < 2) ? mt : 0;
            const unsigned fh = (unsigned)WS_FH + (ph1 * 8 + mt) * FS;
            const int b0 = (int)(tabf & 63), b1 = (int)((tabf >> 6) & 63);
            const int q = lane & 3;
            unsigned fa0 = fk0 - 1024 * FS + (blk >> 1) * FS, fa1 = fk0 + (b0 >> 1) * FS, fa2 = fk0 + (b1 >> 1) * FS, fa3 = fh;
            asm volatile("" : "+s"(fa0), "+s"(fa1), "+s"(fa2), "+s"(fa3));
            const unsigned lo_ = (q & 1) ? fa1 : fa0, hi_ = (q & 1) ? fa3 : fa2;
            const u64 fm = flag_wait_some(ws, (q & 2) ? hi_ : lo_, tag, 3);
            glds4k_dev(kv + b0 * 4096, l16, dbeg);
            const char* qp = ws + WS_QF + (size_t)((head * 64 + blk) * 2) * 1024;
            glds16_dev(qp, l16, ex); glds16_dev(qp + 1024, l16, ex + 1024);
            if (fm & 4) glds4k_dev(kv + b1 * 4096, l16, dbeg + K2_SLOT);
            LOCAL_BLOCK(1);
#undef LOCAL_BLOCK
            if (fm & 4) WAITBAR_(4);
            else {
                WAITBAR_(0);
                flag_wait(ws, fk0 + (b1 >> 1) * FS, tag);
                glds4k_dev(kv + b1 * 4096, l16, dbeg + K2_SLOT);
            }
            const bf16x8 q0 = ldsrd128(ex + l16), q1 = ldsrd128(ex + 1024 + l16);
            if (hl && !(fm & 8)) flag_wait(ws, fh, tag);
            FIRST_TILE(lbase, q0, q1);
            u32x16 h0; u32x8 h1;
            L6C(hl, lbase);
            if (hl) {
                local_prev(bq[0][0], bq[0][1], asbf(u32x4{h0[0], h0[1], h0[2], h0[3]}), asbf(u32x4{h0[4], h0[5], h0[6], h0[7]}), asbf(u32x4{h0[8], h0[9], h0[10], h0[11]}), lane, lp[0]);
                local_prev(bq[1][0], bq[1][1], asbf(u32x4{h0[12], h0[13], h0[14], h0[15]}), asbf(u32x4{h1[0], h1[1], h1[2], h1[3]}), asbf(u32x4{h1[4], h1[5], h1[6], h1[7]}), lane, lp[1]);
                local_fin(lp[0]); local_fin(lp[1]);
            }
            const char* op = ws + WS_OF + (size_t)(((64 + pb1) * 32 + ph1) * 2) * 1024;
            st16_dev<0>(op, l16, quad(lp[0].o, 0)); st16_dev<1024>(op, l16, quad(lp[0].o, 1)); st16_dev<0>(op + 65536, l16, quad(lp[1].o, 0)); st16_dev<1024>(op + 65536, l16, quad(lp[1].o, 1));
        }
#undef FIRST_TILE
#undef L6_OUT
#undef L6C
        FRESH();
        l16 = lane * 16;
        const int isx = br & (1 - g);
        unsigned cnt = 7, dst = dbeg + 2 * K2_SLOT, nx = K2_SLOT;
        unsigned mode = isx << 1;
        const unsigned late = (1 - br) & g;
        const unsigned act = 2u | (isx * 12u) | ((br | (1 - g)) << 5) | (late << 6), hk = 32u | (late << 7);
        const u64 tab = tabf >> (6 + 6 * br);
        u32x8 adr = {lds0 + l16, lds0 + l16 + K2_SLOT, l16, lds0 + l16, c_wb + wid * 16384, (c_wb ^ 128) + wid * 16384 + 7168,
                     br ? (unsigned)WS_FO + ((64 + pb1 + (lane & 1)) * 32 + ph1) * FS : fxo, c_tw};
        const unsigned kvrec = (unsigned)WS_KV + (ph1 * 64 + pb1) * 4096 + 3072, pv2 = c_pv;
        f32x16 xo; f32x2 xl;
        asm volatile(ATT_MAIN
                     : "+{v[0:15]}"(o), "+{v[32:47]}"(sa), "+{v[64:79]}"(qol), "+{v[80:95]}"(kvf), "+{v[104:111]}"(ph), "+{v[112:119]}"(adr), "={v[122:137]}"(xo), "={v[138:139]}"(xl),
                       [cnt] "+s"(cnt), [dst] "+s"(dst), [nx] "+s"(nx), [mode] "+s"(mode)
                     : "{v[16:31]}"(negm), "{v[174:175]}"(u32x2{sidx, (unsigned)WS_FK + own}), "{v[140:143]}"(u32x4{(unsigned)tag, (unsigned)(tag >> 32), fxo, xrec0 + l16}),
                       "{v[170:173]}"(u32x4{c_fl, kvrec + pv2 + (lane & 8) * 16, kvrec + pv2, fld + l16}), "{v[176:191]}"(pub0), "{v[192:207]}"(pub1),
                       [srclo] "s"((unsigned)(uintptr_t)kv), [srchi] "s"((unsigned)((uintptr_t)kv >> 32)), [dbeg] "s"(dbeg), [dend] "s"(dbeg + 3 * K2_SLOT),
                       [wo01] "s"(wo + (size_t)k30 * 131072), [wo23] "s"(wo + (size_t)((k30 + 2) & 15) * 131072), [wo] "s"(wo), [wdst] "s"(__builtin_amdgcn_readfirstlane(lds0 + 98304 + wid * 2048)), [act] "s"(act), [hk] "s"(hk), [tab] "s"(tab), [k0] "s"(br), [ws] "s"(ws)
                     : ATT_TMP, "v120", "v121", "v168", "v169", "s40", "s41", "s42", "s43", "s44", "s45", "s46", "s47", "s48", "s49", "s50", "s51", "vcc", "scc", "memory");
        FRESH();
        l16 = lane * 16;
        const unsigned xrec = xrec0 + l16;
        const bf16x8 q0 = asbf(u32x4{qol[0], qol[1], qol[2], qol[3]}), q1 = asbf(u32x4{qol[4], qol[5], qol[6], qol[7]});
        const unsigned one2 = (((lane >> 4) & 1) == ((lane >> 2) & 1)) ? 0x3F803F80u : 0u;
        const u32x4 ones = {one2, one2, one2, one2};
        float lsum = __uint_as_float(qol[12]) + __uint_as_float(qol[13]);
        bool ok = lsum < 1.0e18f;
        if (isx) {
            if (__builtin_expect(!(mode & 4), 0)) {
                flag_wait(ws, fxo, tag);
                f32x4 a0, a1, a2, a3;
                asm volatile("global_load_dwordx4 %0, %5, %6 offset:-2048 sc1\n\tglobal_load_dwordx4 %1, %5, %6 offset:-1024 sc1\n\tglobal_load_dwordx4 %2, %5, %6 sc1\n\t"
                             "global_load_dwordx4 %3, %5, %6 offset:1024 sc1\n\tglobal_load_dwordx2 %4, %5, %6 offset:2048 sc1\n\ts_waitcnt vmcnt(0)"
                             : "=&v"(a0), "=&v"(a1), "=&v"(a2), "=&v"(a3), "=&v"(xl) : "v"(xrec), "s"(ws) : "memory");
#pragma unroll
                for (int r = 0; r < 4; ++r) { xo[r] = a0[r]; xo[4 + r] = a1[r]; xo[8 + r] = a2[r]; xo[12 + r] = a3[r]; }
            }
            const float nm = __builtin_fminf(negm[0], xl[1]), fa = EXP2(nm - negm[0]), fx = EXP2(nm - xl[1]);
            ok = ok && (xl[0] < 1.0e18f);
            lsum = lsum * fa + xl[0] * fx;
#pragma unroll
            for (int r = 0; r < 16; ++r) o[r] = o[r] * fa + xo[r] * fx;
        }
#pragma unroll
        for (int r = 0; r < 16; ++r) ok = ok && (__builtin_fabsf(o[r]) < 3.0e38f);
        if (__builtin_expect(__any(!ok), 0)) {
            float m = -__builtin_inff();
            f32x4 l;
            for (int pass = 0; pass < 2; ++pass) {
                o = z; l = f32x4{};
                for (int t = 0; t < 64; ++t) {
                    bf16x8 f0, f1, f2, f3;
                    asm volatile("global_load_dwordx4 %0, %4, %5 sc1\n\tglobal_load_dwordx4 %1, %4, %5 offset:1024 sc1\n\tglobal_load_dwordx4 %2, %4, %5 offset:2048 sc1\n\t"
                                 "global_load_dwordx4 %3, %4, %5 offset:3072 sc1\n\ts_waitcnt vmcnt(0)" : "=&v"(f0), "=&v"(f1), "=&v"(f2), "=&v"(f3) : "v"(l16 + t * 4096), "s"(kv) : "memory");
                    f32x16 s = MFMA32(f0, q0, z); s = MFMA32(f1, q1, s);
                    if (pass == 0) {
#pragma unroll
                        for (int r = 0; r < 16; ++r) m = __builtin_fmaxf(m, s[r]);
                    } else {
#pragma unroll
                        for (int r = 0; r < 16; ++r) s[r] = EXP2(s[r] - m);
                        const bf16x8 p0 = asbf(pack8(s, 0)), p1 = asbf(pack8(s, 1));
                        o = MFMA32(f2, p0, o); o = MFMA32(f3, p1, o);
                        l = MFMA16(asbf(ones), p0, l); l = MFMA16(asbf(ones), p1, l);
                    }
                }
                if (pass == 0) m = swap_max(m);
            }
            lsum = l[0];
        }
        const float inv = __builtin_amdgcn_rcpf(lsum);
#pragma unroll
        for (int r = 0; r < 16; ++r) o[r] *= inv;
        const char* op = ws + WS_OF + (size_t)((blk * 32 + head) * 2) * 1024;
        st16_dev<0>(op, l16, pack8(o, 0)); st16_dev<1024>(op, l16, pack8(o, 1));
    }
    {
        FRESH();
        const int g = wid >> 2, wi = wid & 3, wm = wi >> 1, wn = wi & 1, g4 = lane >> 4;
        const char* of = ws + WS_OF + (size_t)(br * 64 + mt3 * 4 + wi) * 65536;
        const unsigned vw4[4] = {c_wb + wi * 32768, (c_wb ^ 128) + wi * 32768 + 7168, c_wb + wi * 32768 + 14336, (c_wb ^ 128) + wi * 32768 + 21504};
        const unsigned l16 = lane * 16, sdst = lds0 + wi * 4096;
#define STAGE_O(t) glds4k_dev(of + (((t) + k30) & 15) * 4096, l16, sdst + ((t) & 3) * 16384)
#define STAGE_WO(t, slot) do { const char* s_ = wo + (size_t)(((t) + k30) & 15) * 131072; glds_run<0>(s_, vw4[0], sdst + (slot)); glds_run<1>(s_, vw4[1], 0); glds_run<2>(s_, vw4[2], 0); glds_run<3>(s_, vw4[3], 0); } while (0)
        const unsigned fo = c_fo;
        drain();
        if (lane == 0) flag_set(ws + WS_FO, ((mt * 8 + wid) * 32 + 2 * hp + br) * FS, tag);
        if (b == 0 && wid == 0) {
            const unsigned fa = (unsigned)WS_FK + ((2 * (lane >> 3)) * 32 + (lane & 7) * 4) * FS, fb = (unsigned)WS_FO + ((64 + (lane & 7) * 8) * 32 + 2 * (lane >> 3)) * FS;
            const u64 t0 = __builtin_amdgcn_s_memrealtime();
            for (;;) {
                u64 v0, v1, v2, v3;
                asm volatile("global_load_dwordx2 %0, %4, %8 sc1\n\tglobal_load_dwordx2 %1, %5, %8 sc1\n\tglobal_load_dwordx2 %2, %6, %8 sc1\n\tglobal_load_dwordx2 %3, %7, %8 sc1\n\ts_waitcnt vmcnt(0)"
                             : "=&v"(v0), "=&v"(v1), "=&v"(v2), "=&v"(v3) : "v"(fa), "v"(fa + 16 * 32 * FS), "v"(fb), "v"(fb + 16 * FS), "s"(ws0) : "memory");
                if (__all(v0 == tag && v1 == tag && v2 == tag && v3 == tag) || __builtin_amdgcn_s_memrealtime() - t0 > 2000000ull) break;
                __builtin_amdgcn_s_sleep(SLP);
            }
            if (lane == 0) flag_set(ws0, (unsigned)WS_EP, tag);
        }
        asm volatile("s_barrier" ::: "memory");
        const u64 fm = flag_wait_some(ws, fo, tag, (3ull << (2 * nt3)) | (g ? 3ull << (2 * ((nt3 + 1) & 7)) : 0ull));
        STAGE_O(1 - g);
        if (g == 1) STAGE_O(2);
        STAGE_WO(5 - g, 65536 + (1 - g) * 16384);
        if (fm != ~0ull) flag_wait(ws, fo, tag);
        unsigned ao = lds0 + g * 16384 + wm * 8192 + c_pv;
        unsigned aw[4] = {c_aw, c_aw ^ 32, c_aw ^ 64, c_aw ^ 96};
        const char* so = of + ((3 + g + k30) & 15) * 4096; const char* sw = wo + (size_t)((5 + g + k30) & 15) * 131072;
        unsigned dob = sdst + ((3 + g) & 3) * 16384, dwb = sdst + p3_wslot(6);
        f32x4 acc[4][4];
#pragma unroll
        for (int mb = 0; mb < 4; ++mb)
#pragma unroll
            for (int nb = 0; nb < 4; ++nb) acc[mb][nb] = f32x4{};
        if (g == 1) { WAITBAR_(8); asm volatile("s_barrier" ::: "memory"); } else asm volatile("s_barrier" ::: "memory");
        SBAR();
#pragma unroll
        for (int u = 0; u < 8; ++u) {
            bf16x8 of_[4][2], wf[4][2];
            const bool doo = u < 6 || (u == 6 && g == 0), dow = (u >= 1 || g == 1) && (u < 5 || (u == 5 && g == 0));
#define P3RD(i) do { of_[(i) & 3][(i) >> 2] = ldsrd128(ao + (((i) & 3) >> 1) * 4096 + ((i) & 1) * 256 + ((i) >> 2) * 2048); \
                wf[(i) & 3][(i) >> 2] = cat8(ldstr(aw[(i) & 3] + ((i) >> 2) * 8192), ldstr(aw[(i) & 3] + ((i) >> 2) * 8192 + 2048)); } while (0)
            P3RD(0); if (u < 7 && doo) glds_run_dev<0>(so, l16, dob);
            P3RD(1); if (u < 7 && doo) glds_run_dev<1>(so, l16, 0);
            P3RD(2); if (u < 7 && doo) glds_run_dev<2>(so, l16, 0);
            P3RD(3); if (u < 7 && doo) glds_run_dev<3>(so, l16, 0);
            P3RD(4); if (u < 6 && dow) glds_run<0>(sw, vw4[0], dwb);
            P3RD(5); if (u < 6 && dow) glds_run<1>(sw, vw4[1], 0);
            P3RD(6); if (u < 6 && dow) glds_run<2>(sw, vw4[2], 0);
            P3RD(7); if (u < 6 && dow) glds_run<3>(sw, vw4[3], 0);
#undef P3RD
            SBAR();
            if (u < 7) {
                { const int to = 2 * u + 5 + g; so = of + ((to + k30) & 15) * 4096; sw = wo + (size_t)((to + 2 + k30) & 15) * 131072;
                  dob = sdst + (to & 3) * 16384; dwb = sdst + (g ? p3_wslot(2 * u + 8) : p3_wslot(2 * u + 7)); }
                ao ^= 32768;
#pragma unroll
                for (int q = 0; q < 4; ++q) aw[q] += p3_wslot(2 * u + 2) - p3_wslot(2 * u);
                asm volatile("" : "+v"(ao), "+v"(aw[0]), "+v"(aw[1]), "+v"(aw[2]), "+v"(aw[3]), "+s"(so), "+s"(sw), "+s"(dob), "+s"(dwb));
            }
            if (p3_allow(2 * u) == p3_allow(2 * u + 1)) WAITBAR(p3_allow(2 * u)); else if (g == 0) WAITBAR(p3_allow(2 * u)); else WAITBAR(p3_allow(2 * u + 1));
            SBAR();
            __builtin_amdgcn_s_setprio(1);
#pragma unroll
            for (int mb = 0; mb < 4; ++mb)
#pragma unroll
                for (int nb = 0; nb < 4; ++nb) { acc[mb][nb] = MFMA16(wf[nb][0], of_[mb][0], acc[mb][nb]); acc[mb][nb] = MFMA16(wf[nb][1], of_[mb][1], acc[mb][nb]); CHAIN();
                }
            __builtin_amdgcn_s_setprio(0);
            SBAR();
            if (u < 7 || g == 0) asm volatile("s_barrier" ::: "memory");
            SBAR();
        }
        FRESH();
        const unsigned xw = lds0 + wid * 8192 + l16, xr = lds0 + (wid ^ 4) * 8192 + l16;
#pragma unroll
        for (int i = 0; i < 8; ++i) { const f32x4 v = g ? acc[i >> 2][i & 3] : acc[2 + (i >> 2)][i & 3]; ldswr128(xw + i * 1024, __builtin_bit_cast(u32x4, v)); }
        BARRIER_L();
        const unsigned st = lds0 + 65536 + wid * 4096;
#pragma unroll
        for (int i = 0; i < 8; ++i) {
            const f32x4 v = (g ? acc[2 + (i >> 2)][i & 3] : acc[i >> 2][i & 3]) + __builtin_bit_cast(f32x4, ldsrd128(xr + i * 1024));
            const int row = 16 * (i >> 2) + (lane & 15), c = 2 * (i & 3) + (g4 >> 1);
            *(LAS u32x2*)(uintptr_t)(st + row * 128 + ((c ^ (row & 7)) << 4) + (g4 & 1) * 8) = u32x2{cvtpk(v[0], v[1]), cvtpk(v[2], v[3])};
        }
        const int blk = mt3 * 4 + 2 * wm + g;
        char* op = (char*)out + br * 2048 + nt3 * 256 + wn * 128 + (lane & 7) * 16;
#pragma unroll
        for (int i = 0; i < 4; ++i) {
            const int r = 8 * i + (lane >> 3), row = br ? ((blk >> 1) * 64 + 2 * r + (blk & 1)) : (blk * 32 + r);
            st16_dev<0>(out, (unsigned)(row * 4096 + br * 2048 + nt3 * 256 + wn * 128 + (lane & 7) * 16), __builtin_bit_cast(u32x4, ldsrd128(st + r * 128 + (((lane & 7) ^ (r & 7)) << 4))));
        }
#undef STAGE_O
#undef STAGE_WO
    }
    asm volatile(".Lmk_code_end:");
}

extern "C" void kernel_launch(void* const* d_in, const int* in_sizes, int n_in, void* d_out, int out_size, void* d_ws, size_t ws_size, hipStream_t stream) {
    mk_fused<<<256, 512, 0, stream>>>((const u16*)d_in[0], (const u16*)d_in[1], (const u16*)d_in[2], (const u16*)d_in[3], (const u16*)d_in[4], (const u16*)d_in[5], (char*)d_ws, (u16*)d_out);
}
```
